```python
import torch

def _softmax(x, axis=-1):
    return torch.softmax(x, dim=axis)
def _gelu(x):
    return torch.nn.functional.gelu(x, approximate="tanh")
def _silu(x):
    return torch.nn.functional.silu(x)
def _sigmoid(x):
    return torch.sigmoid(x)
def _layernorm(x, axis=-1, eps=1e-5):
    m = x.mean(dim=axis, keepdim=True)
    v = x.var(dim=axis, keepdim=True, unbiased=False)
    return (x - m) / torch.sqrt(v + eps)
def _scatter_add(tgt, idx, src, axis=0):
    return tgt.clone().index_add_(axis, idx, src)
def _segment_sum(src, seg, n):
    out = torch.zeros((n,) + src.shape[1:], dtype=src.dtype)
    return out.index_add_(0, seg, src)
def _pad(x, pw):
    flat = tuple(v for pair in reversed(pw) for v in pair)
    return torch.nn.functional.pad(x, flat)

kernel_name = 'sequential_preLN__sliding_block__glu_gated_swiglu__prompt'

def setup_inputs():

    _w = (lambda a: (lambda t: t.to(torch.bfloat16) if t.is_floating_point() else t)(torch.as_tensor(a)))
    def _A_setup():
        B, S, D = 1, 2048, 512
        return {
            "x": _w((lambda *s: torch.randn(*s, dtype=torch.bfloat16) * 0.05)(B, S, D)),
            "wqkv": _w((lambda *s: torch.randn(*s, dtype=torch.bfloat16) * 0.05)(D, 3 * D)),
            "wo": _w((lambda *s: torch.randn(*s, dtype=torch.bfloat16) * 0.05)(D, D)),
        }
    _A_in = _A_setup()

    def _B_setup():
        B, S, D, F = 1, 2048, 512, 1792
        return {
            "x": _w((lambda *s: torch.randn(*s, dtype=torch.bfloat16) * 0.05)(B, S, D)),
            "w1": _w((lambda *s: torch.randn(*s, dtype=torch.bfloat16) * 0.8)(D, F) * (2.0 / D) ** 0.5),
            "w3": _w((lambda *s: torch.randn(*s, dtype=torch.bfloat16) * 0.8)(D, F) * (2.0 / D) ** 0.5),
            "w2": _w((lambda *s: torch.randn(*s, dtype=torch.bfloat16) * 0.8)(F, D) * (2.0 / F) ** 0.5),
        }
    _B_in = _B_setup()

    return {
        "A_x": _A_in["x"],
        "A_wqkv": _A_in["wqkv"],
        "A_wo": _A_in["wo"],
        "B_x": _B_in["x"],
        "B_w1": _B_in["w1"],
        "B_w3": _B_in["w3"],
        "B_w2": _B_in["w2"],
    }

def reference(A_x, A_wqkv, A_wo, B_x, B_w1, B_w3, B_w2):
    def _A_ref(x, wqkv, wo):
        B, S, D = x.shape
        H = 32; Dh = D // H; W = 256
        qkv = (lambda x, *d: x.permute(*d))((x @ wqkv).reshape(B, S, 3, H, Dh), 2, 0, 3, 1, 4)
        q, k, v = qkv[0], qkv[1], qkv[2]
        scores = torch.einsum("bhqd,bhkd->bhqk", q, k) * (Dh ** -0.5)
        idx = torch.arange(S)
        mask = (idx[:, None] - idx[None, :] >= 0) & (idx[:, None] - idx[None, :] < W)
        scores = torch.where(mask[None, None], scores, -1e9)
        p = _softmax(scores, axis=-1)
        o = torch.einsum("bhqk,bhkd->bhqd", p, v)
        return (lambda x, *d: x.permute(*d))(o, 0, 2, 1, 3).reshape(B, S, D) @ wo

    def _B_ref(x, w1, w3, w2):
        gate = torch.einsum("bsd,df->bsf", x, w1)
        up = torch.einsum("bsd,df->bsf", x, w3)
        h = _silu(gate) * up
        out = torch.einsum("bsf,fd->bsd", h, w2)
        return out

    x = A_x
    h = x + _A_ref(_layernorm(x, axis=-1), A_wqkv, A_wo)
    h2 = h + _B_ref(_layernorm(h, axis=-1), B_w1, B_w3, B_w2)
    return h2
```

```cpp
#include <hip/hip_runtime.h>
#include <hip/hip_bf16.h>
#include <stdint.h>

typedef short bf16x8 __attribute__((ext_vector_type(8)));
typedef short s16x4 __attribute__((ext_vector_type(4)));
typedef float f32x4 __attribute__((ext_vector_type(4)));
typedef float f32x2 __attribute__((ext_vector_type(2)));
typedef unsigned u32x4 __attribute__((ext_vector_type(4)));
typedef unsigned u32x2 __attribute__((ext_vector_type(2)));
typedef __bf16 bf16x2 __attribute__((ext_vector_type(2)));
#define LAS __attribute__((address_space(3)))
#define DEV __device__ __forceinline__

constexpr int S_ = 2048, D_ = 512, H_ = 32, F_ = 1792;
constexpr float LN_EPS = 1e-5f;

DEV f32x4 mfma16(bf16x8 a, bf16x8 b, f32x4 c) { return __builtin_amdgcn_mfma_f32_16x16x32_bf16(a, b, c, 0, 0, 0); }
DEV unsigned pack2(float a, float b) {
  f32x2 v = {a, b};
  return __builtin_bit_cast(unsigned, __builtin_convertvector(v, bf16x2));
}
DEV float bflo(unsigned u) { return __builtin_bit_cast(float, u << 16); }
DEV float bfhi(unsigned u) { return __builtin_bit_cast(float, u & 0xffff0000u); }
DEV float dot2(unsigned a, unsigned b, float c) {
  return __builtin_amdgcn_fdot2_f32_bf16(__builtin_bit_cast(bf16x2, a), __builtin_bit_cast(bf16x2, b), c, false);
}
DEV void dma16(const char* gptr, unsigned ldsbase) {
  unsigned sv;
  asm volatile("s_mov_b32 %0, m0\n\ts_mov_b32 m0, %2\n\ts_nop 0\n\tglobal_load_lds_dwordx4 %1, off\n\ts_mov_b32 m0, %0"
               : "=&s"(sv) : "v"(gptr), "s"(ldsbase) : "memory");
}
template <int N> DEV void wait_bar() { asm volatile("s_waitcnt vmcnt(%0) lgkmcnt(0)\n\ts_barrier" ::"n"(N) : "memory"); }
DEV void bar() { asm volatile("s_waitcnt lgkmcnt(0)\n\ts_barrier" ::: "memory"); }
DEV bf16x8 lds128(const char* p) { return *(const LAS bf16x8*)(LAS const char*)p; }
DEV s16x4 ldstr(const char* p) { return __builtin_amdgcn_ds_read_tr16_b64_v4i16((LAS s16x4*)p); }
DEV bf16x8 cat(s16x4 a, s16x4 b) { return __builtin_shufflevector(a, b, 0, 1, 2, 3, 4, 5, 6, 7); }
DEV float xsum(float v) { v += __shfl_xor(v, 16); v += __shfl_xor(v, 32); return v; }
DEV float xmax(float v) { v = fmaxf(v, __shfl_xor(v, 16)); v = fmaxf(v, __shfl_xor(v, 32)); return v; }
DEV unsigned uni(unsigned v) { return __builtin_amdgcn_readfirstlane(v); }

template <int A, int B> struct Min { static constexpr int v = A < B ? A : B; };

template <int KT, int NK, int NS, int P, class Issue, class Compute>
DEV void pipe_steps(Issue& issue, Compute& compute) {
  if constexpr (KT < NK) {
    wait_bar<(Min<KT + NS - 1, NK>::v - KT - 1) * P>();
    if constexpr (KT + NS - 1 < NK) issue((KT + NS - 1) % NS);
    compute(KT % NS, KT);
    pipe_steps<KT + 1, NK, NS, P>(issue, compute);
  }
}

constexpr int K1_STAGE = 16384 + 12288, K1_NS = 5;
constexpr int K1_LDS = K1_STAGE * K1_NS + 1024 + 1536;

__global__ __launch_bounds__(512) void k1_qkv(const char* __restrict__ x, const char* __restrict__ w,
                                              char* __restrict__ qkv) {
  extern __shared__ __attribute__((aligned(16))) char smem[];
  const int tid = threadIdx.x, lane = tid & 63, wv = uni(tid >> 6);
  const int g = lane >> 4, i = lane & 15, q4 = (lane >> 2) & 3, p4 = lane & 3;
  const int tg = wv & 3, nh = wv >> 2;
  const int b = blockIdx.x;
  const int cb = b & 15, tb = b >> 4;
  const unsigned lds0 = (unsigned)(size_t)(LAS char*)smem;

  const char* pt;
  {
    int row = tb * 128 + wv * 16 + (lane >> 3);
    int c = (lane & 7) ^ (lane >> 3);
    pt = x + (size_t)row * 1024 + c * 16;
  }
  const char *pw0, *pw1;
  {
    int qq = wv * 64 + lane;
    int k = qq / 12, c = (qq % 12) ^ (((k >> 3) & 1) << 1);
    pw0 = w + (size_t)k * 3072 + cb * 192 + c * 16;
    qq = 512 + wv * 32 + (lane & 31);
    k = qq / 12; c = (qq % 12) ^ (((k >> 3) & 1) << 1);
    pw1 = w + (size_t)k * 3072 + cb * 192 + c * 16;
  }
  auto issue = [&](int slot) {
    unsigned base = lds0 + slot * K1_STAGE;
    dma16(pt, base + wv * 2048);
    dma16(pt + 8192, base + wv * 2048 + 1024);
    dma16(pw0, base + 16384 + wv * 1024);
    if (lane < 32) dma16(pw1, base + 16384 + 8192 + wv * 512);
    pt += 128; pw0 += 64 * 3072; pw1 += 64 * 3072;
  };
#pragma unroll
  for (int s = 0; s < K1_NS - 1; ++s) issue(s);

  f32x4 acc[2][3], cs[3];
#pragma unroll
  for (int a = 0; a < 3; ++a) { acc[0][a] = f32x4{0, 0, 0, 0}; acc[1][a] = f32x4{0, 0, 0, 0}; cs[a] = f32x4{0, 0, 0, 0}; }
  float s1 = 0.f, s2 = 0.f;
  const bf16x8 ones = {0x3F80, 0x3F80, 0x3F80, 0x3F80, 0x3F80, 0x3F80, 0x3F80, 0x3F80};
  const int toff0 = (tg * 32 + i) * 128 + ((g ^ (i & 7)) << 4);
  const int toff1 = (tg * 32 + i) * 128 + (((4 + g) ^ (i & 7)) << 4);
  int woff[3];
#pragma unroll
  for (int nt = 0; nt < 3; ++nt) woff[nt] = 16384 + (8 * g + q4) * 192 + (((nh * 3 + nt) ^ (g & 1)) << 5) + p4 * 8;

  auto compute = [&](int slot, int kt) {
    const char* sb = smem + slot * K1_STAGE;
#pragma unroll
    for (int ks = 0; ks < 2; ++ks) {
      bf16x8 B[2], A[3];
#pragma unroll
      for (int tt = 0; tt < 2; ++tt) B[tt] = lds128(sb + (ks ? toff1 : toff0) + tt * 2048);
#pragma unroll
      for (int nt = 0; nt < 3; ++nt)
        A[nt] = cat(ldstr(sb + woff[nt] + ks * 6144), ldstr(sb + woff[nt] + ks * 6144 + 768));
#pragma unroll
      for (int nt = 0; nt < 3; ++nt)
#pragma unroll
        for (int tt = 0; tt < 2; ++tt) acc[tt][nt] = mfma16(A[nt], B[tt], acc[tt][nt]);
      {
        u32x4 u = __builtin_bit_cast(u32x4, nh ? B[1] : B[0]);
#pragma unroll
        for (int j = 0; j < 4; ++j) { s1 = dot2(u[j], 0x3F803F80u, s1); s2 = dot2(u[j], u[j], s2); }
      }
      if (((kt * 2 + ks) & 3) == tg) {
#pragma unroll
        for (int nt = 0; nt < 3; ++nt) cs[nt] = mfma16(A[nt], ones, cs[nt]);
      }
    }
  };
  pipe_steps<0, 8, K1_NS, 4>(issue, compute);

  float* st = (float*)(smem + K1_STAGE * K1_NS);
  float* csp = (float*)(smem + K1_STAGE * K1_NS + 1024);
  s1 = xsum(s1); s2 = xsum(s2);
  if (g == 0) {
    float mean = s1 * (1.f / 512.f);
    float var = fmaxf(s2 * (1.f / 512.f) - mean * mean, 0.f);
    float rstd = rsqrtf(var + LN_EPS);
    *(f32x2*)(st + (tg * 32 + nh * 16 + i) * 2) = f32x2{mean, rstd};
  }
  if (i == 0) {
#pragma unroll
    for (int nt = 0; nt < 3; ++nt) *(f32x4*)(csp + tg * 96 + nh * 48 + nt * 16 + 4 * g) = cs[nt];
  }
  bar();
  f32x2 ms[2];
#pragma unroll
  for (int tt = 0; tt < 2; ++tt) ms[tt] = *(const f32x2*)(st + (tg * 32 + tt * 16 + i) * 2);
#pragma unroll
  for (int nt = 0; nt < 3; ++nt) {
    f32x4 c = f32x4{0, 0, 0, 0};
#pragma unroll
    for (int t = 0; t < 4; ++t) c += *(const f32x4*)(csp + t * 96 + nh * 48 + nt * 16 + 4 * g);
    int n0 = cb * 96 + nh * 48 + nt * 16;
    char* dst = qkv + (size_t)(n0 >> 4) * (2048 * 32) + g * 8;
#pragma unroll
    for (int tt = 0; tt < 2; ++tt) {
      f32x4 y = (acc[tt][nt] - ms[tt][0] * c) * ms[tt][1];
      u32x2 o = {pack2(y[0], y[1]), pack2(y[2], y[3])};
      *(u32x2*)(dst + (size_t)(tb * 128 + tg * 32 + tt * 16 + i) * 32) = o;
    }
  }
}

constexpr int K2_LDS = 4 * 12288;
__global__ __launch_bounds__(512) void k2_attn(const char* __restrict__ qkv, char* __restrict__ o) {
  extern __shared__ __attribute__((aligned(16))) char smem[];
  const int tid = threadIdx.x, lane = tid & 63, wv = uni(tid >> 6);
  const int g = lane >> 4, i = lane & 15, q4 = (lane >> 2) & 3, p4 = lane & 3;
  const int b = blockIdx.x;
  const int hp = b & 15, qb = b >> 4;
  const int q0 = qb * 128;
  const unsigned lds0 = (unsigned)(size_t)(LAS char*)smem;
  const size_t HS = 2048 * 32;
  {
    const int r = wv >> 1;
    const char* src = qkv + (size_t)((1 + (r >> 1)) * 32 + hp * 2 + (r & 1)) * HS;
#pragma unroll
    for (int j = 0; j < 6; ++j) {
      int ch = ((wv & 1) * 6 + j) * 64 + lane;
      int key = q0 - 256 + (ch >> 1);
      key = key < 0 ? 0 : key;
      dma16(src + (size_t)key * 32 + (ch & 1) * 16, lds0 + r * 12288 + ((wv & 1) * 6 + j) * 1024);
    }
  }
  const int hh = wv >> 2, wq = wv & 3;
  const int head = hp * 2 + hh;
  bf16x8 Q[2];
#pragma unroll
  for (int u = 0; u < 2; ++u) {
    int qs = q0 + wq * 32 + u * 16;
    u32x4 v = *(const u32x4*)(qkv + (size_t)head * HS + (size_t)(qs + i) * 32 + (g & 1) * 16);
    if (g >= 2) v = u32x4{0, 0, 0, 0};
    Q[u] = __builtin_bit_cast(bf16x8, v);
  }
  wait_bar<0>();
  const char* Kl = smem + hh * 12288;
  const char* Vl = smem + 24576 + hh * 12288;
  const float c = 0.25f * 1.4426950408889634f;
  const float NEG = -1e30f;
#pragma unroll
  for (int u = 0; u < 2; ++u) {
    const int qs = q0 + wq * 32 + u * 16;
    const int lrow = wq * 32 + u * 16;
    f32x4 s[17];
#pragma unroll
    for (int t = 0; t < 17; ++t) {
      bf16x8 A = lds128(Kl + (lrow + t * 16 + i) * 32 + (g & 1) * 16);
      s[t] = mfma16(A, Q[u], f32x4{0, 0, 0, 0});
    }
#pragma unroll
    for (int r = 0; r < 4; ++r) {
      s[0][r] = (4 * g + r > i) ? s[0][r] : NEG;
      s[16][r] = (4 * g + r <= i) ? s[16][r] : NEG;
    }
    if (qs < 256) {
      const int nneg = (256 - qs) >> 4;
#pragma unroll
      for (int t = 0; t < 16; ++t)
        if (t < nneg) s[t] = f32x4{NEG, NEG, NEG, NEG};
    }
    float m = s[16][0];
#pragma unroll
    for (int t = 0; t < 17; ++t)
#pragma unroll
      for (int r = 0; r < 4; ++r) m = fmaxf(m, s[t][r]);
    m = xmax(m);
    const float mc = m * c;
    float sum = 0.f;
    unsigned pk[17][2];
#pragma unroll
    for (int t = 0; t < 17; ++t) {
      float e0 = __builtin_amdgcn_exp2f(s[t][0] * c - mc), e1 = __builtin_amdgcn_exp2f(s[t][1] * c - mc);
      float e2 = __builtin_amdgcn_exp2f(s[t][2] * c - mc), e3 = __builtin_amdgcn_exp2f(s[t][3] * c - mc);
      sum += (e0 + e1) + (e2 + e3);
      pk[t][0] = pack2(e0, e1); pk[t][1] = pack2(e2, e3);
    }
    sum = xsum(sum);
    f32x4 oacc = f32x4{0, 0, 0, 0};
#pragma unroll
    for (int tp = 0; tp < 9; ++tp) {
      const int t0 = 2 * tp, t1 = (2 * tp + 1 < 17) ? 2 * tp + 1 : 16;
      bf16x8 A = cat(ldstr(Vl + (lrow + t0 * 16 + 4 * g + q4) * 32 + p4 * 8), ldstr(Vl + (lrow + t1 * 16 + 4 * g + q4) * 32 + p4 * 8));
      u32x4 pb = {pk[t0][0], pk[t0][1], (2 * tp + 1 < 17) ? pk[t1][0] : 0u, (2 * tp + 1 < 17) ? pk[t1][1] : 0u};
      oacc = mfma16(A, __builtin_bit_cast(bf16x8, pb), oacc);
    }
    const float inv = 1.f / sum;
    u32x2 ov = {pack2(oacc[0] * inv, oacc[1] * inv), pack2(oacc[2] * inv, oacc[3] * inv)};
    *(u32x2*)(o + (size_t)(qs + i) * 1024 + head * 32 + g * 8) = ov;
  }
}

constexpr int G_STAGE = 16384;
template <int NK, int NS, bool STATS>
__global__ __launch_bounds__(512) void k_gemm64(const char* __restrict__ a, const char* __restrict__ w,
                                                const char* __restrict__ resid, char* __restrict__ out,
                                                float* __restrict__ stats) {
  extern __shared__ __attribute__((aligned(16))) char smem[];
  const int tid = threadIdx.x, lane = tid & 63, wv = uni(tid >> 6);
  const int g = lane >> 4, i = lane & 15, q4 = (lane >> 2) & 3, p4 = lane & 3;
  const int tg = wv & 1, ng = (wv >> 1) & 1, kp = wv >> 2;
  const int b = blockIdx.x;
  const int cb = b & 7, tb = b >> 3;
  const unsigned lds0 = (unsigned)(size_t)(LAS char*)smem;
  constexpr int LDA = NK * 128;
  const char* pt = a + (size_t)(tb * 64 + wv * 8 + (lane >> 3)) * LDA + (((lane & 7) ^ (lane >> 3)) << 4);
  const char* pw;
  {
    int k = wv * 8 + (lane >> 3), cc = lane & 7;
    int unit = (cc >> 1) ^ (((k >> 1) & 1) | (((k >> 3) & 1) << 1));
    pw = w + (size_t)k * 1024 + cb * 128 + unit * 32 + (cc & 1) * 16;
  }
  auto issue = [&](int slot) {
    unsigned base = lds0 + slot * G_STAGE;
    dma16(pt, base + wv * 1024);
    dma16(pw, base + 8192 + wv * 1024);
    pt += 128; pw += 64 * 1024;
  };
#pragma unroll
  for (int s = 0; s < (NS - 1 < NK ? NS - 1 : NK); ++s) issue(s);
  f32x4 acc[2][2];
#pragma unroll
  for (int t = 0; t < 2; ++t) { acc[t][0] = f32x4{0, 0, 0, 0}; acc[t][1] = f32x4{0, 0, 0, 0}; }
  const int toff = (tg * 32 + i) * 128 + (((kp * 4 + g) ^ (i & 7)) << 4);
  int woff[2];
#pragma unroll
  for (int nt = 0; nt < 2; ++nt)
    woff[nt] = 8192 + (kp * 32 + 8 * g + q4) * 128 + (((ng * 2 + nt) ^ (((q4 >> 1) & 1) | ((g & 1) << 1))) << 5) + p4 * 8;
  auto compute = [&](int slot, int) {
    const char* sb = smem + slot * G_STAGE;
    bf16x8 B[2], A[2];
#pragma unroll
    for (int tt = 0; tt < 2; ++tt) B[tt] = lds128(sb + toff + tt * 2048);
#pragma unroll
    for (int nt = 0; nt < 2; ++nt) A[nt] = cat(ldstr(sb + woff[nt]), ldstr(sb + woff[nt] + 512));
#pragma unroll
    for (int nt = 0; nt < 2; ++nt)
#pragma unroll
      for (int tt = 0; tt < 2; ++tt) acc[tt][nt] = mfma16(A[nt], B[tt], acc[tt][nt]);
  };
  if constexpr (NK <= NS) {
    pipe_steps<0, NK, NS, 2>(issue, compute);
  } else {
    static_assert(NK % NS == 0, "");
    for (int it = 0; it < NK / NS - 1; ++it) {
#pragma unroll
      for (int s = 0; s < NS; ++s) {
        wait_bar<(NS - 2) * 2>();
        issue((s + NS - 1) % NS);
        compute(s, 0);
      }
    }
    pipe_steps<NK - NS, NK, NS, 2>(issue, compute);
  }
  bar();
  {
    float* xb = (float*)smem;
    f32x4* mine = (f32x4*)xb + wv * 128;
#pragma unroll
    for (int nt = 0; nt < 2; ++nt) mine[nt * 64 + lane] = kp ? acc[0][nt] : acc[1][nt];
    bar();
    const f32x4* oth = (const f32x4*)xb + (wv ^ 4) * 128;
    const int token = tb * 64 + tg * 32 + kp * 16 + i;
    float s1 = 0.f, s2 = 0.f;
#pragma unroll
    for (int nt = 0; nt < 2; ++nt) {
      f32x4 v = (kp ? acc[1][nt] : acc[0][nt]) + oth[nt * 64 + lane];
      const size_t off = (size_t)token * 1024 + (cb * 64 + ng * 32 + nt * 16 + 4 * g) * 2;
      u32x2 r = *(const u32x2*)(resid + off);
      v[0] += bflo(r[0]); v[1] += bfhi(r[0]); v[2] += bflo(r[1]); v[3] += bfhi(r[1]);
      u32x2 ov = {pack2(v[0], v[1]), pack2(v[2], v[3])};
      *(u32x2*)(out + off) = ov;
      if constexpr (STATS) {
        s1 = dot2(ov[0], 0x3F803F80u, s1); s1 = dot2(ov[1], 0x3F803F80u, s1);
        s2 = dot2(ov[0], ov[0], s2); s2 = dot2(ov[1], ov[1], s2);
      }
    }
    if constexpr (STATS) {
      s1 = xsum(s1); s2 = xsum(s2);
      if (g == 0) *(f32x2*)(stats + ((size_t)token * 16 + cb * 2 + ng) * 2) = f32x2{s1, s2};
    }
  }
}

constexpr int K4_STAGE = 16384 + 28672, K4_NS = 3;
constexpr int K4_LDS = K4_STAGE * K4_NS + 4 * 224 * 4;

template <int NT, class Issue>
DEV void k4_body(char* smem, Issue& issue, int tg, int half, int lane, const f32x2* ms, char* __restrict__ hh, int tok0, int col0) {
  const int g = lane >> 4, i = lane & 15, q4 = (lane >> 2) & 3, p4 = lane & 3;
  f32x4 acc[2][2 * NT], cs[2 * NT];
#pragma unroll
  for (int a = 0; a < 2 * NT; ++a) { acc[0][a] = f32x4{0, 0, 0, 0}; acc[1][a] = f32x4{0, 0, 0, 0}; cs[a] = f32x4{0, 0, 0, 0}; }
  const bf16x8 ones = {0x3F80, 0x3F80, 0x3F80, 0x3F80, 0x3F80, 0x3F80, 0x3F80, 0x3F80};
  const int toff0 = (tg * 32 + i) * 128 + ((g ^ (i & 7)) << 4);
  const int toff1 = (tg * 32 + i) * 128 + (((4 + g) ^ (i & 7)) << 4);
  const int wrow = 16384 + (8 * g + q4) * 448 + p4 * 8;
  auto compute = [&](int slot, int kt) {
    const char* sb = smem + slot * K4_STAGE;
#pragma unroll
    for (int ks = 0; ks < 2; ++ks) {
      bf16x8 B[2];
#pragma unroll
      for (int tt = 0; tt < 2; ++tt) B[tt] = lds128(sb + (ks ? toff1 : toff0) + tt * 2048);
      const bool docs = ((kt * 2 + ks) & 3) == tg;
#pragma unroll
      for (int a = 0; a < 2 * NT; ++a) {
        const int unit = (a < NT) ? (half * 4 + a) : (7 + half * 4 + a - NT);
        const char* wp = sb + wrow + ks * (32 * 448) + ((unit ^ (g & 1)) << 5);
        bf16x8 A = cat(ldstr(wp), ldstr(wp + 4 * 448));
        acc[0][a] = mfma16(A, B[0], acc[0][a]);
        acc[1][a] = mfma16(A, B[1], acc[1][a]);
        if (docs) cs[a] = mfma16(A, ones, cs[a]);
      }
    }
  };
  pipe_steps<0, 8, K4_NS, 6>(issue, compute);
  float* csp = (float*)(smem + K4_STAGE * K4_NS);
  if (i == 0) {
#pragma unroll
    for (int a = 0; a < 2 * NT; ++a) {
      const int unit = (a < NT) ? (half * 4 + a) : (7 + half * 4 + a - NT);
      *(f32x4*)(csp + tg * 224 + unit * 16 + 4 * g) = cs[a];
    }
  }
  bar();
#pragma unroll
  for (int a = 0; a < NT; ++a) {
    f32x4 cg = f32x4{0, 0, 0, 0}, cu = f32x4{0, 0, 0, 0};
#pragma unroll
    for (int t = 0; t < 4; ++t) {
      cg += *(const f32x4*)(csp + t * 224 + (half * 4 + a) * 16 + 4 * g);
      cu += *(const f32x4*)(csp + t * 224 + (7 + half * 4 + a) * 16 + 4 * g);
    }
#pragma unroll
    for (int tt = 0; tt < 2; ++tt) {
      f32x4 ga = (acc[tt][a] - ms[tt][0] * cg) * ms[tt][1];
      f32x4 up = (acc[tt][NT + a] - ms[tt][0] * cu) * ms[tt][1];
      float r[4];
#pragma unroll
      for (int e = 0; e < 4; ++e) {
        float sg = __builtin_amdgcn_rcpf(1.f + __builtin_amdgcn_exp2f(-1.4426950408889634f * ga[e]));
        r[e] = ga[e] * sg * up[e];
      }
      u32x2 ov = {pack2(r[0], r[1]), pack2(r[2], r[3])};
      *(u32x2*)(hh + (size_t)(tok0 + tg * 32 + tt * 16 + i) * (F_ * 2) + (col0 + (half * 4 + a) * 16 + 4 * g) * 2) = ov;
    }
  }
}

__global__ __launch_bounds__(512) void k4_mlp(const char* __restrict__ h, const char* __restrict__ w1,
                                              const char* __restrict__ w3, const float* __restrict__ stats,
                                              char* __restrict__ hh) {
  extern __shared__ __attribute__((aligned(16))) char smem[];
  const int tid = threadIdx.x, lane = tid & 63, wv = uni(tid >> 6);
  const int g = lane >> 4, i = lane & 15;
  const int tg = wv & 3, half = wv >> 2;
  const int b = blockIdx.x;
  const int cb = b & 15, tb = b >> 4;
  const unsigned lds0 = (unsigned)(size_t)(LAS char*)smem;
  const char* pt = h + (size_t)(tb * 128 + wv * 16 + (lane >> 3)) * 1024 + (((lane & 7) ^ (lane >> 3)) << 4);
  const char* pw[4];
#pragma unroll
  for (int j = 0; j < 4; ++j) {
    int qq = (j < 3) ? ((wv * 3 + j) * 64 + lane) : (1536 + wv * 32 + (lane & 31));
    int k = qq / 28, c = (qq % 28) ^ (((k >> 3) & 1) << 1);
    const char* base = (c < 14) ? w1 : w3;
    int cc = (c < 14) ? c : c - 14;
    pw[j] = base + (size_t)k * (F_ * 2) + cb * 224 + cc * 16;
  }
  auto issue = [&](int slot) {
    unsigned base = lds0 + slot * K4_STAGE;
    dma16(pt, base + wv * 2048);
    dma16(pt + 8192, base + wv * 2048 + 1024);
#pragma unroll
    for (int j = 0; j < 3; ++j) dma16(pw[j], base + 16384 + (wv * 3 + j) * 1024);
    if (lane < 32) dma16(pw[3], base + 16384 + 1536 * 16 + wv * 512);
    pt += 128;
#pragma unroll
    for (int j = 0; j < 4; ++j) pw[j] += 64 * F_ * 2;
  };
#pragma unroll
  for (int s = 0; s < K4_NS - 1; ++s) issue(s);
  f32x2 ms[2];
#pragma unroll
  for (int tt = 0; tt < 2; ++tt) {
    const f32x4* sp = (const f32x4*)(stats + ((size_t)(tb * 128 + tg * 32 + tt * 16 + i) * 16 + 4 * g) * 2);
    f32x4 a = sp[0], c = sp[1];
    float s1 = (a[0] + a[2]) + (c[0] + c[2]), s2 = (a[1] + a[3]) + (c[1] + c[3]);
    s1 = xsum(s1); s2 = xsum(s2);
    float mean = s1 * (1.f / 512.f);
    float var = fmaxf(s2 * (1.f / 512.f) - mean * mean, 0.f);
    ms[tt] = f32x2{mean, rsqrtf(var + LN_EPS)};
  }
  if (half == 0) k4_body<4>(smem, issue, tg, 0, lane, ms, hh, tb * 128, cb * 112);
  else k4_body<3>(smem, issue, tg, 1, lane, ms, hh, tb * 128, cb * 112);
}

extern "C" void kernel_launch(void* const* d_in, const int* in_sizes, int n_in, void* d_out, int out_size,
                              void* d_ws, size_t ws_size, hipStream_t stream) {
  const char* x = (const char*)d_in[0];
  const char* wqkv = (const char*)d_in[1];
  const char* wo = (const char*)d_in[2];
  const char* w1 = (const char*)d_in[4];
  const char* w3 = (const char*)d_in[5];
  const char* w2 = (const char*)d_in[6];
  char* ws = (char*)d_ws;
  char* qkv = ws;
  char* o = ws + (6u << 20);
  char* h = ws + (8u << 20);
  float* stats = (float*)(ws + (10u << 20));
  char* hh = ws + (11u << 20);
  constexpr int G3_NS = 8, G5_NS = 7;
  (void)hipFuncSetAttribute((const void*)k1_qkv, hipFuncAttributeMaxDynamicSharedMemorySize, K1_LDS);
  (void)hipFuncSetAttribute((const void*)k2_attn, hipFuncAttributeMaxDynamicSharedMemorySize, K2_LDS);
  (void)hipFuncSetAttribute((const void*)k_gemm64<8, G3_NS, true>, hipFuncAttributeMaxDynamicSharedMemorySize, G_STAGE * G3_NS);
  (void)hipFuncSetAttribute((const void*)k4_mlp, hipFuncAttributeMaxDynamicSharedMemorySize, K4_LDS);
  (void)hipFuncSetAttribute((const void*)k_gemm64<28, G5_NS, false>, hipFuncAttributeMaxDynamicSharedMemorySize, G_STAGE * G5_NS);
  k1_qkv<<<256, 512, K1_LDS, stream>>>(x, wqkv, qkv);
  k2_attn<<<256, 512, K2_LDS, stream>>>(qkv, o);
  k_gemm64<8, G3_NS, true><<<256, 512, G_STAGE * G3_NS, stream>>>(o, wo, x, h, stats);
  k4_mlp<<<256, 512, K4_LDS, stream>>>(h, w1, w3, stats, hh);
  k_gemm64<28, G5_NS, false><<<256, 512, G_STAGE * G5_NS, stream>>>(hh, w2, h, (char*)d_out, nullptr);
}
```

```cpp
#include <hip/hip_runtime.h>
#include <hip/hip_bf16.h>
#include <stdint.h>

typedef short bf16x8 __attribute__((ext_vector_type(8)));
typedef short s16x4 __attribute__((ext_vector_type(4)));
typedef float f32x4 __attribute__((ext_vector_type(4)));
typedef float f32x2 __attribute__((ext_vector_type(2)));
typedef unsigned u32x4 __attribute__((ext_vector_type(4)));
typedef unsigned u32x2 __attribute__((ext_vector_type(2)));
typedef __bf16 bf16x2 __attribute__((ext_vector_type(2)));
#define LAS __attribute__((address_space(3)))
#define DEV __device__ __forceinline__

constexpr int S_ = 2048, D_ = 512, H_ = 32, F_ = 1792;
constexpr float LN_EPS = 1e-5f;

DEV f32x4 mfma16(bf16x8 a, bf16x8 b, f32x4 c) { return __builtin_amdgcn_mfma_f32_16x16x32_bf16(a, b, c, 0, 0, 0); }
DEV unsigned pack2(float a, float b) {
  f32x2 v = {a, b};
  return __builtin_bit_cast(unsigned, __builtin_convertvector(v, bf16x2));
}
DEV float bflo(unsigned u) { return __builtin_bit_cast(float, u << 16); }
DEV float bfhi(unsigned u) { return __builtin_bit_cast(float, u & 0xffff0000u); }
DEV float dot2(unsigned a, unsigned b, float c) {
  return __builtin_amdgcn_fdot2_f32_bf16(__builtin_bit_cast(bf16x2, a), __builtin_bit_cast(bf16x2, b), c, false);
}
DEV void dma16(const char* gptr, unsigned ldsbase) {
  unsigned sv;
  asm volatile("s_mov_b32 %0, m0\n\ts_mov_b32 m0, %2\n\ts_nop 0\n\tglobal_load_lds_dwordx4 %1, off\n\ts_mov_b32 m0, %0"
               : "=&s"(sv) : "v"(gptr), "s"(ldsbase) : "memory");
}
template <int N> DEV void wait_bar() { asm volatile("s_waitcnt vmcnt(%0) lgkmcnt(0)\n\ts_barrier" ::"n"(N) : "memory"); }
DEV void bar() { asm volatile("s_waitcnt lgkmcnt(0)\n\ts_barrier" ::: "memory"); }
DEV bf16x8 lds128(const char* p) { return *(const LAS bf16x8*)(LAS const char*)p; }
DEV s16x4 ldstr(const char* p) { return __builtin_amdgcn_ds_read_tr16_b64_v4i16((LAS s16x4*)p); }
DEV bf16x8 cat(s16x4 a, s16x4 b) { return __builtin_shufflevector(a, b, 0, 1, 2, 3, 4, 5, 6, 7); }
DEV float xsum(float v) { v += __shfl_xor(v, 16); v += __shfl_xor(v, 32); return v; }
DEV float xmax(float v) { v = fmaxf(v, __shfl_xor(v, 16)); v = fmaxf(v, __shfl_xor(v, 32)); return v; }
DEV unsigned uni(unsigned v) { return __builtin_amdgcn_readfirstlane(v); }

template <int A, int B> struct Min { static constexpr int v = A < B ? A : B; };

template <int KT, int NK, int NS, int P, class Issue, class Compute>
DEV void pipe_steps(Issue& issue, Compute& compute) {
  if constexpr (KT < NK) {
    wait_bar<(Min<KT + NS - 1, NK>::v - KT - 1) * P>();
    if constexpr (KT + NS - 1 < NK) issue((KT + NS - 1) % NS);
    compute(KT % NS, KT);
    pipe_steps<KT + 1, NK, NS, P>(issue, compute);
  }
}

constexpr int K1_STAGE = 16384 + 12288, K1_NS = 5;
constexpr int K1_LDS = K1_STAGE * K1_NS + 1024 + 1536;

__global__ __launch_bounds__(512) void k1_qkv(const char* __restrict__ x, const char* __restrict__ w,
                                              char* __restrict__ qkv) {
  extern __shared__ __attribute__((aligned(16))) char smem[];
  const int tid = threadIdx.x, lane = tid & 63, wv = uni(tid >> 6);
  const int g = lane >> 4, i = lane & 15, q4 = (lane >> 2) & 3, p4 = lane & 3;
  const int tg = wv & 3, nh = wv >> 2;
  const int b = blockIdx.x, xcd = b & 7, bi = b >> 3;
  const int cb = (xcd & 1) * 8 + (bi & 7), tb = (xcd >> 1) * 4 + (bi >> 3);
  const unsigned lds0 = (unsigned)(size_t)(LAS char*)smem;

  const char* pt;
  {
    int row = tb * 128 + wv * 16 + (lane >> 3);
    int c = (lane & 7) ^ (lane >> 3);
    pt = x + (size_t)row * 1024 + c * 16;
  }
  const char *pw0, *pw1;
  {
    int qq = wv * 64 + lane;
    int k = qq / 12, c = (qq % 12) ^ (((k >> 3) & 1) << 1);
    pw0 = w + (size_t)k * 3072 + cb * 192 + c * 16;
    qq = 512 + wv * 32 + (lane & 31);
    k = qq / 12; c = (qq % 12) ^ (((k >> 3) & 1) << 1);
    pw1 = w + (size_t)k * 3072 + cb * 192 + c * 16;
  }
  int kk = bi & 7;
  auto issue = [&](int slot) {
    unsigned base = lds0 + slot * K1_STAGE;
    dma16(pt + kk * 128, base + wv * 2048);
    dma16(pt + kk * 128 + 8192, base + wv * 2048 + 1024);
    dma16(pw0 + (size_t)kk * (64 * 3072), base + 16384 + wv * 1024);
    if (lane < 32) dma16(pw1 + (size_t)kk * (64 * 3072), base + 16384 + 8192 + wv * 512);
    kk = (kk + 1) & 7;
  };
#pragma unroll
  for (int s = 0; s < K1_NS - 1; ++s) issue(s);

  f32x4 acc[2][3], cs = f32x4{0, 0, 0, 0};
#pragma unroll
  for (int a = 0; a < 3; ++a) { acc[0][a] = f32x4{0, 0, 0, 0}; acc[1][a] = f32x4{0, 0, 0, 0}; }
  float s1 = 0.f, s2 = 0.f;
  const bf16x8 ones = {0x3F80, 0x3F80, 0x3F80, 0x3F80, 0x3F80, 0x3F80, 0x3F80, 0x3F80};
  const int toff0 = (tg * 32 + i) * 128 + ((g ^ (i & 7)) << 4);
  const int toff1 = (tg * 32 + i) * 128 + (((4 + g) ^ (i & 7)) << 4);
  int woff[3], un[3];
#pragma unroll
  for (int nt = 0; nt < 3; ++nt) {
    int a = nt + tg; a = a >= 3 ? a - 3 : a; a = a >= 3 ? a - 3 : a;
    un[nt] = nh * 3 + a;
    woff[nt] = 16384 + (8 * g + q4) * 192 + ((un[nt] ^ (g & 1)) << 5) + p4 * 8;
  }

  auto compute = [&](int slot, int kt) {
    const char* sb = smem + slot * K1_STAGE;
#pragma unroll
    for (int ks = 0; ks < 2; ++ks) {
      bf16x8 B[2], A[3];
#pragma unroll
      for (int tt = 0; tt < 2; ++tt) B[tt] = lds128(sb + (ks ? toff1 : toff0) + tt * 2048);
#pragma unroll
      for (int nt = 0; nt < 3; ++nt)
        A[nt] = cat(ldstr(sb + woff[nt] + ks * 6144), ldstr(sb + woff[nt] + ks * 6144 + 768));
#pragma unroll
      for (int nt = 0; nt < 3; ++nt)
#pragma unroll
        for (int tt = 0; tt < 2; ++tt) acc[tt][nt] = mfma16(A[nt], B[tt], acc[tt][nt]);
      {
        u32x4 u = __builtin_bit_cast(u32x4, nh ? B[1] : B[0]);
#pragma unroll
        for (int j = 0; j < 4; ++j) { s1 = dot2(u[j], 0x3F803F80u, s1); s2 = dot2(u[j], u[j], s2); }
      }
      cs = mfma16(A[0], ones, cs);
    }
  };
  pipe_steps<0, 8, K1_NS, 4>(issue, compute);

  float* st = (float*)(smem + K1_STAGE * K1_NS);
  float* csp = (float*)(smem + K1_STAGE * K1_NS + 1024);
  s1 = xsum(s1); s2 = xsum(s2);
  if (g == 0) {
    float mean = s1 * (1.f / 512.f);
    float var = fmaxf(s2 * (1.f / 512.f) - mean * mean, 0.f);
    float rstd = rsqrtf(var + LN_EPS);
    *(f32x2*)(st + (tg * 32 + nh * 16 + i) * 2) = f32x2{mean, rstd};
  }
  if (i == 0 && tg < 3) *(f32x4*)(csp + un[0] * 16 + 4 * g) = cs;
  bar();
  f32x2 ms[2];
#pragma unroll
  for (int tt = 0; tt < 2; ++tt) ms[tt] = *(const f32x2*)(st + (tg * 32 + tt * 16 + i) * 2);
#pragma unroll
  for (int nt = 0; nt < 3; ++nt) {
    f32x4 c = *(const f32x4*)(csp + un[nt] * 16 + 4 * g);
    int n0 = cb * 96 + un[nt] * 16;
    char* dst = qkv + (size_t)(n0 >> 4) * (2048 * 32) + g * 8;
#pragma unroll
    for (int tt = 0; tt < 2; ++tt) {
      f32x4 y = (acc[tt][nt] - ms[tt][0] * c) * ms[tt][1];
      u32x2 o = {pack2(y[0], y[1]), pack2(y[2], y[3])};
      *(u32x2*)(dst + (size_t)(tb * 128 + tg * 32 + tt * 16 + i) * 32) = o;
    }
  }
}

constexpr int K2_LDS = 4 * 12288;
__global__ __launch_bounds__(512) void k2_attn(const char* __restrict__ qkv, char* __restrict__ o) {
  extern __shared__ __attribute__((aligned(16))) char smem[];
  const int tid = threadIdx.x, lane = tid & 63, wv = uni(tid >> 6);
  const int g = lane >> 4, i = lane & 15, q4 = (lane >> 2) & 3, p4 = lane & 3;
  const int b = blockIdx.x;
  const int hp = (b & 7) * 2 + ((b >> 3) & 1), qb = b >> 4;
  const int q0 = qb * 128;
  const unsigned lds0 = (unsigned)(size_t)(LAS char*)smem;
  const size_t HS = 2048 * 32;
  {
    const int r = wv >> 1;
    const char* src = qkv + (size_t)((1 + (r >> 1)) * 32 + hp * 2 + (r & 1)) * HS;
#pragma unroll
    for (int j = 0; j < 6; ++j) {
      int ch = ((wv & 1) * 6 + j) * 64 + lane;
      int key = q0 - 256 + (ch >> 1);
      key = key < 0 ? 0 : key;
      dma16(src + (size_t)key * 32 + (ch & 1) * 16, lds0 + r * 12288 + ((wv & 1) * 6 + j) * 1024);
    }
  }
  const int hh = wv >> 2, wq = wv & 3;
  const int head = hp * 2 + hh;
  bf16x8 Q[2];
#pragma unroll
  for (int u = 0; u < 2; ++u) {
    int qs = q0 + wq * 32 + u * 16;
    u32x4 v = *(const u32x4*)(qkv + (size_t)head * HS + (size_t)(qs + i) * 32 + (g & 1) * 16);
    if (g >= 2) v = u32x4{0, 0, 0, 0};
    Q[u] = __builtin_bit_cast(bf16x8, v);
  }
  wait_bar<0>();
  const char* Kl = smem + hh * 12288;
  const char* Vl = smem + 24576 + hh * 12288;
  const float c = 0.25f * 1.4426950408889634f;
  const float NEG = -1e30f;
#pragma unroll
  for (int u = 0; u < 2; ++u) {
    const int qs = q0 + wq * 32 + u * 16;
    const int lrow = wq * 32 + u * 16;
    f32x4 s[17];
#pragma unroll
    for (int t = 0; t < 17; ++t) {
      bf16x8 A = lds128(Kl + (lrow + t * 16 + i) * 32 + (g & 1) * 16);
      s[t] = mfma16(A, Q[u], f32x4{0, 0, 0, 0});
    }
#pragma unroll
    for (int r = 0; r < 4; ++r) {
      s[0][r] = (4 * g + r > i) ? s[0][r] : NEG;
      s[16][r] = (4 * g + r <= i) ? s[16][r] : NEG;
    }
    if (qs < 256) {
      const int nneg = (256 - qs) >> 4;
#pragma unroll
      for (int t = 0; t < 16; ++t)
        if (t < nneg) s[t] = f32x4{NEG, NEG, NEG, NEG};
    }
    float m = s[16][0];
#pragma unroll
    for (int t = 0; t < 17; ++t)
#pragma unroll
      for (int r = 0; r < 4; ++r) m = fmaxf(m, s[t][r]);
    m = xmax(m);
    const float mc = m * c;
    float sum = 0.f;
    unsigned pk[17][2];
#pragma unroll
    for (int t = 0; t < 17; ++t) {
      float e0 = __builtin_amdgcn_exp2f(s[t][0] * c - mc), e1 = __builtin_amdgcn_exp2f(s[t][1] * c - mc);
      float e2 = __builtin_amdgcn_exp2f(s[t][2] * c - mc), e3 = __builtin_amdgcn_exp2f(s[t][3] * c - mc);
      sum += (e0 + e1) + (e2 + e3);
      pk[t][0] = pack2(e0, e1); pk[t][1] = pack2(e2, e3);
    }
    sum = xsum(sum);
    f32x4 oacc = f32x4{0, 0, 0, 0};
#pragma unroll
    for (int tp = 0; tp < 9; ++tp) {
      const int t0 = 2 * tp, t1 = (2 * tp + 1 < 17) ? 2 * tp + 1 : 16;
      bf16x8 A = cat(ldstr(Vl + (lrow + t0 * 16 + 4 * g + q4) * 32 + p4 * 8), ldstr(Vl + (lrow + t1 * 16 + 4 * g + q4) * 32 + p4 * 8));
      u32x4 pb = {pk[t0][0], pk[t0][1], (2 * tp + 1 < 17) ? pk[t1][0] : 0u, (2 * tp + 1 < 17) ? pk[t1][1] : 0u};
      oacc = mfma16(A, __builtin_bit_cast(bf16x8, pb), oacc);
    }
    const float inv = 1.f / sum;
    u32x2 ov = {pack2(oacc[0] * inv, oacc[1] * inv), pack2(oacc[2] * inv, oacc[3] * inv)};
    *(u32x2*)(o + (size_t)(qs + i) * 1024 + head * 32 + g * 8) = ov;
  }
}

constexpr int G_STAGE = 16384;
template <int NK, int NS, bool STATS>
__global__ __launch_bounds__(512) void k_gemm64(const char* __restrict__ a, const char* __restrict__ w,
                                                const char* __restrict__ resid, char* __restrict__ out,
                                                float* __restrict__ stats) {
  extern __shared__ __attribute__((aligned(16))) char smem[];
  const int tid = threadIdx.x, lane = tid & 63, wv = uni(tid >> 6);
  const int g = lane >> 4, i = lane & 15, q4 = (lane >> 2) & 3, p4 = lane & 3;
  const int tg = wv & 1, ng = (wv >> 1) & 1, kp = wv >> 2;
  const int b = blockIdx.x;
  const int cb = (b >> 3) & 7, tb = (b & 7) * 4 + (b >> 6);
  const unsigned lds0 = (unsigned)(size_t)(LAS char*)smem;
  constexpr int LDA = NK * 128;
  const char* pt = a + (size_t)(tb * 64 + wv * 8 + (lane >> 3)) * LDA + (((lane & 7) ^ (lane >> 3)) << 4);
  const char* pw;
  {
    int k = wv * 8 + (lane >> 3), cc = lane & 7;
    int unit = (cc >> 1) ^ (((k >> 1) & 1) | (((k >> 3) & 1) << 1));
    pw = w + (size_t)k * 1024 + cb * 128 + unit * 32 + (cc & 1) * 16;
  }
  int kk = ((b >> 3) * 5) % NK;
  auto issue = [&](int slot) {
    unsigned base = lds0 + slot * G_STAGE;
    dma16(pt + kk * 128, base + wv * 1024);
    dma16(pw + (size_t)kk * (64 * 1024), base + 8192 + wv * 1024);
    kk = (kk + 1 == NK) ? 0 : kk + 1;
  };
#pragma unroll
  for (int s = 0; s < (NS - 1 < NK ? NS - 1 : NK); ++s) issue(s);
  f32x4 acc[2][2];
#pragma unroll
  for (int t = 0; t < 2; ++t) { acc[t][0] = f32x4{0, 0, 0, 0}; acc[t][1] = f32x4{0, 0, 0, 0}; }
  const int toff = (tg * 32 + i) * 128 + (((kp * 4 + g) ^ (i & 7)) << 4);
  int woff[2];
#pragma unroll
  for (int nt = 0; nt < 2; ++nt)
    woff[nt] = 8192 + (kp * 32 + 8 * g + q4) * 128 + (((ng * 2 + nt) ^ (((q4 >> 1) & 1) | ((g & 1) << 1))) << 5) + p4 * 8;
  auto compute = [&](int slot, int) {
    const char* sb = smem + slot * G_STAGE;
    bf16x8 B[2], A[2];
#pragma unroll
    for (int tt = 0; tt < 2; ++tt) B[tt] = lds128(sb + toff + tt * 2048);
#pragma unroll
    for (int nt = 0; nt < 2; ++nt) A[nt] = cat(ldstr(sb + woff[nt]), ldstr(sb + woff[nt] + 512));
#pragma unroll
    for (int nt = 0; nt < 2; ++nt)
#pragma unroll
      for (int tt = 0; tt < 2; ++tt) acc[tt][nt] = mfma16(A[nt], B[tt], acc[tt][nt]);
  };
  if constexpr (NK <= NS) {
    pipe_steps<0, NK, NS, 2>(issue, compute);
  } else {
    static_assert(NK % NS == 0, "");
    for (int it = 0; it < NK / NS - 1; ++it) {
#pragma unroll
      for (int s = 0; s < NS; ++s) {
        wait_bar<(NS - 2) * 2>();
        issue((s + NS - 1) % NS);
        compute(s, 0);
      }
    }
    pipe_steps<NK - NS, NK, NS, 2>(issue, compute);
  }
  bar();
  {
    float* xb = (float*)smem;
    f32x4* mine = (f32x4*)xb + wv * 128;
#pragma unroll
    for (int nt = 0; nt < 2; ++nt) mine[nt * 64 + lane] = kp ? acc[0][nt] : acc[1][nt];
    bar();
    const f32x4* oth = (const f32x4*)xb + (wv ^ 4) * 128;
    const int token = tb * 64 + tg * 32 + kp * 16 + i;
    float s1 = 0.f, s2 = 0.f;
#pragma unroll
    for (int nt = 0; nt < 2; ++nt) {
      f32x4 v = (kp ? acc[1][nt] : acc[0][nt]) + oth[nt * 64 + lane];
      const size_t off = (size_t)token * 1024 + (cb * 64 + ng * 32 + nt * 16 + 4 * g) * 2;
      u32x2 r = *(const u32x2*)(resid + off);
      v[0] += bflo(r[0]); v[1] += bfhi(r[0]); v[2] += bflo(r[1]); v[3] += bfhi(r[1]);
      u32x2 ov = {pack2(v[0], v[1]), pack2(v[2], v[3])};
      *(u32x2*)(out + off) = ov;
      if constexpr (STATS) {
        s1 = dot2(ov[0], 0x3F803F80u, s1); s1 = dot2(ov[1], 0x3F803F80u, s1);
        s2 = dot2(ov[0], ov[0], s2); s2 = dot2(ov[1], ov[1], s2);
      }
    }
    if constexpr (STATS) {
      s1 = xsum(s1); s2 = xsum(s2);
      if (g == 0) *(f32x2*)(stats + ((size_t)token * 16 + cb * 2 + ng) * 2) = f32x2{s1, s2};
    }
  }
}

constexpr int K4_STAGE = 16384 + 28672, K4_NS = 3;
constexpr int K4_LDS = K4_STAGE * K4_NS + 4 * 224 * 4;

template <int NT, class Issue>
DEV void k4_body(char* smem, Issue& issue, int tg, int half, int lane, const f32x2* ms, char* __restrict__ hh, int tok0, int col0) {
  const int g = lane >> 4, i = lane & 15, q4 = (lane >> 2) & 3, p4 = lane & 3;
  f32x4 acc[2][2 * NT], csg = f32x4{0, 0, 0, 0}, csu = f32x4{0, 0, 0, 0};
#pragma unroll
  for (int a = 0; a < 2 * NT; ++a) { acc[0][a] = f32x4{0, 0, 0, 0}; acc[1][a] = f32x4{0, 0, 0, 0}; }
  const bf16x8 ones = {0x3F80, 0x3F80, 0x3F80, 0x3F80, 0x3F80, 0x3F80, 0x3F80, 0x3F80};
  const int toff0 = (tg * 32 + i) * 128 + ((g ^ (i & 7)) << 4);
  const int toff1 = (tg * 32 + i) * 128 + (((4 + g) ^ (i & 7)) << 4);
  const int wrow = 16384 + (8 * g + q4) * 448 + p4 * 8;
  int pj[NT], wg[NT], wu[NT];
#pragma unroll
  for (int a = 0; a < NT; ++a) {
    int j = a + tg; j = j >= NT ? j - NT : j; j = j >= NT ? j - NT : j;
    pj[a] = half * 4 + j;
    wg[a] = wrow + ((pj[a] ^ (g & 1)) << 5);
    wu[a] = wrow + (((7 + pj[a]) ^ (g & 1)) << 5);
  }
  auto compute = [&](int slot, int kt) {
    const char* sb = smem + slot * K4_STAGE;
#pragma unroll
    for (int ks = 0; ks < 2; ++ks) {
      bf16x8 B[2];
#pragma unroll
      for (int tt = 0; tt < 2; ++tt) B[tt] = lds128(sb + (ks ? toff1 : toff0) + tt * 2048);
#pragma unroll
      for (int a = 0; a < 2 * NT; ++a) {
        const char* wp = sb + ((a < NT) ? wg[a] : wu[a - NT]) + ks * (32 * 448);
        bf16x8 A = cat(ldstr(wp), ldstr(wp + 4 * 448));
        acc[0][a] = mfma16(A, B[0], acc[0][a]);
        acc[1][a] = mfma16(A, B[1], acc[1][a]);
        if (a == 0) csg = mfma16(A, ones, csg);
        if (a == NT) csu = mfma16(A, ones, csu);
      }
    }
  };
  pipe_steps<0, 8, K4_NS, 6>(issue, compute);
  float* csp = (float*)(smem + K4_STAGE * K4_NS);
  if (i == 0 && tg < NT) {
    *(f32x4*)(csp + pj[0] * 16 + 4 * g) = csg;
    *(f32x4*)(csp + (7 + pj[0]) * 16 + 4 * g) = csu;
  }
  bar();
#pragma unroll
  for (int a = 0; a < NT; ++a) {
    f32x4 cg = *(const f32x4*)(csp + pj[a] * 16 + 4 * g);
    f32x4 cu = *(const f32x4*)(csp + (7 + pj[a]) * 16 + 4 * g);
#pragma unroll
    for (int tt = 0; tt < 2; ++tt) {
      f32x4 ga = (acc[tt][a] - ms[tt][0] * cg) * ms[tt][1];
      f32x4 up = (acc[tt][NT + a] - ms[tt][0] * cu) * ms[tt][1];
      float r[4];
#pragma unroll
      for (int e = 0; e < 4; ++e) {
        float sg = __builtin_amdgcn_rcpf(1.f + __builtin_amdgcn_exp2f(-1.4426950408889634f * ga[e]));
        r[e] = ga[e] * sg * up[e];
      }
      u32x2 ov = {pack2(r[0], r[1]), pack2(r[2], r[3])};
      *(u32x2*)(hh + (size_t)(tok0 + tg * 32 + tt * 16 + i) * (F_ * 2) + (col0 + pj[a] * 16 + 4 * g) * 2) = ov;
    }
  }
}

__global__ __launch_bounds__(512) void k4_mlp(const char* __restrict__ h, const char* __restrict__ w1,
                                              const char* __restrict__ w3, const float* __restrict__ stats,
                                              char* __restrict__ hh) {
  extern __shared__ __attribute__((aligned(16))) char smem[];
  const int tid = threadIdx.x, lane = tid & 63, wv = uni(tid >> 6);
  const int g = lane >> 4, i = lane & 15;
  const int tg = wv & 3, half = wv >> 2;
  const int b = blockIdx.x, xcd = b & 7, bi = b >> 3;
  const int cb = (xcd & 3) * 4 + (bi & 3), tb = (xcd >> 2) * 8 + (bi >> 2);
  const unsigned lds0 = (unsigned)(size_t)(LAS char*)smem;
  const char* pt = h + (size_t)(tb * 128 + wv * 16 + (lane >> 3)) * 1024 + (((lane & 7) ^ (lane >> 3)) << 4);
  const char* pw[4];
#pragma unroll
  for (int j = 0; j < 4; ++j) {
    int qq = (j < 3) ? ((wv * 3 + j) * 64 + lane) : (1536 + wv * 32 + (lane & 31));
    int k = qq / 28, c = (qq % 28) ^ (((k >> 3) & 1) << 1);
    const char* base = (c < 14) ? w1 : w3;
    int cc = (c < 14) ? c : c - 14;
    pw[j] = base + (size_t)k * (F_ * 2) + cb * 224 + cc * 16;
  }
  int kk = bi & 7;
  auto issue = [&](int slot) {
    unsigned base = lds0 + slot * K4_STAGE;
    dma16(pt + kk * 128, base + wv * 2048);
    dma16(pt + kk * 128 + 8192, base + wv * 2048 + 1024);
#pragma unroll
    for (int j = 0; j < 3; ++j) dma16(pw[j] + (size_t)kk * (64 * F_ * 2), base + 16384 + (wv * 3 + j) * 1024);
    if (lane < 32) dma16(pw[3] + (size_t)kk * (64 * F_ * 2), base + 16384 + 1536 * 16 + wv * 512);
    kk = (kk + 1) & 7;
  };
#pragma unroll
  for (int s = 0; s < K4_NS - 1; ++s) issue(s);
  f32x2 ms[2];
#pragma unroll
  for (int tt = 0; tt < 2; ++tt) {
    const f32x4* sp = (const f32x4*)(stats + ((size_t)(tb * 128 + tg * 32 + tt * 16 + i) * 16 + 4 * g) * 2);
    f32x4 a = sp[0], c = sp[1];
    float s1 = (a[0] + a[2]) + (c[0] + c[2]), s2 = (a[1] + a[3]) + (c[1] + c[3]);
    s1 = xsum(s1); s2 = xsum(s2);
    float mean = s1 * (1.f / 512.f);
    float var = fmaxf(s2 * (1.f / 512.f) - mean * mean, 0.f);
    ms[tt] = f32x2{mean, rsqrtf(var + LN_EPS)};
  }
  if (half == 0) k4_body<4>(smem, issue, tg, 0, lane, ms, hh, tb * 128, cb * 112);
  else k4_body<3>(smem, issue, tg, 1, lane, ms, hh, tb * 128, cb * 112);
}

extern "C" void kernel_launch(void* const* d_in, const int* in_sizes, int n_in, void* d_out, int out_size,
                              void* d_ws, size_t ws_size, hipStream_t stream) {
  const char* x = (const char*)d_in[0];
  const char* wqkv = (const char*)d_in[1];
  const char* wo = (const char*)d_in[2];
  const char* w1 = (const char*)d_in[4];
  const char* w3 = (const char*)d_in[5];
  const char* w2 = (const char*)d_in[6];
  char* ws = (char*)d_ws;
  char* qkv = ws;
  char* o = ws + (6u << 20);
  char* h = ws + (8u << 20);
  float* stats = (float*)(ws + (10u << 20));
  char* hh = ws + (11u << 20);
  constexpr int G3_NS = 8, G5_NS = 7;
  (void)hipFuncSetAttribute((const void*)k1_qkv, hipFuncAttributeMaxDynamicSharedMemorySize, K1_LDS);
  (void)hipFuncSetAttribute((const void*)k2_attn, hipFuncAttributeMaxDynamicSharedMemorySize, K2_LDS);
  (void)hipFuncSetAttribute((const void*)k_gemm64<8, G3_NS, true>, hipFuncAttributeMaxDynamicSharedMemorySize, G_STAGE * G3_NS);
  (void)hipFuncSetAttribute((const void*)k4_mlp, hipFuncAttributeMaxDynamicSharedMemorySize, K4_LDS);
  (void)hipFuncSetAttribute((const void*)k_gemm64<28, G5_NS, false>, hipFuncAttributeMaxDynamicSharedMemorySize, G_STAGE * G5_NS);
  k1_qkv<<<256, 512, K1_LDS, stream>>>(x, wqkv, qkv);
  k2_attn<<<256, 512, K2_LDS, stream>>>(qkv, o);
  k_gemm64<8, G3_NS, true><<<256, 512, G_STAGE * G3_NS, stream>>>(o, wo, x, h, stats);
  k4_mlp<<<256, 512, K4_LDS, stream>>>(h, w1, w3, stats, hh);
  k_gemm64<28, G5_NS, false><<<256, 512, G_STAGE * G5_NS, stream>>>(hh, w2, h, (char*)d_out, nullptr);
}
```

```cpp
#include <hip/hip_runtime.h>
#include <hip/hip_bf16.h>
#include <stdint.h>

typedef short bf16x8 __attribute__((ext_vector_type(8)));
typedef short s16x4 __attribute__((ext_vector_type(4)));
typedef float f32x4 __attribute__((ext_vector_type(4)));
typedef float f32x2 __attribute__((ext_vector_type(2)));
typedef unsigned u32x4 __attribute__((ext_vector_type(4)));
typedef unsigned u32x2 __attribute__((ext_vector_type(2)));
typedef __bf16 bf16x2 __attribute__((ext_vector_type(2)));
#define LAS __attribute__((address_space(3)))
#define DEV __device__ __forceinline__

constexpr int S_ = 2048, D_ = 512, H_ = 32, F_ = 1792;
constexpr float LN_EPS = 1e-5f;

DEV f32x4 mfma16(bf16x8 a, bf16x8 b, f32x4 c) { return __builtin_amdgcn_mfma_f32_16x16x32_bf16(a, b, c, 0, 0, 0); }
DEV unsigned pack2(float a, float b) {
  f32x2 v = {a, b};
  return __builtin_bit_cast(unsigned, __builtin_convertvector(v, bf16x2));
}
DEV float bflo(unsigned u) { return __builtin_bit_cast(float, u << 16); }
DEV float bfhi(unsigned u) { return __builtin_bit_cast(float, u & 0xffff0000u); }
DEV float dot2(unsigned a, unsigned b, float c) {
  return __builtin_amdgcn_fdot2_f32_bf16(__builtin_bit_cast(bf16x2, a), __builtin_bit_cast(bf16x2, b), c, false);
}
DEV void dma16(const char* gptr, unsigned ldsbase) {
  unsigned sv;
  asm volatile("s_mov_b32 %0, m0\n\ts_mov_b32 m0, %2\n\ts_nop 0\n\tglobal_load_lds_dwordx4 %1, off\n\ts_mov_b32 m0, %0"
               : "=&s"(sv) : "v"(gptr), "s"(ldsbase) : "memory");
}
template <int N> DEV void wait_bar() { asm volatile("s_waitcnt vmcnt(%0) lgkmcnt(0)\n\ts_barrier" ::"n"(N) : "memory"); }
DEV void bar() { asm volatile("s_waitcnt lgkmcnt(0)\n\ts_barrier" ::: "memory"); }
DEV bf16x8 lds128(const char* p) { return *(const LAS bf16x8*)(LAS const char*)p; }
DEV s16x4 ldstr(const char* p) { return __builtin_amdgcn_ds_read_tr16_b64_v4i16((LAS s16x4*)p); }
DEV bf16x8 cat(s16x4 a, s16x4 b) { return __builtin_shufflevector(a, b, 0, 1, 2, 3, 4, 5, 6, 7); }
DEV float xsum(float v) { v += __shfl_xor(v, 16); v += __shfl_xor(v, 32); return v; }
DEV float xmax(float v) { v = fmaxf(v, __shfl_xor(v, 16)); v = fmaxf(v, __shfl_xor(v, 32)); return v; }
DEV unsigned uni(unsigned v) { return __builtin_amdgcn_readfirstlane(v); }

template <int A, int B> struct Min { static constexpr int v = A < B ? A : B; };

template <int KT, int NK, int NS, int P, class Issue, class Compute>
DEV void pipe_steps(Issue& issue, Compute& compute) {
  if constexpr (KT < NK) {
    wait_bar<(Min<KT + NS - 1, NK>::v - KT - 1) * P>();
    if constexpr (KT + NS - 1 < NK) issue((KT + NS - 1) % NS);
    compute(KT % NS, KT);
    pipe_steps<KT + 1, NK, NS, P>(issue, compute);
  }
}

constexpr int K1_STAGE = 16384 + 12288, K1_NS = 5;
constexpr int K1_LDS = K1_STAGE * K1_NS + 1024 + 1536;

__global__ __launch_bounds__(512) void k1_qkv(const char* __restrict__ x, const char* __restrict__ w,
                                              char* __restrict__ qkv) {
  extern __shared__ __attribute__((aligned(16))) char smem[];
  const int tid = threadIdx.x, lane = tid & 63, wv = uni(tid >> 6);
  const int g = lane >> 4, i = lane & 15, q4 = (lane >> 2) & 3, p4 = lane & 3;
  const int tg = wv & 3, nh = wv >> 2;
  const int b = blockIdx.x, xcd = b & 7, bi = b >> 3;
  const int cb = (xcd & 1) * 8 + (bi & 7), tb = (xcd >> 1) * 4 + (bi >> 3);
  const unsigned lds0 = (unsigned)(size_t)(LAS char*)smem;

  const char* pt;
  {
    int row = tb * 128 + wv * 16 + (lane >> 3);
    int c = (lane & 7) ^ (lane >> 3);
    pt = x + (size_t)row * 1024 + c * 16;
  }
  const char *pw0, *pw1;
  {
    int qq = wv * 64 + lane;
    int k = qq / 12, c = (qq % 12) ^ (((k >> 3) & 1) << 1);
    pw0 = w + (size_t)k * 3072 + cb * 192 + c * 16;
    qq = 512 + wv * 32 + (lane & 31);
    k = qq / 12; c = (qq % 12) ^ (((k >> 3) & 1) << 1);
    pw1 = w + (size_t)k * 3072 + cb * 192 + c * 16;
  }
  int kk = bi & 7;
  auto issue = [&](int slot) {
    unsigned base = lds0 + slot * K1_STAGE;
    dma16(pt + kk * 128, base + wv * 2048);
    dma16(pt + kk * 128 + 8192, base + wv * 2048 + 1024);
    dma16(pw0 + (size_t)kk * (64 * 3072), base + 16384 + wv * 1024);
    if (lane < 32) dma16(pw1 + (size_t)kk * (64 * 3072), base + 16384 + 8192 + wv * 512);
    kk = (kk + 1) & 7;
  };
#pragma unroll
  for (int s = 0; s < K1_NS - 1; ++s) issue(s);

  f32x4 acc[2][3], cs = f32x4{0, 0, 0, 0};
#pragma unroll
  for (int a = 0; a < 3; ++a) { acc[0][a] = f32x4{0, 0, 0, 0}; acc[1][a] = f32x4{0, 0, 0, 0}; }
  float s1 = 0.f, s2 = 0.f;
  const bf16x8 ones = {0x3F80, 0x3F80, 0x3F80, 0x3F80, 0x3F80, 0x3F80, 0x3F80, 0x3F80};
  const int toff0 = (tg * 32 + i) * 128 + ((g ^ (i & 7)) << 4);
  const int toff1 = (tg * 32 + i) * 128 + (((4 + g) ^ (i & 7)) << 4);
  int woff[3], un[3];
#pragma unroll
  for (int nt = 0; nt < 3; ++nt) {
    int a = nt + tg; a = a >= 3 ? a - 3 : a; a = a >= 3 ? a - 3 : a;
    un[nt] = nh * 3 + a;
    woff[nt] = 16384 + (8 * g + q4) * 192 + ((un[nt] ^ (g & 1)) << 5) + p4 * 8;
  }

  auto compute = [&](int slot, int kt) {
    const char* sb = smem + slot * K1_STAGE;
#pragma unroll
    for (int ks = 0; ks < 2; ++ks) {
      bf16x8 B[2], A[3];
#pragma unroll
      for (int tt = 0; tt < 2; ++tt) B[tt] = lds128(sb + (ks ? toff1 : toff0) + tt * 2048);
#pragma unroll
      for (int nt = 0; nt < 3; ++nt)
        A[nt] = cat(ldstr(sb + woff[nt] + ks * 6144), ldstr(sb + woff[nt] + ks * 6144 + 768));
#pragma unroll
      for (int nt = 0; nt < 3; ++nt)
#pragma unroll
        for (int tt = 0; tt < 2; ++tt) acc[tt][nt] = mfma16(A[nt], B[tt], acc[tt][nt]);
      {
        u32x4 u = __builtin_bit_cast(u32x4, nh ? B[1] : B[0]);
#pragma unroll
        for (int j = 0; j < 4; ++j) { s1 = dot2(u[j], 0x3F803F80u, s1); s2 = dot2(u[j], u[j], s2); }
      }
      cs = mfma16(A[0], ones, cs);
    }
  };
  pipe_steps<0, 8, K1_NS, 4>(issue, compute);

  float* st = (float*)(smem + K1_STAGE * K1_NS);
  float* csp = (float*)(smem + K1_STAGE * K1_NS + 1024);
  s1 = xsum(s1); s2 = xsum(s2);
  if (g == 0) {
    float mean = s1 * (1.f / 512.f);
    float var = fmaxf(s2 * (1.f / 512.f) - mean * mean, 0.f);
    float rstd = rsqrtf(var + LN_EPS);
    *(f32x2*)(st + (tg * 32 + nh * 16 + i) * 2) = f32x2{mean, rstd};
  }
  if (i == 0 && tg < 3) *(f32x4*)(csp + un[0] * 16 + 4 * g) = cs;
  bar();
  f32x2 ms[2];
#pragma unroll
  for (int tt = 0; tt < 2; ++tt) ms[tt] = *(const f32x2*)(st + (tg * 32 + tt * 16 + i) * 2);
#pragma unroll
  for (int nt = 0; nt < 3; ++nt) {
    f32x4 c = *(const f32x4*)(csp + un[nt] * 16 + 4 * g);
    int n0 = cb * 96 + un[nt] * 16;
    char* dst = qkv + (size_t)(n0 >> 4) * (2048 * 32) + g * 8;
#pragma unroll
    for (int tt = 0; tt < 2; ++tt) {
      f32x4 y = (acc[tt][nt] - ms[tt][0] * c) * ms[tt][1];
      u32x2 o = {pack2(y[0], y[1]), pack2(y[2], y[3])};
      *(u32x2*)(dst + (size_t)(tb * 128 + tg * 32 + tt * 16 + i) * 32) = o;
    }
  }
}

constexpr int K2_LDS = 4 * 12288;
__global__ __launch_bounds__(512) void k2_attn(const char* __restrict__ qkv, char* __restrict__ o,
    const char* __restrict__ wo, const char* __restrict__ w1, const char* __restrict__ w3, const char* __restrict__ w2) {
  extern __shared__ __attribute__((aligned(16))) char smem[];
  const int tid = threadIdx.x, lane = tid & 63, wv = uni(tid >> 6);
  const int g = lane >> 4, i = lane & 15, q4 = (lane >> 2) & 3, p4 = lane & 3;
  const int b = blockIdx.x;
  const int hp = (b & 7) * 2 + ((b >> 3) & 1), qb = b >> 4;
  const int q0 = qb * 128;
  const unsigned lds0 = (unsigned)(size_t)(LAS char*)smem;
  const size_t HS = 2048 * 32;
  {
    const int r = wv >> 1;
    const char* src = qkv + (size_t)((1 + (r >> 1)) * 32 + hp * 2 + (r & 1)) * HS;
#pragma unroll
    for (int j = 0; j < 6; ++j) {
      int ch = ((wv & 1) * 6 + j) * 64 + lane;
      int key = q0 - 256 + (ch >> 1);
      key = key < 0 ? 0 : key;
      dma16(src + (size_t)key * 32 + (ch & 1) * 16, lds0 + r * 12288 + ((wv & 1) * 6 + j) * 1024);
    }
  }
  const int hh = wv >> 2, wq = wv & 3;
  const int head = hp * 2 + hh;
  bf16x8 Q[2];
#pragma unroll
  for (int u = 0; u < 2; ++u) {
    int qs = q0 + wq * 32 + u * 16;
    u32x4 v = *(const u32x4*)(qkv + (size_t)head * HS + (size_t)(qs + i) * 32 + (g & 1) * 16);
    if (g >= 2) v = u32x4{0, 0, 0, 0};
    Q[u] = __builtin_bit_cast(bf16x8, v);
  }
  wait_bar<0>();
  {
    const unsigned gt = b * 512 + tid;
    const unsigned sc = lds0 + K2_LDS + wv * 1024;
    if (b < 64) dma16(wo + (size_t)gt * 16, sc);
    if (b < 224) { dma16(w1 + (size_t)gt * 16, sc); dma16(w3 + (size_t)gt * 16, sc); dma16(w2 + (size_t)gt * 16, sc); }
  }
  const char* Kl = smem + hh * 12288;
  const char* Vl = smem + 24576 + hh * 12288;
  const float c = 0.25f * 1.4426950408889634f;
  const float NEG = -1e30f;
#pragma unroll
  for (int u = 0; u < 2; ++u) {
    const int qs = q0 + wq * 32 + u * 16;
    const int lrow = wq * 32 + u * 16;
    f32x4 s[17];
#pragma unroll
    for (int t = 0; t < 17; ++t) {
      bf16x8 A = lds128(Kl + (lrow + t * 16 + i) * 32 + (g & 1) * 16);
      s[t] = mfma16(A, Q[u], f32x4{0, 0, 0, 0});
    }
#pragma unroll
    for (int r = 0; r < 4; ++r) {
      s[0][r] = (4 * g + r > i) ? s[0][r] : NEG;
      s[16][r] = (4 * g + r <= i) ? s[16][r] : NEG;
    }
    if (qs < 256) {
      const int nneg = (256 - qs) >> 4;
#pragma unroll
      for (int t = 0; t < 16; ++t)
        if (t < nneg) s[t] = f32x4{NEG, NEG, NEG, NEG};
    }
    float m = s[16][0];
#pragma unroll
    for (int t = 0; t < 17; ++t)
#pragma unroll
      for (int r = 0; r < 4; ++r) m = fmaxf(m, s[t][r]);
    m = xmax(m);
    const float mc = m * c;
    float sum = 0.f;
    unsigned pk[17][2];
#pragma unroll
    for (int t = 0; t < 17; ++t) {
      float e0 = __builtin_amdgcn_exp2f(s[t][0] * c - mc), e1 = __builtin_amdgcn_exp2f(s[t][1] * c - mc);
      float e2 = __builtin_amdgcn_exp2f(s[t][2] * c - mc), e3 = __builtin_amdgcn_exp2f(s[t][3] * c - mc);
      sum += (e0 + e1) + (e2 + e3);
      pk[t][0] = pack2(e0, e1); pk[t][1] = pack2(e2, e3);
    }
    sum = xsum(sum);
    f32x4 oacc = f32x4{0, 0, 0, 0};
#pragma unroll
    for (int tp = 0; tp < 9; ++tp) {
      const int t0 = 2 * tp, t1 = (2 * tp + 1 < 17) ? 2 * tp + 1 : 16;
      bf16x8 A = cat(ldstr(Vl + (lrow + t0 * 16 + 4 * g + q4) * 32 + p4 * 8), ldstr(Vl + (lrow + t1 * 16 + 4 * g + q4) * 32 + p4 * 8));
      u32x4 pb = {pk[t0][0], pk[t0][1], (2 * tp + 1 < 17) ? pk[t1][0] : 0u, (2 * tp + 1 < 17) ? pk[t1][1] : 0u};
      oacc = mfma16(A, __builtin_bit_cast(bf16x8, pb), oacc);
    }
    const float inv = 1.f / sum;
    u32x2 ov = {pack2(oacc[0] * inv, oacc[1] * inv), pack2(oacc[2] * inv, oacc[3] * inv)};
    *(u32x2*)(o + (size_t)(qs + i) * 1024 + head * 32 + g * 8) = ov;
  }
  asm volatile("s_waitcnt vmcnt(0)" ::: "memory");
}

constexpr int G_STAGE = 16384;
template <int NK, int NS, bool STATS>
__global__ __launch_bounds__(512) void k_gemm64(const char* __restrict__ a, const char* __restrict__ w,
                                                const char* __restrict__ resid, char* __restrict__ out,
                                                float* __restrict__ stats) {
  extern __shared__ __attribute__((aligned(16))) char smem[];
  const int tid = threadIdx.x, lane = tid & 63, wv = uni(tid >> 6);
  const int g = lane >> 4, i = lane & 15, q4 = (lane >> 2) & 3, p4 = lane & 3;
  const int tg = wv & 1, ng = (wv >> 1) & 1, kp = wv >> 2;
  const int b = blockIdx.x;
  const int cb = (b >> 3) & 7, tb = (b & 7) * 4 + (b >> 6);
  const unsigned lds0 = (unsigned)(size_t)(LAS char*)smem;
  constexpr int LDA = NK * 128;
  const char* pt = a + (size_t)(tb * 64 + wv * 8 + (lane >> 3)) * LDA + (((lane & 7) ^ (lane >> 3)) << 4);
  const char* pw;
  {
    int k = wv * 8 + (lane >> 3), cc = lane & 7;
    int unit = (cc >> 1) ^ (((k >> 1) & 1) | (((k >> 3) & 1) << 1));
    pw = w + (size_t)k * 1024 + cb * 128 + unit * 32 + (cc & 1) * 16;
  }
  int kk = ((b >> 3) * 5) % NK;
  auto issue = [&](int slot) {
    unsigned base = lds0 + slot * G_STAGE;
    dma16(pt + kk * 128, base + wv * 1024);
    dma16(pw + (size_t)kk * (64 * 1024), base + 8192 + wv * 1024);
    kk = (kk + 1 == NK) ? 0 : kk + 1;
  };
#pragma unroll
  for (int s = 0; s < (NS - 1 < NK ? NS - 1 : NK); ++s) issue(s);
  f32x4 acc[2][2];
#pragma unroll
  for (int t = 0; t < 2; ++t) { acc[t][0] = f32x4{0, 0, 0, 0}; acc[t][1] = f32x4{0, 0, 0, 0}; }
  const int toff = (tg * 32 + i) * 128 + (((kp * 4 + g) ^ (i & 7)) << 4);
  int woff[2];
#pragma unroll
  for (int nt = 0; nt < 2; ++nt)
    woff[nt] = 8192 + (kp * 32 + 8 * g + q4) * 128 + (((ng * 2 + nt) ^ (((q4 >> 1) & 1) | ((g & 1) << 1))) << 5) + p4 * 8;
  auto compute = [&](int slot, int) {
    const char* sb = smem + slot * G_STAGE;
    bf16x8 B[2], A[2];
#pragma unroll
    for (int tt = 0; tt < 2; ++tt) B[tt] = lds128(sb + toff + tt * 2048);
#pragma unroll
    for (int nt = 0; nt < 2; ++nt) A[nt] = cat(ldstr(sb + woff[nt]), ldstr(sb + woff[nt] + 512));
#pragma unroll
    for (int nt = 0; nt < 2; ++nt)
#pragma unroll
      for (int tt = 0; tt < 2; ++tt) acc[tt][nt] = mfma16(A[nt], B[tt], acc[tt][nt]);
  };
  if constexpr (NK <= NS) {
    pipe_steps<0, NK, NS, 2>(issue, compute);
  } else {
    static_assert(NK % NS == 0, "");
    for (int it = 0; it < NK / NS - 1; ++it) {
#pragma unroll
      for (int s = 0; s < NS; ++s) {
        wait_bar<(NS - 2) * 2>();
        issue((s + NS - 1) % NS);
        compute(s, 0);
      }
    }
    pipe_steps<NK - NS, NK, NS, 2>(issue, compute);
  }
  bar();
  {
    float* xb = (float*)smem;
    f32x4* mine = (f32x4*)xb + wv * 128;
#pragma unroll
    for (int nt = 0; nt < 2; ++nt) mine[nt * 64 + lane] = kp ? acc[0][nt] : acc[1][nt];
    bar();
    const f32x4* oth = (const f32x4*)xb + (wv ^ 4) * 128;
    const int token = tb * 64 + tg * 32 + kp * 16 + i;
    float s1 = 0.f, s2 = 0.f;
#pragma unroll
    for (int nt = 0; nt < 2; ++nt) {
      f32x4 v = (kp ? acc[1][nt] : acc[0][nt]) + oth[nt * 64 + lane];
      const size_t off = (size_t)token * 1024 + (cb * 64 + ng * 32 + nt * 16 + 4 * g) * 2;
      u32x2 r = *(const u32x2*)(resid + off);
      v[0] += bflo(r[0]); v[1] += bfhi(r[0]); v[2] += bflo(r[1]); v[3] += bfhi(r[1]);
      u32x2 ov = {pack2(v[0], v[1]), pack2(v[2], v[3])};
      *(u32x2*)(out + off) = ov;
      if constexpr (STATS) {
        s1 = dot2(ov[0], 0x3F803F80u, s1); s1 = dot2(ov[1], 0x3F803F80u, s1);
        s2 = dot2(ov[0], ov[0], s2); s2 = dot2(ov[1], ov[1], s2);
      }
    }
    if constexpr (STATS) {
      s1 = xsum(s1); s2 = xsum(s2);
      if (g == 0) *(f32x2*)(stats + ((size_t)token * 16 + cb * 2 + ng) * 2) = f32x2{s1, s2};
    }
  }
}

constexpr int K4_STAGE = 16384 + 28672, K4_NS = 3;
constexpr int K4_LDS = K4_STAGE * K4_NS + 4 * 224 * 4;

template <int NT, class Issue>
DEV void k4_body(char* smem, Issue& issue, int tg, int half, int lane, const f32x2* ms, char* __restrict__ hh, int tok0, int col0) {
  const int g = lane >> 4, i = lane & 15, q4 = (lane >> 2) & 3, p4 = lane & 3;
  f32x4 acc[2][2 * NT], csg = f32x4{0, 0, 0, 0}, csu = f32x4{0, 0, 0, 0};
#pragma unroll
  for (int a = 0; a < 2 * NT; ++a) { acc[0][a] = f32x4{0, 0, 0, 0}; acc[1][a] = f32x4{0, 0, 0, 0}; }
  const bf16x8 ones = {0x3F80, 0x3F80, 0x3F80, 0x3F80, 0x3F80, 0x3F80, 0x3F80, 0x3F80};
  const int toff0 = (tg * 32 + i) * 128 + ((g ^ (i & 7)) << 4);
  const int toff1 = (tg * 32 + i) * 128 + (((4 + g) ^ (i & 7)) << 4);
  const int wrow = 16384 + (8 * g + q4) * 448 + p4 * 8;
  int pj[NT], wg[NT], wu[NT];
#pragma unroll
  for (int a = 0; a < NT; ++a) {
    int j = a + tg; j = j >= NT ? j - NT : j; j = j >= NT ? j - NT : j;
    pj[a] = half * 4 + j;
    wg[a] = wrow + ((pj[a] ^ (g & 1)) << 5);
    wu[a] = wrow + (((7 + pj[a]) ^ (g & 1)) << 5);
  }
  auto compute = [&](int slot, int kt) {
    const char* sb = smem + slot * K4_STAGE;
#pragma unroll
    for (int ks = 0; ks < 2; ++ks) {
      bf16x8 B[2];
#pragma unroll
      for (int tt = 0; tt < 2; ++tt) B[tt] = lds128(sb + (ks ? toff1 : toff0) + tt * 2048);
#pragma unroll
      for (int a = 0; a < 2 * NT; ++a) {
        const char* wp = sb + ((a < NT) ? wg[a] : wu[a - NT]) + ks * (32 * 448);
        bf16x8 A = cat(ldstr(wp), ldstr(wp + 4 * 448));
        acc[0][a] = mfma16(A, B[0], acc[0][a]);
        acc[1][a] = mfma16(A, B[1], acc[1][a]);
        if (a == 0) csg = mfma16(A, ones, csg);
        if (a == NT) csu = mfma16(A, ones, csu);
      }
    }
  };
  pipe_steps<0, 8, K4_NS, 6>(issue, compute);
  float* csp = (float*)(smem + K4_STAGE * K4_NS);
  if (i == 0 && tg < NT) {
    *(f32x4*)(csp + pj[0] * 16 + 4 * g) = csg;
    *(f32x4*)(csp + (7 + pj[0]) * 16 + 4 * g) = csu;
  }
  bar();
#pragma unroll
  for (int a = 0; a < NT; ++a) {
    f32x4 cg = *(const f32x4*)(csp + pj[a] * 16 + 4 * g);
    f32x4 cu = *(const f32x4*)(csp + (7 + pj[a]) * 16 + 4 * g);
#pragma unroll
    for (int tt = 0; tt < 2; ++tt) {
      f32x4 ga = (acc[tt][a] - ms[tt][0] * cg) * ms[tt][1];
      f32x4 up = (acc[tt][NT + a] - ms[tt][0] * cu) * ms[tt][1];
      float r[4];
#pragma unroll
      for (int e = 0; e < 4; ++e) {
        float sg = __builtin_amdgcn_rcpf(1.f + __builtin_amdgcn_exp2f(-1.4426950408889634f * ga[e]));
        r[e] = ga[e] * sg * up[e];
      }
      u32x2 ov = {pack2(r[0], r[1]), pack2(r[2], r[3])};
      *(u32x2*)(hh + (size_t)(tok0 + tg * 32 + tt * 16 + i) * (F_ * 2) + (col0 + pj[a] * 16 + 4 * g) * 2) = ov;
    }
  }
}

__global__ __launch_bounds__(512) void k4_mlp(const char* __restrict__ h, const char* __restrict__ w1,
                                              const char* __restrict__ w3, const float* __restrict__ stats,
                                              char* __restrict__ hh) {
  extern __shared__ __attribute__((aligned(16))) char smem[];
  const int tid = threadIdx.x, lane = tid & 63, wv = uni(tid >> 6);
  const int g = lane >> 4, i = lane & 15;
  const int tg = wv & 3, half = wv >> 2;
  const int b = blockIdx.x, xcd = b & 7, bi = b >> 3;
  const int cb = (xcd & 3) * 4 + (bi & 3), tb = (xcd >> 2) * 8 + (bi >> 2);
  const unsigned lds0 = (unsigned)(size_t)(LAS char*)smem;
  const char* pt = h + (size_t)(tb * 128 + wv * 16 + (lane >> 3)) * 1024 + (((lane & 7) ^ (lane >> 3)) << 4);
  const char* pw[4];
#pragma unroll
  for (int j = 0; j < 4; ++j) {
    int qq = (j < 3) ? ((wv * 3 + j) * 64 + lane) : (1536 + wv * 32 + (lane & 31));
    int k = qq / 28, c = (qq % 28) ^ (((k >> 3) & 1) << 1);
    const char* base = (c < 14) ? w1 : w3;
    int cc = (c < 14) ? c : c - 14;
    pw[j] = base + (size_t)k * (F_ * 2) + cb * 224 + cc * 16;
  }
  int kk = bi & 7;
  auto issue = [&](int slot) {
    unsigned base = lds0 + slot * K4_STAGE;
    dma16(pt + kk * 128, base + wv * 2048);
    dma16(pt + kk * 128 + 8192, base + wv * 2048 + 1024);
#pragma unroll
    for (int j = 0; j < 3; ++j) dma16(pw[j] + (size_t)kk * (64 * F_ * 2), base + 16384 + (wv * 3 + j) * 1024);
    if (lane < 32) dma16(pw[3] + (size_t)kk * (64 * F_ * 2), base + 16384 + 1536 * 16 + wv * 512);
    kk = (kk + 1) & 7;
  };
#pragma unroll
  for (int s = 0; s < K4_NS - 1; ++s) issue(s);
  f32x2 ms[2];
#pragma unroll
  for (int tt = 0; tt < 2; ++tt) {
    const f32x4* sp = (const f32x4*)(stats + ((size_t)(tb * 128 + tg * 32 + tt * 16 + i) * 16 + 4 * g) * 2);
    f32x4 a = sp[0], c = sp[1];
    float s1 = (a[0] + a[2]) + (c[0] + c[2]), s2 = (a[1] + a[3]) + (c[1] + c[3]);
    s1 = xsum(s1); s2 = xsum(s2);
    float mean = s1 * (1.f / 512.f);
    float var = fmaxf(s2 * (1.f / 512.f) - mean * mean, 0.f);
    ms[tt] = f32x2{mean, rsqrtf(var + LN_EPS)};
  }
  if (half == 0) k4_body<4>(smem, issue, tg, 0, lane, ms, hh, tb * 128, cb * 112);
  else k4_body<3>(smem, issue, tg, 1, lane, ms, hh, tb * 128, cb * 112);
}

extern "C" void kernel_launch(void* const* d_in, const int* in_sizes, int n_in, void* d_out, int out_size,
                              void* d_ws, size_t ws_size, hipStream_t stream) {
  const char* x = (const char*)d_in[0];
  const char* wqkv = (const char*)d_in[1];
  const char* wo = (const char*)d_in[2];
  const char* w1 = (const char*)d_in[4];
  const char* w3 = (const char*)d_in[5];
  const char* w2 = (const char*)d_in[6];
  char* ws = (char*)d_ws;
  char* qkv = ws;
  char* o = ws + (6u << 20);
  char* h = ws + (8u << 20);
  float* stats = (float*)(ws + (10u << 20));
  char* hh = ws + (11u << 20);
  constexpr int G3_NS = 8, G5_NS = 7;
  (void)hipFuncSetAttribute((const void*)k1_qkv, hipFuncAttributeMaxDynamicSharedMemorySize, K1_LDS);
  (void)hipFuncSetAttribute((const void*)k2_attn, hipFuncAttributeMaxDynamicSharedMemorySize, K2_LDS + 8192);
  (void)hipFuncSetAttribute((const void*)k_gemm64<8, G3_NS, true>, hipFuncAttributeMaxDynamicSharedMemorySize, G_STAGE * G3_NS);
  (void)hipFuncSetAttribute((const void*)k4_mlp, hipFuncAttributeMaxDynamicSharedMemorySize, K4_LDS);
  (void)hipFuncSetAttribute((const void*)k_gemm64<28, G5_NS, false>, hipFuncAttributeMaxDynamicSharedMemorySize, G_STAGE * G5_NS);
  k1_qkv<<<256, 512, K1_LDS, stream>>>(x, wqkv, qkv);
  k2_attn<<<256, 512, K2_LDS + 8192, stream>>>(qkv, o, wo, w1, w3, w2);
  k_gemm64<8, G3_NS, true><<<256, 512, G_STAGE * G3_NS, stream>>>(o, wo, x, h, stats);
  k4_mlp<<<256, 512, K4_LDS, stream>>>(h, w1, w3, stats, hh);
  k_gemm64<28, G5_NS, false><<<256, 512, G_STAGE * G5_NS, stream>>>(hh, w2, h, (char*)d_out, nullptr);
}
```

```cpp
#include <hip/hip_runtime.h>
#include <hip/hip_bf16.h>
#include <stdint.h>

typedef short bf16x8 __attribute__((ext_vector_type(8)));
typedef short s16x4 __attribute__((ext_vector_type(4)));
typedef float f32x4 __attribute__((ext_vector_type(4)));
typedef float f32x2 __attribute__((ext_vector_type(2)));
typedef unsigned u32x4 __attribute__((ext_vector_type(4)));
typedef unsigned u32x2 __attribute__((ext_vector_type(2)));
typedef __bf16 bf16x2 __attribute__((ext_vector_type(2)));
#define LAS __attribute__((address_space(3)))
#define DEV __device__ __forceinline__

constexpr int S_ = 2048, D_ = 512, H_ = 32, F_ = 1792;
constexpr float LN_EPS = 1e-5f;

DEV f32x4 mfma16(bf16x8 a, bf16x8 b, f32x4 c) { return __builtin_amdgcn_mfma_f32_16x16x32_bf16(a, b, c, 0, 0, 0); }
DEV unsigned pack2(float a, float b) {
  f32x2 v = {a, b};
  return __builtin_bit_cast(unsigned, __builtin_convertvector(v, bf16x2));
}
DEV float bflo(unsigned u) { return __builtin_bit_cast(float, u << 16); }
DEV float bfhi(unsigned u) { return __builtin_bit_cast(float, u & 0xffff0000u); }
DEV float dot2(unsigned a, unsigned b, float c) {
  return __builtin_amdgcn_fdot2_f32_bf16(__builtin_bit_cast(bf16x2, a), __builtin_bit_cast(bf16x2, b), c, false);
}
DEV void dma16(const char* gptr, unsigned ldsbase) {
  unsigned sv;
  asm volatile("s_mov_b32 %0, m0\n\ts_mov_b32 m0, %2\n\ts_nop 0\n\tglobal_load_lds_dwordx4 %1, off\n\ts_mov_b32 m0, %0"
               : "=&s"(sv) : "v"(gptr), "s"(ldsbase) : "memory");
}
DEV void dma16s(unsigned voff, const char* sbase, unsigned ldsbase) {
  unsigned sv;
  asm volatile("s_mov_b32 %0, m0\n\ts_mov_b32 m0, %3\n\ts_nop 0\n\tglobal_load_lds_dwordx4 %1, %2\n\ts_mov_b32 m0, %0"
               : "=&s"(sv) : "v"(voff), "s"(sbase), "s"(ldsbase) : "memory");
}
DEV void dma16sm(unsigned voff, const char* sbase, unsigned ldsbase, unsigned long long mask) {
  unsigned sv;
  asm volatile("s_mov_b32 %0, m0\n\ts_mov_b32 m0, %3\n\ts_mov_b64 exec, %4\n\tglobal_load_lds_dwordx4 %1, %2\n\ts_mov_b64 exec, -1\n\ts_mov_b32 m0, %0"
               : "=&s"(sv) : "v"(voff), "s"(sbase), "s"(ldsbase), "s"(mask) : "memory");
}
template <int N> DEV void wait_bar() { asm volatile("s_waitcnt vmcnt(%0) lgkmcnt(0)\n\ts_barrier" ::"n"(N) : "memory"); }
DEV void bar() { asm volatile("s_waitcnt lgkmcnt(0)\n\ts_barrier" ::: "memory"); }
DEV bf16x8 lds128(const char* p) { return *(const LAS bf16x8*)(LAS const char*)p; }
DEV s16x4 ldstr(const char* p) { return __builtin_amdgcn_ds_read_tr16_b64_v4i16((LAS s16x4*)p); }
DEV bf16x8 cat(s16x4 a, s16x4 b) { return __builtin_shufflevector(a, b, 0, 1, 2, 3, 4, 5, 6, 7); }
DEV float xsum(float v) { v += __shfl_xor(v, 16); v += __shfl_xor(v, 32); return v; }
DEV float xmax(float v) { v = fmaxf(v, __shfl_xor(v, 16)); v = fmaxf(v, __shfl_xor(v, 32)); return v; }
DEV unsigned uni(unsigned v) { return __builtin_amdgcn_readfirstlane(v); }

#define STAMP_DECL
#define STAMP(i)
#define STAMP_OUT(n)
template <int A, int B> struct Min { static constexpr int v = A < B ? A : B; };

template <int KT, int NK, int NS, int P, class Issue, class Compute>
DEV void pipe_steps(Issue& issue, Compute& compute) {
  if constexpr (KT < NK) {
    wait_bar<(Min<KT + NS - 1, NK>::v - KT - 1) * P>();
    if constexpr (KT + NS - 1 < NK) issue((KT + NS - 1) % NS);
    compute(KT % NS, KT);
    pipe_steps<KT + 1, NK, NS, P>(issue, compute);
  }
}

constexpr int K1_STAGE = 16384 + 12288, K1_NS = 5;
constexpr int K1_LDS = K1_STAGE * K1_NS + 1024 + 1536;

__global__ __launch_bounds__(512) void k1_qkv(const char* __restrict__ x, const char* __restrict__ w,
                                              char* __restrict__ qkv) {
  extern __shared__ __attribute__((aligned(16))) char smem[];
  const int tid = threadIdx.x, lane = tid & 63, wv = uni(tid >> 6);
  const int g = lane >> 4, i = lane & 15, q4 = (lane >> 2) & 3, p4 = lane & 3;
  const int tg = wv & 3, nh = wv >> 2;
  const int b = blockIdx.x, xcd = b & 7, bi = b >> 3;
  const int cb = (xcd & 1) * 8 + (bi & 7), tb = (xcd >> 1) * 4 + (bi >> 3);
  const unsigned lds0 = (unsigned)(size_t)(LAS char*)smem;

  const char* pt;
  {
    int row = tb * 128 + wv * 16 + (lane >> 3);
    int c = (lane & 7) ^ (lane >> 3);
    pt = x + (size_t)row * 1024 + c * 16;
  }
  const char *pw0, *pw1;
  {
    int qq = wv * 64 + lane;
    int k = qq / 12, c = (qq % 12) ^ (((k >> 3) & 1) << 1);
    pw0 = w + (size_t)k * 3072 + cb * 192 + c * 16;
    qq = 512 + wv * 32 + (lane & 31);
    k = qq / 12; c = (qq % 12) ^ (((k >> 3) & 1) << 1);
    pw1 = w + (size_t)k * 3072 + cb * 192 + c * 16;
  }
  int kk = bi & 7;
  auto issue = [&](int slot) {
    unsigned base = lds0 + slot * K1_STAGE;
    dma16(pt + kk * 128, base + wv * 2048);
    dma16(pt + kk * 128 + 8192, base + wv * 2048 + 1024);
    dma16(pw0 + (size_t)kk * (64 * 3072), base + 16384 + wv * 1024);
    if (lane < 32) dma16(pw1 + (size_t)kk * (64 * 3072), base + 16384 + 8192 + wv * 512);
    kk = (kk + 1) & 7;
  };
#pragma unroll
  for (int s = 0; s < K1_NS - 1; ++s) issue(s);

  f32x4 acc[2][3], cs = f32x4{0, 0, 0, 0};
#pragma unroll
  for (int a = 0; a < 3; ++a) { acc[0][a] = f32x4{0, 0, 0, 0}; acc[1][a] = f32x4{0, 0, 0, 0}; }
  float s1 = 0.f, s2 = 0.f;
  const bf16x8 ones = {0x3F80, 0x3F80, 0x3F80, 0x3F80, 0x3F80, 0x3F80, 0x3F80, 0x3F80};
  const int toff0 = (tg * 32 + i) * 128 + ((g ^ (i & 7)) << 4);
  const int toff1 = (tg * 32 + i) * 128 + (((4 + g) ^ (i & 7)) << 4);
  int woff[3], un[3];
#pragma unroll
  for (int nt = 0; nt < 3; ++nt) {
    int a = nt + tg; a = a >= 3 ? a - 3 : a; a = a >= 3 ? a - 3 : a;
    un[nt] = nh * 3 + a;
    woff[nt] = 16384 + (8 * g + q4) * 192 + ((un[nt] ^ (g & 1)) << 5) + p4 * 8;
  }

  auto compute = [&](int slot, int kt) {
    const char* sb = smem + slot * K1_STAGE;
#pragma unroll
    for (int ks = 0; ks < 2; ++ks) {
      bf16x8 B[2], A[3];
#pragma unroll
      for (int tt = 0; tt < 2; ++tt) B[tt] = lds128(sb + (ks ? toff1 : toff0) + tt * 2048);
#pragma unroll
      for (int nt = 0; nt < 3; ++nt)
        A[nt] = cat(ldstr(sb + woff[nt] + ks * 6144), ldstr(sb + woff[nt] + ks * 6144 + 768));
#pragma unroll
      for (int nt = 0; nt < 3; ++nt)
#pragma unroll
        for (int tt = 0; tt < 2; ++tt) acc[tt][nt] = mfma16(A[nt], B[tt], acc[tt][nt]);
      {
        u32x4 u = __builtin_bit_cast(u32x4, nh ? B[1] : B[0]);
#pragma unroll
        for (int j = 0; j < 4; ++j) { s1 = dot2(u[j], 0x3F803F80u, s1); s2 = dot2(u[j], u[j], s2); }
      }
      cs = mfma16(A[0], ones, cs);
    }
  };
  pipe_steps<0, 8, K1_NS, 4>(issue, compute);

  float* st = (float*)(smem + K1_STAGE * K1_NS);
  float* csp = (float*)(smem + K1_STAGE * K1_NS + 1024);
  s1 = xsum(s1); s2 = xsum(s2);
  if (g == 0) {
    float mean = s1 * (1.f / 512.f);
    float var = fmaxf(s2 * (1.f / 512.f) - mean * mean, 0.f);
    float rstd = rsqrtf(var + LN_EPS);
    *(f32x2*)(st + (tg * 32 + nh * 16 + i) * 2) = f32x2{mean, rstd};
  }
  if (i == 0 && tg < 3) *(f32x4*)(csp + un[0] * 16 + 4 * g) = cs;
  bar();
  f32x2 ms[2];
#pragma unroll
  for (int tt = 0; tt < 2; ++tt) ms[tt] = *(const f32x2*)(st + (tg * 32 + tt * 16 + i) * 2);
#pragma unroll
  for (int nt = 0; nt < 3; ++nt) {
    f32x4 c = *(const f32x4*)(csp + un[nt] * 16 + 4 * g);
    int n0 = cb * 96 + un[nt] * 16;
    char* dst = qkv + (size_t)(n0 >> 4) * (2048 * 32) + g * 8;
#pragma unroll
    for (int tt = 0; tt < 2; ++tt) {
      f32x4 y = (acc[tt][nt] - ms[tt][0] * c) * ms[tt][1];
      u32x2 o = {pack2(y[0], y[1]), pack2(y[2], y[3])};
      *(u32x2*)(dst + (size_t)(tb * 128 + tg * 32 + tt * 16 + i) * 32) = o;
    }
  }
}

constexpr int K2_LDS = 4 * 12288;
__global__ __launch_bounds__(512) void k2_attn(const char* __restrict__ qkv, char* __restrict__ o) {
  extern __shared__ __attribute__((aligned(16))) char smem[];
  const int tid = threadIdx.x, lane = tid & 63, wv = uni(tid >> 6);
  const int g = lane >> 4, i = lane & 15, q4 = (lane >> 2) & 3, p4 = lane & 3;
  const int b = blockIdx.x;
  const int hp = (b & 7) * 2 + ((b >> 3) & 1), qb = b >> 4;
  const int q0 = qb * 128;
  const unsigned lds0 = (unsigned)(size_t)(LAS char*)smem;
  const size_t HS = 2048 * 32;
  {
    const int r = wv >> 1;
    const char* src = qkv + (size_t)((1 + (r >> 1)) * 32 + hp * 2 + (r & 1)) * HS;
#pragma unroll
    for (int j = 0; j < 6; ++j) {
      int ch = ((wv & 1) * 6 + j) * 64 + lane;
      int key = q0 - 256 + (ch >> 1);
      key = key < 0 ? 0 : key;
      dma16(src + (size_t)key * 32 + (ch & 1) * 16, lds0 + r * 12288 + ((wv & 1) * 6 + j) * 1024);
    }
  }
  const int hh = wv >> 2, wq = wv & 3;
  const int head = hp * 2 + hh;
  bf16x8 Q[2];
#pragma unroll
  for (int u = 0; u < 2; ++u) {
    int qs = q0 + wq * 32 + u * 16;
    u32x4 v = *(const u32x4*)(qkv + (size_t)head * HS + (size_t)(qs + i) * 32 + (g & 1) * 16);
    if (g >= 2) v = u32x4{0, 0, 0, 0};
    Q[u] = __builtin_bit_cast(bf16x8, v);
  }
  wait_bar<0>();
  const char* Kl = smem + hh * 12288;
  const char* Vl = smem + 24576 + hh * 12288;
  const float c = 0.25f * 1.4426950408889634f;
  const float NEG = -1e30f;
#pragma unroll
  for (int u = 0; u < 2; ++u) {
    const int qs = q0 + wq * 32 + u * 16;
    const int lrow = wq * 32 + u * 16;
    f32x4 s[17];
#pragma unroll
    for (int t = 0; t < 17; ++t) {
      bf16x8 A = lds128(Kl + (lrow + t * 16 + i) * 32 + (g & 1) * 16);
      s[t] = mfma16(A, Q[u], f32x4{0, 0, 0, 0});
    }
#pragma unroll
    for (int r = 0; r < 4; ++r) {
      s[0][r] = (4 * g + r > i) ? s[0][r] : NEG;
      s[16][r] = (4 * g + r <= i) ? s[16][r] : NEG;
    }
    if (qs < 256) {
      const int nneg = (256 - qs) >> 4;
#pragma unroll
      for (int t = 0; t < 16; ++t)
        if (t < nneg) s[t] = f32x4{NEG, NEG, NEG, NEG};
    }
    float m = s[16][0];
#pragma unroll
    for (int t = 0; t < 17; ++t)
#pragma unroll
      for (int r = 0; r < 4; ++r) m = fmaxf(m, s[t][r]);
    m = xmax(m);
    const float mc = m * c;
    float sum = 0.f;
    unsigned pk[17][2];
#pragma unroll
    for (int t = 0; t < 17; ++t) {
      float e0 = __builtin_amdgcn_exp2f(s[t][0] * c - mc), e1 = __builtin_amdgcn_exp2f(s[t][1] * c - mc);
      float e2 = __builtin_amdgcn_exp2f(s[t][2] * c - mc), e3 = __builtin_amdgcn_exp2f(s[t][3] * c - mc);
      sum += (e0 + e1) + (e2 + e3);
      pk[t][0] = pack2(e0, e1); pk[t][1] = pack2(e2, e3);
    }
    sum = xsum(sum);
    f32x4 oacc = f32x4{0, 0, 0, 0};
#pragma unroll
    for (int tp = 0; tp < 9; ++tp) {
      const int t0 = 2 * tp, t1 = (2 * tp + 1 < 17) ? 2 * tp + 1 : 16;
      bf16x8 A = cat(ldstr(Vl + (lrow + t0 * 16 + 4 * g + q4) * 32 + p4 * 8), ldstr(Vl + (lrow + t1 * 16 + 4 * g + q4) * 32 + p4 * 8));
      u32x4 pb = {pk[t0][0], pk[t0][1], (2 * tp + 1 < 17) ? pk[t1][0] : 0u, (2 * tp + 1 < 17) ? pk[t1][1] : 0u};
      oacc = mfma16(A, __builtin_bit_cast(bf16x8, pb), oacc);
    }
    const float inv = 1.f / sum;
    u32x2 ov = {pack2(oacc[0] * inv, oacc[1] * inv), pack2(oacc[2] * inv, oacc[3] * inv)};
    *(u32x2*)(o + (size_t)(qs + i) * 1024 + head * 32 + g * 8) = ov;
  }
}

constexpr int G_STAGE = 16384;
template <int NK, int NS, bool STATS>
__global__ __launch_bounds__(512) void k_gemm64(const char* __restrict__ a, const char* __restrict__ w,
                                                const char* __restrict__ resid, char* __restrict__ out,
                                                float* __restrict__ stats) {
  extern __shared__ __attribute__((aligned(16))) char smem[];
  const int tid = threadIdx.x, lane = tid & 63, wv = uni(tid >> 6);
  const int g = lane >> 4, i = lane & 15, q4 = (lane >> 2) & 3, p4 = lane & 3;
  const int tg = wv & 1, ng = (wv >> 1) & 1, kp = wv >> 2;
  const int b = blockIdx.x;
  const int cb = (b >> 3) & 7, tb = (b & 7) * 4 + (b >> 6);
  const unsigned lds0 = (unsigned)(size_t)(LAS char*)smem;
  constexpr int LDA = NK * 128;
  const char* pt = a + (size_t)(tb * 64 + wv * 8 + (lane >> 3)) * LDA + (((lane & 7) ^ (lane >> 3)) << 4);
  const char* pw;
  {
    int k = wv * 8 + (lane >> 3), cc = lane & 7;
    int unit = (cc >> 1) ^ (((k >> 1) & 1) | (((k >> 3) & 1) << 1));
    pw = w + (size_t)k * 1024 + cb * 128 + unit * 32 + (cc & 1) * 16;
  }
  int kk = ((b >> 3) * 5) % NK;
  auto issue = [&](int slot) {
    unsigned base = lds0 + slot * G_STAGE;
    dma16(pt + kk * 128, base + wv * 1024);
    dma16(pw + (size_t)kk * (64 * 1024), base + 8192 + wv * 1024);
    kk = (kk + 1 == NK) ? 0 : kk + 1;
  };
#pragma unroll
  for (int s = 0; s < (NS - 1 < NK ? NS - 1 : NK); ++s) issue(s);
  f32x4 acc[2][2];
#pragma unroll
  for (int t = 0; t < 2; ++t) { acc[t][0] = f32x4{0, 0, 0, 0}; acc[t][1] = f32x4{0, 0, 0, 0}; }
  const int toff = (tg * 32 + i) * 128 + (((kp * 4 + g) ^ (i & 7)) << 4);
  int woff[2];
#pragma unroll
  for (int nt = 0; nt < 2; ++nt)
    woff[nt] = 8192 + (kp * 32 + 8 * g + q4) * 128 + (((ng * 2 + nt) ^ (((q4 >> 1) & 1) | ((g & 1) << 1))) << 5) + p4 * 8;
  auto compute = [&](int slot, int) {
    const char* sb = smem + slot * G_STAGE;
    bf16x8 B[2], A[2];
#pragma unroll
    for (int tt = 0; tt < 2; ++tt) B[tt] = lds128(sb + toff + tt * 2048);
#pragma unroll
    for (int nt = 0; nt < 2; ++nt) A[nt] = cat(ldstr(sb + woff[nt]), ldstr(sb + woff[nt] + 512));
#pragma unroll
    for (int nt = 0; nt < 2; ++nt)
#pragma unroll
      for (int tt = 0; tt < 2; ++tt) acc[tt][nt] = mfma16(A[nt], B[tt], acc[tt][nt]);
  };
  if constexpr (NK <= NS) {
    pipe_steps<0, NK, NS, 2>(issue, compute);
  } else {
    static_assert(NK % NS == 0, "");
    for (int it = 0; it < NK / NS - 1; ++it) {
#pragma unroll
      for (int s = 0; s < NS; ++s) {
        wait_bar<(NS - 2) * 2>();
        issue((s + NS - 1) % NS);
        compute(s, 0);
      }
    }
    pipe_steps<NK - NS, NK, NS, 2>(issue, compute);
  }
  bar();
  {
    float* xb = (float*)smem;
    f32x4* mine = (f32x4*)xb + wv * 128;
#pragma unroll
    for (int nt = 0; nt < 2; ++nt) mine[nt * 64 + lane] = kp ? acc[0][nt] : acc[1][nt];
    bar();
    const f32x4* oth = (const f32x4*)xb + (wv ^ 4) * 128;
    const int token = tb * 64 + tg * 32 + kp * 16 + i;
    float s1 = 0.f, s2 = 0.f;
#pragma unroll
    for (int nt = 0; nt < 2; ++nt) {
      f32x4 v = (kp ? acc[1][nt] : acc[0][nt]) + oth[nt * 64 + lane];
      const size_t off = (size_t)token * 1024 + (cb * 64 + ng * 32 + nt * 16 + 4 * g) * 2;
      u32x2 r = *(const u32x2*)(resid + off);
      v[0] += bflo(r[0]); v[1] += bfhi(r[0]); v[2] += bflo(r[1]); v[3] += bfhi(r[1]);
      u32x2 ov = {pack2(v[0], v[1]), pack2(v[2], v[3])};
      *(u32x2*)(out + off) = ov;
      if constexpr (STATS) {
        s1 = dot2(ov[0], 0x3F803F80u, s1); s1 = dot2(ov[1], 0x3F803F80u, s1);
        s2 = dot2(ov[0], ov[0], s2); s2 = dot2(ov[1], ov[1], s2);
      }
    }
    if constexpr (STATS) {
      s1 = xsum(s1); s2 = xsum(s2);
      if (g == 0) *(f32x2*)(stats + ((size_t)token * 16 + cb * 2 + ng) * 2) = f32x2{s1, s2};
    }
  }
}

constexpr int K4_STAGE = 49152, K4_NS = 3;
constexpr int K4_LDS = K4_STAGE * K4_NS + 4 * 224 * 4;

template <int NT>
DEV void k4_body(char* smem, const char* gt, const char* g1, const char* g3, unsigned vt, unsigned vw,
                 unsigned long long wmask, unsigned ldsw, int tg, int half, int lane, const float* __restrict__ stats,
                 char* __restrict__ hh, int tok0, int col0, unsigned st0) {
  STAMP_DECL;
  const int g = lane >> 4, i = lane & 15, q4 = (lane >> 2) & 3, p4 = lane & 3;
  f32x4 acc[2][2 * NT], csg = f32x4{0, 0, 0, 0}, csu = f32x4{0, 0, 0, 0};
#pragma unroll
  for (int a = 0; a < 2 * NT; ++a) { acc[0][a] = f32x4{0, 0, 0, 0}; acc[1][a] = f32x4{0, 0, 0, 0}; }
  const bf16x8 ones = {0x3F80, 0x3F80, 0x3F80, 0x3F80, 0x3F80, 0x3F80, 0x3F80, 0x3F80};
  const int toff0 = (tg * 32 + i) * 128 + ((g ^ (i & 7)) << 4);
  const int toff1 = (tg * 32 + i) * 128 + (((4 + g) ^ (i & 7)) << 4);
  const int fsw = q4 | ((g & 1) << 2);
  int pj[NT], wa[NT];
#pragma unroll
  for (int a = 0; a < NT; ++a) {
    int j = a + tg; j = j >= NT ? j - NT : j; j = j >= NT ? j - NT : j;
    pj[a] = half * 4 + j;
    wa[a] = 16384 + (8 * g + q4) * 256 + ((pj[a] ^ fsw) << 5) + p4 * 8;
  }
  auto dma = [&](int kt, int j) {
    const unsigned base = ldsw + (kt % K4_NS) * K4_STAGE;
    if (j < 2) dma16s(vt, gt + kt * 128 + j * 8192, base + j * 1024);
    else if (j < 4) dma16sm(vw, g1 + (size_t)(kt * 64 + (j - 2) * 4) * (F_ * 2), base + 16384 + (j - 2) * 1024, wmask);
    else dma16sm(vw, g3 + (size_t)(kt * 64 + (j - 4) * 4) * (F_ * 2), base + 32768 + (j - 4) * 1024, wmask);
  };
  auto ldB = [&](bf16x8* B, int kt, int ks) {
    const char* sb = smem + (kt % K4_NS) * K4_STAGE + (ks ? toff1 : toff0);
    B[0] = lds128(sb); B[1] = lds128(sb + 2048);
  };
  auto ldA = [&](int a, int kt, int ks) -> bf16x8 {
    const char* wp = smem + (kt % K4_NS) * K4_STAGE + wa[a < NT ? a : a - NT] + (a < NT ? 0 : 16384) + ks * 8192;
    return cat(ldstr(wp), ldstr(wp + 1024));
  };
  auto mma = [&](int a, bf16x8 A, const bf16x8* B) {
    acc[0][a] = mfma16(A, B[0], acc[0][a]);
    acc[1][a] = mfma16(A, B[1], acc[1][a]);
    if (a == 0) csg = mfma16(A, ones, csg);
    if (a == NT) csu = mfma16(A, ones, csu);
  };
  bf16x8 A0[2 * NT], A1[2 * NT], B0[2], B1[2];
  f32x4 sa[2], sc[2];
  STAMP(1);
#pragma unroll
  for (int j = 0; j < 6; ++j) dma(0, j);
#pragma unroll
  for (int j = 0; j < 6; ++j) dma(1, j);
  STAMP(2);
  wait_bar<6>();
  STAMP(3);
  ldB(B0, 0, 0);
#pragma unroll
  for (int a = 0; a < 2 * NT; ++a) A0[a] = ldA(a, 0, 0);
#pragma unroll
  for (int kt = 0; kt < 8; ++kt) {
    ldB(B1, kt, 1);
#pragma unroll
    for (int a = 0; a < 2 * NT; ++a) {
      if (a < NT) { A1[2 * a] = ldA(2 * a, kt, 1); A1[2 * a + 1] = ldA(2 * a + 1, kt, 1); }
      mma(a, A0[a], B0);
      if (kt == 0 && a >= 2 * NT - 6) dma(2, a - (2 * NT - 6));
      __builtin_amdgcn_sched_barrier(0);
    }
    if (kt < 6) wait_bar<6>();
    else if (kt == 6) wait_bar<0>();
    STAMP(4 + kt);
    if (kt < 7) ldB(B0, kt + 1, 0);
    if (kt == 6) {
#pragma unroll
      for (int tt = 0; tt < 2; ++tt) {
        const f32x4* sp = (const f32x4*)(stats + ((size_t)(tok0 + tg * 32 + tt * 16 + i) * 16 + 4 * g) * 2);
        sa[tt] = sp[0]; sc[tt] = sp[1];
      }
    }
#pragma unroll
    for (int a = 0; a < 2 * NT; ++a) {
      if (kt < 7 && a < NT) { A0[2 * a] = ldA(2 * a, kt + 1, 0); A0[2 * a + 1] = ldA(2 * a + 1, kt + 1, 0); }
      mma(a, A1[a], B1);
      if (kt + 3 < 8 && a >= 2 * NT - 6) dma(kt + 3, a - (2 * NT - 6));
      __builtin_amdgcn_sched_barrier(0);
    }
  }
  STAMP(12);
  float* csp = (float*)(smem + K4_STAGE * K4_NS);
  if (i == 0 && tg < NT) {
    *(f32x4*)(csp + pj[0] * 16 + 4 * g) = csg;
    *(f32x4*)(csp + (7 + pj[0]) * 16 + 4 * g) = csu;
  }
  float rs[2], mr[2];
#pragma unroll
  for (int tt = 0; tt < 2; ++tt) {
    float s1 = (sa[tt][0] + sa[tt][2]) + (sc[tt][0] + sc[tt][2]), s2 = (sa[tt][1] + sa[tt][3]) + (sc[tt][1] + sc[tt][3]);
    s1 = xsum(s1); s2 = xsum(s2);
    float mean = s1 * (1.f / 512.f);
    float var = fmaxf(s2 * (1.f / 512.f) - mean * mean, 0.f);
    rs[tt] = rsqrtf(var + LN_EPS); mr[tt] = mean * rs[tt];
  }
  bar();
  STAMP(13);
  char* tw = smem + (half * 4 + tg) * 4096;
#pragma unroll
  for (int a = 0; a < NT; ++a) {
    f32x4 cg = *(const f32x4*)(csp + pj[a] * 16 + 4 * g);
    f32x4 cu = *(const f32x4*)(csp + (7 + pj[a]) * 16 + 4 * g);
    const int lc = (pj[a] - half * 4) * 2 + (g >> 1);
#pragma unroll
    for (int tt = 0; tt < 2; ++tt) {
      float r[4];
#pragma unroll
      for (int e = 0; e < 4; ++e) {
        float ga = acc[tt][a][e] * rs[tt] - mr[tt] * cg[e];
        float up = acc[tt][NT + a][e] * rs[tt] - mr[tt] * cu[e];
        float sg = __builtin_amdgcn_rcpf(1.f + __builtin_amdgcn_exp2f(-1.4426950408889634f * ga));
        r[e] = ga * sg * up;
      }
      u32x2 ov = {pack2(r[0], r[1]), pack2(r[2], r[3])};
      *(LAS u32x2*)(LAS char*)(tw + (tt * 16 + i) * 128 + ((lc ^ (i & 7)) << 4) + (g & 1) * 8) = ov;
    }
  }
  asm volatile("s_waitcnt lgkmcnt(0)" ::: "memory");
  STAMP(14);
#pragma unroll
  for (int j = 0; j < NT; ++j) {
    const int c = j * 64 + lane, row = c / (2 * NT), ch = c % (2 * NT);
    u32x4 v = *(const LAS u32x4*)(LAS char*)(tw + row * 128 + ((ch ^ (row & 7)) << 4));
    *(u32x4*)(hh + (size_t)(tok0 + tg * 32 + row) * (F_ * 2) + (col0 + half * 64) * 2 + ch * 16) = v;
  }
  STAMP(15);
  STAMP_OUT(17);
}

__global__ __launch_bounds__(512) void k4_mlp(const char* __restrict__ h, const char* __restrict__ w1,
                                              const char* __restrict__ w3, const float* __restrict__ stats,
                                              char* __restrict__ hh) {
  extern __shared__ __attribute__((aligned(16))) char smem[];
  unsigned st0 = 0;
  const int tid = threadIdx.x, lane = tid & 63, wv = uni(tid >> 6);
  const int tg = wv & 3, half = wv >> 2;
  const int b = blockIdx.x, xcd = b & 7, bi = b >> 3;
  const int cb = (xcd & 3) * 4 + (bi & 3), tb = (xcd >> 2) * 8 + (bi >> 2);
  const unsigned lds0 = (unsigned)(size_t)(LAS char*)smem;
  const char* gt = h + (size_t)(tb * 128 + wv * 16) * 1024;
  const unsigned vt = (lane >> 3) * 1024 + (((lane & 7) ^ (lane >> 3)) << 4);
  const char* g1 = w1 + (size_t)(wv * 8) * (F_ * 2) + cb * 224;
  const char* g3 = w3 + (size_t)(wv * 8) * (F_ * 2) + cb * 224;
  const int unit = ((lane >> 1) & 7) ^ ((lane >> 4) | ((wv & 1) << 2));
  const unsigned vw = (lane >> 4) * (F_ * 2) + unit * 32 + (lane & 1) * 16;
  const unsigned long long wmask = __builtin_amdgcn_ballot_w64(unit < 7);
  const unsigned ldsw = lds0 + wv * 2048;
  if (half == 0) k4_body<4>(smem, gt, g1, g3, vt, vw, wmask, ldsw, tg, 0, lane, stats, hh, tb * 128, cb * 112, st0);
  else k4_body<3>(smem, gt, g1, g3, vt, vw, wmask, ldsw, tg, 1, lane, stats, hh, tb * 128, cb * 112, st0);
}

extern "C" void kernel_launch(void* const* d_in, const int* in_sizes, int n_in, void* d_out, int out_size,
                              void* d_ws, size_t ws_size, hipStream_t stream) {
  const char* x = (const char*)d_in[0];
  const char* wqkv = (const char*)d_in[1];
  const char* wo = (const char*)d_in[2];
  const char* w1 = (const char*)d_in[4];
  const char* w3 = (const char*)d_in[5];
  const char* w2 = (const char*)d_in[6];
  char* ws = (char*)d_ws;
  char* qkv = ws;
  char* o = ws + (6u << 20);
  char* h = ws + (8u << 20);
  float* stats = (float*)(ws + (10u << 20));
  char* hh = ws + (11u << 20);
  constexpr int G3_NS = 8, G5_NS = 7;
  (void)hipFuncSetAttribute((const void*)k1_qkv, hipFuncAttributeMaxDynamicSharedMemorySize, K1_LDS);
  (void)hipFuncSetAttribute((const void*)k2_attn, hipFuncAttributeMaxDynamicSharedMemorySize, K2_LDS);
  (void)hipFuncSetAttribute((const void*)k_gemm64<8, G3_NS, true>, hipFuncAttributeMaxDynamicSharedMemorySize, G_STAGE * G3_NS);
  (void)hipFuncSetAttribute((const void*)k4_mlp, hipFuncAttributeMaxDynamicSharedMemorySize, K4_LDS);
  (void)hipFuncSetAttribute((const void*)k_gemm64<28, G5_NS, false>, hipFuncAttributeMaxDynamicSharedMemorySize, G_STAGE * G5_NS);
  k1_qkv<<<256, 512, K1_LDS, stream>>>(x, wqkv, qkv);
  k2_attn<<<256, 512, K2_LDS, stream>>>(qkv, o);
  k_gemm64<8, G3_NS, true><<<256, 512, G_STAGE * G3_NS, stream>>>(o, wo, x, h, stats);
  k4_mlp<<<256, 512, K4_LDS, stream>>>(h, w1, w3, stats, hh);
  k_gemm64<28, G5_NS, false><<<256, 512, G_STAGE * G5_NS, stream>>>(hh, w2, h, (char*)d_out, nullptr);
}
```

```cpp
#include <hip/hip_runtime.h>
#include <hip/hip_bf16.h>
#include <stdint.h>

typedef short bf16x8 __attribute__((ext_vector_type(8)));
typedef short s16x4 __attribute__((ext_vector_type(4)));
typedef float f32x4 __attribute__((ext_vector_type(4)));
typedef float f32x2 __attribute__((ext_vector_type(2)));
typedef unsigned u32x4 __attribute__((ext_vector_type(4)));
typedef unsigned u32x2 __attribute__((ext_vector_type(2)));
typedef __bf16 bf16x2 __attribute__((ext_vector_type(2)));
#define LAS __attribute__((address_space(3)))
#define DEV __device__ __forceinline__

constexpr int S_ = 2048, D_ = 512, H_ = 32, F_ = 1792;
constexpr float LN_EPS = 1e-5f;

DEV f32x4 mfma16(bf16x8 a, bf16x8 b, f32x4 c) { return __builtin_amdgcn_mfma_f32_16x16x32_bf16(a, b, c, 0, 0, 0); }
DEV unsigned pack2(float a, float b) {
  f32x2 v = {a, b};
  return __builtin_bit_cast(unsigned, __builtin_convertvector(v, bf16x2));
}
DEV float bflo(unsigned u) { return __builtin_bit_cast(float, u << 16); }
DEV float bfhi(unsigned u) { return __builtin_bit_cast(float, u & 0xffff0000u); }
DEV float dot2(unsigned a, unsigned b, float c) {
  return __builtin_amdgcn_fdot2_f32_bf16(__builtin_bit_cast(bf16x2, a), __builtin_bit_cast(bf16x2, b), c, false);
}
DEV void dma16(const char* gptr, unsigned ldsbase) {
  unsigned sv;
  asm volatile("s_mov_b32 %0, m0\n\ts_mov_b32 m0, %2\n\ts_nop 0\n\tglobal_load_lds_dwordx4 %1, off\n\ts_mov_b32 m0, %0"
               : "=&s"(sv) : "v"(gptr), "s"(ldsbase) : "memory");
}
DEV void dma16s(unsigned voff, const char* sbase, unsigned ldsbase) {
  unsigned sv;
  asm volatile("s_mov_b32 %0, m0\n\ts_mov_b32 m0, %3\n\ts_nop 0\n\tglobal_load_lds_dwordx4 %1, %2\n\ts_mov_b32 m0, %0"
               : "=&s"(sv) : "v"(voff), "s"(sbase), "s"(ldsbase) : "memory");
}
DEV void dma16sm(unsigned voff, const char* sbase, unsigned ldsbase, unsigned long long mask) {
  unsigned sv;
  asm volatile("s_mov_b32 %0, m0\n\ts_mov_b32 m0, %3\n\ts_mov_b64 exec, %4\n\tglobal_load_lds_dwordx4 %1, %2\n\ts_mov_b64 exec, -1\n\ts_mov_b32 m0, %0"
               : "=&s"(sv) : "v"(voff), "s"(sbase), "s"(ldsbase), "s"(mask) : "memory");
}
DEV void gld16(bf16x8& d, unsigned voff, const char* sbase) {
  asm volatile("global_load_dwordx4 %0, %1, %2" : "=v"(d) : "v"(voff), "s"(sbase) : "memory");
}
template <int N> DEV void wait_bar() { asm volatile("s_waitcnt vmcnt(%0) lgkmcnt(0)\n\ts_barrier" ::"n"(N) : "memory"); }
DEV void bar() { asm volatile("s_waitcnt lgkmcnt(0)\n\ts_barrier" ::: "memory"); }
DEV bf16x8 lds128(const char* p) { return *(const LAS bf16x8*)(LAS const char*)p; }
DEV s16x4 ldstr(const char* p) { return __builtin_amdgcn_ds_read_tr16_b64_v4i16((LAS s16x4*)p); }
DEV bf16x8 cat(s16x4 a, s16x4 b) { return __builtin_shufflevector(a, b, 0, 1, 2, 3, 4, 5, 6, 7); }
DEV float xsum(float v) { v += __shfl_xor(v, 16); v += __shfl_xor(v, 32); return v; }
DEV float xmax(float v) { v = fmaxf(v, __shfl_xor(v, 16)); v = fmaxf(v, __shfl_xor(v, 32)); return v; }
DEV unsigned uni(unsigned v) { return __builtin_amdgcn_readfirstlane(v); }

#define TL0
#define TL1(k)
#define TLARG
#define TLPASS
template <int A, int B> struct Min { static constexpr int v = A < B ? A : B; };

template <int KT, int NK, int NS, int P, class Issue, class Compute>
DEV void pipe_steps(Issue& issue, Compute& compute) {
  if constexpr (KT < NK) {
    wait_bar<(Min<KT + NS - 1, NK>::v - KT - 1) * P>();
    if constexpr (KT + NS - 1 < NK) issue((KT + NS - 1) % NS);
    compute(KT % NS, KT);
    pipe_steps<KT + 1, NK, NS, P>(issue, compute);
  }
}

constexpr int K1_STAGE = 16384 + 12288, K1_NS = 5;
constexpr int K1_LDS = K1_STAGE * K1_NS + 1024 + 1536;

__global__ __launch_bounds__(512) void k1_qkv(const char* __restrict__ x, const char* __restrict__ w,
                                              char* __restrict__ qkv) {
  extern __shared__ __attribute__((aligned(16))) char smem[];
  TL0;
  const int tid = threadIdx.x, lane = tid & 63, wv = uni(tid >> 6);
  const int g = lane >> 4, i = lane & 15, q4 = (lane >> 2) & 3, p4 = lane & 3;
  const int tg = wv & 3, nh = wv >> 2;
  const int b = blockIdx.x, xcd = b & 7, bi = b >> 3;
  const int cb = (xcd & 1) * 8 + (bi & 7), tb = (xcd >> 1) * 4 + (bi >> 3);
  const unsigned lds0 = (unsigned)(size_t)(LAS char*)smem;

  const char* pt;
  {
    int row = tb * 128 + wv * 16 + (lane >> 3);
    int c = (lane & 7) ^ (lane >> 3);
    pt = x + (size_t)row * 1024 + c * 16;
  }
  const char *pw0, *pw1;
  {
    int qq = wv * 64 + lane;
    int k = qq / 12, c = (qq % 12) ^ (((k >> 3) & 1) << 1);
    pw0 = w + (size_t)k * 3072 + cb * 192 + c * 16;
    qq = 512 + wv * 32 + (lane & 31);
    k = qq / 12; c = (qq % 12) ^ (((k >> 3) & 1) << 1);
    pw1 = w + (size_t)k * 3072 + cb * 192 + c * 16;
  }
  int kk = bi & 7;
  auto issue = [&](int slot) {
    unsigned base = lds0 + slot * K1_STAGE;
    dma16(pt + kk * 128, base + wv * 2048);
    dma16(pt + kk * 128 + 8192, base + wv * 2048 + 1024);
    dma16(pw0 + (size_t)kk * (64 * 3072), base + 16384 + wv * 1024);
    if (lane < 32) dma16(pw1 + (size_t)kk * (64 * 3072), base + 16384 + 8192 + wv * 512);
    kk = (kk + 1) & 7;
  };
#pragma unroll
  for (int s = 0; s < K1_NS - 1; ++s) issue(s);

  f32x4 acc[2][3], cs = f32x4{0, 0, 0, 0};
#pragma unroll
  for (int a = 0; a < 3; ++a) { acc[0][a] = f32x4{0, 0, 0, 0}; acc[1][a] = f32x4{0, 0, 0, 0}; }
  float s1 = 0.f, s2 = 0.f;
  const bf16x8 ones = {0x3F80, 0x3F80, 0x3F80, 0x3F80, 0x3F80, 0x3F80, 0x3F80, 0x3F80};
  const int toff0 = (tg * 32 + i) * 128 + ((g ^ (i & 7)) << 4);
  const int toff1 = (tg * 32 + i) * 128 + (((4 + g) ^ (i & 7)) << 4);
  int woff[3], un[3];
#pragma unroll
  for (int nt = 0; nt < 3; ++nt) {
    int a = nt + tg; a = a >= 3 ? a - 3 : a; a = a >= 3 ? a - 3 : a;
    un[nt] = nh * 3 + a;
    woff[nt] = 16384 + (8 * g + q4) * 192 + ((un[nt] ^ (g & 1)) << 5) + p4 * 8;
  }

  auto compute = [&](int slot, int kt) {
    const char* sb = smem + slot * K1_STAGE;
#pragma unroll
    for (int ks = 0; ks < 2; ++ks) {
      bf16x8 B[2], A[3];
#pragma unroll
      for (int tt = 0; tt < 2; ++tt) B[tt] = lds128(sb + (ks ? toff1 : toff0) + tt * 2048);
#pragma unroll
      for (int nt = 0; nt < 3; ++nt)
        A[nt] = cat(ldstr(sb + woff[nt] + ks * 6144), ldstr(sb + woff[nt] + ks * 6144 + 768));
#pragma unroll
      for (int nt = 0; nt < 3; ++nt)
#pragma unroll
        for (int tt = 0; tt < 2; ++tt) acc[tt][nt] = mfma16(A[nt], B[tt], acc[tt][nt]);
      {
        u32x4 u = __builtin_bit_cast(u32x4, nh ? B[1] : B[0]);
#pragma unroll
        for (int j = 0; j < 4; ++j) { s1 = dot2(u[j], 0x3F803F80u, s1); s2 = dot2(u[j], u[j], s2); }
      }
      cs = mfma16(A[0], ones, cs);
    }
  };
  pipe_steps<0, 8, K1_NS, 4>(issue, compute);

  float* st = (float*)(smem + K1_STAGE * K1_NS);
  float* csp = (float*)(smem + K1_STAGE * K1_NS + 1024);
  s1 = xsum(s1); s2 = xsum(s2);
  if (g == 0) {
    float mean = s1 * (1.f / 512.f);
    float var = fmaxf(s2 * (1.f / 512.f) - mean * mean, 0.f);
    float rstd = rsqrtf(var + LN_EPS);
    *(f32x2*)(st + (tg * 32 + nh * 16 + i) * 2) = f32x2{mean, rstd};
  }
  if (i == 0 && tg < 3) *(f32x4*)(csp + un[0] * 16 + 4 * g) = cs;
  bar();
  f32x2 ms[2];
#pragma unroll
  for (int tt = 0; tt < 2; ++tt) ms[tt] = *(const f32x2*)(st + (tg * 32 + tt * 16 + i) * 2);
#pragma unroll
  for (int nt = 0; nt < 3; ++nt) {
    f32x4 c = *(const f32x4*)(csp + un[nt] * 16 + 4 * g);
    int n0 = cb * 96 + un[nt] * 16;
    char* dst = qkv + (size_t)(n0 >> 4) * (2048 * 32) + g * 8;
#pragma unroll
    for (int tt = 0; tt < 2; ++tt) {
      f32x4 y = (acc[tt][nt] - ms[tt][0] * c) * ms[tt][1];
      u32x2 o = {pack2(y[0], y[1]), pack2(y[2], y[3])};
      *(u32x2*)(dst + (size_t)(tb * 128 + tg * 32 + tt * 16 + i) * 32) = o;
    }
  }
  TL1(0);
}

constexpr int K2_LDS = 96 * 1024;
__global__ __launch_bounds__(512) void k2_attn(const char* __restrict__ qkv, char* __restrict__ o,
    const char* __restrict__ wo, const char* __restrict__ w1, const char* __restrict__ w3, const char* __restrict__ w2) {
  extern __shared__ __attribute__((aligned(16))) char smem[];
  TL0;
  const int tid = threadIdx.x, lane = tid & 63, wv = uni(tid >> 6);
  const int g = lane >> 4, i = lane & 15, q4 = (lane >> 2) & 3, p4 = lane & 3;
  const int b = blockIdx.x;
  const int hp = (b & 7) * 2 + ((b >> 3) & 1), qb = b >> 4;
  const int q0 = qb * 128;
  const unsigned lds0 = (unsigned)(size_t)(LAS char*)smem;
  const size_t HS = 2048 * 32;
  {
    const int r = wv >> 1;
    const char* src = qkv + (size_t)((1 + (r >> 1)) * 32 + hp * 2 + (r & 1)) * HS;
#pragma unroll
    for (int j = 0; j < 6; ++j) {
      int ch = ((wv & 1) * 6 + j) * 64 + lane;
      int key = q0 - 256 + (ch >> 1);
      key = key < 0 ? 0 : key;
      dma16(src + (size_t)key * 32 + (ch & 1) * 16, lds0 + r * 12288 + ((wv & 1) * 6 + j) * 1024);
    }
  }
  const int qs = q0 + wv * 16;
  bf16x8 Q[2];
  gld16(Q[0], (unsigned)(i * 32 + (g & 1) * 16), qkv + (size_t)(hp * 2) * HS + (size_t)qs * 32);
  gld16(Q[1], (unsigned)(i * 32 + (g & 1) * 16), qkv + (size_t)(hp * 2 + 1) * HS + (size_t)qs * 32);
  {
    const int c0 = b * 8 + wv;
    const unsigned sc = lds0 + 49152 + wv * 1024;
    const char* p0 = c0 < 512 ? wo + (size_t)c0 * 1024 : w1 + (size_t)(c0 - 512) * 1024;
    const char* p1 = c0 < 256 ? w1 + (size_t)(c0 + 1536) * 1024 : w3 + (size_t)(c0 - 256) * 1024;
    const char* p2 = w2 + (size_t)(c0 < 1792 ? c0 : c0 - 1792) * 1024;
    dma16s(lane * 16, p0, sc); dma16s(lane * 16, p1, sc); dma16s(lane * 16, p2, sc);
  }
  asm volatile("s_waitcnt vmcnt(3)\n\ts_barrier" : "+v"(Q[0]), "+v"(Q[1]) : : "memory");
  if (g >= 2) { Q[0] = bf16x8{0, 0, 0, 0, 0, 0, 0, 0}; Q[1] = bf16x8{0, 0, 0, 0, 0, 0, 0, 0}; }
  const float c = 0.25f * 1.4426950408889634f;
  const float NEG = -1e30f;
  const int lrow = wv * 16;
  u32x4 ov;
#pragma unroll
  for (int u = 0; u < 2; ++u) {
    const char* Kl = smem + u * 12288;
    const char* Vl = smem + 24576 + u * 12288;
    f32x4 s[17];
#pragma unroll
    for (int t = 0; t < 17; ++t) {
      bf16x8 A = lds128(Kl + (lrow + t * 16 + i) * 32 + (g & 1) * 16);
      s[t] = mfma16(A, Q[u], f32x4{0, 0, 0, 0});
    }
#pragma unroll
    for (int r = 0; r < 4; ++r) {
      s[0][r] = (4 * g + r > i) ? s[0][r] : NEG;
      s[16][r] = (4 * g + r <= i) ? s[16][r] : NEG;
    }
    if (qs < 256) {
      const int nneg = (256 - qs) >> 4;
#pragma unroll
      for (int t = 0; t < 16; ++t)
        if (t < nneg) s[t] = f32x4{NEG, NEG, NEG, NEG};
    }
    float m = s[16][0];
#pragma unroll
    for (int t = 0; t < 17; ++t)
#pragma unroll
      for (int r = 0; r < 4; ++r) m = fmaxf(m, s[t][r]);
    m = xmax(m);
    const float mc = m * c;
    float sum = 0.f;
    unsigned pk[17][2];
#pragma unroll
    for (int t = 0; t < 17; ++t) {
      float e0 = __builtin_amdgcn_exp2f(s[t][0] * c - mc), e1 = __builtin_amdgcn_exp2f(s[t][1] * c - mc);
      float e2 = __builtin_amdgcn_exp2f(s[t][2] * c - mc), e3 = __builtin_amdgcn_exp2f(s[t][3] * c - mc);
      sum += (e0 + e1) + (e2 + e3);
      pk[t][0] = pack2(e0, e1); pk[t][1] = pack2(e2, e3);
    }
    sum = xsum(sum);
    f32x4 oacc = f32x4{0, 0, 0, 0};
#pragma unroll
    for (int tp = 0; tp < 9; ++tp) {
      const int t0 = 2 * tp, t1 = (2 * tp + 1 < 17) ? 2 * tp + 1 : 16;
      bf16x8 A = cat(ldstr(Vl + (lrow + t0 * 16 + 4 * g + q4) * 32 + p4 * 8), ldstr(Vl + (lrow + t1 * 16 + 4 * g + q4) * 32 + p4 * 8));
      u32x4 pb = {pk[t0][0], pk[t0][1], (2 * tp + 1 < 17) ? pk[t1][0] : 0u, (2 * tp + 1 < 17) ? pk[t1][1] : 0u};
      oacc = mfma16(A, __builtin_bit_cast(bf16x8, pb), oacc);
    }
    const float inv = 1.f / sum;
    ov[2 * u] = pack2(oacc[0] * inv, oacc[1] * inv);
    ov[2 * u + 1] = pack2(oacc[2] * inv, oacc[3] * inv);
  }
  *(u32x4*)(o + ((size_t)(qs >> 4) * 16 + hp) * 1024 + lane * 16) = ov;
  asm volatile("s_waitcnt vmcnt(0)" ::: "memory");
  TL1(1);
}

template <int N> DEV void wait_frag(bf16x8* B) {
  asm volatile("s_waitcnt vmcnt(%4)" : "+v"(B[0]), "+v"(B[1]), "+v"(B[2]), "+v"(B[3]) : "n"(N) : "memory");
}
template <int NKS, int R> struct GK { static constexpr int REGION = (R * 4096 > 16384) ? R * 4096 : 16384, LDS = 4 * REGION > 98304 ? 4 * REGION : 98304; };

template <int J, int NKS, int R, class Issue, class LdA>
DEV void gks_steps(Issue& issue, LdA& lda, bf16x8 (&B)[R][4], bf16x8 (&A)[2][4], f32x4 (&acc)[4][4]) {
  if constexpr (J < NKS) {
    if constexpr (J + 1 < NKS) {
      wait_frag<(Min<J + R - 1, NKS - 1>::v - (J + 1)) * 8>(B[(J + 1) % R]);
      lda(A[(J + 1) & 1], (J + 1) % R);
    }
#pragma unroll
    for (int nt = 0; nt < 4; ++nt)
#pragma unroll
      for (int tt = 0; tt < 4; ++tt) acc[tt][nt] = mfma16(A[J & 1][nt], B[J % R][tt], acc[tt][nt]);
    __builtin_amdgcn_sched_barrier(0);
    if constexpr (J + R < NKS) issue(J + R);
    gks_steps<J + 1, NKS, R>(issue, lda, B, A, acc);
  }
}

template <int NKS, int R, bool STATS>
__global__ __launch_bounds__(256) void k_gks(const char* __restrict__ a, const char* __restrict__ w,
                                             const char* __restrict__ resid, char* __restrict__ out,
                                             float* __restrict__ stats) {
  extern __shared__ __attribute__((aligned(16))) char smem[];
  TL0;
  const int tid = threadIdx.x, lane = tid & 63, kp = uni(tid >> 6);
  const int g = lane >> 4, i = lane & 15, q4 = (lane >> 2) & 3, p4 = lane & 3;
  const int b = blockIdx.x;
  const int cb = (b >> 3) & 7, tb = (b & 7) * 4 + (b >> 6);
  constexpr int KT = NKS * 4, REGION = GK<NKS, R>::REGION;
  const unsigned ldsw = (unsigned)(size_t)(LAS char*)smem + kp * REGION;
  const char* ga = a + ((size_t)(tb * 4) * KT + kp * NKS) * 1024;
  const char* gw = w + (size_t)(kp * NKS * 32) * 1024 + cb * 128;
  const unsigned va = lane * 16;
  const unsigned vw = (lane >> 3) * 1024 + ((((lane & 7) >> 1) ^ ((lane >> 4) & 3)) << 5) + (lane & 1) * 16;
  bf16x8 B[R][4], A[2][4], rs[2];
  const int rot = (NKS > R) ? ((tb ^ cb) & 1) * (NKS / 2) : 0;
  auto issue = [&](int j) {
    const int s = j < NKS / 2 ? j + rot : j - rot;
#pragma unroll
    for (int q = 0; q < 4; ++q) dma16s(vw, gw + (size_t)(s * 32 + q * 8) * 1024, ldsw + (j % R) * 4096 + q * 1024);
#pragma unroll
    for (int tt = 0; tt < 4; ++tt) gld16(B[j % R][tt], va, ga + (size_t)(tt * KT + s) * 1024);
  };
  const size_t ooff = (size_t)(tb * 64 + kp * 16) * 1024 + cb * 128;
  const unsigned vo = (lane >> 3) * 1024 + (lane & 7) * 16;
  gld16(rs[0], vo, resid + ooff);
  gld16(rs[1], vo, resid + ooff + 8192);
#pragma unroll
  for (int j = 0; j < R; ++j) issue(j);
  f32x4 acc[4][4];
#pragma unroll
  for (int t = 0; t < 4; ++t)
#pragma unroll
    for (int n = 0; n < 4; ++n) acc[t][n] = f32x4{0, 0, 0, 0};
  const int sw = (2 * g + (q4 >> 1)) & 3;
  const char* wr = smem + kp * REGION + (4 * g + q4) * 128 + p4 * 8;
  auto lda = [&](bf16x8* Af, int slot) {
#pragma unroll
    for (int nt = 0; nt < 4; ++nt) {
      const char* p = wr + slot * 4096 + ((nt ^ sw) << 5);
      Af[nt] = cat(ldstr(p), ldstr(p + 2048));
    }
  };
  wait_frag<(R - 1) * 8>(B[0]);
  asm volatile("" : "+v"(rs[0]), "+v"(rs[1]));
  lda(A[0], 0);
  gks_steps<0, NKS, R>(issue, lda, B, A, acc);
  {
    const int mo = kp * REGION + g * 256 + ((i ^ ((g >> 1) * 2)) << 4);
#pragma unroll
    for (int tt = 0; tt < 4; ++tt)
#pragma unroll
      for (int nt = 0; nt < 4; ++nt)
        *(LAS f32x4*)(LAS char*)(smem + (mo ^ (nt * 64)) + (tt * 4 + nt) * 1024) = acc[tt][nt];
  }
  bar();
  const int c = lane & 7;
#pragma unroll
  for (int it = 0; it < 2; ++it) {
    const int tok = (lane >> 3) + 8 * it;
    const char* rp = smem + (kp * 4 + (c >> 1)) * 1024 + (c & 1) * 512 + ((tok ^ (2 * c)) << 4);
    f32x4 v0 = f32x4{0, 0, 0, 0}, v1 = f32x4{0, 0, 0, 0};
#pragma unroll
    for (int p = 0; p < 4; ++p) {
      v0 += *(const LAS f32x4*)(LAS const char*)(rp + p * REGION);
      v1 += *(const LAS f32x4*)(LAS const char*)(rp + p * REGION + 256);
    }
    u32x4 r = __builtin_bit_cast(u32x4, rs[it]);
    u32x4 ov = {pack2(v0[0] + bflo(r[0]), v0[1] + bfhi(r[0])), pack2(v0[2] + bflo(r[1]), v0[3] + bfhi(r[1])),
                pack2(v1[0] + bflo(r[2]), v1[1] + bfhi(r[2])), pack2(v1[2] + bflo(r[3]), v1[3] + bfhi(r[3]))};
    *(u32x4*)(out + ooff + it * 8192 + vo) = ov;
    if constexpr (STATS) {
      float s1 = 0.f, s2 = 0.f;
#pragma unroll
      for (int e = 0; e < 4; ++e) { s1 = dot2(ov[e], 0x3F803F80u, s1); s2 = dot2(ov[e], ov[e], s2); }
#pragma unroll
      for (int m = 1; m < 8; m <<= 1) { s1 += __shfl_xor(s1, m); s2 += __shfl_xor(s2, m); }
      if (c == 0) *(f32x2*)(stats + ((size_t)(tb * 64 + kp * 16 + tok) * 8 + cb) * 2) = f32x2{s1, s2};
    }
  }
  TL1(STATS ? 2 : 4);
}

constexpr int K4_STAGE = 49152, K4_NS = 3;
constexpr int K4_LDS = K4_STAGE * K4_NS + 4 * 224 * 4;

template <int NT>
DEV void k4_body(char* smem, const char* gt, const char* g1, const char* g3, unsigned vt, unsigned vw,
                 unsigned long long wmask, unsigned ldsw, int tg, int lane, const float* __restrict__ stats,
                 char* __restrict__ hh, int tok0, int cb TLARG) {
  const int g = lane >> 4, i = lane & 15, q4 = (lane >> 2) & 3, p4 = lane & 3;
  f32x4 acc[2][2 * NT], csg = f32x4{0, 0, 0, 0}, csu = f32x4{0, 0, 0, 0};
#pragma unroll
  for (int a = 0; a < 2 * NT; ++a) { acc[0][a] = f32x4{0, 0, 0, 0}; acc[1][a] = f32x4{0, 0, 0, 0}; }
  const bf16x8 ones = {0x3F80, 0x3F80, 0x3F80, 0x3F80, 0x3F80, 0x3F80, 0x3F80, 0x3F80};
  const int toff0 = (tg * 32 + i) * 128 + ((g ^ (i & 7)) << 4);
  const int toff1 = (tg * 32 + i) * 128 + (((4 + g) ^ (i & 7)) << 4);
  const int odd = cb & 1;
  const int fsw = q4 | ((g & 1) << 2);
  int T[NT], wa[NT];
#pragma unroll
  for (int a = 0; a < NT; ++a) {
    T[a] = NT == 4 ? odd * 3 + a : (a < 2 ? (odd ? 1 : 4) + a : (odd ? 0 : 6));
    wa[a] = 16384 + (8 * g + q4) * 256 + ((T[a] ^ fsw) << 5) + p4 * 8;
  }
  const int rot = 0;
  auto dma = [&](int kt, int j) {
    const unsigned base = ldsw + (kt % K4_NS) * K4_STAGE;
    const int ke = (kt + rot) & 7;
    if (j < 2) dma16s(vt, gt + ke * 128 + j * 8192, base + j * 1024);
    else if (j < 4) dma16sm(vw, g1 + (size_t)(ke * 64 + (j - 2) * 4) * (F_ * 2), base + 16384 + (j - 2) * 1024, wmask);
    else dma16sm(vw, g3 + (size_t)(ke * 64 + (j - 4) * 4) * (F_ * 2), base + 32768 + (j - 4) * 1024, wmask);
  };
  auto ldB = [&](bf16x8* B, int kt, int ks) {
    const char* sb = smem + (kt % K4_NS) * K4_STAGE + (ks ? toff1 : toff0);
    B[0] = lds128(sb); B[1] = lds128(sb + 2048);
  };
  auto ldA = [&](int a, int kt, int ks) -> bf16x8 {
    const char* wp = smem + (kt % K4_NS) * K4_STAGE + wa[a < NT ? a : a - NT] + (a < NT ? 0 : 16384) + ks * 8192;
    return cat(ldstr(wp), ldstr(wp + 1024));
  };
  auto mma = [&](int a, bf16x8 A, const bf16x8* B) {
    acc[0][a] = mfma16(A, B[0], acc[0][a]);
    acc[1][a] = mfma16(A, B[1], acc[1][a]);
  };
  auto csum = [&](const bf16x8* A) {
    if (tg == 0) { csg = mfma16(A[0], ones, csg); csu = mfma16(A[NT], ones, csu); }
    else if (tg == 1) { csg = mfma16(A[1], ones, csg); csu = mfma16(A[NT + 1], ones, csu); }
    else if (tg == 2) { csg = mfma16(A[2], ones, csg); csu = mfma16(A[NT + 2], ones, csu); }
    else if (NT == 4) { csg = mfma16(A[NT - 1], ones, csg); csu = mfma16(A[2 * NT - 1], ones, csu); }
  };
  bf16x8 A0[2 * NT], A1[2 * NT], B0[2], B1[2];
  f32x4 sa[2];
#pragma unroll
  for (int j = 0; j < 6; ++j) dma(0, j);
#pragma unroll
  for (int j = 0; j < 6; ++j) dma(1, j);
  wait_bar<6>();
  ldB(B0, 0, 0);
#pragma unroll
  for (int a = 0; a < 2 * NT; ++a) A0[a] = ldA(a, 0, 0);
#pragma unroll
  for (int kt = 0; kt < 8; ++kt) {
    ldB(B1, kt, 1);
#pragma unroll
    for (int a = 0; a < 2 * NT; ++a) {
      if (a < NT) { A1[2 * a] = ldA(2 * a, kt, 1); A1[2 * a + 1] = ldA(2 * a + 1, kt, 1); }
      mma(a, A0[a], B0);
      if (kt == 0 && a >= 2 * NT - 6) dma(2, a - (2 * NT - 6));
      __builtin_amdgcn_sched_barrier(0);
    }
    csum(A0);
    if (kt < 6) wait_bar<6>();
    else if (kt == 6) wait_bar<0>();
    if (kt < 7) ldB(B0, kt + 1, 0);
    if (kt == 6) {
#pragma unroll
      for (int tt = 0; tt < 2; ++tt) sa[tt] = *(const f32x4*)(stats + ((size_t)(tok0 + tg * 32 + tt * 16 + i) * 8 + 2 * g) * 2);
    }
    if (kt == 7) csum(A1);
#pragma unroll
    for (int a = 0; a < 2 * NT; ++a) {
      if (kt < 7 && a < NT) { A0[2 * a] = ldA(2 * a, kt + 1, 0); A0[2 * a + 1] = ldA(2 * a + 1, kt + 1, 0); }
      mma(a, A1[a], B1);
      if (kt + 3 < 8 && a >= 2 * NT - 6) dma(kt + 3, a - (2 * NT - 6));
      __builtin_amdgcn_sched_barrier(0);
    }
    if (kt < 7) csum(A1);
  }
  float* csp = (float*)(smem + K4_STAGE * K4_NS);
  if (i == 0 && tg < NT) {
    const int tt_ = NT == 4 ? odd * 3 + tg : (tg < 2 ? (odd ? 1 : 4) + tg : (odd ? 0 : 6));
    *(f32x4*)(csp + tt_ * 16 + 4 * g) = csg;
    *(f32x4*)(csp + (7 + tt_) * 16 + 4 * g) = csu;
  }
  float rs[2], mr[2];
#pragma unroll
  for (int tt = 0; tt < 2; ++tt) {
    float s1 = sa[tt][0] + sa[tt][2], s2 = sa[tt][1] + sa[tt][3];
    s1 = xsum(s1); s2 = xsum(s2);
    float mean = s1 * (1.f / 512.f);
    float var = fmaxf(s2 * (1.f / 512.f) - mean * mean, 0.f);
    rs[tt] = rsqrtf(var + LN_EPS); mr[tt] = mean * rs[tt];
  }
  bar();
  unsigned pk[2][NT][2];
#pragma unroll
  for (int tt = 0; tt < 2; ++tt) {
    char* dst = hh + ((size_t)((tok0 >> 4) + tg * 2 + tt) * 56) * 1024 + lane * 16;
#pragma unroll
    for (int a = 0; a < NT; ++a) {
      f32x4 cg = *(const f32x4*)(csp + T[a] * 16 + 4 * g);
      f32x4 cu = *(const f32x4*)(csp + (7 + T[a]) * 16 + 4 * g);
      float r[4];
#pragma unroll
      for (int e = 0; e < 4; ++e) {
        float ga = acc[tt][a][e] * rs[tt] - mr[tt] * cg[e];
        float up = acc[tt][NT + a][e] * rs[tt] - mr[tt] * cu[e];
        float sg = __builtin_amdgcn_rcpf(1.f + __builtin_amdgcn_exp2f(-1.4426950408889634f * ga));
        r[e] = ga * sg * up;
      }
      pk[tt][a][0] = pack2(r[0], r[1]); pk[tt][a][1] = pack2(r[2], r[3]);
      const int G = cb * 7 + T[a];
      if (a & 1) {
        *(u32x4*)(dst + (size_t)(G >> 1) * 1024) = u32x4{pk[tt][a - 1][0], pk[tt][a - 1][1], pk[tt][a][0], pk[tt][a][1]};
      } else if (a == NT - 1) {
        *(u32x2*)(dst + (size_t)(G >> 1) * 1024 + (G & 1) * 8) = u32x2{pk[tt][a][0], pk[tt][a][1]};
      }
    }
  }
  TL1(3);
}

__global__ __launch_bounds__(512) void k4_mlp(const char* __restrict__ h, const char* __restrict__ w1,
                                              const char* __restrict__ w3, const float* __restrict__ stats,
                                              char* __restrict__ hh) {
  extern __shared__ __attribute__((aligned(16))) char smem[];
  TL0;
  const int tid = threadIdx.x, lane = tid & 63, wv = uni(tid >> 6);
  const int tg = wv & 3, half = wv >> 2;
  const int b = blockIdx.x, xcd = b & 7, bi = b >> 3;
  const int cb = (xcd & 3) * 4 + (bi & 3), tb = (xcd >> 2) * 8 + (bi >> 2);
  const unsigned lds0 = (unsigned)(size_t)(LAS char*)smem;
  const char* gt = h + (size_t)(tb * 128 + wv * 16) * 1024;
  const unsigned vt = (lane >> 3) * 1024 + (((lane & 7) ^ (lane >> 3)) << 4);
  const char* g1 = w1 + (size_t)(wv * 8) * (F_ * 2) + cb * 224;
  const char* g3 = w3 + (size_t)(wv * 8) * (F_ * 2) + cb * 224;
  const int unit = ((lane >> 1) & 7) ^ ((lane >> 4) | ((wv & 1) << 2));
  const unsigned vw = (lane >> 4) * (F_ * 2) + unit * 32 + (lane & 1) * 16;
  const unsigned long long wmask = __builtin_amdgcn_ballot_w64(unit < 7);
  const unsigned ldsw = lds0 + wv * 2048;
  if (half == 0) k4_body<4>(smem, gt, g1, g3, vt, vw, wmask, ldsw, tg, lane, stats, hh, tb * 128, cb TLPASS);
  else k4_body<3>(smem, gt, g1, g3, vt, vw, wmask, ldsw, tg, lane, stats, hh, tb * 128, cb TLPASS);
}

extern "C" void kernel_launch(void* const* d_in, const int* in_sizes, int n_in, void* d_out, int out_size,
                              void* d_ws, size_t ws_size, hipStream_t stream) {
  const char* x = (const char*)d_in[0];
  const char* wqkv = (const char*)d_in[1];
  const char* wo = (const char*)d_in[2];
  const char* w1 = (const char*)d_in[4];
  const char* w3 = (const char*)d_in[5];
  const char* w2 = (const char*)d_in[6];
  char* ws = (char*)d_ws;
  char* qkv = ws;
  char* o = ws + (6u << 20);
  char* h = ws + (8u << 20);
  float* stats = (float*)(ws + (10u << 20));
  char* hh = ws + (11u << 20);
  (void)hipFuncSetAttribute((const void*)k1_qkv, hipFuncAttributeMaxDynamicSharedMemorySize, K1_LDS);
  (void)hipFuncSetAttribute((const void*)k2_attn, hipFuncAttributeMaxDynamicSharedMemorySize, K2_LDS);
  (void)hipFuncSetAttribute((const void*)k_gks<4, 4, true>, hipFuncAttributeMaxDynamicSharedMemorySize, GK<4, 4>::LDS);
  (void)hipFuncSetAttribute((const void*)k4_mlp, hipFuncAttributeMaxDynamicSharedMemorySize, K4_LDS);
  (void)hipFuncSetAttribute((const void*)k_gks<14, 7, false>, hipFuncAttributeMaxDynamicSharedMemorySize, GK<14, 7>::LDS);
  k1_qkv<<<256, 512, K1_LDS, stream>>>(x, wqkv, qkv);
  k2_attn<<<256, 512, K2_LDS, stream>>>(qkv, o, wo, w1, w3, w2);
  k_gks<4, 4, true><<<256, 256, GK<4, 4>::LDS, stream>>>(o, wo, x, h, stats);
  k4_mlp<<<256, 512, K4_LDS, stream>>>(h, w1, w3, stats, hh);
  k_gks<14, 7, false><<<256, 256, GK<14, 7>::LDS, stream>>>(hh, w2, h, (char*)d_out, nullptr);
}
```

```cpp
#define WT 31
#include <hip/hip_runtime.h>
#include <hip/hip_bf16.h>
#include <stdint.h>

typedef short bf16x8 __attribute__((ext_vector_type(8)));
typedef short s16x4 __attribute__((ext_vector_type(4)));
typedef float f32x4 __attribute__((ext_vector_type(4)));
typedef float f32x2 __attribute__((ext_vector_type(2)));
typedef unsigned u32x4 __attribute__((ext_vector_type(4)));
typedef unsigned u32x2 __attribute__((ext_vector_type(2)));
typedef __bf16 bf16x2 __attribute__((ext_vector_type(2)));
#define LAS __attribute__((address_space(3)))
#define DEV __device__ __forceinline__

constexpr int S_ = 2048, D_ = 512, H_ = 32, F_ = 1792;
constexpr float LN_EPS = 1e-5f;

DEV f32x4 mfma16(bf16x8 a, bf16x8 b, f32x4 c) { return __builtin_amdgcn_mfma_f32_16x16x32_bf16(a, b, c, 0, 0, 0); }
DEV unsigned pack2(float a, float b) {
  f32x2 v = {a, b};
  return __builtin_bit_cast(unsigned, __builtin_convertvector(v, bf16x2));
}
DEV float bflo(unsigned u) { return __builtin_bit_cast(float, u << 16); }
DEV float bfhi(unsigned u) { return __builtin_bit_cast(float, u & 0xffff0000u); }
DEV float dot2(unsigned a, unsigned b, float c) {
  return __builtin_amdgcn_fdot2_f32_bf16(__builtin_bit_cast(bf16x2, a), __builtin_bit_cast(bf16x2, b), c, false);
}
DEV void dma16(const char* gptr, unsigned ldsbase) {
  unsigned sv;
  asm volatile("s_mov_b32 %0, m0\n\ts_mov_b32 m0, %2\n\ts_nop 0\n\tglobal_load_lds_dwordx4 %1, off\n\ts_mov_b32 m0, %0"
               : "=&s"(sv) : "v"(gptr), "s"(ldsbase) : "memory");
}
DEV void dma16s(unsigned voff, const char* sbase, unsigned ldsbase) {
  unsigned sv;
  asm volatile("s_mov_b32 %0, m0\n\ts_mov_b32 m0, %3\n\ts_nop 0\n\tglobal_load_lds_dwordx4 %1, %2\n\ts_mov_b32 m0, %0"
               : "=&s"(sv) : "v"(voff), "s"(sbase), "s"(ldsbase) : "memory");
}
DEV void dma16sm(unsigned voff, const char* sbase, unsigned ldsbase, unsigned long long mask) {
  unsigned sv;
  asm volatile("s_mov_b32 %0, m0\n\ts_mov_b32 m0, %3\n\ts_mov_b64 exec, %4\n\tglobal_load_lds_dwordx4 %1, %2\n\ts_mov_b64 exec, -1\n\ts_mov_b32 m0, %0"
               : "=&s"(sv) : "v"(voff), "s"(sbase), "s"(ldsbase), "s"(mask) : "memory");
}
DEV void gld16(bf16x8& d, unsigned voff, const char* sbase) {
  asm volatile("global_load_dwordx4 %0, %1, %2" : "=v"(d) : "v"(voff), "s"(sbase) : "memory");
}
template <int N> DEV void wait_bar() { asm volatile("s_waitcnt vmcnt(%0) lgkmcnt(0)\n\ts_barrier" ::"n"(N) : "memory"); }
DEV void bar() { asm volatile("s_waitcnt lgkmcnt(0)\n\ts_barrier" ::: "memory"); }
DEV bf16x8 lds128(const char* p) { return *(const LAS bf16x8*)(LAS const char*)p; }
DEV s16x4 ldstr(const char* p) { return __builtin_amdgcn_ds_read_tr16_b64_v4i16((LAS s16x4*)p); }
DEV bf16x8 cat(s16x4 a, s16x4 b) { return __builtin_shufflevector(a, b, 0, 1, 2, 3, 4, 5, 6, 7); }
DEV float xsum(float v) { v += __shfl_xor(v, 16); v += __shfl_xor(v, 32); return v; }
DEV float xmax(float v) { v = fmaxf(v, __shfl_xor(v, 16)); v = fmaxf(v, __shfl_xor(v, 32)); return v; }
DEV unsigned uni(unsigned v) { return __builtin_amdgcn_readfirstlane(v); }
template <bool W> DEV void st16(char* p, u32x4 v) {
  if constexpr (W) asm volatile("global_store_dwordx4 %0, %1, off sc0 sc1" ::"v"(p), "v"(v) : "memory");
  else *(u32x4*)p = v;
}
template <bool W> DEV void st8(char* p, u32x2 v) {
  if constexpr (W) asm volatile("global_store_dwordx2 %0, %1, off sc0 sc1" ::"v"(p), "v"(v) : "memory");
  else *(u32x2*)p = v;
}

#define TL0
#define TL1(k)
#define TLARG
#define TLPASS
template <int A, int B> struct Min { static constexpr int v = A < B ? A : B; };

template <int KT, int NK, int NS, int P, class Issue, class Compute>
DEV void pipe_steps(Issue& issue, Compute& compute) {
  if constexpr (KT < NK) {
    wait_bar<(Min<KT + NS - 1, NK>::v - KT - 1) * P>();
    if constexpr (KT + NS - 1 < NK) issue((KT + NS - 1) % NS);
    compute(KT % NS, KT);
    pipe_steps<KT + 1, NK, NS, P>(issue, compute);
  }
}

constexpr int K1_STAGE = 16384 + 12288, K1_NS = 5;
constexpr int K1_LDS = K1_STAGE * K1_NS + 1024 + 1536;

__global__ __launch_bounds__(512) void k1_qkv(const char* __restrict__ x, const char* __restrict__ w,
                                              char* __restrict__ qkv) {
  extern __shared__ __attribute__((aligned(16))) char smem[];
  TL0;
  const int tid = threadIdx.x, lane = tid & 63, wv = uni(tid >> 6);
  const int g = lane >> 4, i = lane & 15, q4 = (lane >> 2) & 3, p4 = lane & 3;
  const int tg = wv & 3, nh = wv >> 2;
  const int b = blockIdx.x, xcd = b & 7, bi = b >> 3;
  const int cb = (xcd & 1) * 8 + (bi & 7), tb = (xcd >> 1) * 4 + (bi >> 3);
  const unsigned lds0 = (unsigned)(size_t)(LAS char*)smem;

  const char* pt;
  {
    int row = tb * 128 + wv * 16 + (lane >> 3);
    int c = (lane & 7) ^ (lane >> 3);
    pt = x + (size_t)row * 1024 + c * 16;
  }
  const char *pw0, *pw1;
  {
    int qq = wv * 64 + lane;
    int k = qq / 12, c = (qq % 12) ^ (((k >> 3) & 1) << 1);
    pw0 = w + (size_t)k * 3072 + cb * 192 + c * 16;
    qq = 512 + wv * 32 + (lane & 31);
    k = qq / 12; c = (qq % 12) ^ (((k >> 3) & 1) << 1);
    pw1 = w + (size_t)k * 3072 + cb * 192 + c * 16;
  }
  int kk = bi & 7;
  auto issue = [&](int slot) {
    unsigned base = lds0 + slot * K1_STAGE;
    dma16(pt + kk * 128, base + wv * 2048);
    dma16(pt + kk * 128 + 8192, base + wv * 2048 + 1024);
    dma16(pw0 + (size_t)kk * (64 * 3072), base + 16384 + wv * 1024);
    if (lane < 32) dma16(pw1 + (size_t)kk * (64 * 3072), base + 16384 + 8192 + wv * 512);
    kk = (kk + 1) & 7;
  };
#pragma unroll
  for (int s = 0; s < K1_NS - 1; ++s) issue(s);

  f32x4 acc[2][3], cs = f32x4{0, 0, 0, 0};
#pragma unroll
  for (int a = 0; a < 3; ++a) { acc[0][a] = f32x4{0, 0, 0, 0}; acc[1][a] = f32x4{0, 0, 0, 0}; }
  float s1 = 0.f, s2 = 0.f;
  const bf16x8 ones = {0x3F80, 0x3F80, 0x3F80, 0x3F80, 0x3F80, 0x3F80, 0x3F80, 0x3F80};
  const int toff0 = (tg * 32 + i) * 128 + ((g ^ (i & 7)) << 4);
  const int toff1 = (tg * 32 + i) * 128 + (((4 + g) ^ (i & 7)) << 4);
  int woff[3], un[3];
#pragma unroll
  for (int nt = 0; nt < 3; ++nt) {
    int a = nt + tg; a = a >= 3 ? a - 3 : a; a = a >= 3 ? a - 3 : a;
    un[nt] = nh * 3 + a;
    woff[nt] = 16384 + (8 * g + q4) * 192 + ((un[nt] ^ (g & 1)) << 5) + p4 * 8;
  }

  auto compute = [&](int slot, int kt) {
    const char* sb = smem + slot * K1_STAGE;
#pragma unroll
    for (int ks = 0; ks < 2; ++ks) {
      bf16x8 B[2], A[3];
#pragma unroll
      for (int tt = 0; tt < 2; ++tt) B[tt] = lds128(sb + (ks ? toff1 : toff0) + tt * 2048);
#pragma unroll
      for (int nt = 0; nt < 3; ++nt)
        A[nt] = cat(ldstr(sb + woff[nt] + ks * 6144), ldstr(sb + woff[nt] + ks * 6144 + 768));
#pragma unroll
      for (int nt = 0; nt < 3; ++nt)
#pragma unroll
        for (int tt = 0; tt < 2; ++tt) acc[tt][nt] = mfma16(A[nt], B[tt], acc[tt][nt]);
      {
        u32x4 u = __builtin_bit_cast(u32x4, nh ? B[1] : B[0]);
#pragma unroll
        for (int j = 0; j < 4; ++j) { s1 = dot2(u[j], 0x3F803F80u, s1); s2 = dot2(u[j], u[j], s2); }
      }
      cs = mfma16(A[0], ones, cs);
    }
  };
  pipe_steps<0, 8, K1_NS, 4>(issue, compute);

  float* st = (float*)(smem + K1_STAGE * K1_NS);
  float* csp = (float*)(smem + K1_STAGE * K1_NS + 1024);
  s1 = xsum(s1); s2 = xsum(s2);
  if (g == 0) {
    float mean = s1 * (1.f / 512.f);
    float var = fmaxf(s2 * (1.f / 512.f) - mean * mean, 0.f);
    float rstd = rsqrtf(var + LN_EPS);
    *(f32x2*)(st + (tg * 32 + nh * 16 + i) * 2) = f32x2{mean, rstd};
  }
  if (i == 0 && tg < 3) *(f32x4*)(csp + un[0] * 16 + 4 * g) = cs;
  bar();
  f32x2 ms[2];
#pragma unroll
  for (int tt = 0; tt < 2; ++tt) ms[tt] = *(const f32x2*)(st + (tg * 32 + tt * 16 + i) * 2);
#pragma unroll
  for (int nt = 0; nt < 3; ++nt) {
    f32x4 c = *(const f32x4*)(csp + un[nt] * 16 + 4 * g);
    int n0 = cb * 96 + un[nt] * 16;
    char* dst = qkv + (size_t)(n0 >> 4) * (2048 * 32) + g * 8;
#pragma unroll
    for (int tt = 0; tt < 2; ++tt) {
      f32x4 y = (acc[tt][nt] - ms[tt][0] * c) * ms[tt][1];
      u32x2 o = {pack2(y[0], y[1]), pack2(y[2], y[3])};
      st8<(WT & 1) != 0>(dst + (size_t)(tb * 128 + tg * 32 + tt * 16 + i) * 32, o);
    }
  }
  TL1(0);
}

constexpr int K2_LDS = 96 * 1024;
__global__ __launch_bounds__(512) void k2_attn(const char* __restrict__ qkv, char* __restrict__ o,
    const char* __restrict__ wo, const char* __restrict__ w1, const char* __restrict__ w3, const char* __restrict__ w2) {
  extern __shared__ __attribute__((aligned(16))) char smem[];
  TL0;
  const int tid = threadIdx.x, lane = tid & 63, wv = uni(tid >> 6);
  const int g = lane >> 4, i = lane & 15, q4 = (lane >> 2) & 3, p4 = lane & 3;
  const int b = blockIdx.x;
  const int hp = (b & 7) * 2 + ((b >> 3) & 1), qb = b >> 4;
  const int q0 = qb * 128;
  const unsigned lds0 = (unsigned)(size_t)(LAS char*)smem;
  const size_t HS = 2048 * 32;
  {
    const int r = wv >> 1;
    const char* src = qkv + (size_t)((1 + (r >> 1)) * 32 + hp * 2 + (r & 1)) * HS;
#pragma unroll
    for (int j = 0; j < 6; ++j) {
      int ch = ((wv & 1) * 6 + j) * 64 + lane;
      int key = q0 - 256 + (ch >> 1);
      key = key < 0 ? 0 : key;
      dma16(src + (size_t)key * 32 + (ch & 1) * 16, lds0 + r * 12288 + ((wv & 1) * 6 + j) * 1024);
    }
  }
  const int qs = q0 + wv * 16;
  bf16x8 Q[2];
  gld16(Q[0], (unsigned)(i * 32 + (g & 1) * 16), qkv + (size_t)(hp * 2) * HS + (size_t)qs * 32);
  gld16(Q[1], (unsigned)(i * 32 + (g & 1) * 16), qkv + (size_t)(hp * 2 + 1) * HS + (size_t)qs * 32);
  {
    const int c0 = b * 8 + wv;
    const unsigned sc = lds0 + 49152 + wv * 1024;
    const char* p0 = c0 < 512 ? wo + (size_t)c0 * 1024 : w1 + (size_t)(c0 - 512) * 1024;
    const char* p1 = c0 < 256 ? w1 + (size_t)(c0 + 1536) * 1024 : w3 + (size_t)(c0 - 256) * 1024;
    const char* p2 = w2 + (size_t)(c0 < 1792 ? c0 : c0 - 1792) * 1024;
    dma16s(lane * 16, p0, sc); dma16s(lane * 16, p1, sc); dma16s(lane * 16, p2, sc);
  }
  asm volatile("s_waitcnt vmcnt(3)\n\ts_barrier" : "+v"(Q[0]), "+v"(Q[1]) : : "memory");
  if (g >= 2) { Q[0] = bf16x8{0, 0, 0, 0, 0, 0, 0, 0}; Q[1] = bf16x8{0, 0, 0, 0, 0, 0, 0, 0}; }
  const float c = 0.25f * 1.4426950408889634f;
  const float NEG = -1e30f;
  const int lrow = wv * 16;
  u32x4 ov;
#pragma unroll
  for (int u = 0; u < 2; ++u) {
    const char* Kl = smem + u * 12288;
    const char* Vl = smem + 24576 + u * 12288;
    f32x4 s[17];
#pragma unroll
    for (int t = 0; t < 17; ++t) {
      bf16x8 A = lds128(Kl + (lrow + t * 16 + i) * 32 + (g & 1) * 16);
      s[t] = mfma16(A, Q[u], f32x4{0, 0, 0, 0});
    }
#pragma unroll
    for (int r = 0; r < 4; ++r) {
      s[0][r] = (4 * g + r > i) ? s[0][r] : NEG;
      s[16][r] = (4 * g + r <= i) ? s[16][r] : NEG;
    }
    if (qs < 256) {
      const int nneg = (256 - qs) >> 4;
#pragma unroll
      for (int t = 0; t < 16; ++t)
        if (t < nneg) s[t] = f32x4{NEG, NEG, NEG, NEG};
    }
    float m = s[16][0];
#pragma unroll
    for (int t = 0; t < 17; ++t)
#pragma unroll
      for (int r = 0; r < 4; ++r) m = fmaxf(m, s[t][r]);
    m = xmax(m);
    const float mc = m * c;
    float sum = 0.f;
    unsigned pk[17][2];
#pragma unroll
    for (int t = 0; t < 17; ++t) {
      float e0 = __builtin_amdgcn_exp2f(s[t][0] * c - mc), e1 = __builtin_amdgcn_exp2f(s[t][1] * c - mc);
      float e2 = __builtin_amdgcn_exp2f(s[t][2] * c - mc), e3 = __builtin_amdgcn_exp2f(s[t][3] * c - mc);
      sum += (e0 + e1) + (e2 + e3);
      pk[t][0] = pack2(e0, e1); pk[t][1] = pack2(e2, e3);
    }
    sum = xsum(sum);
    f32x4 oacc = f32x4{0, 0, 0, 0};
#pragma unroll
    for (int tp = 0; tp < 9; ++tp) {
      const int t0 = 2 * tp, t1 = (2 * tp + 1 < 17) ? 2 * tp + 1 : 16;
      bf16x8 A = cat(ldstr(Vl + (lrow + t0 * 16 + 4 * g + q4) * 32 + p4 * 8), ldstr(Vl + (lrow + t1 * 16 + 4 * g + q4) * 32 + p4 * 8));
      u32x4 pb = {pk[t0][0], pk[t0][1], (2 * tp + 1 < 17) ? pk[t1][0] : 0u, (2 * tp + 1 < 17) ? pk[t1][1] : 0u};
      oacc = mfma16(A, __builtin_bit_cast(bf16x8, pb), oacc);
    }
    const float inv = 1.f / sum;
    ov[2 * u] = pack2(oacc[0] * inv, oacc[1] * inv);
    ov[2 * u + 1] = pack2(oacc[2] * inv, oacc[3] * inv);
  }
  st16<(WT & 2) != 0>(o + ((size_t)(qs >> 4) * 16 + hp) * 1024 + lane * 16, ov);
  asm volatile("s_waitcnt vmcnt(0)" ::: "memory");
  TL1(1);
}

template <int N> DEV void wait_frag(bf16x8* B) {
  asm volatile("s_waitcnt vmcnt(%4)" : "+v"(B[0]), "+v"(B[1]), "+v"(B[2]), "+v"(B[3]) : "n"(N) : "memory");
}
template <int NKS, int R> struct GK { static constexpr int REGION = (R * 4096 > 16384) ? R * 4096 : 16384, LDS = 4 * REGION > 98304 ? 4 * REGION : 98304; };

template <int J, int NKS, int R, class Issue, class LdA>
DEV void gks_steps(Issue& issue, LdA& lda, bf16x8 (&B)[R][4], bf16x8 (&A)[2][4], f32x4 (&acc)[4][4]) {
  if constexpr (J < NKS) {
    if constexpr (J + 1 < NKS) {
      wait_frag<(Min<J + R - 1, NKS - 1>::v - (J + 1)) * 8>(B[(J + 1) % R]);
      lda(A[(J + 1) & 1], (J + 1) % R);
    }
#pragma unroll
    for (int nt = 0; nt < 4; ++nt)
#pragma unroll
      for (int tt = 0; tt < 4; ++tt) acc[tt][nt] = mfma16(A[J & 1][nt], B[J % R][tt], acc[tt][nt]);
    __builtin_amdgcn_sched_barrier(0);
    if constexpr (J + R < NKS) issue(J + R);
    gks_steps<J + 1, NKS, R>(issue, lda, B, A, acc);
  }
}

template <int NKS, int R, bool STATS>
__global__ __launch_bounds__(256) void k_gks(const char* __restrict__ a, const char* __restrict__ w,
                                             const char* __restrict__ resid, char* __restrict__ out,
                                             float* __restrict__ stats) {
  extern __shared__ __attribute__((aligned(16))) char smem[];
  TL0;
  const int tid = threadIdx.x, lane = tid & 63, kp = uni(tid >> 6);
  const int g = lane >> 4, i = lane & 15, q4 = (lane >> 2) & 3, p4 = lane & 3;
  const int b = blockIdx.x;
  const int cb = (b >> 3) & 7, tb = (b & 7) * 4 + (b >> 6);
  constexpr int KT = NKS * 4, REGION = GK<NKS, R>::REGION;
  const unsigned ldsw = (unsigned)(size_t)(LAS char*)smem + kp * REGION;
  const char* ga = a + ((size_t)(tb * 4) * KT + kp * NKS) * 1024;
  const char* gw = w + (size_t)(kp * NKS * 32) * 1024 + cb * 128;
  const unsigned va = lane * 16;
  const unsigned vw = (lane >> 3) * 1024 + ((((lane & 7) >> 1) ^ ((lane >> 4) & 3)) << 5) + (lane & 1) * 16;
  bf16x8 B[R][4], A[2][4], rs[2];
  const int rot = (NKS > R) ? ((tb ^ cb) & 1) * (NKS / 2) : 0;
  auto issue = [&](int j) {
    const int s = j < NKS / 2 ? j + rot : j - rot;
#pragma unroll
    for (int q = 0; q < 4; ++q) dma16s(vw, gw + (size_t)(s * 32 + q * 8) * 1024, ldsw + (j % R) * 4096 + q * 1024);
#pragma unroll
    for (int tt = 0; tt < 4; ++tt) gld16(B[j % R][tt], va, ga + (size_t)(tt * KT + s) * 1024);
  };
  const size_t ooff = (size_t)(tb * 64 + kp * 16) * 1024 + cb * 128;
  const unsigned vo = (lane >> 3) * 1024 + (lane & 7) * 16;
  gld16(rs[0], vo, resid + ooff);
  gld16(rs[1], vo, resid + ooff + 8192);
#pragma unroll
  for (int j = 0; j < R; ++j) issue(j);
  f32x4 acc[4][4];
#pragma unroll
  for (int t = 0; t < 4; ++t)
#pragma unroll
    for (int n = 0; n < 4; ++n) acc[t][n] = f32x4{0, 0, 0, 0};
  const int sw = (2 * g + (q4 >> 1)) & 3;
  const char* wr = smem + kp * REGION + (4 * g + q4) * 128 + p4 * 8;
  auto lda = [&](bf16x8* Af, int slot) {
#pragma unroll
    for (int nt = 0; nt < 4; ++nt) {
      const char* p = wr + slot * 4096 + ((nt ^ sw) << 5);
      Af[nt] = cat(ldstr(p), ldstr(p + 2048));
    }
  };
  wait_frag<(R - 1) * 8>(B[0]);
  asm volatile("" : "+v"(rs[0]), "+v"(rs[1]));
  lda(A[0], 0);
  gks_steps<0, NKS, R>(issue, lda, B, A, acc);
  {
    const int mo = kp * REGION + g * 256 + ((i ^ ((g >> 1) * 2)) << 4);
#pragma unroll
    for (int tt = 0; tt < 4; ++tt)
#pragma unroll
      for (int nt = 0; nt < 4; ++nt)
        *(LAS f32x4*)(LAS char*)(smem + (mo ^ (nt * 64)) + (tt * 4 + nt) * 1024) = acc[tt][nt];
  }
  bar();
  const int c = lane & 7;
#pragma unroll
  for (int it = 0; it < 2; ++it) {
    const int tok = (lane >> 3) + 8 * it;
    const char* rp = smem + (kp * 4 + (c >> 1)) * 1024 + (c & 1) * 512 + ((tok ^ (2 * c)) << 4);
    f32x4 v0 = f32x4{0, 0, 0, 0}, v1 = f32x4{0, 0, 0, 0};
#pragma unroll
    for (int p = 0; p < 4; ++p) {
      v0 += *(const LAS f32x4*)(LAS const char*)(rp + p * REGION);
      v1 += *(const LAS f32x4*)(LAS const char*)(rp + p * REGION + 256);
    }
    u32x4 r = __builtin_bit_cast(u32x4, rs[it]);
    u32x4 ov = {pack2(v0[0] + bflo(r[0]), v0[1] + bfhi(r[0])), pack2(v0[2] + bflo(r[1]), v0[3] + bfhi(r[1])),
                pack2(v1[0] + bflo(r[2]), v1[1] + bfhi(r[2])), pack2(v1[2] + bflo(r[3]), v1[3] + bfhi(r[3]))};
    st16<(WT & (STATS ? 4 : 16)) != 0>(out + ooff + it * 8192 + vo, ov);
    if constexpr (STATS) {
      float s1 = 0.f, s2 = 0.f;
#pragma unroll
      for (int e = 0; e < 4; ++e) { s1 = dot2(ov[e], 0x3F803F80u, s1); s2 = dot2(ov[e], ov[e], s2); }
#pragma unroll
      for (int m = 1; m < 8; m <<= 1) { s1 += __shfl_xor(s1, m); s2 += __shfl_xor(s2, m); }
      if (c == 0) *(f32x2*)(stats + ((size_t)(tb * 64 + kp * 16 + tok) * 8 + cb) * 2) = f32x2{s1, s2};
    }
  }
  TL1(STATS ? 2 : 4);
}

constexpr int K4_STAGE = 49152, K4_NS = 3;
constexpr int K4_LDS = K4_STAGE * K4_NS + 4 * 224 * 4;

template <int NT>
DEV void k4_body(char* smem, const char* gt, const char* g1, const char* g3, unsigned vt, unsigned vw,
                 unsigned long long wmask, unsigned ldsw, int tg, int lane, const float* __restrict__ stats,
                 char* __restrict__ hh, int tok0, int cb TLARG) {
  const int g = lane >> 4, i = lane & 15, q4 = (lane >> 2) & 3, p4 = lane & 3;
  f32x4 acc[2][2 * NT], csg = f32x4{0, 0, 0, 0}, csu = f32x4{0, 0, 0, 0};
#pragma unroll
  for (int a = 0; a < 2 * NT; ++a) { acc[0][a] = f32x4{0, 0, 0, 0}; acc[1][a] = f32x4{0, 0, 0, 0}; }
  const bf16x8 ones = {0x3F80, 0x3F80, 0x3F80, 0x3F80, 0x3F80, 0x3F80, 0x3F80, 0x3F80};
  const int toff0 = (tg * 32 + i) * 128 + ((g ^ (i & 7)) << 4);
  const int toff1 = (tg * 32 + i) * 128 + (((4 + g) ^ (i & 7)) << 4);
  const int odd = cb & 1;
  const int fsw = q4 | ((g & 1) << 2);
  int T[NT], wa[NT];
#pragma unroll
  for (int a = 0; a < NT; ++a) {
    T[a] = NT == 4 ? odd * 3 + a : (a < 2 ? (odd ? 1 : 4) + a : (odd ? 0 : 6));
    wa[a] = 16384 + (8 * g + q4) * 256 + ((T[a] ^ fsw) << 5) + p4 * 8;
  }
  const int rot = 0;
  auto dma = [&](int kt, int j) {
    const unsigned base = ldsw + (kt % K4_NS) * K4_STAGE;
    const int ke = (kt + rot) & 7;
    if (j < 2) dma16s(vt, gt + ke * 128 + j * 8192, base + j * 1024);
    else if (j < 4) dma16sm(vw, g1 + (size_t)(ke * 64 + (j - 2) * 4) * (F_ * 2), base + 16384 + (j - 2) * 1024, wmask);
    else dma16sm(vw, g3 + (size_t)(ke * 64 + (j - 4) * 4) * (F_ * 2), base + 32768 + (j - 4) * 1024, wmask);
  };
  auto ldB = [&](bf16x8* B, int kt, int ks) {
    const char* sb = smem + (kt % K4_NS) * K4_STAGE + (ks ? toff1 : toff0);
    B[0] = lds128(sb); B[1] = lds128(sb + 2048);
  };
  auto ldA = [&](int a, int kt, int ks) -> bf16x8 {
    const char* wp = smem + (kt % K4_NS) * K4_STAGE + wa[a < NT ? a : a - NT] + (a < NT ? 0 : 16384) + ks * 8192;
    return cat(ldstr(wp), ldstr(wp + 1024));
  };
  auto mma = [&](int a, bf16x8 A, const bf16x8* B) {
    acc[0][a] = mfma16(A, B[0], acc[0][a]);
    acc[1][a] = mfma16(A, B[1], acc[1][a]);
  };
  auto csum = [&](const bf16x8* A) {
    if (tg == 0) { csg = mfma16(A[0], ones, csg); csu = mfma16(A[NT], ones, csu); }
    else if (tg == 1) { csg = mfma16(A[1], ones, csg); csu = mfma16(A[NT + 1], ones, csu); }
    else if (tg == 2) { csg = mfma16(A[2], ones, csg); csu = mfma16(A[NT + 2], ones, csu); }
    else if (NT == 4) { csg = mfma16(A[NT - 1], ones, csg); csu = mfma16(A[2 * NT - 1], ones, csu); }
  };
  bf16x8 A0[2 * NT], A1[2 * NT], B0[2], B1[2];
  f32x4 sa[2];
#pragma unroll
  for (int j = 0; j < 6; ++j) dma(0, j);
#pragma unroll
  for (int j = 0; j < 6; ++j) dma(1, j);
  wait_bar<6>();
  ldB(B0, 0, 0);
#pragma unroll
  for (int a = 0; a < 2 * NT; ++a) A0[a] = ldA(a, 0, 0);
#pragma unroll
  for (int kt = 0; kt < 8; ++kt) {
    ldB(B1, kt, 1);
#pragma unroll
    for (int a = 0; a < 2 * NT; ++a) {
      if (a < NT) { A1[2 * a] = ldA(2 * a, kt, 1); A1[2 * a + 1] = ldA(2 * a + 1, kt, 1); }
      mma(a, A0[a], B0);
      if (kt == 0 && a >= 2 * NT - 6) dma(2, a - (2 * NT - 6));
      __builtin_amdgcn_sched_barrier(0);
    }
    csum(A0);
    if (kt < 6) wait_bar<6>();
    else if (kt == 6) wait_bar<0>();
    if (kt < 7) ldB(B0, kt + 1, 0);
    if (kt == 6) {
#pragma unroll
      for (int tt = 0; tt < 2; ++tt) sa[tt] = *(const f32x4*)(stats + ((size_t)(tok0 + tg * 32 + tt * 16 + i) * 8 + 2 * g) * 2);
    }
    if (kt == 7) csum(A1);
#pragma unroll
    for (int a = 0; a < 2 * NT; ++a) {
      if (kt < 7 && a < NT) { A0[2 * a] = ldA(2 * a, kt + 1, 0); A0[2 * a + 1] = ldA(2 * a + 1, kt + 1, 0); }
      mma(a, A1[a], B1);
      if (kt + 3 < 8 && a >= 2 * NT - 6) dma(kt + 3, a - (2 * NT - 6));
      __builtin_amdgcn_sched_barrier(0);
    }
    if (kt < 7) csum(A1);
  }
  float* csp = (float*)(smem + K4_STAGE * K4_NS);
  if (i == 0 && tg < NT) {
    const int tt_ = NT == 4 ? odd * 3 + tg : (tg < 2 ? (odd ? 1 : 4) + tg : (odd ? 0 : 6));
    *(f32x4*)(csp + tt_ * 16 + 4 * g) = csg;
    *(f32x4*)(csp + (7 + tt_) * 16 + 4 * g) = csu;
  }
  float rs[2], mr[2];
#pragma unroll
  for (int tt = 0; tt < 2; ++tt) {
    float s1 = sa[tt][0] + sa[tt][2], s2 = sa[tt][1] + sa[tt][3];
    s1 = xsum(s1); s2 = xsum(s2);
    float mean = s1 * (1.f / 512.f);
    float var = fmaxf(s2 * (1.f / 512.f) - mean * mean, 0.f);
    rs[tt] = rsqrtf(var + LN_EPS); mr[tt] = mean * rs[tt];
  }
  bar();
  unsigned pk[2][NT][2];
#pragma unroll
  for (int tt = 0; tt < 2; ++tt) {
    char* dst = hh + ((size_t)((tok0 >> 4) + tg * 2 + tt) * 56) * 1024 + lane * 16;
#pragma unroll
    for (int a = 0; a < NT; ++a) {
      f32x4 cg = *(const f32x4*)(csp + T[a] * 16 + 4 * g);
      f32x4 cu = *(const f32x4*)(csp + (7 + T[a]) * 16 + 4 * g);
      float r[4];
#pragma unroll
      for (int e = 0; e < 4; ++e) {
        float ga = acc[tt][a][e] * rs[tt] - mr[tt] * cg[e];
        float up = acc[tt][NT + a][e] * rs[tt] - mr[tt] * cu[e];
        float sg = __builtin_amdgcn_rcpf(1.f + __builtin_amdgcn_exp2f(-1.4426950408889634f * ga));
        r[e] = ga * sg * up;
      }
      pk[tt][a][0] = pack2(r[0], r[1]); pk[tt][a][1] = pack2(r[2], r[3]);
      const int G = cb * 7 + T[a];
      if (a & 1) {
        st16<(WT & 8) != 0>(dst + (size_t)(G >> 1) * 1024, u32x4{pk[tt][a - 1][0], pk[tt][a - 1][1], pk[tt][a][0], pk[tt][a][1]});
      } else if (a == NT - 1) {
        st8<(WT & 8) != 0>(dst + (size_t)(G >> 1) * 1024 + (G & 1) * 8, u32x2{pk[tt][a][0], pk[tt][a][1]});
      }
    }
  }
  TL1(3);
}

__global__ __launch_bounds__(512) void k4_mlp(const char* __restrict__ h, const char* __restrict__ w1,
                                              const char* __restrict__ w3, const float* __restrict__ stats,
                                              char* __restrict__ hh) {
  extern __shared__ __attribute__((aligned(16))) char smem[];
  TL0;
  const int tid = threadIdx.x, lane = tid & 63, wv = uni(tid >> 6);
  const int tg = wv & 3, half = wv >> 2;
  const int b = blockIdx.x, xcd = b & 7, bi = b >> 3;
  const int cb = (xcd & 3) * 4 + (bi & 3), tb = (xcd >> 2) * 8 + (bi >> 2);
  const unsigned lds0 = (unsigned)(size_t)(LAS char*)smem;
  const char* gt = h + (size_t)(tb * 128 + wv * 16) * 1024;
  const unsigned vt = (lane >> 3) * 1024 + (((lane & 7) ^ (lane >> 3)) << 4);
  const char* g1 = w1 + (size_t)(wv * 8) * (F_ * 2) + cb * 224;
  const char* g3 = w3 + (size_t)(wv * 8) * (F_ * 2) + cb * 224;
  const int unit = ((lane >> 1) & 7) ^ ((lane >> 4) | ((wv & 1) << 2));
  const unsigned vw = (lane >> 4) * (F_ * 2) + unit * 32 + (lane & 1) * 16;
  const unsigned long long wmask = __builtin_amdgcn_ballot_w64(unit < 7);
  const unsigned ldsw = lds0 + wv * 2048;
  if (half == 0) k4_body<4>(smem, gt, g1, g3, vt, vw, wmask, ldsw, tg, lane, stats, hh, tb * 128, cb TLPASS);
  else k4_body<3>(smem, gt, g1, g3, vt, vw, wmask, ldsw, tg, lane, stats, hh, tb * 128, cb TLPASS);
}

extern "C" void kernel_launch(void* const* d_in, const int* in_sizes, int n_in, void* d_out, int out_size,
                              void* d_ws, size_t ws_size, hipStream_t stream) {
  const char* x = (const char*)d_in[0];
  const char* wqkv = (const char*)d_in[1];
  const char* wo = (const char*)d_in[2];
  const char* w1 = (const char*)d_in[4];
  const char* w3 = (const char*)d_in[5];
  const char* w2 = (const char*)d_in[6];
  char* ws = (char*)d_ws;
  char* qkv = ws;
  char* o = ws + (6u << 20);
  char* h = ws + (8u << 20);
  float* stats = (float*)(ws + (10u << 20));
  char* hh = ws + (11u << 20);
  (void)hipFuncSetAttribute((const void*)k1_qkv, hipFuncAttributeMaxDynamicSharedMemorySize, K1_LDS);
  (void)hipFuncSetAttribute((const void*)k2_attn, hipFuncAttributeMaxDynamicSharedMemorySize, K2_LDS);
  (void)hipFuncSetAttribute((const void*)k_gks<4, 4, true>, hipFuncAttributeMaxDynamicSharedMemorySize, GK<4, 4>::LDS);
  (void)hipFuncSetAttribute((const void*)k4_mlp, hipFuncAttributeMaxDynamicSharedMemorySize, K4_LDS);
  (void)hipFuncSetAttribute((const void*)k_gks<14, 7, false>, hipFuncAttributeMaxDynamicSharedMemorySize, GK<14, 7>::LDS);
  k1_qkv<<<256, 512, K1_LDS, stream>>>(x, wqkv, qkv);
  k2_attn<<<256, 512, K2_LDS, stream>>>(qkv, o, wo, w1, w3, w2);
  k_gks<4, 4, true><<<256, 256, GK<4, 4>::LDS, stream>>>(o, wo, x, h, stats);
  k4_mlp<<<256, 512, K4_LDS, stream>>>(h, w1, w3, stats, hh);
  k_gks<14, 7, false><<<256, 256, GK<14, 7>::LDS, stream>>>(hh, w2, h, (char*)d_out, nullptr);
}
```

```cpp
#include <hip/hip_runtime.h>
#include <hip/hip_bf16.h>
#include <stdint.h>

typedef short bf16x8 __attribute__((ext_vector_type(8)));
typedef short s16x4 __attribute__((ext_vector_type(4)));
typedef float f32x4 __attribute__((ext_vector_type(4)));
typedef float f32x2 __attribute__((ext_vector_type(2)));
typedef unsigned u32x4 __attribute__((ext_vector_type(4)));
typedef unsigned u32x2 __attribute__((ext_vector_type(2)));
typedef __bf16 bf16x2 __attribute__((ext_vector_type(2)));
#define LAS __attribute__((address_space(3)))
#define DEV __device__ __forceinline__

constexpr int S_ = 2048, D_ = 512, H_ = 32, F_ = 1792;
constexpr float LN_EPS = 1e-5f;

DEV f32x4 mfma16(bf16x8 a, bf16x8 b, f32x4 c) { return __builtin_amdgcn_mfma_f32_16x16x32_bf16(a, b, c, 0, 0, 0); }
DEV unsigned pack2(float a, float b) {
  f32x2 v = {a, b};
  return __builtin_bit_cast(unsigned, __builtin_convertvector(v, bf16x2));
}
DEV float bflo(unsigned u) { return __builtin_bit_cast(float, u << 16); }
DEV float bfhi(unsigned u) { return __builtin_bit_cast(float, u & 0xffff0000u); }
DEV float dot2(unsigned a, unsigned b, float c) {
  return __builtin_amdgcn_fdot2_f32_bf16(__builtin_bit_cast(bf16x2, a), __builtin_bit_cast(bf16x2, b), c, false);
}
DEV void dma16(const char* gptr, unsigned ldsbase) {
  unsigned sv;
  asm volatile("s_mov_b32 %0, m0\n\ts_mov_b32 m0, %2\n\ts_nop 0\n\tglobal_load_lds_dwordx4 %1, off\n\ts_mov_b32 m0, %0"
               : "=&s"(sv) : "v"(gptr), "s"(ldsbase) : "memory");
}
DEV void dma16s(unsigned voff, const char* sbase, unsigned ldsbase) {
  unsigned sv;
  asm volatile("s_mov_b32 %0, m0\n\ts_mov_b32 m0, %3\n\ts_nop 0\n\tglobal_load_lds_dwordx4 %1, %2\n\ts_mov_b32 m0, %0"
               : "=&s"(sv) : "v"(voff), "s"(sbase), "s"(ldsbase) : "memory");
}
DEV void dma16sm(unsigned voff, const char* sbase, unsigned ldsbase, unsigned long long mask) {
  unsigned sv;
  asm volatile("s_mov_b32 %0, m0\n\ts_mov_b32 m0, %3\n\ts_mov_b64 exec, %4\n\tglobal_load_lds_dwordx4 %1, %2\n\ts_mov_b64 exec, -1\n\ts_mov_b32 m0, %0"
               : "=&s"(sv) : "v"(voff), "s"(sbase), "s"(ldsbase), "s"(mask) : "memory");
}
DEV void gld16(bf16x8& d, unsigned voff, const char* sbase) {
  asm volatile("global_load_dwordx4 %0, %1, %2" : "=v"(d) : "v"(voff), "s"(sbase) : "memory");
}
template <int N> DEV void wait_bar() { asm volatile("s_waitcnt vmcnt(%0) lgkmcnt(0)\n\ts_barrier" ::"n"(N) : "memory"); }
DEV void bar() { asm volatile("s_waitcnt lgkmcnt(0)\n\ts_barrier" ::: "memory"); }
DEV bf16x8 lds128(const char* p) { return *(const LAS bf16x8*)(LAS const char*)p; }
DEV s16x4 ldstr(const char* p) { return __builtin_amdgcn_ds_read_tr16_b64_v4i16((LAS s16x4*)p); }
DEV bf16x8 cat(s16x4 a, s16x4 b) { return __builtin_shufflevector(a, b, 0, 1, 2, 3, 4, 5, 6, 7); }
DEV float xsum(float v) { v += __shfl_xor(v, 16); v += __shfl_xor(v, 32); return v; }
DEV float xmax(float v) { v = fmaxf(v, __shfl_xor(v, 16)); v = fmaxf(v, __shfl_xor(v, 32)); return v; }
DEV unsigned uni(unsigned v) { return __builtin_amdgcn_readfirstlane(v); }
#define WT 31
template <bool W> DEV void st16(char* p, u32x4 v) {
  if constexpr (W) asm volatile("global_store_dwordx4 %0, %1, off sc0 sc1" ::"v"(p), "v"(v) : "memory");
  else *(u32x4*)p = v;
}
template <bool W> DEV void st8(char* p, u32x2 v) {
  if constexpr (W) asm volatile("global_store_dwordx2 %0, %1, off sc0 sc1" ::"v"(p), "v"(v) : "memory");
  else *(u32x2*)p = v;
}

#define TL0
#define TL1(k)
#define TLARG
#define TLPASS
template <int A, int B> struct Min { static constexpr int v = A < B ? A : B; };

template <int KT, int NK, int NS, int P, class Issue, class Compute>
DEV void pipe_steps(Issue& issue, Compute& compute) {
  if constexpr (KT < NK) {
    wait_bar<(Min<KT + NS - 1, NK>::v - KT - 1) * P>();
    if constexpr (KT + NS - 1 < NK) issue((KT + NS - 1) % NS);
    compute(KT % NS, KT);
    pipe_steps<KT + 1, NK, NS, P>(issue, compute);
  }
}

constexpr int K1_STAGE = 16384 + 12288, K1_NS = 5;
constexpr int K1_LDS = K1_STAGE * K1_NS + 1024 + 1536;

__global__ __launch_bounds__(512) void k1_qkv(const char* __restrict__ x, const char* __restrict__ w,
                                              char* __restrict__ qkv) {
  extern __shared__ __attribute__((aligned(16))) char smem[];
  TL0;
  const int tid = threadIdx.x, lane = tid & 63, wv = uni(tid >> 6);
  const int g = lane >> 4, i = lane & 15, q4 = (lane >> 2) & 3, p4 = lane & 3;
  const int tg = wv & 3, nh = wv >> 2;
  const int b = blockIdx.x, xcd = b & 7, bi = b >> 3;
  const int cb = (xcd & 1) * 8 + (bi & 7), tb = (xcd >> 1) * 4 + (bi >> 3);
  const unsigned lds0 = (unsigned)(size_t)(LAS char*)smem;

  const char* pt;
  {
    int row = tb * 128 + wv * 16 + (lane >> 3);
    int c = (lane & 7) ^ (lane >> 3);
    pt = x + (size_t)row * 1024 + c * 16;
  }
  const char *pw0, *pw1;
  {
    int qq = wv * 64 + lane;
    int k = qq / 12, c = (qq % 12) ^ (((k >> 3) & 1) << 1);
    pw0 = w + (size_t)k * 3072 + cb * 192 + c * 16;
    qq = 512 + wv * 32 + (lane & 31);
    k = qq / 12; c = (qq % 12) ^ (((k >> 3) & 1) << 1);
    pw1 = w + (size_t)k * 3072 + cb * 192 + c * 16;
  }
  int kk = bi & 7;
  auto issue = [&](int slot) {
    unsigned base = lds0 + slot * K1_STAGE;
    dma16(pt + kk * 128, base + wv * 2048);
    dma16(pt + kk * 128 + 8192, base + wv * 2048 + 1024);
    dma16(pw0 + (size_t)kk * (64 * 3072), base + 16384 + wv * 1024);
    if (lane < 32) dma16(pw1 + (size_t)kk * (64 * 3072), base + 16384 + 8192 + wv * 512);
    kk = (kk + 1) & 7;
  };
#pragma unroll
  for (int s = 0; s < K1_NS - 1; ++s) issue(s);

  f32x4 acc[2][3], cs = f32x4{0, 0, 0, 0};
#pragma unroll
  for (int a = 0; a < 3; ++a) { acc[0][a] = f32x4{0, 0, 0, 0}; acc[1][a] = f32x4{0, 0, 0, 0}; }
  float s1 = 0.f, s2 = 0.f;
  const bf16x8 ones = {0x3F80, 0x3F80, 0x3F80, 0x3F80, 0x3F80, 0x3F80, 0x3F80, 0x3F80};
  const int toff0 = (tg * 32 + i) * 128 + ((g ^ (i & 7)) << 4);
  const int toff1 = (tg * 32 + i) * 128 + (((4 + g) ^ (i & 7)) << 4);
  int woff[3], un[3];
#pragma unroll
  for (int nt = 0; nt < 3; ++nt) {
    int a = nt + tg; a = a >= 3 ? a - 3 : a; a = a >= 3 ? a - 3 : a;
    un[nt] = nh * 3 + a;
    woff[nt] = 16384 + (8 * g + q4) * 192 + ((un[nt] ^ (g & 1)) << 5) + p4 * 8;
  }

  auto compute = [&](int slot, int kt) {
    const char* sb = smem + slot * K1_STAGE;
#pragma unroll
    for (int ks = 0; ks < 2; ++ks) {
      bf16x8 B[2], A[3];
#pragma unroll
      for (int tt = 0; tt < 2; ++tt) B[tt] = lds128(sb + (ks ? toff1 : toff0) + tt * 2048);
#pragma unroll
      for (int nt = 0; nt < 3; ++nt)
        A[nt] = cat(ldstr(sb + woff[nt] + ks * 6144), ldstr(sb + woff[nt] + ks * 6144 + 768));
#pragma unroll
      for (int nt = 0; nt < 3; ++nt)
#pragma unroll
        for (int tt = 0; tt < 2; ++tt) acc[tt][nt] = mfma16(A[nt], B[tt], acc[tt][nt]);
      {
        u32x4 u = __builtin_bit_cast(u32x4, nh ? B[1] : B[0]);
#pragma unroll
        for (int j = 0; j < 4; ++j) { s1 = dot2(u[j], 0x3F803F80u, s1); s2 = dot2(u[j], u[j], s2); }
      }
      cs = mfma16(A[0], ones, cs);
    }
  };
  pipe_steps<0, 8, K1_NS, 4>(issue, compute);

  float* st = (float*)(smem + K1_STAGE * K1_NS);
  float* csp = (float*)(smem + K1_STAGE * K1_NS + 1024);
  s1 = xsum(s1); s2 = xsum(s2);
  if (g == 0) {
    float mean = s1 * (1.f / 512.f);
    float var = fmaxf(s2 * (1.f / 512.f) - mean * mean, 0.f);
    float rstd = rsqrtf(var + LN_EPS);
    *(f32x2*)(st + (tg * 32 + nh * 16 + i) * 2) = f32x2{mean, rstd};
  }
  if (i == 0 && tg < 3) *(f32x4*)(csp + un[0] * 16 + 4 * g) = cs;
  bar();
  f32x2 ms[2];
#pragma unroll
  for (int tt = 0; tt < 2; ++tt) ms[tt] = *(const f32x2*)(st + (tg * 32 + tt * 16 + i) * 2);
#pragma unroll
  for (int nt = 0; nt < 3; ++nt) {
    f32x4 c = *(const f32x4*)(csp + un[nt] * 16 + 4 * g);
    int n0 = cb * 96 + un[nt] * 16;
    char* dst = qkv + (size_t)(n0 >> 4) * (2048 * 32) + g * 8;
#pragma unroll
    for (int tt = 0; tt < 2; ++tt) {
      f32x4 y = (acc[tt][nt] - ms[tt][0] * c) * ms[tt][1];
      u32x2 o = {pack2(y[0], y[1]), pack2(y[2], y[3])};
      st8<(WT & 1) != 0>(dst + (size_t)(tb * 128 + tg * 32 + tt * 16 + i) * 32, o);
    }
  }
  TL1(0);
}

constexpr int K2_LDS = 96 * 1024;
__global__ __launch_bounds__(512) void k2_attn(const char* __restrict__ qkv, char* __restrict__ o,
    const char* __restrict__ wo, const char* __restrict__ w1, const char* __restrict__ w3, const char* __restrict__ w2) {
  extern __shared__ __attribute__((aligned(16))) char smem[];
  TL0;
  const int tid = threadIdx.x, lane = tid & 63, wv = uni(tid >> 6);
  const int g = lane >> 4, i = lane & 15, q4 = (lane >> 2) & 3, p4 = lane & 3;
  const int b = blockIdx.x;
  const int hp = (b & 7) * 2 + ((b >> 3) & 1), qb = b >> 4;
  const int q0 = qb * 128;
  const unsigned lds0 = (unsigned)(size_t)(LAS char*)smem;
  const size_t HS = 2048 * 32;
  {
    const int r = wv >> 1;
    const char* src = qkv + (size_t)((1 + (r >> 1)) * 32 + hp * 2 + (r & 1)) * HS;
#pragma unroll
    for (int j = 0; j < 6; ++j) {
      int ch = ((wv & 1) * 6 + j) * 64 + lane;
      int key = q0 - 256 + (ch >> 1);
      key = key < 0 ? 0 : key;
      dma16(src + (size_t)key * 32 + (ch & 1) * 16, lds0 + r * 12288 + ((wv & 1) * 6 + j) * 1024);
    }
  }
  const int qs = q0 + wv * 16;
  bf16x8 Q[2];
  gld16(Q[0], (unsigned)(i * 32 + (g & 1) * 16), qkv + (size_t)(hp * 2) * HS + (size_t)qs * 32);
  gld16(Q[1], (unsigned)(i * 32 + (g & 1) * 16), qkv + (size_t)(hp * 2 + 1) * HS + (size_t)qs * 32);
  {
    const int c0 = b * 8 + wv;
    const unsigned sc = lds0 + 49152 + wv * 1024;
    const char* p0 = c0 < 512 ? wo + (size_t)c0 * 1024 : w1 + (size_t)(c0 - 512) * 1024;
    const char* p1 = c0 < 256 ? w1 + (size_t)(c0 + 1536) * 1024 : w3 + (size_t)(c0 - 256) * 1024;
    const char* p2 = w2 + (size_t)(c0 < 1792 ? c0 : c0 - 1792) * 1024;
    dma16s(lane * 16, p0, sc); dma16s(lane * 16, p1, sc); dma16s(lane * 16, p2, sc);
  }
  asm volatile("s_waitcnt vmcnt(3)\n\ts_barrier" : "+v"(Q[0]), "+v"(Q[1]) : : "memory");
  if (g >= 2) { Q[0] = bf16x8{0, 0, 0, 0, 0, 0, 0, 0}; Q[1] = bf16x8{0, 0, 0, 0, 0, 0, 0, 0}; }
  const float c = 0.25f * 1.4426950408889634f;
  const float NEG = -1e30f;
  const int lrow = wv * 16;
  f32x4 cin0, cin16;
#pragma unroll
  for (int r = 0; r < 4; ++r) { cin0[r] = (4 * g + r > i) ? 0.f : NEG; cin16[r] = (4 * g + r <= i) ? 0.f : NEG; }
  const bf16x8 ones = {0x3F80, 0x3F80, 0x3F80, 0x3F80, 0x3F80, 0x3F80, 0x3F80, 0x3F80};
  u32x4 ov;
#pragma unroll
  for (int u = 0; u < 2; ++u) {
    const char* Kl = smem + u * 12288;
    const char* Vl = smem + 24576 + u * 12288;
    f32x4 s[17];
#pragma unroll
    for (int t = 0; t < 17; ++t) {
      bf16x8 A = lds128(Kl + (lrow + t * 16 + i) * 32 + (g & 1) * 16);
      s[t] = mfma16(A, Q[u], t == 0 ? cin0 : t == 16 ? cin16 : f32x4{0, 0, 0, 0});
    }
    if (qs < 256) {
      asm volatile("" ::: "memory");
      const int nneg = (256 - qs) >> 4;
#pragma unroll
      for (int t = 0; t < 16; ++t)
        if (t < nneg) s[t] = f32x4{NEG, NEG, NEG, NEG};
    }
    float m = s[16][0];
#pragma unroll
    for (int t = 0; t < 17; ++t)
#pragma unroll
      for (int r = 0; r < 4; ++r) m = fmaxf(m, s[t][r]);
    m = xmax(m);
    const f32x2 c2 = {c, c}, m2 = {-m * c, -m * c};
    unsigned pk[17][2];
#pragma unroll
    for (int t = 0; t < 17; ++t) {
      f32x2 a0 = __builtin_elementwise_fma(f32x2{s[t][0], s[t][1]}, c2, m2);
      f32x2 a1 = __builtin_elementwise_fma(f32x2{s[t][2], s[t][3]}, c2, m2);
      pk[t][0] = pack2(__builtin_amdgcn_exp2f(a0[0]), __builtin_amdgcn_exp2f(a0[1]));
      pk[t][1] = pack2(__builtin_amdgcn_exp2f(a1[0]), __builtin_amdgcn_exp2f(a1[1]));
    }
    f32x4 oacc = f32x4{0, 0, 0, 0}, lsum = f32x4{0, 0, 0, 0};
#pragma unroll
    for (int tp = 0; tp < 9; ++tp) {
      const int t0 = 2 * tp, t1 = (2 * tp + 1 < 17) ? 2 * tp + 1 : 16;
      bf16x8 A = cat(ldstr(Vl + (lrow + t0 * 16 + 4 * g + q4) * 32 + p4 * 8), ldstr(Vl + (lrow + t1 * 16 + 4 * g + q4) * 32 + p4 * 8));
      u32x4 pb = {pk[t0][0], pk[t0][1], (2 * tp + 1 < 17) ? pk[t1][0] : 0u, (2 * tp + 1 < 17) ? pk[t1][1] : 0u};
      oacc = mfma16(A, __builtin_bit_cast(bf16x8, pb), oacc);
      lsum = mfma16(ones, __builtin_bit_cast(bf16x8, pb), lsum);
    }
    const float inv = __builtin_amdgcn_rcpf(lsum[0]);
    ov[2 * u] = pack2(oacc[0] * inv, oacc[1] * inv);
    ov[2 * u + 1] = pack2(oacc[2] * inv, oacc[3] * inv);
  }
  st16<(WT & 2) != 0>(o + ((size_t)(qs >> 4) * 16 + hp) * 1024 + lane * 16, ov);
  asm volatile("s_waitcnt vmcnt(0)" ::: "memory");
  TL1(1);
}

template <int N> DEV void wait_frag(bf16x8* B) {
  asm volatile("s_waitcnt vmcnt(%4)" : "+v"(B[0]), "+v"(B[1]), "+v"(B[2]), "+v"(B[3]) : "n"(N) : "memory");
}
template <int NKS, int R> struct GK { static constexpr int REGION = (R * 4096 > 16384) ? R * 4096 : 16384, LDS = 4 * REGION > 98304 ? 4 * REGION : 98304; };

template <int J, int NKS, int R, class Issue, class LdA>
DEV void gks_steps(Issue& issue, LdA& lda, bf16x8 (&B)[R][4], bf16x8 (&A)[2][4], f32x4 (&acc)[4][4]) {
  if constexpr (J < NKS) {
    if constexpr (J + 1 < NKS) {
      wait_frag<(Min<J + R - 1, NKS - 1>::v - (J + 1)) * 8>(B[(J + 1) % R]);
      lda(A[(J + 1) & 1], (J + 1) % R);
    }
#pragma unroll
    for (int nt = 0; nt < 4; ++nt)
#pragma unroll
      for (int tt = 0; tt < 4; ++tt) acc[tt][nt] = mfma16(A[J & 1][nt], B[J % R][tt], acc[tt][nt]);
    __builtin_amdgcn_sched_barrier(0);
    if constexpr (J + R < NKS) issue(J + R);
    gks_steps<J + 1, NKS, R>(issue, lda, B, A, acc);
  }
}

template <int NKS, int R, bool STATS>
__global__ __launch_bounds__(256) void k_gks(const char* __restrict__ a, const char* __restrict__ w,
                                             const char* __restrict__ resid, char* __restrict__ out,
                                             float* __restrict__ stats) {
  extern __shared__ __attribute__((aligned(16))) char smem[];
  TL0;
  const int tid = threadIdx.x, lane = tid & 63, kp = uni(tid >> 6);
  const int g = lane >> 4, i = lane & 15, q4 = (lane >> 2) & 3, p4 = lane & 3;
  const int b = blockIdx.x;
  const int cb = (b >> 3) & 7, tb = (b & 7) * 4 + (b >> 6);
  constexpr int KT = NKS * 4, REGION = GK<NKS, R>::REGION;
  const unsigned ldsw = (unsigned)(size_t)(LAS char*)smem + kp * REGION;
  const char* ga = a + ((size_t)(tb * 4) * KT + kp * NKS) * 1024;
  const char* gw = w + (size_t)(kp * NKS * 32) * 1024 + cb * 128;
  const unsigned va = lane * 16;
  const unsigned vw = (lane >> 3) * 1024 + ((((lane & 7) >> 1) ^ ((lane >> 4) & 3)) << 5) + (lane & 1) * 16;
  bf16x8 B[R][4], A[2][4], rs[2];
  const int rot = (NKS > R) ? ((tb ^ cb) & 1) * (NKS / 2) : 0;
  auto issue = [&](int j) {
    const int s = j < NKS / 2 ? j + rot : j - rot;
#pragma unroll
    for (int q = 0; q < 4; ++q) dma16s(vw, gw + (size_t)(s * 32 + q * 8) * 1024, ldsw + (j % R) * 4096 + q * 1024);
#pragma unroll
    for (int tt = 0; tt < 4; ++tt) gld16(B[j % R][tt], va, ga + (size_t)(tt * KT + s) * 1024);
  };
  const size_t ooff = (size_t)(tb * 64 + kp * 16) * 1024 + cb * 128;
  const unsigned vo = (lane >> 3) * 1024 + (lane & 7) * 16;
  gld16(rs[0], vo, resid + ooff);
  gld16(rs[1], vo, resid + ooff + 8192);
#pragma unroll
  for (int j = 0; j < R; ++j) issue(j);
  f32x4 acc[4][4];
#pragma unroll
  for (int t = 0; t < 4; ++t)
#pragma unroll
    for (int n = 0; n < 4; ++n) acc[t][n] = f32x4{0, 0, 0, 0};
  const int sw = (2 * g + (q4 >> 1)) & 3;
  const char* wr = smem + kp * REGION + (4 * g + q4) * 128 + p4 * 8;
  auto lda = [&](bf16x8* Af, int slot) {
#pragma unroll
    for (int nt = 0; nt < 4; ++nt) {
      const char* p = wr + slot * 4096 + ((nt ^ sw) << 5);
      Af[nt] = cat(ldstr(p), ldstr(p + 2048));
    }
  };
  wait_frag<(R - 1) * 8>(B[0]);
  asm volatile("" : "+v"(rs[0]), "+v"(rs[1]));
  lda(A[0], 0);
  gks_steps<0, NKS, R>(issue, lda, B, A, acc);
  {
    const int mo = kp * REGION + g * 256 + ((i ^ ((g >> 1) * 2)) << 4);
#pragma unroll
    for (int tt = 0; tt < 4; ++tt)
#pragma unroll
      for (int nt = 0; nt < 4; ++nt)
        *(LAS f32x4*)(LAS char*)(smem + (mo ^ (nt * 64)) + (tt * 4 + nt) * 1024) = acc[tt][nt];
  }
  bar();
  const int c = lane & 7;
#pragma unroll
  for (int it = 0; it < 2; ++it) {
    const int tok = (lane >> 3) + 8 * it;
    const char* rp = smem + (kp * 4 + (c >> 1)) * 1024 + (c & 1) * 512 + ((tok ^ (2 * c)) << 4);
    f32x4 v0 = f32x4{0, 0, 0, 0}, v1 = f32x4{0, 0, 0, 0};
#pragma unroll
    for (int p = 0; p < 4; ++p) {
      v0 += *(const LAS f32x4*)(LAS const char*)(rp + p * REGION);
      v1 += *(const LAS f32x4*)(LAS const char*)(rp + p * REGION + 256);
    }
    u32x4 r = __builtin_bit_cast(u32x4, rs[it]);
    u32x4 ov = {pack2(v0[0] + bflo(r[0]), v0[1] + bfhi(r[0])), pack2(v0[2] + bflo(r[1]), v0[3] + bfhi(r[1])),
                pack2(v1[0] + bflo(r[2]), v1[1] + bfhi(r[2])), pack2(v1[2] + bflo(r[3]), v1[3] + bfhi(r[3]))};
    st16<(WT & (STATS ? 4 : 16)) != 0>(out + ooff + it * 8192 + vo, ov);
    if constexpr (STATS) {
      float s1 = 0.f, s2 = 0.f;
#pragma unroll
      for (int e = 0; e < 4; ++e) { s1 = dot2(ov[e], 0x3F803F80u, s1); s2 = dot2(ov[e], ov[e], s2); }
#pragma unroll
      for (int m = 1; m < 8; m <<= 1) { s1 += __shfl_xor(s1, m); s2 += __shfl_xor(s2, m); }
      if (c == 0) *(f32x2*)(stats + ((size_t)(tb * 64 + kp * 16 + tok) * 8 + cb) * 2) = f32x2{s1, s2};
    }
  }
  TL1(STATS ? 2 : 4);
}

constexpr int K4_STAGE = 49152, K4_NS = 3;
constexpr int K4_LDS = K4_STAGE * K4_NS + 4 * 224 * 4;

template <int NT>
DEV void k4_body(char* smem, const char* gt, const char* g1, const char* g3, unsigned vt, unsigned vw,
                 unsigned long long wmask, unsigned ldsw, int tg, int lane, const float* __restrict__ stats,
                 char* __restrict__ hh, int tok0, int cb TLARG) {
  const int g = lane >> 4, i = lane & 15, q4 = (lane >> 2) & 3, p4 = lane & 3;
  f32x4 acc[2][2 * NT], csg = f32x4{0, 0, 0, 0}, csu = f32x4{0, 0, 0, 0};
#pragma unroll
  for (int a = 0; a < 2 * NT; ++a) { acc[0][a] = f32x4{0, 0, 0, 0}; acc[1][a] = f32x4{0, 0, 0, 0}; }
  const bf16x8 ones = {0x3F80, 0x3F80, 0x3F80, 0x3F80, 0x3F80, 0x3F80, 0x3F80, 0x3F80};
  const int toff0 = (tg * 32 + i) * 128 + ((g ^ (i & 7)) << 4);
  const int toff1 = (tg * 32 + i) * 128 + (((4 + g) ^ (i & 7)) << 4);
  const int odd = cb & 1;
  const int fsw = q4 | ((g & 1) << 2);
  int T[NT], wa[NT];
#pragma unroll
  for (int a = 0; a < NT; ++a) {
    T[a] = NT == 4 ? odd * 3 + a : (a < 2 ? (odd ? 1 : 4) + a : (odd ? 0 : 6));
    wa[a] = 16384 + (8 * g + q4) * 256 + ((T[a] ^ fsw) << 5) + p4 * 8;
  }
  const int rot = 0;
  auto dma = [&](int kt, int j) {
    const unsigned base = ldsw + (kt % K4_NS) * K4_STAGE;
    const int ke = (kt + rot) & 7;
    if (j < 2) dma16s(vt, gt + ke * 128 + j * 8192, base + j * 1024);
    else if (j < 4) dma16sm(vw, g1 + (size_t)(ke * 64 + (j - 2) * 4) * (F_ * 2), base + 16384 + (j - 2) * 1024, wmask);
    else dma16sm(vw, g3 + (size_t)(ke * 64 + (j - 4) * 4) * (F_ * 2), base + 32768 + (j - 4) * 1024, wmask);
  };
  auto ldB = [&](bf16x8* B, int kt, int ks) {
    const char* sb = smem + (kt % K4_NS) * K4_STAGE + (ks ? toff1 : toff0);
    B[0] = lds128(sb); B[1] = lds128(sb + 2048);
  };
  auto ldA = [&](int a, int kt, int ks) -> bf16x8 {
    const char* wp = smem + (kt % K4_NS) * K4_STAGE + wa[a < NT ? a : a - NT] + (a < NT ? 0 : 16384) + ks * 8192;
    return cat(ldstr(wp), ldstr(wp + 1024));
  };
  auto mma = [&](int a, bf16x8 A, const bf16x8* B) {
    acc[0][a] = mfma16(A, B[0], acc[0][a]);
    acc[1][a] = mfma16(A, B[1], acc[1][a]);
  };
  auto csum = [&](const bf16x8* A) {
    if (tg == 0) { csg = mfma16(A[0], ones, csg); csu = mfma16(A[NT], ones, csu); }
    else if (tg == 1) { csg = mfma16(A[1], ones, csg); csu = mfma16(A[NT + 1], ones, csu); }
    else if (tg == 2) { csg = mfma16(A[2], ones, csg); csu = mfma16(A[NT + 2], ones, csu); }
    else if (NT == 4) { csg = mfma16(A[NT - 1], ones, csg); csu = mfma16(A[2 * NT - 1], ones, csu); }
  };
  bf16x8 A0[2 * NT], A1[2 * NT], B0[2], B1[2];
  f32x4 sa[2];
#pragma unroll
  for (int j = 0; j < 6; ++j) dma(0, j);
#pragma unroll
  for (int j = 0; j < 6; ++j) dma(1, j);
  wait_bar<6>();
  ldB(B0, 0, 0);
#pragma unroll
  for (int a = 0; a < 2 * NT; ++a) A0[a] = ldA(a, 0, 0);
#pragma unroll
  for (int kt = 0; kt < 8; ++kt) {
    ldB(B1, kt, 1);
#pragma unroll
    for (int a = 0; a < 2 * NT; ++a) {
      if (a < NT) { A1[2 * a] = ldA(2 * a, kt, 1); A1[2 * a + 1] = ldA(2 * a + 1, kt, 1); }
      mma(a, A0[a], B0);
      if (kt == 0 && a >= 2 * NT - 6) dma(2, a - (2 * NT - 6));
      __builtin_amdgcn_sched_barrier(0);
    }
    csum(A0);
    if (kt < 6) wait_bar<6>();
    else if (kt == 6) wait_bar<0>();
    if (kt < 7) ldB(B0, kt + 1, 0);
    if (kt == 6) {
#pragma unroll
      for (int tt = 0; tt < 2; ++tt) sa[tt] = *(const f32x4*)(stats + ((size_t)(tok0 + tg * 32 + tt * 16 + i) * 8 + 2 * g) * 2);
    }
    if (kt == 7) csum(A1);
#pragma unroll
    for (int a = 0; a < 2 * NT; ++a) {
      if (kt < 7 && a < NT) { A0[2 * a] = ldA(2 * a, kt + 1, 0); A0[2 * a + 1] = ldA(2 * a + 1, kt + 1, 0); }
      mma(a, A1[a], B1);
      if (kt + 3 < 8 && a >= 2 * NT - 6) dma(kt + 3, a - (2 * NT - 6));
      __builtin_amdgcn_sched_barrier(0);
    }
    if (kt < 7) csum(A1);
  }
  float* csp = (float*)(smem + K4_STAGE * K4_NS);
  if (i == 0 && tg < NT) {
    const int tt_ = NT == 4 ? odd * 3 + tg : (tg < 2 ? (odd ? 1 : 4) + tg : (odd ? 0 : 6));
    *(f32x4*)(csp + tt_ * 16 + 4 * g) = csg;
    *(f32x4*)(csp + (7 + tt_) * 16 + 4 * g) = csu;
  }
  float rs[2], mr[2];
#pragma unroll
  for (int tt = 0; tt < 2; ++tt) {
    float s1 = sa[tt][0] + sa[tt][2], s2 = sa[tt][1] + sa[tt][3];
    s1 = xsum(s1); s2 = xsum(s2);
    float mean = s1 * (1.f / 512.f);
    float var = fmaxf(s2 * (1.f / 512.f) - mean * mean, 0.f);
    rs[tt] = rsqrtf(var + LN_EPS); mr[tt] = mean * rs[tt];
  }
  bar();
  unsigned pk[2][NT][2];
#pragma unroll
  for (int tt = 0; tt < 2; ++tt) {
    char* dst = hh + ((size_t)((tok0 >> 4) + tg * 2 + tt) * 56) * 1024 + lane * 16;
#pragma unroll
    for (int a = 0; a < NT; ++a) {
      f32x4 cg = *(const f32x4*)(csp + T[a] * 16 + 4 * g);
      f32x4 cu = *(const f32x4*)(csp + (7 + T[a]) * 16 + 4 * g);
      float r[4];
#pragma unroll
      for (int e = 0; e < 4; ++e) {
        float ga = acc[tt][a][e] * rs[tt] - mr[tt] * cg[e];
        float up = acc[tt][NT + a][e] * rs[tt] - mr[tt] * cu[e];
        float sg = __builtin_amdgcn_rcpf(1.f + __builtin_amdgcn_exp2f(-1.4426950408889634f * ga));
        r[e] = ga * sg * up;
      }
      pk[tt][a][0] = pack2(r[0], r[1]); pk[tt][a][1] = pack2(r[2], r[3]);
      const int G = cb * 7 + T[a];
      if (a & 1) {
        st16<(WT & 8) != 0>(dst + (size_t)(G >> 1) * 1024, u32x4{pk[tt][a - 1][0], pk[tt][a - 1][1], pk[tt][a][0], pk[tt][a][1]});
      } else if (a == NT - 1) {
        st8<(WT & 8) != 0>(dst + (size_t)(G >> 1) * 1024 + (G & 1) * 8, u32x2{pk[tt][a][0], pk[tt][a][1]});
      }
    }
  }
  TL1(3);
}

__global__ __launch_bounds__(512) void k4_mlp(const char* __restrict__ h, const char* __restrict__ w1,
                                              const char* __restrict__ w3, const float* __restrict__ stats,
                                              char* __restrict__ hh) {
  extern __shared__ __attribute__((aligned(16))) char smem[];
  TL0;
  const int tid = threadIdx.x, lane = tid & 63, wv = uni(tid >> 6);
  const int tg = wv & 3, half = wv >> 2;
  const int b = blockIdx.x, xcd = b & 7, bi = b >> 3;
  const int cb = (xcd & 3) * 4 + (bi & 3), tb = (xcd >> 2) * 8 + (bi >> 2);
  const unsigned lds0 = (unsigned)(size_t)(LAS char*)smem;
  const char* gt = h + (size_t)(tb * 128 + wv * 16) * 1024;
  const unsigned vt = (lane >> 3) * 1024 + (((lane & 7) ^ (lane >> 3)) << 4);
  const char* g1 = w1 + (size_t)(wv * 8) * (F_ * 2) + cb * 224;
  const char* g3 = w3 + (size_t)(wv * 8) * (F_ * 2) + cb * 224;
  const int unit = ((lane >> 1) & 7) ^ ((lane >> 4) | ((wv & 1) << 2));
  const unsigned vw = (lane >> 4) * (F_ * 2) + unit * 32 + (lane & 1) * 16;
  const unsigned long long wmask = __builtin_amdgcn_ballot_w64(unit < 7);
  const unsigned ldsw = lds0 + wv * 2048;
  if (half == 0) k4_body<4>(smem, gt, g1, g3, vt, vw, wmask, ldsw, tg, lane, stats, hh, tb * 128, cb TLPASS);
  else k4_body<3>(smem, gt, g1, g3, vt, vw, wmask, ldsw, tg, lane, stats, hh, tb * 128, cb TLPASS);
}

extern "C" void kernel_launch(void* const* d_in, const int* in_sizes, int n_in, void* d_out, int out_size,
                              void* d_ws, size_t ws_size, hipStream_t stream) {
  const char* x = (const char*)d_in[0];
  const char* wqkv = (const char*)d_in[1];
  const char* wo = (const char*)d_in[2];
  const char* w1 = (const char*)d_in[4];
  const char* w3 = (const char*)d_in[5];
  const char* w2 = (const char*)d_in[6];
  char* ws = (char*)d_ws;
  char* qkv = ws;
  char* o = ws + (6u << 20);
  char* h = ws + (8u << 20);
  float* stats = (float*)(ws + (10u << 20));
  char* hh = ws + (11u << 20);
  (void)hipFuncSetAttribute((const void*)k1_qkv, hipFuncAttributeMaxDynamicSharedMemorySize, K1_LDS);
  (void)hipFuncSetAttribute((const void*)k2_attn, hipFuncAttributeMaxDynamicSharedMemorySize, K2_LDS);
  (void)hipFuncSetAttribute((const void*)k_gks<4, 4, true>, hipFuncAttributeMaxDynamicSharedMemorySize, GK<4, 4>::LDS);
  (void)hipFuncSetAttribute((const void*)k4_mlp, hipFuncAttributeMaxDynamicSharedMemorySize, K4_LDS);
  (void)hipFuncSetAttribute((const void*)k_gks<14, 7, false>, hipFuncAttributeMaxDynamicSharedMemorySize, GK<14, 7>::LDS);
  k1_qkv<<<256, 512, K1_LDS, stream>>>(x, wqkv, qkv);
  k2_attn<<<256, 512, K2_LDS, stream>>>(qkv, o, wo, w1, w3, w2);
  k_gks<4, 4, true><<<256, 256, GK<4, 4>::LDS, stream>>>(o, wo, x, h, stats);
  k4_mlp<<<256, 512, K4_LDS, stream>>>(h, w1, w3, stats, hh);
  k_gks<14, 7, false><<<256, 256, GK<14, 7>::LDS, stream>>>(hh, w2, h, (char*)d_out, nullptr);
}
```

```cpp
#include <hip/hip_runtime.h>
#include <hip/hip_bf16.h>
#include <stdint.h>

typedef short bf16x8 __attribute__((ext_vector_type(8)));
typedef short s16x4 __attribute__((ext_vector_type(4)));
typedef float f32x4 __attribute__((ext_vector_type(4)));
typedef float f32x2 __attribute__((ext_vector_type(2)));
typedef unsigned u32x4 __attribute__((ext_vector_type(4)));
typedef unsigned u32x2 __attribute__((ext_vector_type(2)));
typedef __bf16 bf16x2 __attribute__((ext_vector_type(2)));
#define LAS __attribute__((address_space(3)))
#define DEV __device__ __forceinline__

constexpr int S_ = 2048, D_ = 512, H_ = 32, F_ = 1792;
constexpr float LN_EPS = 1e-5f;

DEV f32x4 mfma16(bf16x8 a, bf16x8 b, f32x4 c) { return __builtin_amdgcn_mfma_f32_16x16x32_bf16(a, b, c, 0, 0, 0); }
DEV unsigned pack2(float a, float b) {
  f32x2 v = {a, b};
  return __builtin_bit_cast(unsigned, __builtin_convertvector(v, bf16x2));
}
DEV float bflo(unsigned u) { return __builtin_bit_cast(float, u << 16); }
DEV float bfhi(unsigned u) { return __builtin_bit_cast(float, u & 0xffff0000u); }
DEV float dot2(unsigned a, unsigned b, float c) {
  return __builtin_amdgcn_fdot2_f32_bf16(__builtin_bit_cast(bf16x2, a), __builtin_bit_cast(bf16x2, b), c, false);
}
DEV void dma16(const char* gptr, unsigned ldsbase) {
  unsigned sv;
  asm volatile("s_mov_b32 %0, m0\n\ts_mov_b32 m0, %2\n\ts_nop 0\n\tglobal_load_lds_dwordx4 %1, off\n\ts_mov_b32 m0, %0"
               : "=&s"(sv) : "v"(gptr), "s"(ldsbase) : "memory");
}
DEV void dma16s(unsigned voff, const char* sbase, unsigned ldsbase) {
  unsigned sv;
  asm volatile("s_mov_b32 %0, m0\n\ts_mov_b32 m0, %3\n\ts_nop 0\n\tglobal_load_lds_dwordx4 %1, %2\n\ts_mov_b32 m0, %0"
               : "=&s"(sv) : "v"(voff), "s"(sbase), "s"(ldsbase) : "memory");
}
DEV void dma16sm(unsigned voff, const char* sbase, unsigned ldsbase, unsigned long long mask) {
  unsigned sv;
  asm volatile("s_mov_b32 %0, m0\n\ts_mov_b32 m0, %3\n\ts_mov_b64 exec, %4\n\tglobal_load_lds_dwordx4 %1, %2\n\ts_mov_b64 exec, -1\n\ts_mov_b32 m0, %0"
               : "=&s"(sv) : "v"(voff), "s"(sbase), "s"(ldsbase), "s"(mask) : "memory");
}
DEV void gld16(bf16x8& d, unsigned voff, const char* sbase) {
  asm volatile("global_load_dwordx4 %0, %1, %2" : "=v"(d) : "v"(voff), "s"(sbase) : "memory");
}
template <int N> DEV void wait_bar() { asm volatile("s_waitcnt vmcnt(%0) lgkmcnt(0)\n\ts_barrier" ::"n"(N) : "memory"); }
DEV void bar() { asm volatile("s_waitcnt lgkmcnt(0)\n\ts_barrier" ::: "memory"); }
DEV bf16x8 lds128(const char* p) { return *(const LAS bf16x8*)(LAS const char*)p; }
DEV s16x4 ldstr(const char* p) { return __builtin_amdgcn_ds_read_tr16_b64_v4i16((LAS s16x4*)p); }
DEV bf16x8 cat(s16x4 a, s16x4 b) { return __builtin_shufflevector(a, b, 0, 1, 2, 3, 4, 5, 6, 7); }
DEV float xsum(float v) { v += __shfl_xor(v, 16); v += __shfl_xor(v, 32); return v; }
DEV float xmax(float v) { v = fmaxf(v, __shfl_xor(v, 16)); v = fmaxf(v, __shfl_xor(v, 32)); return v; }
DEV unsigned uni(unsigned v) { return __builtin_amdgcn_readfirstlane(v); }
#define WT 31
template <bool W> DEV void st16(char* p, u32x4 v) {
  if constexpr (W) asm volatile("global_store_dwordx4 %0, %1, off sc0 sc1" ::"v"(p), "v"(v) : "memory");
  else *(u32x4*)p = v;
}
template <bool W> DEV void st8(char* p, u32x2 v) {
  if constexpr (W) asm volatile("global_store_dwordx2 %0, %1, off sc0 sc1" ::"v"(p), "v"(v) : "memory");
  else *(u32x2*)p = v;
}

#define TL0
#define TL1(k)
#define TLARG
#define TLPASS
template <int A, int B> struct Min { static constexpr int v = A < B ? A : B; };

template <int KT, int NK, int NS, int P, class Issue, class Compute>
DEV void pipe_steps(Issue& issue, Compute& compute) {
  if constexpr (KT < NK) {
    wait_bar<(Min<KT + NS - 1, NK>::v - KT - 1) * P>();
    if constexpr (KT + NS - 1 < NK) issue((KT + NS - 1) % NS);
    compute(KT % NS, KT);
    pipe_steps<KT + 1, NK, NS, P>(issue, compute);
  }
}

constexpr int K1_STAGE = 16384 + 12288, K1_NS = 5;
constexpr int K1_LDS = K1_STAGE * K1_NS + 1024 + 1536;

__global__ __launch_bounds__(512) void k1_qkv(const char* __restrict__ x, const char* __restrict__ w,
                                              char* __restrict__ qkv) {
  extern __shared__ __attribute__((aligned(16))) char smem[];
  TL0;
  const int tid = threadIdx.x, lane = tid & 63, wv = uni(tid >> 6);
  const int g = lane >> 4, i = lane & 15, q4 = (lane >> 2) & 3, p4 = lane & 3;
  const int tg = wv & 3, nh = wv >> 2;
  const int b = blockIdx.x, xcd = b & 7, bi = b >> 3;
  const int cb = (xcd & 1) * 8 + (bi & 7), tb = (xcd >> 1) * 4 + (bi >> 3);
  const unsigned lds0 = (unsigned)(size_t)(LAS char*)smem;

  const char* pt;
  {
    int row = tb * 128 + wv * 16 + (lane >> 3);
    int c = (lane & 7) ^ (lane >> 3);
    pt = x + (size_t)row * 1024 + c * 16;
  }
  const char *pw0, *pw1;
  {
    int qq = wv * 64 + lane;
    int k = qq / 12, c = (qq % 12) ^ (((k >> 3) & 1) << 1);
    pw0 = w + (size_t)k * 3072 + cb * 192 + c * 16;
    qq = 512 + wv * 32 + (lane & 31);
    k = qq / 12; c = (qq % 12) ^ (((k >> 3) & 1) << 1);
    pw1 = w + (size_t)k * 3072 + cb * 192 + c * 16;
  }
  int kk = bi & 7;
  auto issue = [&](int slot) {
    unsigned base = lds0 + slot * K1_STAGE;
    dma16(pt + kk * 128, base + wv * 2048);
    dma16(pt + kk * 128 + 8192, base + wv * 2048 + 1024);
    dma16(pw0 + (size_t)kk * (64 * 3072), base + 16384 + wv * 1024);
    if (lane < 32) dma16(pw1 + (size_t)kk * (64 * 3072), base + 16384 + 8192 + wv * 512);
    kk = (kk + 1) & 7;
  };
#pragma unroll
  for (int s = 0; s < K1_NS - 1; ++s) issue(s);

  f32x4 acc[2][3], cs = f32x4{0, 0, 0, 0};
#pragma unroll
  for (int a = 0; a < 3; ++a) { acc[0][a] = f32x4{0, 0, 0, 0}; acc[1][a] = f32x4{0, 0, 0, 0}; }
  float s1 = 0.f, s2 = 0.f;
  const bf16x8 ones = {0x3F80, 0x3F80, 0x3F80, 0x3F80, 0x3F80, 0x3F80, 0x3F80, 0x3F80};
  const int toff0 = (tg * 32 + i) * 128 + ((g ^ (i & 7)) << 4);
  const int toff1 = (tg * 32 + i) * 128 + (((4 + g) ^ (i & 7)) << 4);
  int woff[3], un[3];
#pragma unroll
  for (int nt = 0; nt < 3; ++nt) {
    int a = nt + tg; a = a >= 3 ? a - 3 : a; a = a >= 3 ? a - 3 : a;
    un[nt] = nh * 3 + a;
    woff[nt] = 16384 + (8 * g + q4) * 192 + ((un[nt] ^ (g & 1)) << 5) + p4 * 8;
  }

  auto compute = [&](int slot, int kt) {
    const char* sb = smem + slot * K1_STAGE;
#pragma unroll
    for (int ks = 0; ks < 2; ++ks) {
      bf16x8 B[2], A[3];
#pragma unroll
      for (int tt = 0; tt < 2; ++tt) B[tt] = lds128(sb + (ks ? toff1 : toff0) + tt * 2048);
#pragma unroll
      for (int nt = 0; nt < 3; ++nt)
        A[nt] = cat(ldstr(sb + woff[nt] + ks * 6144), ldstr(sb + woff[nt] + ks * 6144 + 768));
#pragma unroll
      for (int nt = 0; nt < 3; ++nt)
#pragma unroll
        for (int tt = 0; tt < 2; ++tt) acc[tt][nt] = mfma16(A[nt], B[tt], acc[tt][nt]);
      {
        u32x4 u = __builtin_bit_cast(u32x4, nh ? B[1] : B[0]);
#pragma unroll
        for (int j = 0; j < 4; ++j) { s1 = dot2(u[j], 0x3F803F80u, s1); s2 = dot2(u[j], u[j], s2); }
      }
      cs = mfma16(A[0], ones, cs);
    }
  };
  pipe_steps<0, 8, K1_NS, 4>(issue, compute);

  float* st = (float*)(smem + K1_STAGE * K1_NS);
  float* csp = (float*)(smem + K1_STAGE * K1_NS + 1024);
  s1 = xsum(s1); s2 = xsum(s2);
  if (g == 0) {
    float mean = s1 * (1.f / 512.f);
    float var = fmaxf(s2 * (1.f / 512.f) - mean * mean, 0.f);
    float rstd = rsqrtf(var + LN_EPS);
    *(f32x2*)(st + (tg * 32 + nh * 16 + i) * 2) = f32x2{mean, rstd};
  }
  if (i == 0 && tg < 3) *(f32x4*)(csp + un[0] * 16 + 4 * g) = cs;
  bar();
  f32x2 ms[2];
#pragma unroll
  for (int tt = 0; tt < 2; ++tt) ms[tt] = *(const f32x2*)(st + (tg * 32 + tt * 16 + i) * 2);
#pragma unroll
  for (int nt = 0; nt < 3; ++nt) {
    f32x4 c = *(const f32x4*)(csp + un[nt] * 16 + 4 * g);
    int n0 = cb * 96 + un[nt] * 16;
    char* dst = qkv + (size_t)(n0 >> 4) * (2048 * 32) + g * 8;
#pragma unroll
    for (int tt = 0; tt < 2; ++tt) {
      f32x4 y = (acc[tt][nt] - ms[tt][0] * c) * ms[tt][1];
      u32x2 o = {pack2(y[0], y[1]), pack2(y[2], y[3])};
      st8<(WT & 1) != 0>(dst + (size_t)(tb * 128 + tg * 32 + tt * 16 + i) * 32, o);
    }
  }
  TL1(0);
}

constexpr int K2_LDS = 96 * 1024;
__global__ __launch_bounds__(512) void k2_attn(const char* __restrict__ qkv, char* __restrict__ o,
    const char* __restrict__ wo, const char* __restrict__ w1, const char* __restrict__ w3, const char* __restrict__ w2) {
  extern __shared__ __attribute__((aligned(16))) char smem[];
  TL0;
  const int tid = threadIdx.x, lane = tid & 63, wv = uni(tid >> 6);
  const int g = lane >> 4, i = lane & 15, q4 = (lane >> 2) & 3, p4 = lane & 3;
  const int b = blockIdx.x;
  const int hp = (b & 7) * 2 + ((b >> 3) & 1), qb = b >> 4;
  const int q0 = qb * 128;
  const unsigned lds0 = (unsigned)(size_t)(LAS char*)smem;
  const size_t HS = 2048 * 32;
  {
    const int r = wv >> 1;
    const char* src = qkv + (size_t)((1 + (r >> 1)) * 32 + hp * 2 + (r & 1)) * HS;
#pragma unroll
    for (int j = 0; j < 6; ++j) {
      int ch = ((wv & 1) * 6 + j) * 64 + lane;
      int key = q0 - 256 + (ch >> 1);
      key = key < 0 ? 0 : key;
      dma16(src + (size_t)key * 32 + (ch & 1) * 16, lds0 + r * 12288 + ((wv & 1) * 6 + j) * 1024);
    }
  }
  const int qs = q0 + wv * 16;
  bf16x8 Q[2];
  gld16(Q[0], (unsigned)(i * 32 + (g & 1) * 16), qkv + (size_t)(hp * 2) * HS + (size_t)qs * 32);
  gld16(Q[1], (unsigned)(i * 32 + (g & 1) * 16), qkv + (size_t)(hp * 2 + 1) * HS + (size_t)qs * 32);
  {
    const int c0 = b * 8 + wv;
    const unsigned sc = lds0 + 49152 + wv * 1024;
    const char* p0 = c0 < 512 ? wo + (size_t)c0 * 1024 : w1 + (size_t)(c0 - 512) * 1024;
    const char* p1 = c0 < 256 ? w1 + (size_t)(c0 + 1536) * 1024 : w3 + (size_t)(c0 - 256) * 1024;
    const char* p2 = w2 + (size_t)(c0 < 1792 ? c0 : c0 - 1792) * 1024;
    dma16s(lane * 16, p0, sc); dma16s(lane * 16, p1, sc); dma16s(lane * 16, p2, sc);
  }
  asm volatile("s_waitcnt vmcnt(3)\n\ts_barrier" : "+v"(Q[0]), "+v"(Q[1]) : : "memory");
  if (g >= 2) { Q[0] = bf16x8{0, 0, 0, 0, 0, 0, 0, 0}; Q[1] = bf16x8{0, 0, 0, 0, 0, 0, 0, 0}; }
  const float c = 0.25f * 1.4426950408889634f;
  const float NEG = -1e30f;
  const int lrow = wv * 16;
  u32x4 ov;
#pragma unroll
  for (int u = 0; u < 2; ++u) {
    const char* Kl = smem + u * 12288;
    const char* Vl = smem + 24576 + u * 12288;
    f32x4 s[17];
#pragma unroll
    for (int t = 0; t < 17; ++t) {
      bf16x8 A = lds128(Kl + (lrow + t * 16 + i) * 32 + (g & 1) * 16);
      s[t] = mfma16(A, Q[u], f32x4{0, 0, 0, 0});
    }
#pragma unroll
    for (int r = 0; r < 4; ++r) {
      s[0][r] = (4 * g + r > i) ? s[0][r] : NEG;
      s[16][r] = (4 * g + r <= i) ? s[16][r] : NEG;
    }
    if (qs < 256) {
      const int nneg = (256 - qs) >> 4;
#pragma unroll
      for (int t = 0; t < 16; ++t)
        if (t < nneg) s[t] = f32x4{NEG, NEG, NEG, NEG};
    }
    float m = s[16][0];
#pragma unroll
    for (int t = 0; t < 17; ++t)
#pragma unroll
      for (int r = 0; r < 4; ++r) m = fmaxf(m, s[t][r]);
    m = xmax(m);
    const float mc = m * c;
    float sum = 0.f;
    unsigned pk[17][2];
#pragma unroll
    for (int t = 0; t < 17; ++t) {
      float e0 = __builtin_amdgcn_exp2f(s[t][0] * c - mc), e1 = __builtin_amdgcn_exp2f(s[t][1] * c - mc);
      float e2 = __builtin_amdgcn_exp2f(s[t][2] * c - mc), e3 = __builtin_amdgcn_exp2f(s[t][3] * c - mc);
      sum += (e0 + e1) + (e2 + e3);
      pk[t][0] = pack2(e0, e1); pk[t][1] = pack2(e2, e3);
    }
    sum = xsum(sum);
    f32x4 oacc = f32x4{0, 0, 0, 0};
#pragma unroll
    for (int tp = 0; tp < 9; ++tp) {
      const int t0 = 2 * tp, t1 = (2 * tp + 1 < 17) ? 2 * tp + 1 : 16;
      bf16x8 A = cat(ldstr(Vl + (lrow + t0 * 16 + 4 * g + q4) * 32 + p4 * 8), ldstr(Vl + (lrow + t1 * 16 + 4 * g + q4) * 32 + p4 * 8));
      u32x4 pb = {pk[t0][0], pk[t0][1], (2 * tp + 1 < 17) ? pk[t1][0] : 0u, (2 * tp + 1 < 17) ? pk[t1][1] : 0u};
      oacc = mfma16(A, __builtin_bit_cast(bf16x8, pb), oacc);
    }
    const float inv = 1.f / sum;
    ov[2 * u] = pack2(oacc[0] * inv, oacc[1] * inv);
    ov[2 * u + 1] = pack2(oacc[2] * inv, oacc[3] * inv);
  }
  st16<(WT & 2) != 0>(o + ((size_t)(qs >> 4) * 16 + hp) * 1024 + lane * 16, ov);
  asm volatile("s_waitcnt vmcnt(0)" ::: "memory");
  TL1(1);
}

template <int N> DEV void wait_frag(bf16x8* B) {
  asm volatile("s_waitcnt vmcnt(%4)" : "+v"(B[0]), "+v"(B[1]), "+v"(B[2]), "+v"(B[3]) : "n"(N) : "memory");
}
template <int NKS, int R> struct GK { static constexpr int REGION = (R * 4096 > 16384) ? R * 4096 : 16384, LDS = 4 * REGION > 98304 ? 4 * REGION : 98304; };

template <int J, int NKS, int R, class Issue, class LdA>
DEV void gks_steps(Issue& issue, LdA& lda, bf16x8 (&B)[R][4], bf16x8 (&A)[2][4], f32x4 (&acc)[4][4]) {
  if constexpr (J < NKS) {
    if constexpr (J + 1 < NKS) {
      wait_frag<(Min<J + R - 1, NKS - 1>::v - (J + 1)) * 8>(B[(J + 1) % R]);
      lda(A[(J + 1) & 1], (J + 1) % R);
    }
#pragma unroll
    for (int nt = 0; nt < 4; ++nt)
#pragma unroll
      for (int tt = 0; tt < 4; ++tt) acc[tt][nt] = mfma16(A[J & 1][nt], B[J % R][tt], acc[tt][nt]);
    __builtin_amdgcn_sched_barrier(0);
    if constexpr (J + R < NKS) issue(J + R);
    gks_steps<J + 1, NKS, R>(issue, lda, B, A, acc);
  }
}

template <int NKS, int R, bool STATS>
__global__ __launch_bounds__(256) void k_gks(const char* __restrict__ a, const char* __restrict__ w,
                                             const char* __restrict__ resid, char* __restrict__ out,
                                             float* __restrict__ stats) {
  extern __shared__ __attribute__((aligned(16))) char smem[];
  TL0;
  const int tid = threadIdx.x, lane = tid & 63, kp = uni(tid >> 6);
  const int g = lane >> 4, i = lane & 15, q4 = (lane >> 2) & 3, p4 = lane & 3;
  const int b = blockIdx.x;
  const int cb = (b >> 3) & 7, tb = (b & 7) * 4 + (b >> 6);
  constexpr int KT = NKS * 4, REGION = GK<NKS, R>::REGION;
  const unsigned ldsw = (unsigned)(size_t)(LAS char*)smem + kp * REGION;
  const char* ga = a + ((size_t)(tb * 4) * KT + kp * NKS) * 1024;
  const char* gw = w + (size_t)(kp * NKS * 32) * 1024 + cb * 128;
  const unsigned va = lane * 16;
  const unsigned vw = (lane >> 3) * 1024 + ((((lane & 7) >> 1) ^ ((lane >> 4) & 3)) << 5) + (lane & 1) * 16;
  bf16x8 B[R][4], A[2][4], rs[2];
  const int rot = (NKS > R) ? ((tb ^ cb) & 1) * (NKS / 2) : 0;
  auto issue = [&](int j) {
    const int s = j < NKS / 2 ? j + rot : j - rot;
#pragma unroll
    for (int q = 0; q < 4; ++q) dma16s(vw, gw + (size_t)(s * 32 + q * 8) * 1024, ldsw + (j % R) * 4096 + q * 1024);
#pragma unroll
    for (int tt = 0; tt < 4; ++tt) gld16(B[j % R][tt], va, ga + (size_t)(tt * KT + s) * 1024);
  };
  const size_t ooff = (size_t)(tb * 64 + kp * 16) * 1024 + cb * 128;
  const unsigned vo = (lane >> 3) * 1024 + (lane & 7) * 16;
  gld16(rs[0], vo, resid + ooff);
  gld16(rs[1], vo, resid + ooff + 8192);
#pragma unroll
  for (int j = 0; j < R; ++j) issue(j);
  f32x4 acc[4][4];
#pragma unroll
  for (int t = 0; t < 4; ++t)
#pragma unroll
    for (int n = 0; n < 4; ++n) acc[t][n] = f32x4{0, 0, 0, 0};
  const int sw = (2 * g + (q4 >> 1)) & 3;
  const char* wr = smem + kp * REGION + (4 * g + q4) * 128 + p4 * 8;
  auto lda = [&](bf16x8* Af, int slot) {
#pragma unroll
    for (int nt = 0; nt < 4; ++nt) {
      const char* p = wr + slot * 4096 + ((nt ^ sw) << 5);
      Af[nt] = cat(ldstr(p), ldstr(p + 2048));
    }
  };
  wait_frag<(R - 1) * 8>(B[0]);
  asm volatile("" : "+v"(rs[0]), "+v"(rs[1]));
  lda(A[0], 0);
  gks_steps<0, NKS, R>(issue, lda, B, A, acc);
  {
    const int mo = kp * REGION + g * 256 + ((i ^ ((g >> 1) * 2)) << 4);
#pragma unroll
    for (int tt = 0; tt < 4; ++tt)
#pragma unroll
      for (int nt = 0; nt < 4; ++nt)
        *(LAS f32x4*)(LAS char*)(smem + (mo ^ (nt * 64)) + (tt * 4 + nt) * 1024) = acc[tt][nt];
  }
  bar();
  const int c = lane & 7;
#pragma unroll
  for (int it = 0; it < 2; ++it) {
    const int tok = (lane >> 3) + 8 * it;
    const char* rp = smem + (kp * 4 + (c >> 1)) * 1024 + (c & 1) * 512 + ((tok ^ (2 * c)) << 4);
    f32x4 v0 = f32x4{0, 0, 0, 0}, v1 = f32x4{0, 0, 0, 0};
#pragma unroll
    for (int p = 0; p < 4; ++p) {
      v0 += *(const LAS f32x4*)(LAS const char*)(rp + p * REGION);
      v1 += *(const LAS f32x4*)(LAS const char*)(rp + p * REGION + 256);
    }
    u32x4 r = __builtin_bit_cast(u32x4, rs[it]);
    u32x4 ov = {pack2(v0[0] + bflo(r[0]), v0[1] + bfhi(r[0])), pack2(v0[2] + bflo(r[1]), v0[3] + bfhi(r[1])),
                pack2(v1[0] + bflo(r[2]), v1[1] + bfhi(r[2])), pack2(v1[2] + bflo(r[3]), v1[3] + bfhi(r[3]))};
    st16<(WT & (STATS ? 4 : 16)) != 0>(out + ooff + it * 8192 + vo, ov);
    if constexpr (STATS) {
      float s1 = 0.f, s2 = 0.f;
#pragma unroll
      for (int e = 0; e < 4; ++e) { s1 = dot2(ov[e], 0x3F803F80u, s1); s2 = dot2(ov[e], ov[e], s2); }
#pragma unroll
      for (int m = 1; m < 8; m <<= 1) { s1 += __shfl_xor(s1, m); s2 += __shfl_xor(s2, m); }
      if (c == 0) *(f32x2*)(stats + ((size_t)(tb * 64 + kp * 16 + tok) * 8 + cb) * 2) = f32x2{s1, s2};
    }
  }
  TL1(STATS ? 2 : 4);
}

constexpr int K4_STAGE = 49152, K4_NS = 3;
constexpr int K4_LDS = K4_STAGE * K4_NS + 4 * 224 * 4;

template <int NT>
DEV void k4_body(char* smem, const char* gt, const char* g1, const char* g3, unsigned vt, unsigned vw,
                 unsigned long long wmask, unsigned ldsw, int tg, int lane, const float* __restrict__ stats,
                 char* __restrict__ hh, int tok0, int cb TLARG) {
  const int g = lane >> 4, i = lane & 15, q4 = (lane >> 2) & 3, p4 = lane & 3;
  f32x4 acc[2][2 * NT], csg = f32x4{0, 0, 0, 0}, csu = f32x4{0, 0, 0, 0};
#pragma unroll
  for (int a = 0; a < 2 * NT; ++a) { acc[0][a] = f32x4{0, 0, 0, 0}; acc[1][a] = f32x4{0, 0, 0, 0}; }
  const bf16x8 ones = {0x3F80, 0x3F80, 0x3F80, 0x3F80, 0x3F80, 0x3F80, 0x3F80, 0x3F80};
  const int toff0 = (tg * 32 + i) * 128 + ((g ^ (i & 7)) << 4);
  const int toff1 = (tg * 32 + i) * 128 + (((4 + g) ^ (i & 7)) << 4);
  const int odd = cb & 1;
  const int fsw = q4 | ((g & 1) << 2);
  int T[NT], wa[NT];
#pragma unroll
  for (int a = 0; a < NT; ++a) {
    T[a] = NT == 4 ? odd * 3 + a : (a < 2 ? (odd ? 1 : 4) + a : (odd ? 0 : 6));
    wa[a] = 16384 + (8 * g + q4) * 256 + ((T[a] ^ fsw) << 5) + p4 * 8;
  }
  const int rot = 0;
  auto dma = [&](int kt, int j) {
    const unsigned base = ldsw + (kt % K4_NS) * K4_STAGE;
    const int ke = (kt + rot) & 7;
    if (j < 2) dma16s(vt, gt + ke * 128 + j * 8192, base + j * 1024);
    else if (j < 4) dma16sm(vw, g1 + (size_t)(ke * 64 + (j - 2) * 4) * (F_ * 2), base + 16384 + (j - 2) * 1024, wmask);
    else dma16sm(vw, g3 + (size_t)(ke * 64 + (j - 4) * 4) * (F_ * 2), base + 32768 + (j - 4) * 1024, wmask);
  };
  auto ldB = [&](bf16x8* B, int kt, int ks) {
    const char* sb = smem + (kt % K4_NS) * K4_STAGE + (ks ? toff1 : toff0);
    B[0] = lds128(sb); B[1] = lds128(sb + 2048);
  };
  auto ldA = [&](int a, int kt, int ks) -> bf16x8 {
    const char* wp = smem + (kt % K4_NS) * K4_STAGE + wa[a < NT ? a : a - NT] + (a < NT ? 0 : 16384) + ks * 8192;
    return cat(ldstr(wp), ldstr(wp + 1024));
  };
  auto mma = [&](int a, bf16x8 A, const bf16x8* B) {
    acc[0][a] = mfma16(A, B[0], acc[0][a]);
    acc[1][a] = mfma16(A, B[1], acc[1][a]);
  };
  auto csum = [&](const bf16x8* A) {
    if (tg == 0) { csg = mfma16(A[0], ones, csg); csu = mfma16(A[NT], ones, csu); }
    else if (tg == 1) { csg = mfma16(A[1], ones, csg); csu = mfma16(A[NT + 1], ones, csu); }
    else if (tg == 2) { csg = mfma16(A[2], ones, csg); csu = mfma16(A[NT + 2], ones, csu); }
    else if (NT == 4) { csg = mfma16(A[NT - 1], ones, csg); csu = mfma16(A[2 * NT - 1], ones, csu); }
  };
  bf16x8 A0[2 * NT], A1[2 * NT], B0[2], B1[2];
  f32x4 sa[2];
#pragma unroll
  for (int j = 0; j < 6; ++j) dma(0, j);
#pragma unroll
  for (int j = 0; j < 6; ++j) dma(1, j);
  wait_bar<6>();
  ldB(B0, 0, 0);
#pragma unroll
  for (int a = 0; a < 2 * NT; ++a) A0[a] = ldA(a, 0, 0);
#pragma unroll
  for (int kt = 0; kt < 8; ++kt) {
    ldB(B1, kt, 1);
#pragma unroll
    for (int a = 0; a < 2 * NT; ++a) {
      if (a < NT) { A1[2 * a] = ldA(2 * a, kt, 1); A1[2 * a + 1] = ldA(2 * a + 1, kt, 1); }
      mma(a, A0[a], B0);
      if (kt == 0 && a >= 2 * NT - 6) dma(2, a - (2 * NT - 6));
      __builtin_amdgcn_sched_barrier(0);
    }
    csum(A0);
    if (kt < 6) wait_bar<6>();
    else if (kt == 6) wait_bar<0>();
    if (kt < 7) ldB(B0, kt + 1, 0);
    if (kt == 6) {
#pragma unroll
      for (int tt = 0; tt < 2; ++tt) sa[tt] = *(const f32x4*)(stats + ((size_t)(tok0 + tg * 32 + tt * 16 + i) * 8 + 2 * g) * 2);
    }
    if (kt == 7) csum(A1);
#pragma unroll
    for (int a = 0; a < 2 * NT; ++a) {
      if (kt < 7 && a < NT) { A0[2 * a] = ldA(2 * a, kt + 1, 0); A0[2 * a + 1] = ldA(2 * a + 1, kt + 1, 0); }
      mma(a, A1[a], B1);
      if (kt + 3 < 8 && a >= 2 * NT - 6) dma(kt + 3, a - (2 * NT - 6));
      __builtin_amdgcn_sched_barrier(0);
    }
    if (kt < 7) csum(A1);
  }
  float* csp = (float*)(smem + K4_STAGE * K4_NS);
  if (i == 0 && tg < NT) {
    const int tt_ = NT == 4 ? odd * 3 + tg : (tg < 2 ? (odd ? 1 : 4) + tg : (odd ? 0 : 6));
    *(f32x4*)(csp + tt_ * 16 + 4 * g) = csg;
    *(f32x4*)(csp + (7 + tt_) * 16 + 4 * g) = csu;
  }
  float rs[2], mr[2];
#pragma unroll
  for (int tt = 0; tt < 2; ++tt) {
    float s1 = sa[tt][0] + sa[tt][2], s2 = sa[tt][1] + sa[tt][3];
    s1 = xsum(s1); s2 = xsum(s2);
    float mean = s1 * (1.f / 512.f);
    float var = fmaxf(s2 * (1.f / 512.f) - mean * mean, 0.f);
    rs[tt] = rsqrtf(var + LN_EPS); mr[tt] = mean * rs[tt];
  }
  bar();
  unsigned pk[2][NT][2];
#pragma unroll
  for (int tt = 0; tt < 2; ++tt) {
    char* dst = hh + ((size_t)((tok0 >> 4) + tg * 2 + tt) * 56) * 1024 + lane * 16;
#pragma unroll
    for (int a = 0; a < NT; ++a) {
      f32x4 cg = *(const f32x4*)(csp + T[a] * 16 + 4 * g);
      f32x4 cu = *(const f32x4*)(csp + (7 + T[a]) * 16 + 4 * g);
      float r[4];
#pragma unroll
      for (int e = 0; e < 4; ++e) {
        float ga = acc[tt][a][e] * rs[tt] - mr[tt] * cg[e];
        float up = acc[tt][NT + a][e] * rs[tt] - mr[tt] * cu[e];
        float sg = __builtin_amdgcn_rcpf(1.f + __builtin_amdgcn_exp2f(-1.4426950408889634f * ga));
        r[e] = ga * sg * up;
      }
      pk[tt][a][0] = pack2(r[0], r[1]); pk[tt][a][1] = pack2(r[2], r[3]);
      const int G = cb * 7 + T[a];
      if (a & 1) {
        st16<(WT & 8) != 0>(dst + (size_t)(G >> 1) * 1024, u32x4{pk[tt][a - 1][0], pk[tt][a - 1][1], pk[tt][a][0], pk[tt][a][1]});
      } else if (a == NT - 1) {
        st8<(WT & 8) != 0>(dst + (size_t)(G >> 1) * 1024 + (G & 1) * 8, u32x2{pk[tt][a][0], pk[tt][a][1]});
      }
    }
  }
  TL1(3);
}

__global__ __launch_bounds__(512) void k4_mlp(const char* __restrict__ h, const char* __restrict__ w1,
                                              const char* __restrict__ w3, const float* __restrict__ stats,
                                              char* __restrict__ hh) {
  extern __shared__ __attribute__((aligned(16))) char smem[];
  TL0;
  const int tid = threadIdx.x, lane = tid & 63, wv = uni(tid >> 6);
  const int tg = wv & 3, half = wv >> 2;
  const int b = blockIdx.x, xcd = b & 7, bi = b >> 3;
  const int cb = (xcd & 3) * 4 + (bi & 3), tb = (xcd >> 2) * 8 + (bi >> 2);
  const unsigned lds0 = (unsigned)(size_t)(LAS char*)smem;
  const char* gt = h + (size_t)(tb * 128 + wv * 16) * 1024;
  const unsigned vt = (lane >> 3) * 1024 + (((lane & 7) ^ (lane >> 3)) << 4);
  const char* g1 = w1 + (size_t)(wv * 8) * (F_ * 2) + cb * 224;
  const char* g3 = w3 + (size_t)(wv * 8) * (F_ * 2) + cb * 224;
  const int unit = ((lane >> 1) & 7) ^ ((lane >> 4) | ((wv & 1) << 2));
  const unsigned vw = (lane >> 4) * (F_ * 2) + unit * 32 + (lane & 1) * 16;
  const unsigned long long wmask = __builtin_amdgcn_ballot_w64(unit < 7);
  const unsigned ldsw = lds0 + wv * 2048;
  if (half == 0) k4_body<4>(smem, gt, g1, g3, vt, vw, wmask, ldsw, tg, lane, stats, hh, tb * 128, cb TLPASS);
  else k4_body<3>(smem, gt, g1, g3, vt, vw, wmask, ldsw, tg, lane, stats, hh, tb * 128, cb TLPASS);
}

extern "C" void kernel_launch(void* const* d_in, const int* in_sizes, int n_in, void* d_out, int out_size,
                              void* d_ws, size_t ws_size, hipStream_t stream) {
  const char* x = (const char*)d_in[0];
  const char* wqkv = (const char*)d_in[1];
  const char* wo = (const char*)d_in[2];
  const char* w1 = (const char*)d_in[4];
  const char* w3 = (const char*)d_in[5];
  const char* w2 = (const char*)d_in[6];
  char* ws = (char*)d_ws;
  char* qkv = ws;
  char* o = ws + (6u << 20);
  char* h = ws + (8u << 20);
  float* stats = (float*)(ws + (10u << 20));
  char* hh = ws + (11u << 20);
  (void)hipFuncSetAttribute((const void*)k1_qkv, hipFuncAttributeMaxDynamicSharedMemorySize, K1_LDS);
  (void)hipFuncSetAttribute((const void*)k2_attn, hipFuncAttributeMaxDynamicSharedMemorySize, K2_LDS);
  (void)hipFuncSetAttribute((const void*)k_gks<4, 4, true>, hipFuncAttributeMaxDynamicSharedMemorySize, GK<4, 4>::LDS);
  (void)hipFuncSetAttribute((const void*)k4_mlp, hipFuncAttributeMaxDynamicSharedMemorySize, K4_LDS);
  (void)hipFuncSetAttribute((const void*)k_gks<14, 7, false>, hipFuncAttributeMaxDynamicSharedMemorySize, GK<14, 7>::LDS);
  k1_qkv<<<256, 512, K1_LDS, stream>>>(x, wqkv, qkv);
  k2_attn<<<256, 512, K2_LDS, stream>>>(qkv, o, wo, w1, w3, w2);
  k_gks<4, 4, true><<<256, 256, GK<4, 4>::LDS, stream>>>(o, wo, x, h, stats);
  k4_mlp<<<256, 512, K4_LDS, stream>>>(h, w1, w3, stats, hh);
  k_gks<14, 7, false><<<256, 256, GK<14, 7>::LDS, stream>>>(hh, w2, h, (char*)d_out, nullptr);
}
```

```cpp
#include <hip/hip_runtime.h>
#include <hip/hip_bf16.h>
#include <stdint.h>

typedef short bf16x8 __attribute__((ext_vector_type(8)));
typedef short s16x4 __attribute__((ext_vector_type(4)));
typedef float f32x4 __attribute__((ext_vector_type(4)));
typedef float f32x2 __attribute__((ext_vector_type(2)));
typedef unsigned u32x4 __attribute__((ext_vector_type(4)));
typedef unsigned u32x2 __attribute__((ext_vector_type(2)));
typedef __bf16 bf16x2 __attribute__((ext_vector_type(2)));
#define LAS __attribute__((address_space(3)))
#define DEV __device__ __forceinline__

constexpr int S_ = 2048, D_ = 512, H_ = 32, F_ = 1792;
constexpr float LN_EPS = 1e-5f;

DEV f32x4 mfma16(bf16x8 a, bf16x8 b, f32x4 c) { return __builtin_amdgcn_mfma_f32_16x16x32_bf16(a, b, c, 0, 0, 0); }
DEV unsigned pack2(float a, float b) {
  f32x2 v = {a, b};
  return __builtin_bit_cast(unsigned, __builtin_convertvector(v, bf16x2));
}
DEV float bflo(unsigned u) { return __builtin_bit_cast(float, u << 16); }
DEV float bfhi(unsigned u) { return __builtin_bit_cast(float, u & 0xffff0000u); }
DEV float dot2(unsigned a, unsigned b, float c) {
  return __builtin_amdgcn_fdot2_f32_bf16(__builtin_bit_cast(bf16x2, a), __builtin_bit_cast(bf16x2, b), c, false);
}
DEV void dma16(const char* gptr, unsigned ldsbase) {
  unsigned sv;
  asm volatile("s_mov_b32 %0, m0\n\ts_mov_b32 m0, %2\n\ts_nop 0\n\tglobal_load_lds_dwordx4 %1, off\n\ts_mov_b32 m0, %0"
               : "=&s"(sv) : "v"(gptr), "s"(ldsbase) : "memory");
}
DEV void dma16s(unsigned voff, const char* sbase, unsigned ldsbase) {
  unsigned sv;
  asm volatile("s_mov_b32 %0, m0\n\ts_mov_b32 m0, %3\n\ts_nop 0\n\tglobal_load_lds_dwordx4 %1, %2\n\ts_mov_b32 m0, %0"
               : "=&s"(sv) : "v"(voff), "s"(sbase), "s"(ldsbase) : "memory");
}
DEV void dma16sm(unsigned voff, const char* sbase, unsigned ldsbase, unsigned long long mask) {
  unsigned sv;
  asm volatile("s_mov_b32 %0, m0\n\ts_mov_b32 m0, %3\n\ts_mov_b64 exec, %4\n\tglobal_load_lds_dwordx4 %1, %2\n\ts_mov_b64 exec, -1\n\ts_mov_b32 m0, %0"
               : "=&s"(sv) : "v"(voff), "s"(sbase), "s"(ldsbase), "s"(mask) : "memory");
}
DEV void gld16(bf16x8& d, unsigned voff, const char* sbase) {
  asm volatile("global_load_dwordx4 %0, %1, %2" : "=v"(d) : "v"(voff), "s"(sbase) : "memory");
}
template <int N> DEV void wait_bar() { asm volatile("s_waitcnt vmcnt(%0) lgkmcnt(0)\n\ts_barrier" ::"n"(N) : "memory"); }
DEV void bar() { asm volatile("s_waitcnt lgkmcnt(0)\n\ts_barrier" ::: "memory"); }
DEV bf16x8 lds128(const char* p) { return *(const LAS bf16x8*)(LAS const char*)p; }
DEV s16x4 ldstr(const char* p) { return __builtin_amdgcn_ds_read_tr16_b64_v4i16((LAS s16x4*)p); }
DEV bf16x8 cat(s16x4 a, s16x4 b) { return __builtin_shufflevector(a, b, 0, 1, 2, 3, 4, 5, 6, 7); }
DEV float xsum(float v) { v += __shfl_xor(v, 16); v += __shfl_xor(v, 32); return v; }
DEV float xmax(float v) { v = fmaxf(v, __shfl_xor(v, 16)); v = fmaxf(v, __shfl_xor(v, 32)); return v; }
DEV unsigned uni(unsigned v) { return __builtin_amdgcn_readfirstlane(v); }
#define WT 31
template <bool W> DEV void st16(char* p, u32x4 v) {
  if constexpr (W) asm volatile("global_store_dwordx4 %0, %1, off sc0 sc1\n\ts_nop 1" ::"v"(p), "v"(v) : "memory");
  else *(u32x4*)p = v;
}
template <bool W> DEV void st8(char* p, u32x2 v) {
  if constexpr (W) asm volatile("global_store_dwordx2 %0, %1, off sc0 sc1" ::"v"(p), "v"(v) : "memory");
  else *(u32x2*)p = v;
}

#define TL0
#define TL1(k)
#define TLARG
#define TLPASS
#define STAMP(n)
#define STDECL
#define STOUT
template <int A, int B> struct Min { static constexpr int v = A < B ? A : B; };

template <int KT, int NK, int NS, int P, class Issue, class Compute>
DEV void pipe_steps(Issue& issue, Compute& compute) {
  if constexpr (KT < NK) {
    wait_bar<(Min<KT + NS - 1, NK>::v - KT - 1) * P>();
    if constexpr (KT + NS - 1 < NK) issue((KT + NS - 1) % NS);
    compute(KT % NS, KT);
    pipe_steps<KT + 1, NK, NS, P>(issue, compute);
  }
}

constexpr int K1_STAGE = 16384 + 12288, K1_NS = 5;
constexpr int K1_LDS = K1_STAGE * K1_NS + 1024 + 1536;

__global__ __launch_bounds__(512) void k1_qkv(const char* __restrict__ x, const char* __restrict__ w,
                                              char* __restrict__ qkv) {
  extern __shared__ __attribute__((aligned(16))) char smem[];
  TL0;
  const int tid = threadIdx.x, lane = tid & 63, wv = uni(tid >> 6);
  const int g = lane >> 4, i = lane & 15, q4 = (lane >> 2) & 3, p4 = lane & 3;
  const int tg = wv & 3, nh = wv >> 2;
  const int b = blockIdx.x, xcd = b & 7, bi = b >> 3;
  const int cb = (xcd & 1) * 8 + (bi & 7), tb = (xcd >> 1) * 4 + (bi >> 3);
  const unsigned lds0 = (unsigned)(size_t)(LAS char*)smem;

  const char* pt;
  {
    int row = tb * 128 + wv * 16 + (lane >> 3);
    int c = (lane & 7) ^ (lane >> 3);
    pt = x + (size_t)row * 1024 + c * 16;
  }
  const char *pw0, *pw1;
  {
    int qq = wv * 64 + lane;
    int k = qq / 12, c = (qq % 12) ^ (((k >> 3) & 1) << 1);
    pw0 = w + (size_t)k * 3072 + cb * 192 + c * 16;
    qq = 512 + wv * 32 + (lane & 31);
    k = qq / 12; c = (qq % 12) ^ (((k >> 3) & 1) << 1);
    pw1 = w + (size_t)k * 3072 + cb * 192 + c * 16;
  }
  int kk = bi & 7;
  auto issue = [&](int slot) {
    unsigned base = lds0 + slot * K1_STAGE;
    dma16(pt + kk * 128, base + wv * 2048);
    dma16(pt + kk * 128 + 8192, base + wv * 2048 + 1024);
    dma16(pw0 + (size_t)kk * (64 * 3072), base + 16384 + wv * 1024);
    if (lane < 32) dma16(pw1 + (size_t)kk * (64 * 3072), base + 16384 + 8192 + wv * 512);
    kk = (kk + 1) & 7;
  };
#pragma unroll
  for (int s = 0; s < K1_NS - 1; ++s) issue(s);

  f32x4 acc[2][3], cs = f32x4{0, 0, 0, 0};
#pragma unroll
  for (int a = 0; a < 3; ++a) { acc[0][a] = f32x4{0, 0, 0, 0}; acc[1][a] = f32x4{0, 0, 0, 0}; }
  float s1 = 0.f, s2 = 0.f;
  const bf16x8 ones = {0x3F80, 0x3F80, 0x3F80, 0x3F80, 0x3F80, 0x3F80, 0x3F80, 0x3F80};
  const int toff0 = (tg * 32 + i) * 128 + ((g ^ (i & 7)) << 4);
  const int toff1 = (tg * 32 + i) * 128 + (((4 + g) ^ (i & 7)) << 4);
  int woff[3], un[3];
#pragma unroll
  for (int nt = 0; nt < 3; ++nt) {
    int a = nt + tg; a = a >= 3 ? a - 3 : a; a = a >= 3 ? a - 3 : a;
    un[nt] = nh * 3 + a;
    woff[nt] = 16384 + (8 * g + q4) * 192 + ((un[nt] ^ (g & 1)) << 5) + p4 * 8;
  }

  auto compute = [&](int slot, int kt) {
    const char* sb = smem + slot * K1_STAGE;
#pragma unroll
    for (int ks = 0; ks < 2; ++ks) {
      bf16x8 B[2], A[3];
#pragma unroll
      for (int tt = 0; tt < 2; ++tt) B[tt] = lds128(sb + (ks ? toff1 : toff0) + tt * 2048);
#pragma unroll
      for (int nt = 0; nt < 3; ++nt)
        A[nt] = cat(ldstr(sb + woff[nt] + ks * 6144), ldstr(sb + woff[nt] + ks * 6144 + 768));
#pragma unroll
      for (int nt = 0; nt < 3; ++nt)
#pragma unroll
        for (int tt = 0; tt < 2; ++tt) acc[tt][nt] = mfma16(A[nt], B[tt], acc[tt][nt]);
      {
        u32x4 u = __builtin_bit_cast(u32x4, nh ? B[1] : B[0]);
#pragma unroll
        for (int j = 0; j < 4; ++j) { s1 = dot2(u[j], 0x3F803F80u, s1); s2 = dot2(u[j], u[j], s2); }
      }
      cs = mfma16(A[0], ones, cs);
    }
  };
  pipe_steps<0, 8, K1_NS, 4>(issue, compute);

  float* st = (float*)(smem + K1_STAGE * K1_NS);
  float* csp = (float*)(smem + K1_STAGE * K1_NS + 1024);
  s1 = xsum(s1); s2 = xsum(s2);
  if (g == 0) {
    float mean = s1 * (1.f / 512.f);
    float var = fmaxf(s2 * (1.f / 512.f) - mean * mean, 0.f);
    float rstd = rsqrtf(var + LN_EPS);
    *(f32x2*)(st + (tg * 32 + nh * 16 + i) * 2) = f32x2{mean, rstd};
  }
  if (i == 0 && tg < 3) *(f32x4*)(csp + un[0] * 16 + 4 * g) = cs;
  bar();
  f32x2 ms[2];
#pragma unroll
  for (int tt = 0; tt < 2; ++tt) ms[tt] = *(const f32x2*)(st + (tg * 32 + tt * 16 + i) * 2);
#pragma unroll
  for (int nt = 0; nt < 3; ++nt) {
    f32x4 c = *(const f32x4*)(csp + un[nt] * 16 + 4 * g);
    int n0 = cb * 96 + un[nt] * 16;
    char* dst = qkv + (size_t)(n0 >> 4) * (2048 * 32) + g * 8;
#pragma unroll
    for (int tt = 0; tt < 2; ++tt) {
      f32x4 y = (acc[tt][nt] - ms[tt][0] * c) * ms[tt][1];
      u32x2 o = {pack2(y[0], y[1]), pack2(y[2], y[3])};
      st8<(WT & 1) != 0>(dst + (size_t)(tb * 128 + tg * 32 + tt * 16 + i) * 32, o);
    }
  }
  TL1(0);
}

constexpr int K2_LDS = 96 * 1024;
__global__ __launch_bounds__(512) void k2_attn(const char* __restrict__ qkv, char* __restrict__ o,
    const char* __restrict__ wo, const char* __restrict__ w1, const char* __restrict__ w3, const char* __restrict__ w2) {
  extern __shared__ __attribute__((aligned(16))) char smem[];
  TL0;
  const int tid = threadIdx.x, lane = tid & 63, wv = uni(tid >> 6);
  const int g = lane >> 4, i = lane & 15, q4 = (lane >> 2) & 3, p4 = lane & 3;
  const int b = blockIdx.x;
  const int hp = (b & 7) * 2 + ((b >> 3) & 1), qb = b >> 4;
  const int q0 = qb * 128;
  const unsigned lds0 = (unsigned)(size_t)(LAS char*)smem;
  const size_t HS = 2048 * 32;
  {
    const int r = wv >> 1;
    const char* src = qkv + (size_t)((1 + (r >> 1)) * 32 + hp * 2 + (r & 1)) * HS;
#pragma unroll
    for (int j = 0; j < 6; ++j) {
      int ch = ((wv & 1) * 6 + j) * 64 + lane;
      int key = q0 - 256 + (ch >> 1);
      key = key < 0 ? 0 : key;
      dma16(src + (size_t)key * 32 + (ch & 1) * 16, lds0 + r * 12288 + ((wv & 1) * 6 + j) * 1024);
    }
  }
  const int qs = q0 + wv * 16;
  bf16x8 Q[2];
  gld16(Q[0], (unsigned)(i * 32 + (g & 1) * 16), qkv + (size_t)(hp * 2) * HS + (size_t)qs * 32);
  gld16(Q[1], (unsigned)(i * 32 + (g & 1) * 16), qkv + (size_t)(hp * 2 + 1) * HS + (size_t)qs * 32);
  {
    const int c0 = b * 8 + wv;
    const unsigned sc = lds0 + 49152 + wv * 1024;
    const char* p0 = c0 < 512 ? wo + (size_t)c0 * 1024 : w1 + (size_t)(c0 - 512) * 1024;
    const char* p1 = c0 < 256 ? w1 + (size_t)(c0 + 1536) * 1024 : w3 + (size_t)(c0 - 256) * 1024;
    const char* p2 = w2 + (size_t)(c0 < 1792 ? c0 : c0 - 1792) * 1024;
    dma16s(lane * 16, p0, sc); dma16s(lane * 16, p1, sc); dma16s(lane * 16, p2, sc);
  }
  asm volatile("s_waitcnt vmcnt(3)\n\ts_barrier" : "+v"(Q[0]), "+v"(Q[1]) : : "memory");
  if (g >= 2) { Q[0] = bf16x8{0, 0, 0, 0, 0, 0, 0, 0}; Q[1] = bf16x8{0, 0, 0, 0, 0, 0, 0, 0}; }
  const float c = 0.25f * 1.4426950408889634f;
  const float NEG = -1e30f;
  const int lrow = wv * 16;
  f32x4 cin0, cin16;
#pragma unroll
  for (int r = 0; r < 4; ++r) { cin0[r] = (4 * g + r > i) ? 0.f : NEG; cin16[r] = (4 * g + r <= i) ? 0.f : NEG; }
  const bf16x8 ones = {0x3F80, 0x3F80, 0x3F80, 0x3F80, 0x3F80, 0x3F80, 0x3F80, 0x3F80};
  u32x4 ov;
#pragma unroll
  for (int u = 0; u < 2; ++u) {
    const char* Kl = smem + u * 12288;
    const char* Vl = smem + 24576 + u * 12288;
    f32x4 s[17];
#pragma unroll
    for (int t = 0; t < 17; ++t) {
      bf16x8 A = lds128(Kl + (lrow + t * 16 + i) * 32 + (g & 1) * 16);
      s[t] = mfma16(A, Q[u], t == 0 ? cin0 : t == 16 ? cin16 : f32x4{0, 0, 0, 0});
    }
    if (qs < 256) {
      asm volatile("" ::: "memory");
      const int nneg = (256 - qs) >> 4;
#pragma unroll
      for (int t = 0; t < 16; ++t)
        if (t < nneg) s[t] = f32x4{NEG, NEG, NEG, NEG};
    }
    float m = s[16][0];
#pragma unroll
    for (int t = 0; t < 17; ++t)
#pragma unroll
      for (int r = 0; r < 4; ++r) m = fmaxf(m, s[t][r]);
    m = xmax(m);
    const f32x2 c2 = {c, c}, m2 = {-m * c, -m * c};
    unsigned pk[17][2];
#pragma unroll
    for (int t = 0; t < 17; ++t) {
      f32x2 a0 = __builtin_elementwise_fma(f32x2{s[t][0], s[t][1]}, c2, m2);
      f32x2 a1 = __builtin_elementwise_fma(f32x2{s[t][2], s[t][3]}, c2, m2);
      pk[t][0] = pack2(__builtin_amdgcn_exp2f(a0[0]), __builtin_amdgcn_exp2f(a0[1]));
      pk[t][1] = pack2(__builtin_amdgcn_exp2f(a1[0]), __builtin_amdgcn_exp2f(a1[1]));
    }
    f32x4 oacc = f32x4{0, 0, 0, 0}, lsum = f32x4{0, 0, 0, 0};
#pragma unroll
    for (int tp = 0; tp < 9; ++tp) {
      const int t0 = 2 * tp, t1 = (2 * tp + 1 < 17) ? 2 * tp + 1 : 16;
      bf16x8 A = cat(ldstr(Vl + (lrow + t0 * 16 + 4 * g + q4) * 32 + p4 * 8), ldstr(Vl + (lrow + t1 * 16 + 4 * g + q4) * 32 + p4 * 8));
      u32x4 pb = {pk[t0][0], pk[t0][1], (2 * tp + 1 < 17) ? pk[t1][0] : 0u, (2 * tp + 1 < 17) ? pk[t1][1] : 0u};
      oacc = mfma16(A, __builtin_bit_cast(bf16x8, pb), oacc);
      lsum = mfma16(ones, __builtin_bit_cast(bf16x8, pb), lsum);
    }
    const float inv = __builtin_amdgcn_rcpf(lsum[0]);
    ov[2 * u] = pack2(oacc[0] * inv, oacc[1] * inv);
    ov[2 * u + 1] = pack2(oacc[2] * inv, oacc[3] * inv);
  }
  st16<(WT & 2) != 0>(o + ((size_t)(qs >> 4) * 16 + hp) * 1024 + lane * 16, ov);
  asm volatile("s_waitcnt vmcnt(0)" ::: "memory");
  TL1(1);
}

template <int N> DEV void wait_frag(bf16x8* B) {
  asm volatile("s_waitcnt vmcnt(%4)" : "+v"(B[0]), "+v"(B[1]), "+v"(B[2]), "+v"(B[3]) : "n"(N) : "memory");
}
template <int NKS, int R> struct GK { static constexpr int REGION = (R * 4096 > 16384) ? R * 4096 : 16384, LDS = 4 * REGION > 98304 ? 4 * REGION : 98304; };

template <int J, int NKS, int R, class Issue, class LdA>
DEV void gks_steps(Issue& issue, LdA& lda, bf16x8 (&B)[R][4], bf16x8 (&A)[2][4], f32x4 (&acc)[4][4]) {
  if constexpr (J < NKS) {
    if constexpr (J + 1 < NKS) {
      wait_frag<(Min<J + R - 1, NKS - 1>::v - (J + 1)) * 8>(B[(J + 1) % R]);
      lda(A[(J + 1) & 1], (J + 1) % R);
    }
#pragma unroll
    for (int nt = 0; nt < 4; ++nt)
#pragma unroll
      for (int tt = 0; tt < 4; ++tt) acc[tt][nt] = mfma16(A[J & 1][nt], B[J % R][tt], acc[tt][nt]);
    __builtin_amdgcn_sched_barrier(0);
    if constexpr (J + R < NKS) issue(J + R);
    gks_steps<J + 1, NKS, R>(issue, lda, B, A, acc);
  }
}

template <int NKS, int R, bool STATS>
__global__ __launch_bounds__(256) void k_gks(const char* __restrict__ a, const char* __restrict__ w,
                                             const char* __restrict__ resid, char* __restrict__ out,
                                             float* __restrict__ stats) {
  extern __shared__ __attribute__((aligned(16))) char smem[];
  TL0;
  const int tid = threadIdx.x, lane = tid & 63, kp = uni(tid >> 6);
  const int g = lane >> 4, i = lane & 15, q4 = (lane >> 2) & 3, p4 = lane & 3;
  const int b = blockIdx.x;
  const int cb = (b >> 3) & 7, tb = (b & 7) * 4 + (b >> 6);
  constexpr int KT = NKS * 4, REGION = GK<NKS, R>::REGION;
  const unsigned ldsw = (unsigned)(size_t)(LAS char*)smem + kp * REGION;
  const char* ga = a + ((size_t)(tb * 4) * KT + kp * NKS) * 1024;
  const char* gw = w + (size_t)(kp * NKS * 32) * 1024 + cb * 128;
  const unsigned va = lane * 16;
  const unsigned vw = (lane >> 3) * 1024 + ((((lane & 7) >> 1) ^ ((lane >> 4) & 3)) << 5) + (lane & 1) * 16;
  bf16x8 B[R][4], A[2][4], rs[2];
  const int rot = (NKS > R) ? ((tb ^ cb) & 1) * (NKS / 2) : 0;
  auto issue = [&](int j) {
    const int s = j < NKS / 2 ? j + rot : j - rot;
#pragma unroll
    for (int q = 0; q < 4; ++q) dma16s(vw, gw + (size_t)(s * 32 + q * 8) * 1024, ldsw + (j % R) * 4096 + q * 1024);
#pragma unroll
    for (int tt = 0; tt < 4; ++tt) gld16(B[j % R][tt], va, ga + (size_t)(tt * KT + s) * 1024);
  };
  const size_t ooff = (size_t)(tb * 64 + kp * 16) * 1024 + cb * 128;
  const unsigned vo = (lane >> 3) * 1024 + (lane & 7) * 16;
  gld16(rs[0], vo, resid + ooff);
  gld16(rs[1], vo, resid + ooff + 8192);
#pragma unroll
  for (int j = 0; j < R; ++j) issue(j);
  f32x4 acc[4][4];
#pragma unroll
  for (int t = 0; t < 4; ++t)
#pragma unroll
    for (int n = 0; n < 4; ++n) acc[t][n] = f32x4{0, 0, 0, 0};
  const int sw = (2 * g + (q4 >> 1)) & 3;
  const char* wr = smem + kp * REGION + (4 * g + q4) * 128 + p4 * 8;
  auto lda = [&](bf16x8* Af, int slot) {
#pragma unroll
    for (int nt = 0; nt < 4; ++nt) {
      const char* p = wr + slot * 4096 + ((nt ^ sw) << 5);
      Af[nt] = cat(ldstr(p), ldstr(p + 2048));
    }
  };
  wait_frag<(R - 1) * 8>(B[0]);
  asm volatile("" : "+v"(rs[0]), "+v"(rs[1]));
  lda(A[0], 0);
  gks_steps<0, NKS, R>(issue, lda, B, A, acc);
  {
    const int mo = kp * REGION + g * 256 + ((i ^ ((g >> 1) * 2)) << 4);
#pragma unroll
    for (int tt = 0; tt < 4; ++tt)
#pragma unroll
      for (int nt = 0; nt < 4; ++nt)
        *(LAS f32x4*)(LAS char*)(smem + (mo ^ (nt * 64)) + (tt * 4 + nt) * 1024) = acc[tt][nt];
  }
  bar();
  const int c = lane & 7;
#pragma unroll
  for (int it = 0; it < 2; ++it) {
    const int tok = (lane >> 3) + 8 * it;
    const char* rp = smem + (kp * 4 + (c >> 1)) * 1024 + (c & 1) * 512 + ((tok ^ (2 * c)) << 4);
    f32x4 v0 = f32x4{0, 0, 0, 0}, v1 = f32x4{0, 0, 0, 0};
#pragma unroll
    for (int p = 0; p < 4; ++p) {
      v0 += *(const LAS f32x4*)(LAS const char*)(rp + p * REGION);
      v1 += *(const LAS f32x4*)(LAS const char*)(rp + p * REGION + 256);
    }
    u32x4 r = __builtin_bit_cast(u32x4, rs[it]);
    u32x4 ov = {pack2(v0[0] + bflo(r[0]), v0[1] + bfhi(r[0])), pack2(v0[2] + bflo(r[1]), v0[3] + bfhi(r[1])),
                pack2(v1[0] + bflo(r[2]), v1[1] + bfhi(r[2])), pack2(v1[2] + bflo(r[3]), v1[3] + bfhi(r[3]))};
    st16<(WT & (STATS ? 4 : 16)) != 0>(out + ooff + it * 8192 + vo, ov);
    if constexpr (STATS) {
      float s1 = 0.f, s2 = 0.f;
#pragma unroll
      for (int e = 0; e < 4; ++e) { s1 = dot2(ov[e], 0x3F803F80u, s1); s2 = dot2(ov[e], ov[e], s2); }
#pragma unroll
      for (int m = 1; m < 8; m <<= 1) { s1 += __shfl_xor(s1, m); s2 += __shfl_xor(s2, m); }
      if (c == 0) *(f32x2*)(stats + ((size_t)(tb * 64 + kp * 16 + tok) * 8 + cb) * 2) = f32x2{s1, s2};
    }
  }
  TL1(STATS ? 2 : 4);
}

constexpr int K4_STAGE = 49152, K4_NS = 3;
constexpr int K4_LDS = K4_STAGE * K4_NS + 4 * 224 * 4;

template <int NT>
DEV void k4_body(char* smem, const char* gt, const char* g1, const char* g3, unsigned vt, unsigned vw,
                 unsigned long long wmask, unsigned ldsw, int tg, int lane, const float* __restrict__ stats,
                 char* __restrict__ hh, int tok0, int cb TLARG) {
  const int g = lane >> 4, i = lane & 15, q4 = (lane >> 2) & 3, p4 = lane & 3;
  f32x4 acc[2][2 * NT], csg = f32x4{0, 0, 0, 0}, csu = f32x4{0, 0, 0, 0};
#pragma unroll
  for (int a = 0; a < 2 * NT; ++a) { acc[0][a] = f32x4{0, 0, 0, 0}; acc[1][a] = f32x4{0, 0, 0, 0}; }
  const bf16x8 ones = {0x3F80, 0x3F80, 0x3F80, 0x3F80, 0x3F80, 0x3F80, 0x3F80, 0x3F80};
  const int toff0 = (tg * 32 + i) * 128 + ((g ^ (i & 7)) << 4);
  const int toff1 = (tg * 32 + i) * 128 + (((4 + g) ^ (i & 7)) << 4);
  const int odd = cb & 1;
  const int fsw = q4 | ((g & 1) << 2);
  int T[NT], wa[NT];
#pragma unroll
  for (int a = 0; a < NT; ++a) {
    T[a] = NT == 4 ? odd * 3 + a : (a < 2 ? (odd ? 1 : 4) + a : (odd ? 0 : 6));
    wa[a] = 16384 + (8 * g + q4) * 256 + ((T[a] ^ fsw) << 5) + p4 * 8;
  }
  const int rot = 0;
  auto dma = [&](int kt, int j) {
    const unsigned base = ldsw + (kt % K4_NS) * K4_STAGE;
    const int ke = (kt + rot) & 7;
    if (j < 2) dma16s(vt, gt + ke * 128 + j * 8192, base + j * 1024);
    else if (j < 4) dma16sm(vw, g1 + (size_t)(ke * 64 + (j - 2) * 4) * (F_ * 2), base + 16384 + (j - 2) * 1024, wmask);
    else dma16sm(vw, g3 + (size_t)(ke * 64 + (j - 4) * 4) * (F_ * 2), base + 32768 + (j - 4) * 1024, wmask);
  };
  auto ldB = [&](bf16x8* B, int kt, int ks) {
    const char* sb = smem + (kt % K4_NS) * K4_STAGE + (ks ? toff1 : toff0);
    B[0] = lds128(sb); B[1] = lds128(sb + 2048);
  };
  auto ldA = [&](int a, int kt, int ks) -> bf16x8 {
    const char* wp = smem + (kt % K4_NS) * K4_STAGE + wa[a < NT ? a : a - NT] + (a < NT ? 0 : 16384) + ks * 8192;
    return cat(ldstr(wp), ldstr(wp + 1024));
  };
  auto mma = [&](int a, bf16x8 A, const bf16x8* B) {
    acc[0][a] = mfma16(A, B[0], acc[0][a]);
    acc[1][a] = mfma16(A, B[1], acc[1][a]);
  };
  auto csum = [&](const bf16x8* A) {
    if (tg == 0) { csg = mfma16(A[0], ones, csg); csu = mfma16(A[NT], ones, csu); }
    else if (tg == 1) { csg = mfma16(A[1], ones, csg); csu = mfma16(A[NT + 1], ones, csu); }
    else if (tg == 2) { csg = mfma16(A[2], ones, csg); csu = mfma16(A[NT + 2], ones, csu); }
    else if (NT == 4) { csg = mfma16(A[NT - 1], ones, csg); csu = mfma16(A[2 * NT - 1], ones, csu); }
  };
  bf16x8 A0[2 * NT], A1[2 * NT], B0[2], B1[2];
  f32x4 sa[2];
#pragma unroll
  for (int j = 0; j < 6; ++j) dma(0, j);
#pragma unroll
  for (int j = 0; j < 6; ++j) dma(1, j);
  wait_bar<6>();
  ldB(B0, 0, 0);
#pragma unroll
  for (int a = 0; a < 2 * NT; ++a) A0[a] = ldA(a, 0, 0);
#pragma unroll
  for (int kt = 0; kt < 8; ++kt) {
    ldB(B1, kt, 1);
#pragma unroll
    for (int a = 0; a < 2 * NT; ++a) {
      if (a < NT) { A1[2 * a] = ldA(2 * a, kt, 1); A1[2 * a + 1] = ldA(2 * a + 1, kt, 1); }
      mma(a, A0[a], B0);
      if (kt == 0 && a >= 2 * NT - 6) dma(2, a - (2 * NT - 6));
      __builtin_amdgcn_sched_barrier(0);
    }
    csum(A0);
    if (kt < 6) wait_bar<6>();
    else if (kt == 6) wait_bar<0>();
    if (kt < 7) ldB(B0, kt + 1, 0);
    if (kt == 6) {
#pragma unroll
      for (int tt = 0; tt < 2; ++tt) sa[tt] = *(const f32x4*)(stats + ((size_t)(tok0 + tg * 32 + tt * 16 + i) * 8 + 2 * g) * 2);
    }
    if (kt == 7) csum(A1);
#pragma unroll
    for (int a = 0; a < 2 * NT; ++a) {
      if (kt < 7 && a < NT) { A0[2 * a] = ldA(2 * a, kt + 1, 0); A0[2 * a + 1] = ldA(2 * a + 1, kt + 1, 0); }
      mma(a, A1[a], B1);
      if (kt + 3 < 8 && a >= 2 * NT - 6) dma(kt + 3, a - (2 * NT - 6));
      __builtin_amdgcn_sched_barrier(0);
    }
    if (kt < 7) csum(A1);
  }
  float* csp = (float*)(smem + K4_STAGE * K4_NS);
  if (i == 0 && tg < NT) {
    const int tt_ = NT == 4 ? odd * 3 + tg : (tg < 2 ? (odd ? 1 : 4) + tg : (odd ? 0 : 6));
    *(f32x4*)(csp + tt_ * 16 + 4 * g) = csg;
    *(f32x4*)(csp + (7 + tt_) * 16 + 4 * g) = csu;
  }
  float rs[2], mr[2];
#pragma unroll
  for (int tt = 0; tt < 2; ++tt) {
    float s1 = sa[tt][0] + sa[tt][2], s2 = sa[tt][1] + sa[tt][3];
    s1 = xsum(s1); s2 = xsum(s2);
    float mean = s1 * (1.f / 512.f);
    float var = fmaxf(s2 * (1.f / 512.f) - mean * mean, 0.f);
    rs[tt] = rsqrtf(var + LN_EPS); mr[tt] = mean * rs[tt];
  }
  bar();
  unsigned pk[2][NT][2];
#pragma unroll
  for (int tt = 0; tt < 2; ++tt) {
    char* dst = hh + ((size_t)((tok0 >> 4) + tg * 2 + tt) * 56) * 1024 + lane * 16;
#pragma unroll
    for (int a = 0; a < NT; ++a) {
      f32x4 cg = *(const f32x4*)(csp + T[a] * 16 + 4 * g);
      f32x4 cu = *(const f32x4*)(csp + (7 + T[a]) * 16 + 4 * g);
      float r[4];
#pragma unroll
      for (int e = 0; e < 4; ++e) {
        float ga = acc[tt][a][e] * rs[tt] - mr[tt] * cg[e];
        float up = acc[tt][NT + a][e] * rs[tt] - mr[tt] * cu[e];
        float sg = __builtin_amdgcn_rcpf(1.f + __builtin_amdgcn_exp2f(-1.4426950408889634f * ga));
        r[e] = ga * sg * up;
      }
      pk[tt][a][0] = pack2(r[0], r[1]); pk[tt][a][1] = pack2(r[2], r[3]);
      const int G = cb * 7 + T[a];
      if (a & 1) {
        st16<(WT & 8) != 0>(dst + (size_t)(G >> 1) * 1024, u32x4{pk[tt][a - 1][0], pk[tt][a - 1][1], pk[tt][a][0], pk[tt][a][1]});
      } else if (a == NT - 1) {
        st8<(WT & 8) != 0>(dst + (size_t)(G >> 1) * 1024 + (G & 1) * 8, u32x2{pk[tt][a][0], pk[tt][a][1]});
      }
    }
  }
  TL1(3);
}

__global__ __launch_bounds__(512) void k4_mlp(const char* __restrict__ h, const char* __restrict__ w1,
                                              const char* __restrict__ w3, const float* __restrict__ stats,
                                              char* __restrict__ hh) {
  extern __shared__ __attribute__((aligned(16))) char smem[];
  TL0;
  const int tid = threadIdx.x, lane = tid & 63, wv = uni(tid >> 6);
  const int tg = wv & 3, half = wv >> 2;
  const int b = blockIdx.x, xcd = b & 7, bi = b >> 3;
  const int cb = (xcd & 3) * 4 + (bi & 3), tb = (xcd >> 2) * 8 + (bi >> 2);
  const unsigned lds0 = (unsigned)(size_t)(LAS char*)smem;
  const char* gt = h + (size_t)(tb * 128 + wv * 16) * 1024;
  const unsigned vt = (lane >> 3) * 1024 + (((lane & 7) ^ (lane >> 3)) << 4);
  const char* g1 = w1 + (size_t)(wv * 8) * (F_ * 2) + cb * 224;
  const char* g3 = w3 + (size_t)(wv * 8) * (F_ * 2) + cb * 224;
  const int unit = ((lane >> 1) & 7) ^ ((lane >> 4) | ((wv & 1) << 2));
  const unsigned vw = (lane >> 4) * (F_ * 2) + unit * 32 + (lane & 1) * 16;
  const unsigned long long wmask = __builtin_amdgcn_ballot_w64(unit < 7);
  const unsigned ldsw = lds0 + wv * 2048;
  if (half == 0) k4_body<4>(smem, gt, g1, g3, vt, vw, wmask, ldsw, tg, lane, stats, hh, tb * 128, cb TLPASS);
  else k4_body<3>(smem, gt, g1, g3, vt, vw, wmask, ldsw, tg, lane, stats, hh, tb * 128, cb TLPASS);
}

extern "C" void kernel_launch(void* const* d_in, const int* in_sizes, int n_in, void* d_out, int out_size,
                              void* d_ws, size_t ws_size, hipStream_t stream) {
  const char* x = (const char*)d_in[0];
  const char* wqkv = (const char*)d_in[1];
  const char* wo = (const char*)d_in[2];
  const char* w1 = (const char*)d_in[4];
  const char* w3 = (const char*)d_in[5];
  const char* w2 = (const char*)d_in[6];
  char* ws = (char*)d_ws;
  char* qkv = ws;
  char* o = ws + (6u << 20);
  char* h = ws + (8u << 20);
  float* stats = (float*)(ws + (10u << 20));
  char* hh = ws + (11u << 20);
  (void)hipFuncSetAttribute((const void*)k1_qkv, hipFuncAttributeMaxDynamicSharedMemorySize, K1_LDS);
  (void)hipFuncSetAttribute((const void*)k2_attn, hipFuncAttributeMaxDynamicSharedMemorySize, K2_LDS);
  (void)hipFuncSetAttribute((const void*)k_gks<4, 4, true>, hipFuncAttributeMaxDynamicSharedMemorySize, GK<4, 4>::LDS);
  (void)hipFuncSetAttribute((const void*)k4_mlp, hipFuncAttributeMaxDynamicSharedMemorySize, K4_LDS);
  (void)hipFuncSetAttribute((const void*)k_gks<14, 7, false>, hipFuncAttributeMaxDynamicSharedMemorySize, GK<14, 7>::LDS);
  k1_qkv<<<256, 512, K1_LDS, stream>>>(x, wqkv, qkv);
  k2_attn<<<256, 512, K2_LDS, stream>>>(qkv, o, wo, w1, w3, w2);
  k_gks<4, 4, true><<<256, 256, GK<4, 4>::LDS, stream>>>(o, wo, x, h, stats);
  k4_mlp<<<256, 512, K4_LDS, stream>>>(h, w1, w3, stats, hh);
  k_gks<14, 7, false><<<256, 256, GK<14, 7>::LDS, stream>>>(hh, w2, h, (char*)d_out, nullptr);
}
```

```cpp
#include <hip/hip_runtime.h>
#include <hip/hip_bf16.h>
#include <stdint.h>

typedef short bf16x8 __attribute__((ext_vector_type(8)));
typedef short s16x4 __attribute__((ext_vector_type(4)));
typedef float f32x4 __attribute__((ext_vector_type(4)));
typedef float f32x2 __attribute__((ext_vector_type(2)));
typedef unsigned u32x4 __attribute__((ext_vector_type(4)));
typedef unsigned u32x2 __attribute__((ext_vector_type(2)));
typedef __bf16 bf16x2 __attribute__((ext_vector_type(2)));
#define LAS __attribute__((address_space(3)))
#define DEV __device__ __forceinline__

constexpr int S_ = 2048, D_ = 512, H_ = 32, F_ = 1792;
constexpr float LN_EPS = 1e-5f;

DEV f32x4 mfma16(bf16x8 a, bf16x8 b, f32x4 c) { return __builtin_amdgcn_mfma_f32_16x16x32_bf16(a, b, c, 0, 0, 0); }
DEV unsigned pack2(float a, float b) {
  f32x2 v = {a, b};
  return __builtin_bit_cast(unsigned, __builtin_convertvector(v, bf16x2));
}
DEV float bflo(unsigned u) { return __builtin_bit_cast(float, u << 16); }
DEV float bfhi(unsigned u) { return __builtin_bit_cast(float, u & 0xffff0000u); }
DEV float dot2(unsigned a, unsigned b, float c) {
  return __builtin_amdgcn_fdot2_f32_bf16(__builtin_bit_cast(bf16x2, a), __builtin_bit_cast(bf16x2, b), c, false);
}
DEV void dma16(const char* gptr, unsigned ldsbase) {
  unsigned sv;
  asm volatile("s_mov_b32 %0, m0\n\ts_mov_b32 m0, %2\n\ts_nop 0\n\tglobal_load_lds_dwordx4 %1, off\n\ts_mov_b32 m0, %0"
               : "=&s"(sv) : "v"(gptr), "s"(ldsbase) : "memory");
}
DEV void dma16s(unsigned voff, const char* sbase, unsigned ldsbase) {
  unsigned sv;
  asm volatile("s_mov_b32 %0, m0\n\ts_mov_b32 m0, %3\n\ts_nop 0\n\tglobal_load_lds_dwordx4 %1, %2\n\ts_mov_b32 m0, %0"
               : "=&s"(sv) : "v"(voff), "s"(sbase), "s"(ldsbase) : "memory");
}
DEV void dma16sm(unsigned voff, const char* sbase, unsigned ldsbase, unsigned long long mask) {
  unsigned sv;
  asm volatile("s_mov_b32 %0, m0\n\ts_mov_b32 m0, %3\n\ts_mov_b64 exec, %4\n\tglobal_load_lds_dwordx4 %1, %2\n\ts_mov_b64 exec, -1\n\ts_mov_b32 m0, %0"
               : "=&s"(sv) : "v"(voff), "s"(sbase), "s"(ldsbase), "s"(mask) : "memory");
}
DEV void gld16(bf16x8& d, unsigned voff, const char* sbase) {
  asm volatile("global_load_dwordx4 %0, %1, %2" : "=v"(d) : "v"(voff), "s"(sbase) : "memory");
}
template <int N> DEV void wait_bar() { asm volatile("s_waitcnt vmcnt(%0) lgkmcnt(0)\n\ts_barrier" ::"n"(N) : "memory"); }
DEV void bar() { asm volatile("s_waitcnt lgkmcnt(0)\n\ts_barrier" ::: "memory"); }
DEV bf16x8 lds128(const char* p) { return *(const LAS bf16x8*)(LAS const char*)p; }
DEV s16x4 ldstr(const char* p) { return __builtin_amdgcn_ds_read_tr16_b64_v4i16((LAS s16x4*)p); }
DEV bf16x8 cat(s16x4 a, s16x4 b) { return __builtin_shufflevector(a, b, 0, 1, 2, 3, 4, 5, 6, 7); }
DEV float xsum(float v) { v += __shfl_xor(v, 16); v += __shfl_xor(v, 32); return v; }
DEV float xmax(float v) { v = fmaxf(v, __shfl_xor(v, 16)); v = fmaxf(v, __shfl_xor(v, 32)); return v; }
DEV unsigned uni(unsigned v) { return __builtin_amdgcn_readfirstlane(v); }
#define WT 31
template <bool W> DEV void st16(char* p, u32x4 v) {
  if constexpr (W) asm volatile("global_store_dwordx4 %0, %1, off sc0 sc1\n\ts_nop 1" ::"v"(p), "v"(v) : "memory");
  else *(u32x4*)p = v;
}
template <bool W> DEV void st8(char* p, u32x2 v) {
  if constexpr (W) asm volatile("global_store_dwordx2 %0, %1, off sc0 sc1" ::"v"(p), "v"(v) : "memory");
  else *(u32x2*)p = v;
}

#define TL0
#define TL1(k)
#define TLARG
#define TLPASS
#define STAMP(n)
#define STDECL
#define STOUT
template <int A, int B> struct Min { static constexpr int v = A < B ? A : B; };

template <int KT, int NK, int NS, int P, class Issue, class Compute>
DEV void pipe_steps(Issue& issue, Compute& compute) {
  if constexpr (KT < NK) {
    wait_bar<(Min<KT + NS - 1, NK>::v - KT - 1) * P>();
    if constexpr (KT + NS - 1 < NK) issue((KT + NS - 1) % NS);
    compute(KT % NS, KT);
    pipe_steps<KT + 1, NK, NS, P>(issue, compute);
  }
}

constexpr int K1_STAGE = 16384 + 12288, K1_NS = 5;
constexpr int K1_LDS = K1_STAGE * K1_NS + 1024 + 1536;
#define SB __builtin_amdgcn_sched_barrier(0)
DEV float xsum2(float v) {
  auto a = __builtin_amdgcn_permlane16_swap(__float_as_uint(v), __float_as_uint(v), false, false);
  v = __uint_as_float(a[0]) + __uint_as_float(a[1]);
  auto b = __builtin_amdgcn_permlane32_swap(__float_as_uint(v), __float_as_uint(v), false, false);
  return __uint_as_float(b[0]) + __uint_as_float(b[1]);
}

__global__ __launch_bounds__(512) void k1_qkv(const char* __restrict__ x, const char* __restrict__ w,
                                              char* __restrict__ qkv) {
  extern __shared__ __attribute__((aligned(16))) char smem[];
  TL0;
  STDECL;
  const int tid = threadIdx.x, lane = tid & 63, wv = uni(tid >> 6);
  const int g = lane >> 4, i = lane & 15, q4 = (lane >> 2) & 3, p4 = lane & 3;
  const int tg = wv & 3, nh = wv >> 2;
  const int b = blockIdx.x, xcd = b & 7, bi = b >> 3;
  const int cb = (xcd & 1) * 8 + (bi & 7), tb = (xcd >> 1) * 4 + (bi >> 3);
  const unsigned lds0 = (unsigned)(size_t)(LAS char*)smem;

  const char* gx = x + (size_t)(tb * 128 + wv * 16) * 1024;
  const unsigned vt = (lane >> 3) * 1024 + (((lane & 7) ^ (lane >> 3)) << 4);
  const char* gw = w + cb * 192;
  unsigned vw0, vw1;
  {
    int qq = wv * 64 + lane;
    int k = qq / 12, c = (qq % 12) ^ (((k >> 3) & 1) << 1);
    vw0 = k * 3072 + c * 16;
    qq = 512 + wv * 32 + (lane & 31);
    k = qq / 12; c = (qq % 12) ^ (((k >> 3) & 1) << 1);
    vw1 = k * 3072 + c * 16;
  }
  const int k0 = bi & 7;
  auto dma = [&](int s, int j) {
    const int kk = (k0 + s) & 7;
    const unsigned base = lds0 + (s % K1_NS) * K1_STAGE;
    if (j == 0) dma16s(vt, gx + kk * 128, base + wv * 2048);
    else if (j == 1) dma16s(vt, gx + kk * 128 + 8192, base + wv * 2048 + 1024);
    else if (j == 2) dma16s(vw0, gw + kk * (64 * 3072), base + 16384 + wv * 1024);
    else dma16sm(vw1, gw + kk * (64 * 3072), base + 16384 + 8192 + wv * 512, 0xFFFFFFFFull);
  };
#pragma unroll
  for (int s = 0; s < K1_NS - 1; ++s)
#pragma unroll
    for (int j = 0; j < 4; ++j) dma(s, j);

  f32x4 acc[2][3], cs = f32x4{0, 0, 0, 0}, s1v = f32x4{0, 0, 0, 0};
#pragma unroll
  for (int a = 0; a < 3; ++a) { acc[0][a] = f32x4{0, 0, 0, 0}; acc[1][a] = f32x4{0, 0, 0, 0}; }
  float s2a = 0.f, s2b = 0.f;
  const bf16x8 ones = {0x3F80, 0x3F80, 0x3F80, 0x3F80, 0x3F80, 0x3F80, 0x3F80, 0x3F80};
  int toff[2][2];
#pragma unroll
  for (int tt = 0; tt < 2; ++tt) {
    toff[tt][0] = (tg * 32 + (tt ^ nh) * 16 + i) * 128 + ((g ^ (i & 7)) << 4);
    toff[tt][1] = (tg * 32 + (tt ^ nh) * 16 + i) * 128 + (((4 + g) ^ (i & 7)) << 4);
  }
  int woff[3], un[3];
#pragma unroll
  for (int nt = 0; nt < 3; ++nt) {
    int a = nt + tg; a = a >= 3 ? a - 3 : a; a = a >= 3 ? a - 3 : a;
    un[nt] = nh * 3 + a;
    woff[nt] = 16384 + (8 * g + q4) * 192 + ((un[nt] ^ (g & 1)) << 5) + p4 * 8;
  }
  auto ldB = [&](int tt, int kt, int ks) -> bf16x8 { return lds128(smem + (kt % K1_NS) * K1_STAGE + toff[tt][ks]); };
  auto ldA = [&](int nt, int kt, int ks) -> bf16x8 {
    const char* p = smem + (kt % K1_NS) * K1_STAGE + woff[nt] + ks * 6144;
    return cat(ldstr(p), ldstr(p + 768));
  };
  bf16x8 A0[3], A1[3], B0[2], B1[2];
  auto phase = [&](bf16x8 (&Ac)[3], bf16x8 (&Bc)[2], bf16x8 (&An)[3], bf16x8 (&Bn)[2], int kn, int ksn, int dk, int dj) {
    const u32x4 u = __builtin_bit_cast(u32x4, Bc[0]);
#pragma unroll
    for (int m = 0; m < 8; ++m) {
      if (kn < 8) {
        if (m == 0) { Bn[0] = ldB(0, kn, ksn); An[0] = ldA(0, kn, ksn); }
        else if (m == 1) Bn[1] = ldB(1, kn, ksn);
        else if (m < 4) An[m - 1] = ldA(m - 1, kn, ksn);
      }
      if (m < 6) acc[m & 1][m >> 1] = mfma16(Ac[m >> 1], Bc[m & 1], acc[m & 1][m >> 1]);
      else if (m == 6) cs = mfma16(Ac[0], ones, cs);
      else s1v = mfma16(ones, Bc[0], s1v);
      {
        float f = (m & 1) ? bfhi(u[m >> 1]) : bflo(u[m >> 1]);
        asm volatile("" : "+v"(f));
        if (m & 1) s2b = __builtin_fmaf(f, f, s2b); else s2a = __builtin_fmaf(f, f, s2a);
      }
      if (dk < 8 && (m == 2 || m == 5)) dma(dk, dj + (m == 5));
      SB;
    }
  };
  wait_bar<12>();
  STAMP(0);
  B0[0] = ldB(0, 0, 0); A0[0] = ldA(0, 0, 0); B0[1] = ldB(1, 0, 0); A0[1] = ldA(1, 0, 0); A0[2] = ldA(2, 0, 0);
#pragma unroll
  for (int kt = 0; kt < 8; ++kt) {
    phase(A0, B0, A1, B1, kt, 1, kt + 4, 0);
    if (kt < 4) wait_bar<10>();
    else if (kt == 4) wait_bar<8>();
    else if (kt == 5) wait_bar<4>();
    else if (kt == 6) wait_bar<0>();
    if (kt == 0) STAMP(1);
    if (kt == 3) STAMP(2);
    if (kt == 6) STAMP(3);
    phase(A1, B1, A0, B0, kt + 1, 0, kt + 4, 2);
  }
  STAMP(4);

  float* st = (float*)(smem + K1_STAGE * K1_NS);
  float* csp = (float*)(smem + K1_STAGE * K1_NS + 1024);
  const float s2 = xsum2(s2a + s2b);
  if (g == 0) {
    float mean = s1v[0] * (1.f / 512.f);
    float var = fmaxf(s2 * (1.f / 512.f) - mean * mean, 0.f);
    float rstd = rsqrtf(var + LN_EPS);
    *(f32x2*)(st + (tg * 32 + nh * 16 + i) * 2) = f32x2{-mean * rstd, rstd};
  }
  if (i == 0 && tg < 3) *(f32x4*)(csp + un[0] * 16 + 4 * g) = cs;
  bar();
  f32x2 ms[2];
#pragma unroll
  for (int tt = 0; tt < 2; ++tt) ms[tt] = *(const f32x2*)(st + (tg * 32 + (tt ^ nh) * 16 + i) * 2);
#pragma unroll
  for (int nt = 0; nt < 3; ++nt) {
    f32x4 c = *(const f32x4*)(csp + un[nt] * 16 + 4 * g);
    int n0 = cb * 96 + un[nt] * 16;
    char* dst = qkv + (size_t)(n0 >> 4) * (2048 * 32) + g * 8;
#pragma unroll
    for (int tt = 0; tt < 2; ++tt) {
      f32x4 y = acc[tt][nt] * ms[tt][1] + ms[tt][0] * c;
      u32x2 o = {pack2(y[0], y[1]), pack2(y[2], y[3])};
      st8<(WT & 1) != 0>(dst + (size_t)(tb * 128 + tg * 32 + (tt ^ nh) * 16 + i) * 32, o);
    }
  }
  STAMP(5);
  STOUT;
  TL1(0);
}

constexpr int K2_LDS = 96 * 1024;
__global__ __launch_bounds__(512) void k2_attn(const char* __restrict__ qkv, char* __restrict__ o,
    const char* __restrict__ wo, const char* __restrict__ w1, const char* __restrict__ w3, const char* __restrict__ w2) {
  extern __shared__ __attribute__((aligned(16))) char smem[];
  TL0;
  const int tid = threadIdx.x, lane = tid & 63, wv = uni(tid >> 6);
  const int g = lane >> 4, i = lane & 15, q4 = (lane >> 2) & 3, p4 = lane & 3;
  const int b = blockIdx.x;
  const int hp = (b & 7) * 2 + ((b >> 3) & 1), qb = b >> 4;
  const int q0 = qb * 128;
  const unsigned lds0 = (unsigned)(size_t)(LAS char*)smem;
  const size_t HS = 2048 * 32;
  {
    const int r = wv >> 1;
    const char* src = qkv + (size_t)((1 + (r >> 1)) * 32 + hp * 2 + (r & 1)) * HS;
#pragma unroll
    for (int j = 0; j < 6; ++j) {
      int ch = ((wv & 1) * 6 + j) * 64 + lane;
      int key = q0 - 256 + (ch >> 1);
      key = key < 0 ? 0 : key;
      dma16(src + (size_t)key * 32 + (ch & 1) * 16, lds0 + r * 12288 + ((wv & 1) * 6 + j) * 1024);
    }
  }
  const int qs = q0 + wv * 16;
  bf16x8 Q[2];
  gld16(Q[0], (unsigned)(i * 32 + (g & 1) * 16), qkv + (size_t)(hp * 2) * HS + (size_t)qs * 32);
  gld16(Q[1], (unsigned)(i * 32 + (g & 1) * 16), qkv + (size_t)(hp * 2 + 1) * HS + (size_t)qs * 32);
  {
    const int c0 = b * 8 + wv;
    const unsigned sc = lds0 + 49152 + wv * 1024;
    const char* p0 = c0 < 512 ? wo + (size_t)c0 * 1024 : w1 + (size_t)(c0 - 512) * 1024;
    const char* p1 = c0 < 256 ? w1 + (size_t)(c0 + 1536) * 1024 : w3 + (size_t)(c0 - 256) * 1024;
    const char* p2 = w2 + (size_t)(c0 < 1792 ? c0 : c0 - 1792) * 1024;
    dma16s(lane * 16, p0, sc); dma16s(lane * 16, p1, sc); dma16s(lane * 16, p2, sc);
  }
  asm volatile("s_waitcnt vmcnt(3)\n\ts_barrier" : "+v"(Q[0]), "+v"(Q[1]) : : "memory");
  if (g >= 2) { Q[0] = bf16x8{0, 0, 0, 0, 0, 0, 0, 0}; Q[1] = bf16x8{0, 0, 0, 0, 0, 0, 0, 0}; }
  const float c = 0.25f * 1.4426950408889634f;
  const float NEG = -1e30f;
  const int lrow = wv * 16;
  f32x4 cin0, cin16;
#pragma unroll
  for (int r = 0; r < 4; ++r) { cin0[r] = (4 * g + r > i) ? 0.f : NEG; cin16[r] = (4 * g + r <= i) ? 0.f : NEG; }
  const bf16x8 ones = {0x3F80, 0x3F80, 0x3F80, 0x3F80, 0x3F80, 0x3F80, 0x3F80, 0x3F80};
  u32x4 ov;
#pragma unroll
  for (int u = 0; u < 2; ++u) {
    const char* Kl = smem + u * 12288;
    const char* Vl = smem + 24576 + u * 12288;
    f32x4 s[17];
#pragma unroll
    for (int t = 0; t < 17; ++t) {
      bf16x8 A = lds128(Kl + (lrow + t * 16 + i) * 32 + (g & 1) * 16);
      s[t] = mfma16(A, Q[u], t == 0 ? cin0 : t == 16 ? cin16 : f32x4{0, 0, 0, 0});
    }
    if (qs < 256) {
      asm volatile("" ::: "memory");
      const int nneg = (256 - qs) >> 4;
#pragma unroll
      for (int t = 0; t < 16; ++t)
        if (t < nneg) s[t] = f32x4{NEG, NEG, NEG, NEG};
    }
    float m = s[16][0];
#pragma unroll
    for (int t = 0; t < 17; ++t)
#pragma unroll
      for (int r = 0; r < 4; ++r) m = fmaxf(m, s[t][r]);
    m = xmax(m);
    const f32x2 c2 = {c, c}, m2 = {-m * c, -m * c};
    unsigned pk[17][2];
#pragma unroll
    for (int t = 0; t < 17; ++t) {
      f32x2 a0 = __builtin_elementwise_fma(f32x2{s[t][0], s[t][1]}, c2, m2);
      f32x2 a1 = __builtin_elementwise_fma(f32x2{s[t][2], s[t][3]}, c2, m2);
      pk[t][0] = pack2(__builtin_amdgcn_exp2f(a0[0]), __builtin_amdgcn_exp2f(a0[1]));
      pk[t][1] = pack2(__builtin_amdgcn_exp2f(a1[0]), __builtin_amdgcn_exp2f(a1[1]));
    }
    f32x4 oacc = f32x4{0, 0, 0, 0}, lsum = f32x4{0, 0, 0, 0};
#pragma unroll
    for (int tp = 0; tp < 9; ++tp) {
      const int t0 = 2 * tp, t1 = (2 * tp + 1 < 17) ? 2 * tp + 1 : 16;
      bf16x8 A = cat(ldstr(Vl + (lrow + t0 * 16 + 4 * g + q4) * 32 + p4 * 8), ldstr(Vl + (lrow + t1 * 16 + 4 * g + q4) * 32 + p4 * 8));
      u32x4 pb = {pk[t0][0], pk[t0][1], (2 * tp + 1 < 17) ? pk[t1][0] : 0u, (2 * tp + 1 < 17) ? pk[t1][1] : 0u};
      oacc = mfma16(A, __builtin_bit_cast(bf16x8, pb), oacc);
      lsum = mfma16(ones, __builtin_bit_cast(bf16x8, pb), lsum);
    }
    const float inv = __builtin_amdgcn_rcpf(lsum[0]);
    ov[2 * u] = pack2(oacc[0] * inv, oacc[1] * inv);
    ov[2 * u + 1] = pack2(oacc[2] * inv, oacc[3] * inv);
  }
  st16<(WT & 2) != 0>(o + ((size_t)(qs >> 4) * 16 + hp) * 1024 + lane * 16, ov);
  asm volatile("s_waitcnt vmcnt(0)" ::: "memory");
  TL1(1);
}

template <int N> DEV void wait_frag(bf16x8* B) {
  asm volatile("s_waitcnt vmcnt(%4)" : "+v"(B[0]), "+v"(B[1]), "+v"(B[2]), "+v"(B[3]) : "n"(N) : "memory");
}
template <int NKS, int R> struct GK { static constexpr int REGION = (R * 4096 > 16384) ? R * 4096 : 16384, LDS = 4 * REGION > 98304 ? 4 * REGION : 98304; };

template <int J, int NKS, int R, class Issue, class LdA>
DEV void gks_steps(Issue& issue, LdA& lda, bf16x8 (&B)[R][4], bf16x8 (&A)[2][4], f32x4 (&acc)[4][4]) {
  if constexpr (J < NKS) {
    if constexpr (J + 1 < NKS) {
      wait_frag<(Min<J + R - 1, NKS - 1>::v - (J + 1)) * 8>(B[(J + 1) % R]);
      lda(A[(J + 1) & 1], (J + 1) % R);
    }
#pragma unroll
    for (int nt = 0; nt < 4; ++nt)
#pragma unroll
      for (int tt = 0; tt < 4; ++tt) acc[tt][nt] = mfma16(A[J & 1][nt], B[J % R][tt], acc[tt][nt]);
    __builtin_amdgcn_sched_barrier(0);
    if constexpr (J + R < NKS) issue(J + R);
    gks_steps<J + 1, NKS, R>(issue, lda, B, A, acc);
  }
}

template <int NKS, int R, bool STATS>
__global__ __launch_bounds__(256) void k_gks(const char* __restrict__ a, const char* __restrict__ w,
                                             const char* __restrict__ resid, char* __restrict__ out,
                                             float* __restrict__ stats) {
  extern __shared__ __attribute__((aligned(16))) char smem[];
  TL0;
  const int tid = threadIdx.x, lane = tid & 63, kp = uni(tid >> 6);
  const int g = lane >> 4, i = lane & 15, q4 = (lane >> 2) & 3, p4 = lane & 3;
  const int b = blockIdx.x;
  const int cb = (b >> 3) & 7, tb = (b & 7) * 4 + (b >> 6);
  constexpr int KT = NKS * 4, REGION = GK<NKS, R>::REGION;
  const unsigned ldsw = (unsigned)(size_t)(LAS char*)smem + kp * REGION;
  const char* ga = a + ((size_t)(tb * 4) * KT + kp * NKS) * 1024;
  const char* gw = w + (size_t)(kp * NKS * 32) * 1024 + cb * 128;
  const unsigned va = lane * 16;
  const unsigned vw = (lane >> 3) * 1024 + ((((lane & 7) >> 1) ^ ((lane >> 4) & 3)) << 5) + (lane & 1) * 16;
  bf16x8 B[R][4], A[2][4], rs[2];
  const int rot = (NKS > R) ? ((tb ^ cb) & 1) * (NKS / 2) : 0;
  auto issue = [&](int j) {
    const int s = j < NKS / 2 ? j + rot : j - rot;
#pragma unroll
    for (int q = 0; q < 4; ++q) dma16s(vw, gw + (size_t)(s * 32 + q * 8) * 1024, ldsw + (j % R) * 4096 + q * 1024);
#pragma unroll
    for (int tt = 0; tt < 4; ++tt) gld16(B[j % R][tt], va, ga + (size_t)(tt * KT + s) * 1024);
  };
  const size_t ooff = (size_t)(tb * 64 + kp * 16) * 1024 + cb * 128;
  const unsigned vo = (lane >> 3) * 1024 + (lane & 7) * 16;
  gld16(rs[0], vo, resid + ooff);
  gld16(rs[1], vo, resid + ooff + 8192);
#pragma unroll
  for (int j = 0; j < R; ++j) issue(j);
  f32x4 acc[4][4];
#pragma unroll
  for (int t = 0; t < 4; ++t)
#pragma unroll
    for (int n = 0; n < 4; ++n) acc[t][n] = f32x4{0, 0, 0, 0};
  const int sw = (2 * g + (q4 >> 1)) & 3;
  const char* wr = smem + kp * REGION + (4 * g + q4) * 128 + p4 * 8;
  auto lda = [&](bf16x8* Af, int slot) {
#pragma unroll
    for (int nt = 0; nt < 4; ++nt) {
      const char* p = wr + slot * 4096 + ((nt ^ sw) << 5);
      Af[nt] = cat(ldstr(p), ldstr(p + 2048));
    }
  };
  wait_frag<(R - 1) * 8>(B[0]);
  asm volatile("" : "+v"(rs[0]), "+v"(rs[1]));
  lda(A[0], 0);
  gks_steps<0, NKS, R>(issue, lda, B, A, acc);
  {
    const int mo = kp * REGION + g * 256 + ((i ^ ((g >> 1) * 2)) << 4);
#pragma unroll
    for (int tt = 0; tt < 4; ++tt)
#pragma unroll
      for (int nt = 0; nt < 4; ++nt)
        *(LAS f32x4*)(LAS char*)(smem + (mo ^ (nt * 64)) + (tt * 4 + nt) * 1024) = acc[tt][nt];
  }
  bar();
  const int c = lane & 7;
#pragma unroll
  for (int it = 0; it < 2; ++it) {
    const int tok = (lane >> 3) + 8 * it;
    const char* rp = smem + (kp * 4 + (c >> 1)) * 1024 + (c & 1) * 512 + ((tok ^ (2 * c)) << 4);
    f32x4 v0 = f32x4{0, 0, 0, 0}, v1 = f32x4{0, 0, 0, 0};
#pragma unroll
    for (int p = 0; p < 4; ++p) {
      v0 += *(const LAS f32x4*)(LAS const char*)(rp + p * REGION);
      v1 += *(const LAS f32x4*)(LAS const char*)(rp + p * REGION + 256);
    }
    u32x4 r = __builtin_bit_cast(u32x4, rs[it]);
    u32x4 ov = {pack2(v0[0] + bflo(r[0]), v0[1] + bfhi(r[0])), pack2(v0[2] + bflo(r[1]), v0[3] + bfhi(r[1])),
                pack2(v1[0] + bflo(r[2]), v1[1] + bfhi(r[2])), pack2(v1[2] + bflo(r[3]), v1[3] + bfhi(r[3]))};
    st16<(WT & (STATS ? 4 : 16)) != 0>(out + ooff + it * 8192 + vo, ov);
    if constexpr (STATS) {
      float s1 = 0.f, s2 = 0.f;
#pragma unroll
      for (int e = 0; e < 4; ++e) { s1 = dot2(ov[e], 0x3F803F80u, s1); s2 = dot2(ov[e], ov[e], s2); }
#pragma unroll
      for (int m = 1; m < 8; m <<= 1) { s1 += __shfl_xor(s1, m); s2 += __shfl_xor(s2, m); }
      if (c == 0) *(f32x2*)(stats + ((size_t)(tb * 64 + kp * 16 + tok) * 8 + cb) * 2) = f32x2{s1, s2};
    }
  }
  TL1(STATS ? 2 : 4);
}

constexpr int K4_STAGE = 49152, K4_NS = 3;
constexpr int K4_LDS = K4_STAGE * K4_NS + 4 * 224 * 4;

template <int NT>
DEV void k4_body(char* smem, const char* gt, const char* g1, const char* g3, unsigned vt, unsigned vw,
                 unsigned long long wmask, unsigned ldsw, int tg, int lane, const float* __restrict__ stats,
                 char* __restrict__ hh, int tok0, int cb TLARG) {
  const int g = lane >> 4, i = lane & 15, q4 = (lane >> 2) & 3, p4 = lane & 3;
  f32x4 acc[2][2 * NT], csg = f32x4{0, 0, 0, 0}, csu = f32x4{0, 0, 0, 0};
#pragma unroll
  for (int a = 0; a < 2 * NT; ++a) { acc[0][a] = f32x4{0, 0, 0, 0}; acc[1][a] = f32x4{0, 0, 0, 0}; }
  const bf16x8 ones = {0x3F80, 0x3F80, 0x3F80, 0x3F80, 0x3F80, 0x3F80, 0x3F80, 0x3F80};
  const int toff0 = (tg * 32 + i) * 128 + ((g ^ (i & 7)) << 4);
  const int toff1 = (tg * 32 + i) * 128 + (((4 + g) ^ (i & 7)) << 4);
  const int odd = cb & 1;
  const int fsw = q4 | ((g & 1) << 2);
  int T[NT], wa[NT];
#pragma unroll
  for (int a = 0; a < NT; ++a) {
    T[a] = NT == 4 ? odd * 3 + a : (a < 2 ? (odd ? 1 : 4) + a : (odd ? 0 : 6));
    wa[a] = 16384 + (8 * g + q4) * 256 + ((T[a] ^ fsw) << 5) + p4 * 8;
  }
  const int rot = 0;
  auto dma = [&](int kt, int j) {
    const unsigned base = ldsw + (kt % K4_NS) * K4_STAGE;
    const int ke = (kt + rot) & 7;
    if (j < 2) dma16s(vt, gt + ke * 128 + j * 8192, base + j * 1024);
    else if (j < 4) dma16sm(vw, g1 + (size_t)(ke * 64 + (j - 2) * 4) * (F_ * 2), base + 16384 + (j - 2) * 1024, wmask);
    else dma16sm(vw, g3 + (size_t)(ke * 64 + (j - 4) * 4) * (F_ * 2), base + 32768 + (j - 4) * 1024, wmask);
  };
  auto ldB = [&](bf16x8* B, int kt, int ks) {
    const char* sb = smem + (kt % K4_NS) * K4_STAGE + (ks ? toff1 : toff0);
    B[0] = lds128(sb); B[1] = lds128(sb + 2048);
  };
  auto ldA = [&](int a, int kt, int ks) -> bf16x8 {
    const char* wp = smem + (kt % K4_NS) * K4_STAGE + wa[a < NT ? a : a - NT] + (a < NT ? 0 : 16384) + ks * 8192;
    return cat(ldstr(wp), ldstr(wp + 1024));
  };
  auto mma = [&](int a, bf16x8 A, const bf16x8* B) {
    acc[0][a] = mfma16(A, B[0], acc[0][a]);
    acc[1][a] = mfma16(A, B[1], acc[1][a]);
  };
  auto csum = [&](const bf16x8* A) {
    if (tg == 0) { csg = mfma16(A[0], ones, csg); csu = mfma16(A[NT], ones, csu); }
    else if (tg == 1) { csg = mfma16(A[1], ones, csg); csu = mfma16(A[NT + 1], ones, csu); }
    else if (tg == 2) { csg = mfma16(A[2], ones, csg); csu = mfma16(A[NT + 2], ones, csu); }
    else if (NT == 4) { csg = mfma16(A[NT - 1], ones, csg); csu = mfma16(A[2 * NT - 1], ones, csu); }
  };
  bf16x8 A0[2 * NT], A1[2 * NT], B0[2], B1[2];
  f32x4 sa[2];
#pragma unroll
  for (int j = 0; j < 6; ++j) dma(0, j);
#pragma unroll
  for (int j = 0; j < 6; ++j) dma(1, j);
  wait_bar<6>();
  ldB(B0, 0, 0);
#pragma unroll
  for (int a = 0; a < 2 * NT; ++a) A0[a] = ldA(a, 0, 0);
#pragma unroll
  for (int kt = 0; kt < 8; ++kt) {
    ldB(B1, kt, 1);
#pragma unroll
    for (int a = 0; a < 2 * NT; ++a) {
      if (a < NT) { A1[2 * a] = ldA(2 * a, kt, 1); A1[2 * a + 1] = ldA(2 * a + 1, kt, 1); }
      mma(a, A0[a], B0);
      if (kt == 0 && a >= 2 * NT - 6) dma(2, a - (2 * NT - 6));
      __builtin_amdgcn_sched_barrier(0);
    }
    csum(A0);
    if (kt < 6) wait_bar<6>();
    else if (kt == 6) wait_bar<0>();
    if (kt < 7) ldB(B0, kt + 1, 0);
    if (kt == 6) {
#pragma unroll
      for (int tt = 0; tt < 2; ++tt) sa[tt] = *(const f32x4*)(stats + ((size_t)(tok0 + tg * 32 + tt * 16 + i) * 8 + 2 * g) * 2);
    }
    if (kt == 7) csum(A1);
#pragma unroll
    for (int a = 0; a < 2 * NT; ++a) {
      if (kt < 7 && a < NT) { A0[2 * a] = ldA(2 * a, kt + 1, 0); A0[2 * a + 1] = ldA(2 * a + 1, kt + 1, 0); }
      mma(a, A1[a], B1);
      if (kt + 3 < 8 && a >= 2 * NT - 6) dma(kt + 3, a - (2 * NT - 6));
      __builtin_amdgcn_sched_barrier(0);
    }
    if (kt < 7) csum(A1);
  }
  float* csp = (float*)(smem + K4_STAGE * K4_NS);
  if (i == 0 && tg < NT) {
    const int tt_ = NT == 4 ? odd * 3 + tg : (tg < 2 ? (odd ? 1 : 4) + tg : (odd ? 0 : 6));
    *(f32x4*)(csp + tt_ * 16 + 4 * g) = csg;
    *(f32x4*)(csp + (7 + tt_) * 16 + 4 * g) = csu;
  }
  float rs[2], mr[2];
#pragma unroll
  for (int tt = 0; tt < 2; ++tt) {
    float s1 = sa[tt][0] + sa[tt][2], s2 = sa[tt][1] + sa[tt][3];
    s1 = xsum(s1); s2 = xsum(s2);
    float mean = s1 * (1.f / 512.f);
    float var = fmaxf(s2 * (1.f / 512.f) - mean * mean, 0.f);
    rs[tt] = rsqrtf(var + LN_EPS); mr[tt] = mean * rs[tt];
  }
  bar();
  unsigned pk[2][NT][2];
#pragma unroll
  for (int tt = 0; tt < 2; ++tt) {
    char* dst = hh + ((size_t)((tok0 >> 4) + tg * 2 + tt) * 56) * 1024 + lane * 16;
#pragma unroll
    for (int a = 0; a < NT; ++a) {
      f32x4 cg = *(const f32x4*)(csp + T[a] * 16 + 4 * g);
      f32x4 cu = *(const f32x4*)(csp + (7 + T[a]) * 16 + 4 * g);
      float r[4];
#pragma unroll
      for (int e = 0; e < 4; ++e) {
        float ga = acc[tt][a][e] * rs[tt] - mr[tt] * cg[e];
        float up = acc[tt][NT + a][e] * rs[tt] - mr[tt] * cu[e];
        float sg = __builtin_amdgcn_rcpf(1.f + __builtin_amdgcn_exp2f(-1.4426950408889634f * ga));
        r[e] = ga * sg * up;
      }
      pk[tt][a][0] = pack2(r[0], r[1]); pk[tt][a][1] = pack2(r[2], r[3]);
      const int G = cb * 7 + T[a];
      if (a & 1) {
        st16<(WT & 8) != 0>(dst + (size_t)(G >> 1) * 1024, u32x4{pk[tt][a - 1][0], pk[tt][a - 1][1], pk[tt][a][0], pk[tt][a][1]});
      } else if (a == NT - 1) {
        st8<(WT & 8) != 0>(dst + (size_t)(G >> 1) * 1024 + (G & 1) * 8, u32x2{pk[tt][a][0], pk[tt][a][1]});
      }
    }
  }
  TL1(3);
}

__global__ __launch_bounds__(512) void k4_mlp(const char* __restrict__ h, const char* __restrict__ w1,
                                              const char* __restrict__ w3, const float* __restrict__ stats,
                                              char* __restrict__ hh) {
  extern __shared__ __attribute__((aligned(16))) char smem[];
  TL0;
  const int tid = threadIdx.x, lane = tid & 63, wv = uni(tid >> 6);
  const int tg = wv & 3, half = wv >> 2;
  const int b = blockIdx.x, xcd = b & 7, bi = b >> 3;
  const int cb = (xcd & 3) * 4 + (bi & 3), tb = (xcd >> 2) * 8 + (bi >> 2);
  const unsigned lds0 = (unsigned)(size_t)(LAS char*)smem;
  const char* gt = h + (size_t)(tb * 128 + wv * 16) * 1024;
  const unsigned vt = (lane >> 3) * 1024 + (((lane & 7) ^ (lane >> 3)) << 4);
  const char* g1 = w1 + (size_t)(wv * 8) * (F_ * 2) + cb * 224;
  const char* g3 = w3 + (size_t)(wv * 8) * (F_ * 2) + cb * 224;
  const int unit = ((lane >> 1) & 7) ^ ((lane >> 4) | ((wv & 1) << 2));
  const unsigned vw = (lane >> 4) * (F_ * 2) + unit * 32 + (lane & 1) * 16;
  const unsigned long long wmask = __builtin_amdgcn_ballot_w64(unit < 7);
  const unsigned ldsw = lds0 + wv * 2048;
  if (half == 0) k4_body<4>(smem, gt, g1, g3, vt, vw, wmask, ldsw, tg, lane, stats, hh, tb * 128, cb TLPASS);
  else k4_body<3>(smem, gt, g1, g3, vt, vw, wmask, ldsw, tg, lane, stats, hh, tb * 128, cb TLPASS);
}

extern "C" void kernel_launch(void* const* d_in, const int* in_sizes, int n_in, void* d_out, int out_size,
                              void* d_ws, size_t ws_size, hipStream_t stream) {
  const char* x = (const char*)d_in[0];
  const char* wqkv = (const char*)d_in[1];
  const char* wo = (const char*)d_in[2];
  const char* w1 = (const char*)d_in[4];
  const char* w3 = (const char*)d_in[5];
  const char* w2 = (const char*)d_in[6];
  char* ws = (char*)d_ws;
  char* qkv = ws;
  char* o = ws + (6u << 20);
  char* h = ws + (8u << 20);
  float* stats = (float*)(ws + (10u << 20));
  char* hh = ws + (11u << 20);
  (void)hipFuncSetAttribute((const void*)k1_qkv, hipFuncAttributeMaxDynamicSharedMemorySize, K1_LDS);
  (void)hipFuncSetAttribute((const void*)k2_attn, hipFuncAttributeMaxDynamicSharedMemorySize, K2_LDS);
  (void)hipFuncSetAttribute((const void*)k_gks<4, 4, true>, hipFuncAttributeMaxDynamicSharedMemorySize, GK<4, 4>::LDS);
  (void)hipFuncSetAttribute((const void*)k4_mlp, hipFuncAttributeMaxDynamicSharedMemorySize, K4_LDS);
  (void)hipFuncSetAttribute((const void*)k_gks<14, 7, false>, hipFuncAttributeMaxDynamicSharedMemorySize, GK<14, 7>::LDS);
  k1_qkv<<<256, 512, K1_LDS, stream>>>(x, wqkv, qkv);
  k2_attn<<<256, 512, K2_LDS, stream>>>(qkv, o, wo, w1, w3, w2);
  k_gks<4, 4, true><<<256, 256, GK<4, 4>::LDS, stream>>>(o, wo, x, h, stats);
  k4_mlp<<<256, 512, K4_LDS, stream>>>(h, w1, w3, stats, hh);
  k_gks<14, 7, false><<<256, 256, GK<14, 7>::LDS, stream>>>(hh, w2, h, (char*)d_out, nullptr);
}
```

```cpp
#include <hip/hip_runtime.h>
#include <hip/hip_bf16.h>
#include <stdint.h>

typedef short bf16x8 __attribute__((ext_vector_type(8)));
typedef short s16x4 __attribute__((ext_vector_type(4)));
typedef float f32x4 __attribute__((ext_vector_type(4)));
typedef float f32x2 __attribute__((ext_vector_type(2)));
typedef unsigned u32x4 __attribute__((ext_vector_type(4)));
typedef unsigned u32x2 __attribute__((ext_vector_type(2)));
typedef __bf16 bf16x2 __attribute__((ext_vector_type(2)));
#define LAS __attribute__((address_space(3)))
#define DEV __device__ __forceinline__

constexpr int S_ = 2048, D_ = 512, H_ = 32, F_ = 1792;
constexpr float LN_EPS = 1e-5f;

DEV f32x4 mfma16(bf16x8 a, bf16x8 b, f32x4 c) { return __builtin_amdgcn_mfma_f32_16x16x32_bf16(a, b, c, 0, 0, 0); }
DEV unsigned pack2(float a, float b) {
  f32x2 v = {a, b};
  return __builtin_bit_cast(unsigned, __builtin_convertvector(v, bf16x2));
}
DEV float bflo(unsigned u) { return __builtin_bit_cast(float, u << 16); }
DEV float bfhi(unsigned u) { return __builtin_bit_cast(float, u & 0xffff0000u); }
DEV float dot2(unsigned a, unsigned b, float c) {
  return __builtin_amdgcn_fdot2_f32_bf16(__builtin_bit_cast(bf16x2, a), __builtin_bit_cast(bf16x2, b), c, false);
}
DEV void dma16(const char* gptr, unsigned ldsbase) {
  unsigned sv;
  asm volatile("s_mov_b32 %0, m0\n\ts_mov_b32 m0, %2\n\ts_nop 0\n\tglobal_load_lds_dwordx4 %1, off\n\ts_mov_b32 m0, %0"
               : "=&s"(sv) : "v"(gptr), "s"(ldsbase) : "memory");
}
DEV void dma16s(unsigned voff, const char* sbase, unsigned ldsbase) {
  unsigned sv;
  asm volatile("s_mov_b32 %0, m0\n\ts_mov_b32 m0, %3\n\ts_nop 0\n\tglobal_load_lds_dwordx4 %1, %2\n\ts_mov_b32 m0, %0"
               : "=&s"(sv) : "v"(voff), "s"(sbase), "s"(ldsbase) : "memory");
}
DEV void dma16sm(unsigned voff, const char* sbase, unsigned ldsbase, unsigned long long mask) {
  unsigned sv;
  asm volatile("s_mov_b32 %0, m0\n\ts_mov_b32 m0, %3\n\ts_mov_b64 exec, %4\n\tglobal_load_lds_dwordx4 %1, %2\n\ts_mov_b64 exec, -1\n\ts_mov_b32 m0, %0"
               : "=&s"(sv) : "v"(voff), "s"(sbase), "s"(ldsbase), "s"(mask) : "memory");
}
DEV void gld16(bf16x8& d, unsigned voff, const char* sbase) {
  asm volatile("global_load_dwordx4 %0, %1, %2" : "=v"(d) : "v"(voff), "s"(sbase) : "memory");
}
template <int N> DEV void wait_bar() { asm volatile("s_waitcnt vmcnt(%0) lgkmcnt(0)\n\ts_barrier" ::"n"(N) : "memory"); }
DEV void bar() { asm volatile("s_waitcnt lgkmcnt(0)\n\ts_barrier" ::: "memory"); }
DEV bf16x8 lds128(const char* p) { return *(const LAS bf16x8*)(LAS const char*)p; }
DEV s16x4 ldstr(const char* p) { return __builtin_amdgcn_ds_read_tr16_b64_v4i16((LAS s16x4*)p); }
DEV bf16x8 cat(s16x4 a, s16x4 b) { return __builtin_shufflevector(a, b, 0, 1, 2, 3, 4, 5, 6, 7); }
DEV float xsum(float v) { v += __shfl_xor(v, 16); v += __shfl_xor(v, 32); return v; }
DEV float xmax(float v) { v = fmaxf(v, __shfl_xor(v, 16)); v = fmaxf(v, __shfl_xor(v, 32)); return v; }
DEV unsigned uni(unsigned v) { return __builtin_amdgcn_readfirstlane(v); }
#define WT 31
template <bool W> DEV void st16(char* p, u32x4 v) {
  if constexpr (W) asm volatile("global_store_dwordx4 %0, %1, off sc0 sc1\n\ts_nop 1" ::"v"(p), "v"(v) : "memory");
  else *(u32x4*)p = v;
}
template <bool W> DEV void st8(char* p, u32x2 v) {
  if constexpr (W) asm volatile("global_store_dwordx2 %0, %1, off sc0 sc1" ::"v"(p), "v"(v) : "memory");
  else *(u32x2*)p = v;
}

#define TL0
#define TL1(k)
#define TLARG
#define TLPASS
#define STAMP(n)
#define STDECL
#define STOUT
template <int A, int B> struct Min { static constexpr int v = A < B ? A : B; };

template <int KT, int NK, int NS, int P, class Issue, class Compute>
DEV void pipe_steps(Issue& issue, Compute& compute) {
  if constexpr (KT < NK) {
    wait_bar<(Min<KT + NS - 1, NK>::v - KT - 1) * P>();
    if constexpr (KT + NS - 1 < NK) issue((KT + NS - 1) % NS);
    compute(KT % NS, KT);
    pipe_steps<KT + 1, NK, NS, P>(issue, compute);
  }
}

constexpr int K1_STAGE = 16384 + 12288, K1_NS = 5;
constexpr int K1_LDS = K1_STAGE * K1_NS + 1024 + 1536;
#define SB __builtin_amdgcn_sched_barrier(0)
DEV float xsum2(float v) {
  auto a = __builtin_amdgcn_permlane16_swap(__float_as_uint(v), __float_as_uint(v), false, false);
  v = __uint_as_float(a[0]) + __uint_as_float(a[1]);
  auto b = __builtin_amdgcn_permlane32_swap(__float_as_uint(v), __float_as_uint(v), false, false);
  return __uint_as_float(b[0]) + __uint_as_float(b[1]);
}

__global__ __launch_bounds__(512) void k1_qkv(const char* __restrict__ x, const char* __restrict__ w,
                                              char* __restrict__ qkv, char* __restrict__ flags) {
  extern __shared__ __attribute__((aligned(16))) char smem[];
  TL0;
  STDECL;
  const int tid = threadIdx.x, lane = tid & 63, wv = uni(tid >> 6);
  const int g = lane >> 4, i = lane & 15, q4 = (lane >> 2) & 3, p4 = lane & 3;
  const int tg = wv & 3, nh = wv >> 2;
  const int b = blockIdx.x, xcd = b & 7, bi = b >> 3;
  const int cb = (xcd & 1) * 8 + (bi & 7), tb = (xcd >> 1) * 4 + (bi >> 3);
  const unsigned lds0 = (unsigned)(size_t)(LAS char*)smem;

  const char* gx = x + (size_t)(tb * 128 + wv * 16) * 1024;
  const unsigned vt = (lane >> 3) * 1024 + (((lane & 7) ^ (lane >> 3)) << 4);
  const char* gw = w + cb * 192;
  unsigned vw0, vw1;
  {
    int qq = wv * 64 + lane;
    int k = qq / 12, c = (qq % 12) ^ (((k >> 3) & 1) << 1);
    vw0 = k * 3072 + c * 16;
    qq = 512 + wv * 32 + (lane & 31);
    k = qq / 12; c = (qq % 12) ^ (((k >> 3) & 1) << 1);
    vw1 = k * 3072 + c * 16;
  }
  const int k0 = bi & 7;
  auto dma = [&](int s, int j) {
    const int kk = (k0 + s) & 7;
    const unsigned base = lds0 + (s % K1_NS) * K1_STAGE;
    if (j == 0) dma16s(vt, gx + kk * 128, base + wv * 2048);
    else if (j == 1) dma16s(vt, gx + kk * 128 + 8192, base + wv * 2048 + 1024);
    else if (j == 2) dma16s(vw0, gw + kk * (64 * 3072), base + 16384 + wv * 1024);
    else dma16sm(vw1, gw + kk * (64 * 3072), base + 16384 + 8192 + wv * 512, 0xFFFFFFFFull);
  };
#pragma unroll
  for (int s = 0; s < K1_NS - 1; ++s)
#pragma unroll
    for (int j = 0; j < 4; ++j) dma(s, j);

  f32x4 acc[2][3], cs = f32x4{0, 0, 0, 0}, s1v = f32x4{0, 0, 0, 0};
#pragma unroll
  for (int a = 0; a < 3; ++a) { acc[0][a] = f32x4{0, 0, 0, 0}; acc[1][a] = f32x4{0, 0, 0, 0}; }
  float s2a = 0.f, s2b = 0.f;
  const bf16x8 ones = {0x3F80, 0x3F80, 0x3F80, 0x3F80, 0x3F80, 0x3F80, 0x3F80, 0x3F80};
  int toff[2][2];
#pragma unroll
  for (int tt = 0; tt < 2; ++tt) {
    toff[tt][0] = (tg * 32 + (tt ^ nh) * 16 + i) * 128 + ((g ^ (i & 7)) << 4);
    toff[tt][1] = (tg * 32 + (tt ^ nh) * 16 + i) * 128 + (((4 + g) ^ (i & 7)) << 4);
  }
  int woff[3], un[3];
#pragma unroll
  for (int nt = 0; nt < 3; ++nt) {
    int a = nt + tg; a = a >= 3 ? a - 3 : a; a = a >= 3 ? a - 3 : a;
    un[nt] = nh * 3 + a;
    woff[nt] = 16384 + (8 * g + q4) * 192 + ((un[nt] ^ (g & 1)) << 5) + p4 * 8;
  }
  auto ldB = [&](int tt, int kt, int ks) -> bf16x8 { return lds128(smem + (kt % K1_NS) * K1_STAGE + toff[tt][ks]); };
  auto ldA = [&](int nt, int kt, int ks) -> bf16x8 {
    const char* p = smem + (kt % K1_NS) * K1_STAGE + woff[nt] + ks * 6144;
    return cat(ldstr(p), ldstr(p + 768));
  };
  bf16x8 A0[3], A1[3], B0[2], B1[2];
  auto phase = [&](bf16x8 (&Ac)[3], bf16x8 (&Bc)[2], bf16x8 (&An)[3], bf16x8 (&Bn)[2], int kn, int ksn, int dk, int dj) {
    const u32x4 u = __builtin_bit_cast(u32x4, Bc[0]);
#pragma unroll
    for (int m = 0; m < 8; ++m) {
      if (kn < 8) {
        if (m == 0) { Bn[0] = ldB(0, kn, ksn); An[0] = ldA(0, kn, ksn); }
        else if (m == 1) Bn[1] = ldB(1, kn, ksn);
        else if (m < 4) An[m - 1] = ldA(m - 1, kn, ksn);
      }
      if (m < 6) acc[m & 1][m >> 1] = mfma16(Ac[m >> 1], Bc[m & 1], acc[m & 1][m >> 1]);
      else if (m == 6) cs = mfma16(Ac[0], ones, cs);
      else s1v = mfma16(ones, Bc[0], s1v);
      {
        float f = (m & 1) ? bfhi(u[m >> 1]) : bflo(u[m >> 1]);
        asm volatile("" : "+v"(f));
        if (m & 1) s2b = __builtin_fmaf(f, f, s2b); else s2a = __builtin_fmaf(f, f, s2a);
      }
      if (dk < 8 && (m == 2 || m == 5)) dma(dk, dj + (m == 5));
      SB;
    }
  };
  wait_bar<12>();
  STAMP(0);
  B0[0] = ldB(0, 0, 0); A0[0] = ldA(0, 0, 0); B0[1] = ldB(1, 0, 0); A0[1] = ldA(1, 0, 0); A0[2] = ldA(2, 0, 0);
#pragma unroll
  for (int kt = 0; kt < 8; ++kt) {
    phase(A0, B0, A1, B1, kt, 1, kt + 4, 0);
    if (kt < 4) wait_bar<10>();
    else if (kt == 4) wait_bar<8>();
    else if (kt == 5) wait_bar<4>();
    else if (kt == 6) wait_bar<0>();
    if (kt == 0) STAMP(1);
    if (kt == 3) STAMP(2);
    if (kt == 6) STAMP(3);
    phase(A1, B1, A0, B0, kt + 1, 0, kt + 4, 2);
  }
  STAMP(4);

  float* st = (float*)(smem + K1_STAGE * K1_NS);
  float* csp = (float*)(smem + K1_STAGE * K1_NS + 1024);
  const float s2 = xsum2(s2a + s2b);
  if (g == 0) {
    float mean = s1v[0] * (1.f / 512.f);
    float var = fmaxf(s2 * (1.f / 512.f) - mean * mean, 0.f);
    float rstd = rsqrtf(var + LN_EPS);
    *(f32x2*)(st + (tg * 32 + nh * 16 + i) * 2) = f32x2{-mean * rstd, rstd};
  }
  if (i == 0 && tg < 3) *(f32x4*)(csp + un[0] * 16 + 4 * g) = cs;
  bar();
  f32x2 ms[2];
#pragma unroll
  for (int tt = 0; tt < 2; ++tt) ms[tt] = *(const f32x2*)(st + (tg * 32 + (tt ^ nh) * 16 + i) * 2);
#pragma unroll
  for (int nt = 0; nt < 3; ++nt) {
    f32x4 c = *(const f32x4*)(csp + un[nt] * 16 + 4 * g);
    int n0 = cb * 96 + un[nt] * 16;
    char* dst = qkv + (size_t)(n0 >> 4) * (2048 * 32) + g * 8;
#pragma unroll
    for (int tt = 0; tt < 2; ++tt) {
      f32x4 y = acc[tt][nt] * ms[tt][1] + ms[tt][0] * c;
      u32x2 o = {pack2(y[0], y[1]), pack2(y[2], y[3])};
      st8<(WT & 1) != 0>(dst + (size_t)(tb * 128 + tg * 32 + (tt ^ nh) * 16 + i) * 32, o);
    }
  }
  if (b == 0 && tid < 384) st16<true>(flags + tid * 16, u32x4{0, 0, 0, 0});
  STAMP(5);
  STOUT;
  TL1(0);
}

constexpr int S0 = 0, S1 = 49152, S2 = 98304, CSP = 147456, MG_LDS = 163840;
#define MS(n)
#define MSDECL
#define MSOUT
#define MSARG
#define MSPASS
DEV void flag_set(unsigned* f) {
  unsigned one = 1;
  asm volatile("global_store_dword %0, %1, off sc0 sc1" ::"v"(f), "v"(one) : "memory");
}
DEV void flag_wait(const unsigned* f16, int lane) {
  const unsigned* p = f16 + (lane & 15);
  for (int it = 0; it < (1 << 20); ++it) {
    unsigned c;
    asm volatile("global_load_dword %0, %1, off sc0 sc1\n\ts_waitcnt vmcnt(0)" : "=v"(c) : "v"(p) : "memory");
    if (__builtin_amdgcn_ballot_w64(c != 1u) == 0) break;
  }
}
template <int V> struct IC { static constexpr int value = V; };
DEV int opaque(int v) { asm volatile("" : "+v"(v)); return v; }
DEV int opaque_s(int v) { asm volatile("" : "+s"(v)); return v; }

struct CW {
  const char *g1, *g3; unsigned vw; unsigned long long wmask;
};
DEV CW c_setup(const char* w1, const char* w3, int b, int wv, int lane) {
  const int xcd = b & 7, bi = b >> 3;
  const int cb = (xcd & 3) * 4 + (bi & 3);
  CW c;
  c.g1 = w1 + (size_t)(wv * 8) * (F_ * 2) + cb * 224;
  c.g3 = w3 + (size_t)(wv * 8) * (F_ * 2) + cb * 224;
  const int unit = ((lane >> 1) & 7) ^ ((lane >> 4) | ((wv & 1) << 2));
  c.vw = (lane >> 4) * (F_ * 2) + unit * 32 + (lane & 1) * 16;
  c.wmask = __builtin_amdgcn_ballot_w64(unit < 7);
  return c;
}
DEV void c_dmaw(const CW& c, unsigned ldsw, int kt, int j) {
  const unsigned base = ldsw + (kt % 3) * 49152;
  if (j < 2) dma16sm(c.vw, c.g1 + (size_t)(kt * 64 + j * 4) * (F_ * 2), base + 16384 + j * 1024, c.wmask);
  else dma16sm(c.vw, c.g3 + (size_t)(kt * 64 + (j - 2) * 4) * (F_ * 2), base + 32768 + (j - 2) * 1024, c.wmask);
}
DEV constexpr int d_unit(int u) {
  return u < 3 ? S2 + u * 2048 : u < 6 ? S0 + (u - 3) * 2048 : u < 9 ? S1 + (u - 6) * 2048 : u == 9 ? CSP
       : u < 13 ? S2 + (u - 10) * 2048 : S0;
}
DEV constexpr int d_ustr(int u) { return u == 9 ? 2048 : 6144; }
DEV void d_dma(const char* gw2, unsigned vw, unsigned lds0, int wv, int u, int q) {
  dma16s(vw, gw2 + (size_t)(u * 16 + q * 8) * 1024, lds0 + d_unit(u) + wv * d_ustr(u) + q * 1024);
}

template <int NT>
DEV void c_body(char* smem, unsigned lds0, const char* gt, const CW& cw, unsigned vt, int wv, int lane,
                const char* __restrict__ stats, char* __restrict__ hh, int tok0, int cb, const char* gw2, unsigned vwd MSARG) {
  const int tg = wv & 3;
  const unsigned ldsw = lds0 + wv * 2048;
  const int g = lane >> 4, i = lane & 15, q4 = (lane >> 2) & 3, p4 = lane & 3;
  f32x4 acc[2][2 * NT], csg = f32x4{0, 0, 0, 0}, csu = f32x4{0, 0, 0, 0};
#pragma unroll
  for (int a = 0; a < 2 * NT; ++a) { acc[0][a] = f32x4{0, 0, 0, 0}; acc[1][a] = f32x4{0, 0, 0, 0}; }
  const bf16x8 ones = {0x3F80, 0x3F80, 0x3F80, 0x3F80, 0x3F80, 0x3F80, 0x3F80, 0x3F80};
  const int toff0 = (tg * 32 + i) * 128 + ((g ^ (i & 7)) << 4);
  const int toff1 = (tg * 32 + i) * 128 + (((4 + g) ^ (i & 7)) << 4);
  const int odd = cb & 1;
  const int fsw = q4 | ((g & 1) << 2);
  int T[NT], wa[NT];
#pragma unroll
  for (int a = 0; a < NT; ++a) {
    T[a] = NT == 4 ? odd * 3 + a : (a < 2 ? (odd ? 1 : 4) + a : (odd ? 0 : 6));
    wa[a] = 16384 + (8 * g + q4) * 256 + ((T[a] ^ fsw) << 5) + p4 * 8;
  }
  auto dmat = [&](int kt, int j) { dma16s(vt, gt + kt * 128 + j * 8192, ldsw + (kt % 3) * 49152 + j * 1024); };
  auto dma = [&](int kt, int j) {
    if (j < 2) dmat(kt, j); else c_dmaw(cw, ldsw, kt, j - 2);
  };
  auto ldB = [&](bf16x8* B, int kt, int ks) {
    const char* sb = smem + (kt % 3) * 49152 + (ks ? toff1 : toff0);
    B[0] = lds128(sb); B[1] = lds128(sb + 2048);
  };
  auto ldA = [&](int a, int kt, int ks) -> bf16x8 {
    const char* wp = smem + (kt % 3) * 49152 + wa[a < NT ? a : a - NT] + (a < NT ? 0 : 16384) + ks * 8192;
    return cat(ldstr(wp), ldstr(wp + 1024));
  };
  auto mma = [&](int a, bf16x8 A, const bf16x8* B) {
    acc[0][a] = mfma16(A, B[0], acc[0][a]);
    acc[1][a] = mfma16(A, B[1], acc[1][a]);
  };
  auto csum = [&](const bf16x8* A) {
    if (tg == 0) { csg = mfma16(A[0], ones, csg); csu = mfma16(A[NT], ones, csu); }
    else if (tg == 1) { csg = mfma16(A[1], ones, csg); csu = mfma16(A[NT + 1], ones, csu); }
    else if (tg == 2) { csg = mfma16(A[2], ones, csg); csu = mfma16(A[NT + 2], ones, csu); }
    else if (NT == 4) { csg = mfma16(A[NT - 1], ones, csg); csu = mfma16(A[2 * NT - 1], ones, csu); }
  };
  bf16x8 A0[2 * NT], A1[2 * NT], B0[2], B1[2];
  bf16x8 sa[2];
#pragma unroll
  for (int kt = 0; kt < 3; ++kt) { dmat(kt, 0); dmat(kt, 1); }
  wait_bar<4>();
  MS(10);
  ldB(B0, 0, 0);
#pragma unroll
  for (int a = 0; a < 2 * NT; ++a) A0[a] = ldA(a, 0, 0);
#pragma unroll
  for (int kt = 0; kt < 8; ++kt) {
    ldB(B1, kt, 1);
#pragma unroll
    for (int a = 0; a < 2 * NT; ++a) {
      if (a < NT) { A1[2 * a] = ldA(2 * a, kt, 1); A1[2 * a + 1] = ldA(2 * a + 1, kt, 1); }
      mma(a, A0[a], B0);
      __builtin_amdgcn_sched_barrier(0);
    }
    csum(A0);
    if (kt == 0) wait_bar<2>();
    else if (kt < 7) wait_bar<6>();
    else asm volatile("s_waitcnt vmcnt(6) lgkmcnt(0)\n\ts_barrier" : "+v"(sa[0]), "+v"(sa[1]) : : "memory");
    if (kt == 0) MS(11); if (kt == 3) MS(12); if (kt == 6) MS(13);
    if (kt < 7) ldB(B0, kt + 1, 0);
    if (kt == 6) {
      gld16(sa[0], (unsigned)(i * 64 + g * 16), stats + (size_t)(tok0 + tg * 32) * 64);
      gld16(sa[1], (unsigned)(i * 64 + g * 16), stats + (size_t)(tok0 + tg * 32 + 16) * 64);
    }
    if (kt == 7) csum(A1);
#pragma unroll
    for (int a = 0; a < 2 * NT; ++a) {
      if (kt < 7 && a < NT) { A0[2 * a] = ldA(2 * a, kt + 1, 0); A0[2 * a + 1] = ldA(2 * a + 1, kt + 1, 0); }
      mma(a, A1[a], B1);
      if (a >= 2 * NT - 6) {
        const int j = a - (2 * NT - 6);
        if (kt + 3 < 8) dma(kt + 3, j);
        else d_dma(gw2, vwd, lds0, wv, (kt - 5) * 3 + (j >> 1), j & 1);
      }
      __builtin_amdgcn_sched_barrier(0);
    }
    if (kt < 7) csum(A1);
  }
  MS(14);
  float* csp = (float*)(smem + CSP);
  if (i == 0 && tg < NT) {
    const int tt_ = NT == 4 ? odd * 3 + tg : (tg < 2 ? (odd ? 1 : 4) + tg : (odd ? 0 : 6));
    *(f32x4*)(csp + tt_ * 16 + 4 * g) = csg;
    *(f32x4*)(csp + (7 + tt_) * 16 + 4 * g) = csu;
  }
  float rs[2], mr[2];
#pragma unroll
  for (int tt = 0; tt < 2; ++tt) {
    const f32x4 sv = __builtin_bit_cast(f32x4, sa[tt]);
    float s1 = sv[0] + sv[2], s2 = sv[1] + sv[3];
    s1 = xsum(s1); s2 = xsum(s2);
    float mean = s1 * (1.f / 512.f);
    float var = fmaxf(s2 * (1.f / 512.f) - mean * mean, 0.f);
    rs[tt] = rsqrtf(var + LN_EPS); mr[tt] = mean * rs[tt];
  }
  bar();
  MS(15);
  unsigned pk[2][NT][2];
#pragma unroll
  for (int tt = 0; tt < 2; ++tt) {
    char* dst = hh + ((size_t)((tok0 >> 4) + tg * 2 + tt) * 56) * 1024 + lane * 16;
#pragma unroll
    for (int a = 0; a < NT; ++a) {
      f32x4 cg = *(const f32x4*)(csp + T[a] * 16 + 4 * g);
      f32x4 cu = *(const f32x4*)(csp + (7 + T[a]) * 16 + 4 * g);
      float r[4];
#pragma unroll
      for (int e = 0; e < 4; ++e) {
        float ga = acc[tt][a][e] * rs[tt] - mr[tt] * cg[e];
        float up = acc[tt][NT + a][e] * rs[tt] - mr[tt] * cu[e];
        float sg = __builtin_amdgcn_rcpf(1.f + __builtin_amdgcn_exp2f(-1.4426950408889634f * ga));
        r[e] = ga * sg * up;
      }
      pk[tt][a][0] = pack2(r[0], r[1]); pk[tt][a][1] = pack2(r[2], r[3]);
      const int G = cb * 7 + T[a];
      if (a & 1) {
        st16<true>(dst + (size_t)(G >> 1) * 1024, u32x4{pk[tt][a - 1][0], pk[tt][a - 1][1], pk[tt][a][0], pk[tt][a][1]});
      } else if (a == NT - 1) {
        st8<true>(dst + (size_t)(G >> 1) * 1024 + (G & 1) * 8, u32x2{pk[tt][a][0], pk[tt][a][1]});
      }
    }
  }
}

__global__ __launch_bounds__(512) void k_mega(const char* __restrict__ x, const char* __restrict__ wo,
                                              const char* __restrict__ w1, const char* __restrict__ w3,
                                              const char* __restrict__ w2, char* __restrict__ ws, char* __restrict__ out) {
  extern __shared__ __attribute__((aligned(16))) char smem[];
  MSDECL;
  MS(0);
  const char* qkv = ws;
  char* o = ws + (6u << 20);
  char* h = ws + (8u << 20);
  char* stats = ws + (10u << 20);
  unsigned* flags = (unsigned*)(ws + (10u << 20) + (512u << 10));
  char* hh = ws + (11u << 20);
  const int b = blockIdx.x;
  const unsigned lds0 = (unsigned)(size_t)(LAS char*)smem;
  const int cbG = (b >> 3) & 7, tbG = (b & 7) * 4 + (b >> 6);
  const int cbC = (b & 3) * 4 + ((b >> 3) & 3), tbC = ((b >> 2) & 1) * 8 + (b >> 5);

  {
    const int tid = threadIdx.x, lane = tid & 63, wv = uni(tid >> 6);
    const int g = lane >> 4, i = lane & 15, q4 = (lane >> 2) & 3, p4 = lane & 3;
    const int hp = (b & 7) * 2 + ((b >> 3) & 1), qb = b >> 4;
    const int q0 = qb * 128;
    const size_t HS = 2048 * 32;
    {
      const int r = wv >> 1;
      const char* src = qkv + (size_t)((1 + (r >> 1)) * 32 + hp * 2 + (r & 1)) * HS;
#pragma unroll
      for (int j = 0; j < 6; ++j) {
        int ch = ((wv & 1) * 6 + j) * 64 + lane;
        int key = q0 - 256 + (ch >> 1);
        key = key < 0 ? 0 : key;
        dma16(src + (size_t)key * 32 + (ch & 1) * 16, lds0 + r * 12288 + ((wv & 1) * 6 + j) * 1024);
      }
    }
    const int qs = q0 + wv * 16;
    bf16x8 Q[2];
    gld16(Q[0], (unsigned)(i * 32 + (g & 1) * 16), qkv + (size_t)(hp * 2) * HS + (size_t)qs * 32);
    gld16(Q[1], (unsigned)(i * 32 + (g & 1) * 16), qkv + (size_t)(hp * 2 + 1) * HS + (size_t)qs * 32);
    {
      const unsigned vw = (lane >> 3) * 1024 + ((((lane & 7) >> 1) ^ ((lane >> 4) & 3)) << 5) + (lane & 1) * 16;
      const char* gw = wo + (size_t)(wv * 64) * 1024 + cbG * 128;
#pragma unroll
      for (int q = 0; q < 8; ++q) dma16s(vw, gw + (size_t)(q * 8) * 1024, lds0 + S2 + wv * 8192 + q * 1024);
      const CW cw = c_setup(w1, w3, b, wv, lane);
#pragma unroll
      for (int j = 0; j < 4; ++j) c_dmaw(cw, lds0 + wv * 2048, 1, j);
      const int c0 = b * 8 + wv;
      const unsigned sc = lds0 + S1 + wv * 1024;
      const char* p1 = c0 < 256 ? w1 + (size_t)(c0 + 1536) * 1024 : w3 + (size_t)(c0 - 256) * 1024;
      const char* p0 = w1 + (size_t)(c0 < 1536 ? c0 : c0 - 1536) * 1024;
      const char* p2 = w2 + (size_t)(c0 < 1792 ? c0 : c0 - 1792) * 1024;
      dma16s(lane * 16, p0, sc); dma16s(lane * 16, p1, sc); dma16s(lane * 16, p2, sc);
    }
    asm volatile("s_waitcnt vmcnt(15)\n\ts_barrier" : "+v"(Q[0]), "+v"(Q[1]) : : "memory");
    MS(1);
    if (g >= 2) { Q[0] = bf16x8{0, 0, 0, 0, 0, 0, 0, 0}; Q[1] = bf16x8{0, 0, 0, 0, 0, 0, 0, 0}; }
    const float c = 0.25f * 1.4426950408889634f;
    const float NEG = -1e30f;
    const int lrow = wv * 16;
    f32x4 cin0, cin16;
#pragma unroll
    for (int r = 0; r < 4; ++r) { cin0[r] = (4 * g + r > i) ? 0.f : NEG; cin16[r] = (4 * g + r <= i) ? 0.f : NEG; }
    const bf16x8 ones = {0x3F80, 0x3F80, 0x3F80, 0x3F80, 0x3F80, 0x3F80, 0x3F80, 0x3F80};
    u32x4 ov;
#pragma unroll
    for (int u = 0; u < 2; ++u) {
      const char* Kl = smem + u * 12288;
      const char* Vl = smem + 24576 + u * 12288;
      f32x4 s[17];
#pragma unroll
      for (int t = 0; t < 17; ++t) {
        bf16x8 A = lds128(Kl + (lrow + t * 16 + i) * 32 + (g & 1) * 16);
        s[t] = mfma16(A, Q[u], t == 0 ? cin0 : t == 16 ? cin16 : f32x4{0, 0, 0, 0});
      }
      if (qs < 256) {
        asm volatile("" ::: "memory");
        const int nneg = (256 - qs) >> 4;
#pragma unroll
        for (int t = 0; t < 16; ++t)
          if (t < nneg) s[t] = f32x4{NEG, NEG, NEG, NEG};
      }
      float m = s[16][0];
#pragma unroll
      for (int t = 0; t < 17; ++t)
#pragma unroll
        for (int r = 0; r < 4; ++r) m = fmaxf(m, s[t][r]);
      m = xmax(m);
      const f32x2 c2 = {c, c}, m2 = {-m * c, -m * c};
      unsigned pk[17][2];
#pragma unroll
      for (int t = 0; t < 17; ++t) {
        f32x2 a0 = __builtin_elementwise_fma(f32x2{s[t][0], s[t][1]}, c2, m2);
        f32x2 a1 = __builtin_elementwise_fma(f32x2{s[t][2], s[t][3]}, c2, m2);
        pk[t][0] = pack2(__builtin_amdgcn_exp2f(a0[0]), __builtin_amdgcn_exp2f(a0[1]));
        pk[t][1] = pack2(__builtin_amdgcn_exp2f(a1[0]), __builtin_amdgcn_exp2f(a1[1]));
      }
      f32x4 oacc = f32x4{0, 0, 0, 0}, lsum = f32x4{0, 0, 0, 0};
#pragma unroll
      for (int tp = 0; tp < 9; ++tp) {
        const int t0 = 2 * tp, t1 = (2 * tp + 1 < 17) ? 2 * tp + 1 : 16;
        bf16x8 A = cat(ldstr(Vl + (lrow + t0 * 16 + 4 * g + q4) * 32 + p4 * 8), ldstr(Vl + (lrow + t1 * 16 + 4 * g + q4) * 32 + p4 * 8));
        u32x4 pb = {pk[t0][0], pk[t0][1], (2 * tp + 1 < 17) ? pk[t1][0] : 0u, (2 * tp + 1 < 17) ? pk[t1][1] : 0u};
        oacc = mfma16(A, __builtin_bit_cast(bf16x8, pb), oacc);
        lsum = mfma16(ones, __builtin_bit_cast(bf16x8, pb), lsum);
      }
      const float inv = __builtin_amdgcn_rcpf(lsum[0]);
      ov[2 * u] = pack2(oacc[0] * inv, oacc[1] * inv);
      ov[2 * u + 1] = pack2(oacc[2] * inv, oacc[3] * inv);
    }
    st16<true>(o + ((size_t)(qs >> 4) * 16 + hp) * 1024 + lane * 16, ov);
    MS(2);
    asm volatile("s_waitcnt vmcnt(0) lgkmcnt(0)\n\ts_barrier" ::: "memory");
    if (tid == 0) flag_set(flags + qb * 32 + hp);
    {
      const CW cw = c_setup(w1, w3, b, wv, lane);
#pragma unroll
      for (int j = 0; j < 4; ++j) c_dmaw(cw, lds0 + wv * 2048, 0, j);
    }
    if (wv == 0) flag_wait(flags + (tbG >> 1) * 32, lane);
    asm volatile("s_barrier" ::: "memory");
    MS(3);
  }
  {
    const int tid = opaque(threadIdx.x), lane = tid & 63, wv = uni(tid >> 6);
    const int g = lane >> 4, i = lane & 15, q4 = (lane >> 2) & 3, p4 = lane & 3;
    const int kp = wv & 3, th = wv >> 2;
    bf16x8 B[4][2], rs;
    const char* ga = o + ((size_t)(tbG * 4 + th * 2) * 16 + kp * 4) * 1024;
#pragma unroll
    for (int s = 0; s < 4; ++s) {
      gld16(B[s][0], lane * 16, ga + s * 1024);
      gld16(B[s][1], lane * 16, ga + (16 + s) * 1024);
    }
    const size_t ooff = (size_t)(tbG * 64 + wv * 8) * 1024 + cbG * 128;
    const unsigned vo = (lane >> 3) * 1024 + (lane & 7) * 16;
    gld16(rs, vo, x + ooff);
    const int sw = (2 * g + (q4 >> 1)) & 3;
    const char* wr = smem + S2 + kp * 16384 + (4 * g + q4) * 128 + p4 * 8;
    bf16x8 A[4][4];
#pragma unroll
    for (int s = 0; s < 4; ++s)
#pragma unroll
      for (int nt = 0; nt < 4; ++nt) {
        const char* p = wr + s * 4096 + ((nt ^ sw) << 5);
        A[s][nt] = cat(ldstr(p), ldstr(p + 2048));
      }
    f32x4 acc[2][4];
#pragma unroll
    for (int t = 0; t < 2; ++t)
#pragma unroll
      for (int n = 0; n < 4; ++n) acc[t][n] = f32x4{0, 0, 0, 0};
    asm volatile("s_waitcnt vmcnt(7)" : "+v"(B[0][0]), "+v"(B[0][1]) : : "memory");
#pragma unroll
    for (int nt = 0; nt < 4; ++nt) { acc[0][nt] = mfma16(A[0][nt], B[0][0], acc[0][nt]); acc[1][nt] = mfma16(A[0][nt], B[0][1], acc[1][nt]); }
    asm volatile("s_waitcnt vmcnt(5)" : "+v"(B[1][0]), "+v"(B[1][1]) : : "memory");
#pragma unroll
    for (int nt = 0; nt < 4; ++nt) { acc[0][nt] = mfma16(A[1][nt], B[1][0], acc[0][nt]); acc[1][nt] = mfma16(A[1][nt], B[1][1], acc[1][nt]); }
    asm volatile("s_waitcnt vmcnt(3)" : "+v"(B[2][0]), "+v"(B[2][1]) : : "memory");
#pragma unroll
    for (int nt = 0; nt < 4; ++nt) { acc[0][nt] = mfma16(A[2][nt], B[2][0], acc[0][nt]); acc[1][nt] = mfma16(A[2][nt], B[2][1], acc[1][nt]); }
    asm volatile("s_waitcnt vmcnt(1)" : "+v"(B[3][0]), "+v"(B[3][1]) : : "memory");
#pragma unroll
    for (int nt = 0; nt < 4; ++nt) { acc[0][nt] = mfma16(A[3][nt], B[3][0], acc[0][nt]); acc[1][nt] = mfma16(A[3][nt], B[3][1], acc[1][nt]); }
    bar();
    {
      const int mo = S2 + kp * 16384 + g * 256 + ((i ^ ((g >> 1) * 2)) << 4);
#pragma unroll
      for (int tt = 0; tt < 2; ++tt)
#pragma unroll
        for (int nt = 0; nt < 4; ++nt)
          *(LAS f32x4*)(LAS char*)(smem + (mo ^ (nt * 64)) + ((th * 2 + tt) * 4 + nt) * 1024) = acc[tt][nt];
    }
    asm volatile("s_waitcnt vmcnt(0) lgkmcnt(0)\n\ts_barrier" : "+v"(rs) : : "memory");
    {
      const int c = lane & 7;
      const int tok = (lane >> 3) + 8 * (wv & 1);
      const char* rp = smem + S2 + ((wv >> 1) * 4 + (c >> 1)) * 1024 + (c & 1) * 512 + ((tok ^ (2 * c)) << 4);
      f32x4 v0 = f32x4{0, 0, 0, 0}, v1 = f32x4{0, 0, 0, 0};
#pragma unroll
      for (int p = 0; p < 4; ++p) {
        v0 += *(const LAS f32x4*)(LAS const char*)(rp + p * 16384);
        v1 += *(const LAS f32x4*)(LAS const char*)(rp + p * 16384 + 256);
      }
      u32x4 r = __builtin_bit_cast(u32x4, rs);
      u32x4 ov = {pack2(v0[0] + bflo(r[0]), v0[1] + bfhi(r[0])), pack2(v0[2] + bflo(r[1]), v0[3] + bfhi(r[1])),
                  pack2(v1[0] + bflo(r[2]), v1[1] + bfhi(r[2])), pack2(v1[2] + bflo(r[3]), v1[3] + bfhi(r[3]))};
      st16<true>(h + ooff + vo, ov);
      float s1 = 0.f, s2 = 0.f;
#pragma unroll
      for (int e = 0; e < 4; ++e) { s1 = dot2(ov[e], 0x3F803F80u, s1); s2 = dot2(ov[e], ov[e], s2); }
#pragma unroll
      for (int m = 1; m < 8; m <<= 1) { s1 += __shfl_xor(s1, m); s2 += __shfl_xor(s2, m); }
      if (c == 0) st8<true>(stats + ((size_t)(tbG * 64 + wv * 8 + (lane >> 3)) * 8 + cbG) * 8,
                            u32x2{__float_as_uint(s1), __float_as_uint(s2)});
    }
    MS(4);
    asm volatile("s_waitcnt vmcnt(0) lgkmcnt(0)\n\ts_barrier" ::: "memory");
    if (tid == 0) flag_set(flags + 512 + (tbG >> 1) * 32 + (tbG & 1) * 8 + cbG);
    {
      const CW cw = c_setup(w1, w3, b, wv, lane);
#pragma unroll
      for (int j = 0; j < 4; ++j) c_dmaw(cw, lds0 + wv * 2048, 2, j);
    }
    if (wv == 0) flag_wait(flags + 512 + tbC * 32, lane);
    asm volatile("s_barrier" ::: "memory");
    MS(5);
  }
  const unsigned vwd_ = 0;
  {
    const int tid = opaque(threadIdx.x), lane = tid & 63, wv = uni(tid >> 6);
    const char* gt = h + (size_t)(tbC * 128 + wv * 16) * 1024;
    const unsigned vt = (lane >> 3) * 1024 + (((lane & 7) ^ (lane >> 3)) << 4);
    const CW cw = c_setup(w1, w3, b, wv, lane);
    const unsigned vwd = (lane >> 3) * 1024 + ((((lane & 7) >> 1) ^ ((lane >> 4) & 3)) << 5) + (lane & 1) * 16;
    const char* gw2 = w2 + (size_t)(wv * 224) * 1024 + cbG * 128;
    if ((wv >> 2) == 0) c_body<4>(smem, lds0, gt, cw, vt, wv, lane, stats, hh, tbC * 128, cbC, gw2, vwd MSPASS);
    else c_body<3>(smem, lds0, gt, cw, vt, wv, lane, stats, hh, tbC * 128, cbC, gw2, vwd MSPASS);
    MS(6);
  }
  {
    const int tid = opaque(threadIdx.x), lane = tid & 63, wv = uni(tid >> 6);
    const int g = lane >> 4, i = lane & 15, q4 = (lane >> 2) & 3, p4 = lane & 3;
    asm volatile("s_waitcnt vmcnt(0) lgkmcnt(0)\n\ts_barrier" ::: "memory");
    if (tid == 0) flag_set(flags + 1024 + tbC * 32 + cbC);
    const unsigned vwd = (lane >> 3) * 1024 + ((((lane & 7) >> 1) ^ ((lane >> 4) & 3)) << 5) + (lane & 1) * 16;
    const char* gw2 = w2 + (size_t)(wv * 224) * 1024 + cbG * 128;
    d_dma(gw2, vwd, lds0, wv, 9, 0); d_dma(gw2, vwd, lds0, wv, 9, 1);
    const int sw = (2 * g + (q4 >> 1)) & 3;
    const int ro = (4 * g + q4) * 128 + p4 * 8;
    int rn[4];
#pragma unroll
    for (int nt = 0; nt < 4; ++nt) rn[nt] = ro + ((nt ^ sw) << 5);
    auto lda = [&](bf16x8* Af, int j) {
      const char* p0 = smem + d_unit(2 * j) + wv * d_ustr(2 * j);
      const char* p1 = smem + d_unit(2 * j + 1) + wv * d_ustr(2 * j + 1);
#pragma unroll
      for (int nt = 0; nt < 4; ++nt) Af[nt] = cat(ldstr(p0 + rn[nt]), ldstr(p1 + rn[nt]));
    };
    bf16x8 A[2][4], B[7][4], rs;
    lda(A[0], 0); lda(A[1], 1);
    asm volatile("s_waitcnt lgkmcnt(0)" : "+v"(A[0][0]), "+v"(A[0][1]), "+v"(A[0][2]), "+v"(A[0][3]), "+v"(A[1][0]), "+v"(A[1][1]), "+v"(A[1][2]), "+v"(A[1][3]) : : "memory");
#pragma unroll
    for (int u = 10; u < 14; ++u) { d_dma(gw2, vwd, lds0, wv, u, 0); d_dma(gw2, vwd, lds0, wv, u, 1); }
    if (wv == 0) flag_wait(flags + 1024 + (tbG >> 1) * 32, lane);
    asm volatile("s_barrier" ::: "memory");
    MS(7);
    const char* ga = hh + ((size_t)(tbG * 4) * 56 + wv * 7) * 1024;
#pragma unroll
    for (int j = 0; j < 7; ++j)
#pragma unroll
      for (int tt = 0; tt < 4; ++tt) gld16(B[j][tt], lane * 16, ga + (size_t)(tt * 56 + j) * 1024);
    const size_t ooff = (size_t)(tbG * 64 + wv * 8) * 1024 + cbG * 128;
    const unsigned vo = (lane >> 3) * 1024 + (lane & 7) * 16;
    gld16(rs, vo, h + ooff);
    f32x4 acc[4][4];
#pragma unroll
    for (int t = 0; t < 4; ++t)
#pragma unroll
      for (int n = 0; n < 4; ++n) acc[t][n] = f32x4{0, 0, 0, 0};
    auto step = [&](auto J) {
      constexpr int j = decltype(J)::value;
      asm volatile("s_waitcnt vmcnt(%4)" : "+v"(B[j][0]), "+v"(B[j][1]), "+v"(B[j][2]), "+v"(B[j][3]) : "n"(25 - 4 * j) : "memory");
#pragma unroll
      for (int nt = 0; nt < 4; ++nt)
#pragma unroll
        for (int tt = 0; tt < 4; ++tt) acc[tt][nt] = mfma16(A[j & 1][nt], B[j][tt], acc[tt][nt]);
      __builtin_amdgcn_sched_barrier(0);
      if constexpr (j + 2 < 7) lda(A[j & 1], j + 2);
    };
    step(IC<0>{}); step(IC<1>{}); step(IC<2>{}); step(IC<3>{}); step(IC<4>{}); step(IC<5>{}); step(IC<6>{});
    {
      const int mo = wv * 6144 + g * 256 + ((i ^ ((g >> 1) * 2)) << 4);
#pragma unroll
      for (int tt = 0; tt < 4; ++tt)
#pragma unroll
        for (int nt = 0; nt < 4; ++nt) {
          const int t = tt * 4 + nt;
          const int base = t < 6 ? S2 + t * 1024 : t < 12 ? S0 + (t - 6) * 1024 : S1 + (t - 12) * 1024;
          *(LAS f32x4*)(LAS char*)(smem + (mo ^ (nt * 64)) + base) = acc[tt][nt];
        }
    }
    asm volatile("s_waitcnt vmcnt(0) lgkmcnt(0)\n\ts_barrier" : "+v"(rs) : : "memory");
    {
      const int c = lane & 7;
      const int tok = (lane >> 3) + 8 * (wv & 1);
      const int t = (wv >> 1) * 4 + (c >> 1);
      const int base = t < 6 ? S2 + t * 1024 : t < 12 ? S0 + (t - 6) * 1024 : S1 + (t - 12) * 1024;
      const char* rp = smem + base + (c & 1) * 512 + ((tok ^ (2 * c)) << 4);
      f32x4 v0 = f32x4{0, 0, 0, 0}, v1 = f32x4{0, 0, 0, 0};
#pragma unroll
      for (int p = 0; p < 8; ++p) {
        v0 += *(const LAS f32x4*)(LAS const char*)(rp + p * 6144);
        v1 += *(const LAS f32x4*)(LAS const char*)(rp + p * 6144 + 256);
      }
      u32x4 r = __builtin_bit_cast(u32x4, rs);
      u32x4 ov = {pack2(v0[0] + bflo(r[0]), v0[1] + bfhi(r[0])), pack2(v0[2] + bflo(r[1]), v0[3] + bfhi(r[1])),
                  pack2(v1[0] + bflo(r[2]), v1[1] + bfhi(r[2])), pack2(v1[2] + bflo(r[3]), v1[3] + bfhi(r[3]))};
      *(u32x4*)(out + ooff + vo) = ov;
    }
    MS(8);
  }
}

extern "C" void kernel_launch(void* const* d_in, const int* in_sizes, int n_in, void* d_out, int out_size,
                              void* d_ws, size_t ws_size, hipStream_t stream) {
  const char* x = (const char*)d_in[0];
  const char* wqkv = (const char*)d_in[1];
  const char* wo = (const char*)d_in[2];
  const char* w1 = (const char*)d_in[4];
  const char* w3 = (const char*)d_in[5];
  const char* w2 = (const char*)d_in[6];
  char* ws = (char*)d_ws;
  (void)hipFuncSetAttribute((const void*)k1_qkv, hipFuncAttributeMaxDynamicSharedMemorySize, K1_LDS);
  (void)hipFuncSetAttribute((const void*)k_mega, hipFuncAttributeMaxDynamicSharedMemorySize, MG_LDS);
  k1_qkv<<<256, 512, K1_LDS, stream>>>(x, wqkv, ws, ws + (10u << 20) + (512u << 10));
  k_mega<<<256, 512, MG_LDS, stream>>>(x, wo, w1, w3, w2, ws, (char*)d_out);
}
```

```cpp
#include <hip/hip_runtime.h>
#include <hip/hip_bf16.h>
#include <stdint.h>

typedef short bf16x8 __attribute__((ext_vector_type(8)));
typedef short s16x4 __attribute__((ext_vector_type(4)));
typedef float f32x4 __attribute__((ext_vector_type(4)));
typedef float f32x2 __attribute__((ext_vector_type(2)));
typedef unsigned u32x4 __attribute__((ext_vector_type(4)));
typedef unsigned u32x2 __attribute__((ext_vector_type(2)));
typedef __bf16 bf16x2 __attribute__((ext_vector_type(2)));
#define LAS __attribute__((address_space(3)))
#define DEV __device__ __forceinline__

constexpr int S_ = 2048, D_ = 512, H_ = 32, F_ = 1792;
constexpr float LN_EPS = 1e-5f;

DEV f32x4 mfma16(bf16x8 a, bf16x8 b, f32x4 c) { return __builtin_amdgcn_mfma_f32_16x16x32_bf16(a, b, c, 0, 0, 0); }
DEV unsigned pack2(float a, float b) {
  f32x2 v = {a, b};
  return __builtin_bit_cast(unsigned, __builtin_convertvector(v, bf16x2));
}
DEV float bflo(unsigned u) { return __builtin_bit_cast(float, u << 16); }
DEV float bfhi(unsigned u) { return __builtin_bit_cast(float, u & 0xffff0000u); }
DEV float dot2(unsigned a, unsigned b, float c) {
  return __builtin_amdgcn_fdot2_f32_bf16(__builtin_bit_cast(bf16x2, a), __builtin_bit_cast(bf16x2, b), c, false);
}
DEV void dma16(const char* gptr, unsigned ldsbase) {
  unsigned sv;
  asm volatile("s_mov_b32 %0, m0\n\ts_mov_b32 m0, %2\n\ts_nop 0\n\tglobal_load_lds_dwordx4 %1, off\n\ts_mov_b32 m0, %0"
               : "=&s"(sv) : "v"(gptr), "s"(ldsbase) : "memory");
}
DEV void dma16s(unsigned voff, const char* sbase, unsigned ldsbase) {
  unsigned sv;
  asm volatile("s_mov_b32 %0, m0\n\ts_mov_b32 m0, %3\n\ts_nop 0\n\tglobal_load_lds_dwordx4 %1, %2\n\ts_mov_b32 m0, %0"
               : "=&s"(sv) : "v"(voff), "s"(sbase), "s"(ldsbase) : "memory");
}
DEV void dma16sm(unsigned voff, const char* sbase, unsigned ldsbase, unsigned long long mask) {
  unsigned sv;
  asm volatile("s_mov_b32 %0, m0\n\ts_mov_b32 m0, %3\n\ts_mov_b64 exec, %4\n\tglobal_load_lds_dwordx4 %1, %2\n\ts_mov_b64 exec, -1\n\ts_mov_b32 m0, %0"
               : "=&s"(sv) : "v"(voff), "s"(sbase), "s"(ldsbase), "s"(mask) : "memory");
}
DEV void gld16(bf16x8& d, unsigned voff, const char* sbase) {
  asm volatile("global_load_dwordx4 %0, %1, %2" : "=v"(d) : "v"(voff), "s"(sbase) : "memory");
}
template <int N> DEV void wait_bar() { asm volatile("s_waitcnt vmcnt(%0) lgkmcnt(0)\n\ts_barrier" ::"n"(N) : "memory"); }
DEV void bar() { asm volatile("s_waitcnt lgkmcnt(0)\n\ts_barrier" ::: "memory"); }
DEV bf16x8 lds128(const char* p) { return *(const LAS bf16x8*)(LAS const char*)p; }
DEV s16x4 ldstr(const char* p) { return __builtin_amdgcn_ds_read_tr16_b64_v4i16((LAS s16x4*)p); }
DEV bf16x8 cat(s16x4 a, s16x4 b) { return __builtin_shufflevector(a, b, 0, 1, 2, 3, 4, 5, 6, 7); }
DEV float xsum(float v) { v += __shfl_xor(v, 16); v += __shfl_xor(v, 32); return v; }
DEV float xmax(float v) { v = fmaxf(v, __shfl_xor(v, 16)); v = fmaxf(v, __shfl_xor(v, 32)); return v; }
DEV float dpp_xor1(float v) { return __builtin_bit_cast(float, __builtin_amdgcn_mov_dpp(__builtin_bit_cast(int, v), 0xB1, 0xF, 0xF, true)); }
DEV float dpp_xor2(float v) { return __builtin_bit_cast(float, __builtin_amdgcn_mov_dpp(__builtin_bit_cast(int, v), 0x4E, 0xF, 0xF, true)); }
DEV float dpp_ror4(float v) { return __builtin_bit_cast(float, __builtin_amdgcn_mov_dpp(__builtin_bit_cast(int, v), 0x124, 0xF, 0xF, true)); }
DEV float dpp_ror8(float v) { return __builtin_bit_cast(float, __builtin_amdgcn_mov_dpp(__builtin_bit_cast(int, v), 0x128, 0xF, 0xF, true)); }
DEV unsigned uni(unsigned v) { return __builtin_amdgcn_readfirstlane(v); }
#define WT 31
template <bool W> DEV void st16(char* p, u32x4 v) {
  if constexpr (W) asm volatile("global_store_dwordx4 %0, %1, off sc0 sc1\n\ts_nop 1" ::"v"(p), "v"(v) : "memory");
  else *(u32x4*)p = v;
}
template <bool W> DEV void st8(char* p, u32x2 v) {
  if constexpr (W) asm volatile("global_store_dwordx2 %0, %1, off sc0 sc1" ::"v"(p), "v"(v) : "memory");
  else *(u32x2*)p = v;
}

#define TL0
#define TL1(k)
#define TLARG
#define TLPASS
#define STAMP(n)
#define STDECL
#define STOUT
template <int A, int B> struct Min { static constexpr int v = A < B ? A : B; };

template <int KT, int NK, int NS, int P, class Issue, class Compute>
DEV void pipe_steps(Issue& issue, Compute& compute) {
  if constexpr (KT < NK) {
    wait_bar<(Min<KT + NS - 1, NK>::v - KT - 1) * P>();
    if constexpr (KT + NS - 1 < NK) issue((KT + NS - 1) % NS);
    compute(KT % NS, KT);
    pipe_steps<KT + 1, NK, NS, P>(issue, compute);
  }
}

constexpr int K1_STAGE = 16384 + 12288, K1_NS = 5;
constexpr int K1_LDS = K1_STAGE * K1_NS + 1024 + 1536;
#define SB __builtin_amdgcn_sched_barrier(0)
DEV float xsum2(float v) {
  auto a = __builtin_amdgcn_permlane16_swap(__float_as_uint(v), __float_as_uint(v), false, false);
  v = __uint_as_float(a[0]) + __uint_as_float(a[1]);
  auto b = __builtin_amdgcn_permlane32_swap(__float_as_uint(v), __float_as_uint(v), false, false);
  return __uint_as_float(b[0]) + __uint_as_float(b[1]);
}

__global__ __launch_bounds__(512) void k1_qkv(const char* __restrict__ x, const char* __restrict__ w,
                                              char* __restrict__ qkv, char* __restrict__ flags) {
  extern __shared__ __attribute__((aligned(16))) char smem[];
  TL0;
  STDECL;
  const int tid = threadIdx.x, lane = tid & 63, wv = uni(tid >> 6);
  const int g = lane >> 4, i = lane & 15, q4 = (lane >> 2) & 3, p4 = lane & 3;
  const int tg = wv & 3, nh = wv >> 2;
  const int b = blockIdx.x, xcd = b & 7, bi = b >> 3;
  const int cb = (xcd & 1) * 8 + (bi & 7), tb = (xcd >> 1) * 4 + (bi >> 3);
  const unsigned lds0 = (unsigned)(size_t)(LAS char*)smem;

  const char* gx = x + (size_t)(tb * 128 + wv * 16) * 1024;
  const unsigned vt = (lane >> 3) * 1024 + (((lane & 7) ^ (lane >> 3)) << 4);
  const char* gw = w + cb * 192;
  unsigned vw0, vw1;
  {
    int qq = wv * 64 + lane;
    int k = qq / 12, c = (qq % 12) ^ (((k >> 3) & 1) << 1);
    vw0 = k * 3072 + c * 16;
    qq = 512 + wv * 32 + (lane & 31);
    k = qq / 12; c = (qq % 12) ^ (((k >> 3) & 1) << 1);
    vw1 = k * 3072 + c * 16;
  }
  const int k0 = bi & 7;
  auto dma = [&](int s, int j) {
    const int kk = (k0 + s) & 7;
    const unsigned base = lds0 + (s % K1_NS) * K1_STAGE;
    if (j == 0) dma16s(vt, gx + kk * 128, base + wv * 2048);
    else if (j == 1) dma16s(vt, gx + kk * 128 + 8192, base + wv * 2048 + 1024);
    else if (j == 2) dma16s(vw0, gw + kk * (64 * 3072), base + 16384 + wv * 1024);
    else dma16sm(vw1, gw + kk * (64 * 3072), base + 16384 + 8192 + wv * 512, 0xFFFFFFFFull);
  };
#pragma unroll
  for (int s = 0; s < K1_NS - 1; ++s)
#pragma unroll
    for (int j = 0; j < 4; ++j) dma(s, j);

  f32x4 acc[2][3], cs = f32x4{0, 0, 0, 0}, s1v = f32x4{0, 0, 0, 0};
#pragma unroll
  for (int a = 0; a < 3; ++a) { acc[0][a] = f32x4{0, 0, 0, 0}; acc[1][a] = f32x4{0, 0, 0, 0}; }
  float s2a = 0.f, s2b = 0.f;
  const bf16x8 ones = {0x3F80, 0x3F80, 0x3F80, 0x3F80, 0x3F80, 0x3F80, 0x3F80, 0x3F80};
  int toff[2][2];
#pragma unroll
  for (int tt = 0; tt < 2; ++tt) {
    toff[tt][0] = (tg * 32 + (tt ^ nh) * 16 + i) * 128 + ((g ^ (i & 7)) << 4);
    toff[tt][1] = (tg * 32 + (tt ^ nh) * 16 + i) * 128 + (((4 + g) ^ (i & 7)) << 4);
  }
  int woff[3], un[3];
#pragma unroll
  for (int nt = 0; nt < 3; ++nt) {
    int a = nt + tg; a = a >= 3 ? a - 3 : a; a = a >= 3 ? a - 3 : a;
    un[nt] = nh * 3 + a;
    woff[nt] = 16384 + (8 * g + q4) * 192 + ((un[nt] ^ (g & 1)) << 5) + p4 * 8;
  }
  auto ldB = [&](int tt, int kt, int ks) -> bf16x8 { return lds128(smem + (kt % K1_NS) * K1_STAGE + toff[tt][ks]); };
  auto ldA = [&](int nt, int kt, int ks) -> bf16x8 {
    const char* p = smem + (kt % K1_NS) * K1_STAGE + woff[nt] + ks * 6144;
    return cat(ldstr(p), ldstr(p + 768));
  };
  bf16x8 A0[3], A1[3], B0[2], B1[2];
  auto phase = [&](bf16x8 (&Ac)[3], bf16x8 (&Bc)[2], bf16x8 (&An)[3], bf16x8 (&Bn)[2], int kn, int ksn, int dk, int dj) {
    const u32x4 u = __builtin_bit_cast(u32x4, Bc[0]);
#pragma unroll
    for (int m = 0; m < 8; ++m) {
      if (kn < 8) {
        if (m == 0) { Bn[0] = ldB(0, kn, ksn); An[0] = ldA(0, kn, ksn); }
        else if (m == 1) Bn[1] = ldB(1, kn, ksn);
        else if (m < 4) An[m - 1] = ldA(m - 1, kn, ksn);
      }
      if (m < 6) acc[m & 1][m >> 1] = mfma16(Ac[m >> 1], Bc[m & 1], acc[m & 1][m >> 1]);
      else if (m == 6) cs = mfma16(Ac[0], ones, cs);
      else s1v = mfma16(ones, Bc[0], s1v);
      {
        float f = (m & 1) ? bfhi(u[m >> 1]) : bflo(u[m >> 1]);
        asm volatile("" : "+v"(f));
        if (m & 1) s2b = __builtin_fmaf(f, f, s2b); else s2a = __builtin_fmaf(f, f, s2a);
      }
      if (dk < 8 && (m == 2 || m == 5)) dma(dk, dj + (m == 5));
      SB;
    }
  };
  wait_bar<12>();
  STAMP(0);
  B0[0] = ldB(0, 0, 0); A0[0] = ldA(0, 0, 0); B0[1] = ldB(1, 0, 0); A0[1] = ldA(1, 0, 0); A0[2] = ldA(2, 0, 0);
#pragma unroll
  for (int kt = 0; kt < 8; ++kt) {
    phase(A0, B0, A1, B1, kt, 1, kt + 4, 0);
    if (kt < 4) wait_bar<10>();
    else if (kt == 4) wait_bar<8>();
    else if (kt == 5) wait_bar<4>();
    else if (kt == 6) wait_bar<0>();
    if (kt == 0) STAMP(1);
    if (kt == 3) STAMP(2);
    if (kt == 6) STAMP(3);
    phase(A1, B1, A0, B0, kt + 1, 0, kt + 4, 2);
  }
  STAMP(4);

  float* st = (float*)(smem + K1_STAGE * K1_NS);
  float* csp = (float*)(smem + K1_STAGE * K1_NS + 1024);
  const float s2 = xsum2(s2a + s2b);
  if (g == 0) {
    float mean = s1v[0] * (1.f / 512.f);
    float var = fmaxf(s2 * (1.f / 512.f) - mean * mean, 0.f);
    float rstd = rsqrtf(var + LN_EPS);
    *(f32x2*)(st + (tg * 32 + nh * 16 + i) * 2) = f32x2{-mean * rstd, rstd};
  }
  if (i == 0 && tg < 3) *(f32x4*)(csp + un[0] * 16 + 4 * g) = cs;
  bar();
  f32x2 ms[2];
#pragma unroll
  for (int tt = 0; tt < 2; ++tt) ms[tt] = *(const f32x2*)(st + (tg * 32 + (tt ^ nh) * 16 + i) * 2);
#pragma unroll
  for (int nt = 0; nt < 3; ++nt) {
    f32x4 c = *(const f32x4*)(csp + un[nt] * 16 + 4 * g);
    int n0 = cb * 96 + un[nt] * 16;
    char* dst = qkv + (size_t)(n0 >> 4) * (2048 * 32) + g * 8;
#pragma unroll
    for (int tt = 0; tt < 2; ++tt) {
      f32x4 y = acc[tt][nt] * ms[tt][1] + ms[tt][0] * c;
      u32x2 o = {pack2(y[0], y[1]), pack2(y[2], y[3])};
      st8<(WT & 1) != 0>(dst + (size_t)(tb * 128 + tg * 32 + (tt ^ nh) * 16 + i) * 32, o);
    }
  }
  if (tid < 24) st16<true>(flags + ((tid >> 3) * 256 + b) * 128 + (tid & 7) * 16, u32x4{0, 0, 0, 0});
  STAMP(5);
  STOUT;
  TL1(0);
}

constexpr int S0 = 0, S1 = 49152, S2 = 98304, CSP = 147456, MG_LDS = 163840;
#define MS(n)
#define MSDECL
#define MSOUT
#define MSARG
#define MSPASS
DEV void flag_set(unsigned* f) {
  unsigned one = 1;
  asm volatile("global_store_dword %0, %1, off sc0 sc1" ::"v"(f), "v"(one) : "memory");
}
#define PSLEEP 5
#define STR2(x) #x
#define STR(x) STR2(x)
DEV void flag_wait(const unsigned* p) {
  for (int it = 0; it < (1 << 20); ++it) {
    unsigned c;
    asm volatile("global_load_dword %0, %1, off sc0 sc1\n\ts_waitcnt vmcnt(0)" : "=v"(c) : "v"(p) : "memory");
    if (__builtin_amdgcn_ballot_w64(c != 1u) == 0) break;
  }
  return;
  unsigned cnt;
  asm volatile(
      "s_mov_b32 %0, 0x40000\n\t"
      "global_load_dword v252, %1, off sc0 sc1\n\ts_sleep " STR(PSLEEP) "\n\t"
      "global_load_dword v253, %1, off sc0 sc1\n\ts_sleep " STR(PSLEEP) "\n\t"
      "global_load_dword v254, %1, off sc0 sc1\n\ts_sleep " STR(PSLEEP) "\n"
      "1:\n\t"
      "global_load_dword v255, %1, off sc0 sc1\n\ts_waitcnt vmcnt(3)\n\tv_cmp_ne_u32 vcc, 1, v252\n\ts_cbranch_vccz 2f\n\ts_sleep " STR(PSLEEP) "\n\t"
      "global_load_dword v252, %1, off sc0 sc1\n\ts_waitcnt vmcnt(3)\n\tv_cmp_ne_u32 vcc, 1, v253\n\ts_cbranch_vccz 2f\n\ts_sleep " STR(PSLEEP) "\n\t"
      "global_load_dword v253, %1, off sc0 sc1\n\ts_waitcnt vmcnt(3)\n\tv_cmp_ne_u32 vcc, 1, v254\n\ts_cbranch_vccz 2f\n\ts_sleep " STR(PSLEEP) "\n\t"
      "global_load_dword v254, %1, off sc0 sc1\n\ts_waitcnt vmcnt(3)\n\tv_cmp_ne_u32 vcc, 1, v255\n\ts_cbranch_vccz 2f\n\ts_sleep " STR(PSLEEP) "\n\t"
      "s_sub_u32 %0, %0, 1\n\ts_cmp_lg_u32 %0, 0\n\ts_cbranch_scc1 1b\n"
      "2:"
      : "=&s"(cnt) : "v"(p) : "vcc", "scc", "memory", "v252", "v253", "v254", "v255");
}
template <int V> struct IC { static constexpr int value = V; };
DEV int opaque(int v) { asm volatile("" : "+v"(v)); return v; }
DEV int opaque_s(int v) { asm volatile("" : "+s"(v)); return v; }

struct CW {
  const char *g1, *g3; unsigned vw; unsigned long long wmask;
};
DEV CW c_setup(const char* w1, const char* w3, int b, int wv, int lane) {
  const int xcd = b & 7, bi = b >> 3;
  const int cb = (xcd & 3) * 4 + (bi & 3);
  CW c;
  c.g1 = w1 + (size_t)(wv * 8) * (F_ * 2) + cb * 224;
  c.g3 = w3 + (size_t)(wv * 8) * (F_ * 2) + cb * 224;
  const int unit = ((lane >> 1) & 7) ^ ((lane >> 4) | ((wv & 1) << 2));
  c.vw = (lane >> 4) * (F_ * 2) + unit * 32 + (lane & 1) * 16;
  c.wmask = __builtin_amdgcn_ballot_w64(unit < 7);
  return c;
}
DEV void c_dmaw(const CW& c, unsigned ldsw, int kt, int j) {
  const unsigned base = ldsw + (kt % 3) * 49152;
  if (j < 2) dma16sm(c.vw, c.g1 + (size_t)(kt * 64 + j * 4) * (F_ * 2), base + 16384 + j * 1024, c.wmask);
  else dma16sm(c.vw, c.g3 + (size_t)(kt * 64 + (j - 2) * 4) * (F_ * 2), base + 32768 + (j - 2) * 1024, c.wmask);
}
DEV constexpr int d_unit(int u) {
  return u < 3 ? S2 + u * 2048 : u < 6 ? S0 + (u - 3) * 2048 : u < 9 ? S1 + (u - 6) * 2048 : u == 9 ? CSP
       : u < 13 ? S2 + (u - 10) * 2048 : S0;
}
DEV constexpr int d_ustr(int u) { return u == 9 ? 2048 : 6144; }
DEV void d_dma(const char* gw2, unsigned vw, unsigned lds0, int wv, int u, int q) {
  dma16s(vw, gw2 + (size_t)(u * 16 + q * 8) * 1024, lds0 + d_unit(u) + wv * d_ustr(u) + q * 1024);
}

template <int NT>
DEV void c_body(char* smem, unsigned lds0, const char* gt, const CW& cw, unsigned vt, int wv, int lane,
                const char* __restrict__ stats, const char* __restrict__ csb, char* __restrict__ hh, int tok0, int cb, const char* gw2, unsigned vwd MSARG) {
  const int tg = wv & 3;
  const unsigned ldsw = lds0 + wv * 2048;
  const int g = lane >> 4, i = lane & 15, q4 = (lane >> 2) & 3, p4 = lane & 3;
  f32x4 acc[2][2 * NT];
#pragma unroll
  for (int a = 0; a < 2 * NT; ++a) { acc[0][a] = f32x4{0, 0, 0, 0}; acc[1][a] = f32x4{0, 0, 0, 0}; }
  const int toff0 = (tg * 32 + i) * 128 + ((g ^ (i & 7)) << 4);
  const int toff1 = (tg * 32 + i) * 128 + (((4 + g) ^ (i & 7)) << 4);
  const int odd = cb & 1;
  const int fsw = q4 | ((g & 1) << 2);
  int T[NT], wa[NT];
#pragma unroll
  for (int a = 0; a < NT; ++a) {
    T[a] = NT == 4 ? odd * 3 + a : (a < 2 ? (odd ? 1 : 4) + a : (odd ? 0 : 6));
    wa[a] = 16384 + (8 * g + q4) * 256 + ((T[a] ^ fsw) << 5) + p4 * 8;
  }
  auto dmat = [&](int kt, int j) { dma16s(vt, gt + kt * 128 + j * 8192, ldsw + (kt % 3) * 49152 + j * 1024); };
  auto dma = [&](int kt, int j) {
    if (j < 2) dmat(kt, j); else c_dmaw(cw, ldsw, kt, j - 2);
  };
  auto ldB = [&](bf16x8* B, int kt, int ks) {
    const char* sb = smem + (kt % 3) * 49152 + (ks ? toff1 : toff0);
    B[0] = lds128(sb); B[1] = lds128(sb + 2048);
  };
  auto ldA = [&](int a, int kt, int ks) -> bf16x8 {
    const char* wp = smem + (kt % 3) * 49152 + wa[a < NT ? a : a - NT] + (a < NT ? 0 : 16384) + ks * 8192;
    return cat(ldstr(wp), ldstr(wp + 1024));
  };
  auto mma = [&](int a, bf16x8 A, const bf16x8* B) {
    acc[0][a] = mfma16(A, B[0], acc[0][a]);
    acc[1][a] = mfma16(A, B[1], acc[1][a]);
  };
  bf16x8 A0[2 * NT], A1[2 * NT], B0[2], B1[2];
  bf16x8 sa[2], cp[4];
#pragma unroll
  for (int kt = 0; kt < 3; ++kt) { dmat(kt, 0); dmat(kt, 1); }
  gld16(sa[0], (unsigned)(i * 64 + g * 16), stats + (size_t)(tok0 + tg * 32) * 64);
  gld16(sa[1], (unsigned)(i * 64 + g * 16), stats + (size_t)(tok0 + tg * 32 + 16) * 64);
  const int cq = min(tg * 16 + (lane >> 2), 55);
  if (NT == 3) {
#pragma unroll
    for (int m = 0; m < 4; ++m) gld16(cp[m], (unsigned)(((lane & 3) * 4 + m) * 896 + cq * 16), csb + (size_t)cb * 14336);
  }
  constexpr int X = NT == 3 ? 6 : 2;
  wait_bar<4 + X>();
  MS(10);
  float rs[2], mr[2];
  float* csp = (float*)(smem + CSP);
  ldB(B0, 0, 0);
#pragma unroll
  for (int a = 0; a < 2 * NT; ++a) A0[a] = ldA(a, 0, 0);
#pragma unroll
  for (int kt = 0; kt < 8; ++kt) {
    ldB(B1, kt, 1);
#pragma unroll
    for (int a = 0; a < 2 * NT; ++a) {
      if (a < NT) { A1[2 * a] = ldA(2 * a, kt, 1); A1[2 * a + 1] = ldA(2 * a + 1, kt, 1); }
      mma(a, A0[a], B0);
      __builtin_amdgcn_sched_barrier(0);
    }
    if (kt == 0) wait_bar<2 + X>();
    else if (kt != 1) wait_bar<6>();
    else if (NT == 3) asm volatile("s_waitcnt vmcnt(6) lgkmcnt(0)\n\ts_barrier" : "+v"(sa[0]), "+v"(sa[1]), "+v"(cp[0]), "+v"(cp[1]), "+v"(cp[2]), "+v"(cp[3]) : : "memory");
    else asm volatile("s_waitcnt vmcnt(6) lgkmcnt(0)\n\ts_barrier" : "+v"(sa[0]), "+v"(sa[1]) : : "memory");
    if (kt == 0) MS(11); if (kt == 3) MS(12); if (kt == 6) MS(13);
    if (kt < 7) ldB(B0, kt + 1, 0);
    if (kt == 1) {
#pragma unroll
      for (int tt = 0; tt < 2; ++tt) {
        const f32x4 sv = __builtin_bit_cast(f32x4, sa[tt]);
        const float s1 = xsum2(sv[0] + sv[2]), s2 = xsum2(sv[1] + sv[3]);
        const float mean = s1 * (1.f / 512.f);
        const float var = fmaxf(s2 * (1.f / 512.f) - mean * mean, 0.f);
        rs[tt] = rsqrtf(var + LN_EPS); mr[tt] = mean * rs[tt];
      }
      if (NT == 3) {
        f32x4 c = (__builtin_bit_cast(f32x4, cp[0]) + __builtin_bit_cast(f32x4, cp[1])) +
                  (__builtin_bit_cast(f32x4, cp[2]) + __builtin_bit_cast(f32x4, cp[3]));
#pragma unroll
        for (int e = 0; e < 4; ++e) { c[e] += dpp_xor1(c[e]); c[e] += dpp_xor2(c[e]); }
        if ((lane & 3) == 0) *(f32x4*)(csp + cq * 4) = c;
      }
    }
#pragma unroll
    for (int a = 0; a < 2 * NT; ++a) {
      if (kt < 7 && a < NT) { A0[2 * a] = ldA(2 * a, kt + 1, 0); A0[2 * a + 1] = ldA(2 * a + 1, kt + 1, 0); }
      mma(a, A1[a], B1);
      if (a >= 2 * NT - 6) {
        const int j = a - (2 * NT - 6);
        if (kt + 3 < 8) dma(kt + 3, j);
        else d_dma(gw2, vwd, lds0, wv, (kt - 5) * 3 + (j >> 1), j & 1);
      }
      __builtin_amdgcn_sched_barrier(0);
    }
  }
  MS(14);
  MS(15);
  unsigned pk[2][NT][2];
#pragma unroll
  for (int tt = 0; tt < 2; ++tt) {
    char* dst = hh + ((size_t)((tok0 >> 4) + tg * 2 + tt) * 56) * 1024 + lane * 16;
#pragma unroll
    for (int a = 0; a < NT; ++a) {
      f32x4 cg = *(const f32x4*)(csp + T[a] * 16 + 4 * g);
      f32x4 cu = *(const f32x4*)(csp + (7 + T[a]) * 16 + 4 * g);
      float r[4];
#pragma unroll
      for (int e = 0; e < 4; ++e) {
        float ga = acc[tt][a][e] * rs[tt] - mr[tt] * cg[e];
        float up = acc[tt][NT + a][e] * rs[tt] - mr[tt] * cu[e];
        float sg = __builtin_amdgcn_rcpf(1.f + __builtin_amdgcn_exp2f(-1.4426950408889634f * ga));
        r[e] = ga * sg * up;
      }
      pk[tt][a][0] = pack2(r[0], r[1]); pk[tt][a][1] = pack2(r[2], r[3]);
      const int G = cb * 7 + T[a];
      if (a & 1) {
        st16<true>(dst + (size_t)(G >> 1) * 1024, u32x4{pk[tt][a - 1][0], pk[tt][a - 1][1], pk[tt][a][0], pk[tt][a][1]});
      } else if (a == NT - 1) {
        st8<true>(dst + (size_t)(G >> 1) * 1024 + (G & 1) * 8, u32x2{pk[tt][a][0], pk[tt][a][1]});
      }
    }
  }
}

__global__ __launch_bounds__(512) __attribute__((amdgpu_num_vgpr(252))) void k_mega(const char* __restrict__ x, const char* __restrict__ wo,
                                              const char* __restrict__ w1, const char* __restrict__ w3,
                                              const char* __restrict__ w2, char* __restrict__ ws, char* __restrict__ out) {
  extern __shared__ __attribute__((aligned(16))) char smem[];
  MSDECL;
  MS(0);
  const char* qkv = ws;
  char* o = ws + (6u << 20);
  char* h = ws + (8u << 20);
  char* stats = ws + (10u << 20);
  unsigned* flags = (unsigned*)(ws + (10u << 20) + (512u << 10));
  char* csb = ws + (10u << 20) + (768u << 10);
  char* hh = ws + (11u << 20);
  const int b = blockIdx.x;
  const unsigned lds0 = (unsigned)(size_t)(LAS char*)smem;
  const int cbG = (b >> 3) & 7, tbG = (b & 7) * 4 + (b >> 6);
  const int cbC = (b & 3) * 4 + ((b >> 3) & 3), tbC = ((b >> 2) & 1) * 8 + (b >> 5);

  {
    const int tid = threadIdx.x, lane = tid & 63, wv = uni(tid >> 6);
    const int g = lane >> 4, i = lane & 15, q4 = (lane >> 2) & 3, p4 = lane & 3;
    const int hp = (b & 7) * 2 + ((b >> 3) & 1), qb = b >> 4;
    const int q0 = qb * 128;
    const size_t HS = 2048 * 32;
    {
      const int r = wv >> 1;
      const char* src = qkv + (size_t)((1 + (r >> 1)) * 32 + hp * 2 + (r & 1)) * HS;
#pragma unroll
      for (int j = 0; j < 6; ++j) {
        int ch = ((wv & 1) * 6 + j) * 64 + lane;
        int key = q0 - 256 + (ch >> 1);
        key = key < 0 ? 0 : key;
        dma16(src + (size_t)key * 32 + (ch & 1) * 16, lds0 + r * 12288 + ((wv & 1) * 6 + j) * 1024);
      }
    }
    const int qs = q0 + wv * 16;
    bf16x8 Q[2], cw0, cw1;
    gld16(Q[0], (unsigned)(i * 32 + (g & 1) * 16), qkv + (size_t)(hp * 2) * HS + (size_t)qs * 32);
    gld16(Q[1], (unsigned)(i * 32 + (g & 1) * 16), qkv + (size_t)(hp * 2 + 1) * HS + (size_t)qs * 32);
    asm volatile("s_waitcnt vmcnt(0)\n\ts_barrier" : "+v"(Q[0]), "+v"(Q[1]) : : "memory");
    MS(1);
    const unsigned vwo = (lane >> 3) * 1024 + ((((lane & 7) >> 1) ^ ((lane >> 4) & 3)) << 5) + (lane & 1) * 16;
    const char* gwo = wo + (size_t)(wv * 64) * 1024 + cbG * 128;
    const CW cw1s = c_setup(w1, w3, b, wv, lane);
    auto pf = [&](int k) {
      if (k < 2) {
        const int ch = max(wv - 1, 0) * 4 + (lane & 3);
        const char* pc = (ch >= 14 ? w3 + (ch - 14) * 16 : w1 + ch * 16) + (size_t)(tbC * 32 + (lane >> 2)) * (F_ * 2) + cbC * 224;
        if (k == 0) asm volatile("global_load_dwordx4 %0, %1, off" : "=v"(cw0) : "v"(pc) : "memory");
        else asm volatile("global_load_dwordx4 %0, %1, off" : "=v"(cw1) : "v"(pc + 16 * (F_ * 2)) : "memory");
      } else if (k < 10) dma16s(vwo, gwo + (size_t)((k - 2) * 8) * 1024, lds0 + S2 + wv * 8192 + (k - 2) * 1024);
      else if (k < 14) c_dmaw(cw1s, lds0 + wv * 2048, 1, k - 10);
      else if (k == 14) {
        const int c0 = b * 8 + wv;
        dma16s(lane * 16, w2 + (size_t)(c0 < 1792 ? c0 : c0 - 1792) * 1024, lds0 + S1 + wv * 1024);
      }
    };
    if (g >= 2) { Q[0] = bf16x8{0, 0, 0, 0, 0, 0, 0, 0}; Q[1] = bf16x8{0, 0, 0, 0, 0, 0, 0, 0}; }
    const float c = 0.25f * 1.4426950408889634f;
    const float NEG = -1e30f;
    const int lrow = wv * 16;
    f32x4 cin0, cin16;
#pragma unroll
    for (int r = 0; r < 4; ++r) { cin0[r] = (4 * g + r > i) ? 0.f : NEG; cin16[r] = (4 * g + r <= i) ? 0.f : NEG; }
    const bf16x8 ones = {0x3F80, 0x3F80, 0x3F80, 0x3F80, 0x3F80, 0x3F80, 0x3F80, 0x3F80};
    u32x4 ov;
#pragma unroll
    for (int u = 0; u < 2; ++u) {
      const char* Kl = smem + u * 12288;
      const char* Vl = smem + 24576 + u * 12288;
      f32x4 s[17];
#pragma unroll
      for (int t = 0; t < 17; ++t) {
        bf16x8 A = lds128(Kl + (lrow + t * 16 + i) * 32 + (g & 1) * 16);
        s[t] = mfma16(A, Q[u], t == 0 ? cin0 : t == 16 ? cin16 : f32x4{0, 0, 0, 0});
      }
      float m = s[16][0];
#pragma unroll
      for (int t = 0; t < 17; ++t)
#pragma unroll
        for (int r = 0; r < 4; ++r) m = fmaxf(m, s[t][r]);
      m = xmax(m);
      const f32x2 c2 = {c, c}, m2 = {-m * c, -m * c};
      unsigned pk[17][2];
      auto soft = [&](int t) {
        f32x2 a0 = __builtin_elementwise_fma(f32x2{s[t][0], s[t][1]}, c2, m2);
        f32x2 a1 = __builtin_elementwise_fma(f32x2{s[t][2], s[t][3]}, c2, m2);
        pk[t][0] = pack2(__builtin_amdgcn_exp2f(a0[0]), __builtin_amdgcn_exp2f(a0[1]));
        pk[t][1] = pack2(__builtin_amdgcn_exp2f(a1[0]), __builtin_amdgcn_exp2f(a1[1]));
      };
      if (qs >= 256) {
#pragma unroll
        for (int t = 0; t < 17; ++t) {
          soft(t);
          if (u == 0 && t < 15) pf(t);
        }
      } else {
        const int nneg = (256 - qs) >> 4;
#pragma unroll
        for (int t = 0; t < 17; ++t) {
          soft(t);
          if (t < 16) { const unsigned mk = t < nneg ? 0u : ~0u; pk[t][0] &= mk; pk[t][1] &= mk; }
          if (u == 0 && t < 15) pf(t);
        }
      }
      f32x4 oacc = f32x4{0, 0, 0, 0}, lsum = f32x4{0, 0, 0, 0};
#pragma unroll
      for (int tp = 0; tp < 9; ++tp) {
        const int t0 = 2 * tp, t1 = (2 * tp + 1 < 17) ? 2 * tp + 1 : 16;
        bf16x8 A = cat(ldstr(Vl + (lrow + t0 * 16 + 4 * g + q4) * 32 + p4 * 8), ldstr(Vl + (lrow + t1 * 16 + 4 * g + q4) * 32 + p4 * 8));
        u32x4 pb = {pk[t0][0], pk[t0][1], (2 * tp + 1 < 17) ? pk[t1][0] : 0u, (2 * tp + 1 < 17) ? pk[t1][1] : 0u};
        oacc = mfma16(A, __builtin_bit_cast(bf16x8, pb), oacc);
        lsum = mfma16(ones, __builtin_bit_cast(bf16x8, pb), lsum);
      }
      const float inv = __builtin_amdgcn_rcpf(lsum[0]);
      ov[2 * u] = pack2(oacc[0] * inv, oacc[1] * inv);
      ov[2 * u + 1] = pack2(oacc[2] * inv, oacc[3] * inv);
    }
    st16<true>(o + ((size_t)(qs >> 4) * 16 + hp) * 1024 + lane * 16, ov);
    MS(2);
    asm volatile("s_waitcnt vmcnt(0) lgkmcnt(0)\n\ts_barrier" : "+v"(cw0), "+v"(cw1) : : "memory");
    MS(16);
    if (tid < 16) flag_set(flags + (((qb >> 1) | (tid << 3) | ((qb & 1) << 7)) * 32 + hp));
    {
      const CW cw = c_setup(w1, w3, b, wv, lane);
#pragma unroll
      for (int j = 0; j < 4; ++j) c_dmaw(cw, lds0 + wv * 2048, 0, j);
    }
    if (wv == 0) { flag_wait(flags + b * 32 + (lane & 15)); MS(17); }
    else {
      const u32x4 ua = __builtin_bit_cast(u32x4, cw0), ub = __builtin_bit_cast(u32x4, cw1);
      f32x4 lo, hi;
#pragma unroll
      for (int e = 0; e < 2; ++e) {
        lo[2 * e] = bflo(ua[e]) + bflo(ub[e]); lo[2 * e + 1] = bfhi(ua[e]) + bfhi(ub[e]);
        hi[2 * e] = bflo(ua[2 + e]) + bflo(ub[2 + e]); hi[2 * e + 1] = bfhi(ua[2 + e]) + bfhi(ub[2 + e]);
      }
#pragma unroll
      for (int e = 0; e < 4; ++e) {
        lo[e] += dpp_ror4(lo[e]); lo[e] += dpp_ror8(lo[e]); lo[e] = xsum2(lo[e]);
        hi[e] += dpp_ror4(hi[e]); hi[e] += dpp_ror8(hi[e]); hi[e] = xsum2(hi[e]);
      }
      if (lane < 4) {
        char* d = csb + (size_t)(cbC * 16 + tbC) * 896 + ((wv - 1) * 4 + lane) * 32;
        st16<true>(d, __builtin_bit_cast(u32x4, lo));
        st16<true>(d + 16, __builtin_bit_cast(u32x4, hi));
      }
      asm volatile("s_waitcnt vmcnt(0)" ::: "memory");
    }
    asm volatile("s_barrier" ::: "memory");
    if (tid >= 64 && tid < 80) flag_set(flags + 8192 + ((cbC >> 2) | ((tid & 1) << 2) | ((cbC & 3) << 3) | (((tid & 15) >> 1) << 5)) * 32 + 16 + tbC);
    MS(3);
  }
  {
    const int tid = opaque(threadIdx.x), lane = tid & 63, wv = uni(tid >> 6);
    const int g = lane >> 4, i = lane & 15, q4 = (lane >> 2) & 3, p4 = lane & 3;
    const int kp = wv & 3, th = wv >> 2;
    bf16x8 B[4][2], rs;
    const char* ga = o + ((size_t)(tbG * 4 + th * 2) * 16 + kp * 4) * 1024;
#pragma unroll
    for (int s = 0; s < 4; ++s) {
      gld16(B[s][0], lane * 16, ga + s * 1024);
      gld16(B[s][1], lane * 16, ga + (16 + s) * 1024);
    }
    const size_t ooff = (size_t)(tbG * 64 + wv * 8) * 1024 + cbG * 128;
    const unsigned vo = (lane >> 3) * 1024 + (lane & 7) * 16;
    gld16(rs, vo, x + ooff);
    const int sw = (2 * g + (q4 >> 1)) & 3;
    const char* wr = smem + S2 + kp * 16384 + (4 * g + q4) * 128 + p4 * 8;
    bf16x8 A[4][4];
#pragma unroll
    for (int s = 0; s < 4; ++s)
#pragma unroll
      for (int nt = 0; nt < 4; ++nt) {
        const char* p = wr + s * 4096 + ((nt ^ sw) << 5);
        A[s][nt] = cat(ldstr(p), ldstr(p + 2048));
      }
    f32x4 acc[2][4];
#pragma unroll
    for (int t = 0; t < 2; ++t)
#pragma unroll
      for (int n = 0; n < 4; ++n) acc[t][n] = f32x4{0, 0, 0, 0};
    asm volatile("s_waitcnt vmcnt(7)" : "+v"(B[0][0]), "+v"(B[0][1]) : : "memory");
    MS(22);
#pragma unroll
    for (int nt = 0; nt < 4; ++nt) { acc[0][nt] = mfma16(A[0][nt], B[0][0], acc[0][nt]); acc[1][nt] = mfma16(A[0][nt], B[0][1], acc[1][nt]); }
    asm volatile("s_waitcnt vmcnt(5)" : "+v"(B[1][0]), "+v"(B[1][1]) : : "memory");
#pragma unroll
    for (int nt = 0; nt < 4; ++nt) { acc[0][nt] = mfma16(A[1][nt], B[1][0], acc[0][nt]); acc[1][nt] = mfma16(A[1][nt], B[1][1], acc[1][nt]); }
    asm volatile("s_waitcnt vmcnt(3)" : "+v"(B[2][0]), "+v"(B[2][1]) : : "memory");
#pragma unroll
    for (int nt = 0; nt < 4; ++nt) { acc[0][nt] = mfma16(A[2][nt], B[2][0], acc[0][nt]); acc[1][nt] = mfma16(A[2][nt], B[2][1], acc[1][nt]); }
    asm volatile("s_waitcnt vmcnt(1)" : "+v"(B[3][0]), "+v"(B[3][1]) : : "memory");
#pragma unroll
    for (int nt = 0; nt < 4; ++nt) { acc[0][nt] = mfma16(A[3][nt], B[3][0], acc[0][nt]); acc[1][nt] = mfma16(A[3][nt], B[3][1], acc[1][nt]); }
    MS(23);
    bar();
    {
      const int mo = S2 + kp * 16384 + g * 256 + ((i ^ ((g >> 1) * 2)) << 4);
#pragma unroll
      for (int tt = 0; tt < 2; ++tt)
#pragma unroll
        for (int nt = 0; nt < 4; ++nt)
          *(LAS f32x4*)(LAS char*)(smem + (mo ^ (nt * 64)) + ((th * 2 + tt) * 4 + nt) * 1024) = acc[tt][nt];
    }
    asm volatile("s_waitcnt vmcnt(0) lgkmcnt(0)\n\ts_barrier" : "+v"(rs) : : "memory");
    {
      const int c = lane & 7;
      const int tok = (lane >> 3) + 8 * (wv & 1);
      const char* rp = smem + S2 + ((wv >> 1) * 4 + (c >> 1)) * 1024 + (c & 1) * 512 + ((tok ^ (2 * c)) << 4);
      f32x4 v0 = f32x4{0, 0, 0, 0}, v1 = f32x4{0, 0, 0, 0};
#pragma unroll
      for (int p = 0; p < 4; ++p) {
        v0 += *(const LAS f32x4*)(LAS const char*)(rp + p * 16384);
        v1 += *(const LAS f32x4*)(LAS const char*)(rp + p * 16384 + 256);
      }
      u32x4 r = __builtin_bit_cast(u32x4, rs);
      u32x4 ov = {pack2(v0[0] + bflo(r[0]), v0[1] + bfhi(r[0])), pack2(v0[2] + bflo(r[1]), v0[3] + bfhi(r[1])),
                  pack2(v1[0] + bflo(r[2]), v1[1] + bfhi(r[2])), pack2(v1[2] + bflo(r[3]), v1[3] + bfhi(r[3]))};
      st16<true>(h + ooff + vo, ov);
      float s1 = 0.f, s2 = 0.f;
#pragma unroll
      for (int e = 0; e < 4; ++e) { s1 = dot2(ov[e], 0x3F803F80u, s1); s2 = dot2(ov[e], ov[e], s2); }
#pragma unroll
      for (int m = 1; m < 8; m <<= 1) { s1 += __shfl_xor(s1, m); s2 += __shfl_xor(s2, m); }
      if (c == 0) st8<true>(stats + ((size_t)(tbG * 64 + wv * 8 + (lane >> 3)) * 8 + cbG) * 8,
                            u32x2{__float_as_uint(s1), __float_as_uint(s2)});
    }
    MS(4);
    asm volatile("s_waitcnt vmcnt(0) lgkmcnt(0)\n\ts_barrier" ::: "memory");
    MS(18);
    if (tid < 16) flag_set(flags + 8192 + ((tid & 3) | ((tbG >> 4) << 2) | ((tid >> 2) << 3) | (((tbG >> 1) & 7) << 5)) * 32 + (tbG & 1) * 8 + cbG);
    {
      const CW cw = c_setup(w1, w3, b, wv, lane);
#pragma unroll
      for (int j = 0; j < 4; ++j) c_dmaw(cw, lds0 + wv * 2048, 2, j);
    }
    if (wv == 0) flag_wait(flags + 8192 + b * 32 + (lane & 31)); MS(19);
    asm volatile("s_barrier" ::: "memory");
    MS(5);
  }
  const unsigned vwd_ = 0;
  {
    const int tid = opaque(threadIdx.x), lane = tid & 63, wv = uni(tid >> 6);
    const char* gt = h + (size_t)(tbC * 128 + wv * 16) * 1024;
    const unsigned vt = (lane >> 3) * 1024 + (((lane & 7) ^ (lane >> 3)) << 4);
    const CW cw = c_setup(w1, w3, b, wv, lane);
    const unsigned vwd = (lane >> 3) * 1024 + ((((lane & 7) >> 1) ^ ((lane >> 4) & 3)) << 5) + (lane & 1) * 16;
    const char* gw2 = w2 + (size_t)(wv * 224) * 1024 + cbG * 128;
    if ((wv >> 2) == 0) c_body<4>(smem, lds0, gt, cw, vt, wv, lane, stats, csb, hh, tbC * 128, cbC, gw2, vwd MSPASS);
    else c_body<3>(smem, lds0, gt, cw, vt, wv, lane, stats, csb, hh, tbC * 128, cbC, gw2, vwd MSPASS);
    MS(6);
  }
  {
    const int tid = opaque(threadIdx.x), lane = tid & 63, wv = uni(tid >> 6);
    const int g = lane >> 4, i = lane & 15, q4 = (lane >> 2) & 3, p4 = lane & 3;
    asm volatile("s_waitcnt vmcnt(0) lgkmcnt(0)\n\ts_barrier" ::: "memory");
    MS(20);
    if (tid < 16) flag_set(flags + 16384 + ((tbC >> 1) | (tid << 3) | ((tbC & 1) << 7)) * 32 + cbC);
    const unsigned vwd = (lane >> 3) * 1024 + ((((lane & 7) >> 1) ^ ((lane >> 4) & 3)) << 5) + (lane & 1) * 16;
    const char* gw2 = w2 + (size_t)(wv * 224) * 1024 + cbG * 128;
    d_dma(gw2, vwd, lds0, wv, 9, 0); d_dma(gw2, vwd, lds0, wv, 9, 1);
    const int sw = (2 * g + (q4 >> 1)) & 3;
    const int ro = (4 * g + q4) * 128 + p4 * 8;
    int rn[4];
#pragma unroll
    for (int nt = 0; nt < 4; ++nt) rn[nt] = ro + ((nt ^ sw) << 5);
    auto lda = [&](bf16x8* Af, int j) {
      const char* p0 = smem + d_unit(2 * j) + wv * d_ustr(2 * j);
      const char* p1 = smem + d_unit(2 * j + 1) + wv * d_ustr(2 * j + 1);
#pragma unroll
      for (int nt = 0; nt < 4; ++nt) Af[nt] = cat(ldstr(p0 + rn[nt]), ldstr(p1 + rn[nt]));
    };
    bf16x8 A[2][4], B[7][4], rs;
    lda(A[0], 0); lda(A[1], 1);
    asm volatile("s_waitcnt lgkmcnt(0)" : "+v"(A[0][0]), "+v"(A[0][1]), "+v"(A[0][2]), "+v"(A[0][3]), "+v"(A[1][0]), "+v"(A[1][1]), "+v"(A[1][2]), "+v"(A[1][3]) : : "memory");
#pragma unroll
    for (int u = 10; u < 14; ++u) { d_dma(gw2, vwd, lds0, wv, u, 0); d_dma(gw2, vwd, lds0, wv, u, 1); }
    if (wv == 0) flag_wait(flags + 16384 + b * 32 + (lane & 15)); MS(21);
    asm volatile("s_barrier" ::: "memory");
    MS(7);
    const char* ga = hh + ((size_t)(tbG * 4) * 56 + wv * 7) * 1024;
#pragma unroll
    for (int j = 0; j < 7; ++j)
#pragma unroll
      for (int tt = 0; tt < 4; ++tt) gld16(B[j][tt], lane * 16, ga + (size_t)(tt * 56 + j) * 1024);
    const size_t ooff = (size_t)(tbG * 64 + wv * 8) * 1024 + cbG * 128;
    const unsigned vo = (lane >> 3) * 1024 + (lane & 7) * 16;
    gld16(rs, vo, h + ooff);
    f32x4 acc[4][4];
#pragma unroll
    for (int t = 0; t < 4; ++t)
#pragma unroll
      for (int n = 0; n < 4; ++n) acc[t][n] = f32x4{0, 0, 0, 0};
    auto step = [&](auto J) {
      constexpr int j = decltype(J)::value;
      asm volatile("s_waitcnt vmcnt(%4)" : "+v"(B[j][0]), "+v"(B[j][1]), "+v"(B[j][2]), "+v"(B[j][3]) : "n"(25 - 4 * j) : "memory");
#pragma unroll
      for (int nt = 0; nt < 4; ++nt)
#pragma unroll
        for (int tt = 0; tt < 4; ++tt) acc[tt][nt] = mfma16(A[j & 1][nt], B[j][tt], acc[tt][nt]);
      __builtin_amdgcn_sched_barrier(0);
      if constexpr (j + 2 < 7) lda(A[j & 1], j + 2);
    };
    step(IC<0>{}); step(IC<1>{}); step(IC<2>{}); step(IC<3>{}); step(IC<4>{}); step(IC<5>{}); step(IC<6>{});
    {
      const int mo = wv * 6144 + g * 256 + ((i ^ ((g >> 1) * 2)) << 4);
#pragma unroll
      for (int tt = 0; tt < 4; ++tt)
#pragma unroll
        for (int nt = 0; nt < 4; ++nt) {
          const int t = tt * 4 + nt;
          const int base = t < 6 ? S2 + t * 1024 : t < 12 ? S0 + (t - 6) * 1024 : S1 + (t - 12) * 1024;
          *(LAS f32x4*)(LAS char*)(smem + (mo ^ (nt * 64)) + base) = acc[tt][nt];
        }
    }
    asm volatile("s_waitcnt vmcnt(0) lgkmcnt(0)\n\ts_barrier" : "+v"(rs) : : "memory");
    {
      const int c = lane & 7;
      const int tok = (lane >> 3) + 8 * (wv & 1);
      const int t = (wv >> 1) * 4 + (c >> 1);
      const int base = t < 6 ? S2 + t * 1024 : t < 12 ? S0 + (t - 6) * 1024 : S1 + (t - 12) * 1024;
      const char* rp = smem + base + (c & 1) * 512 + ((tok ^ (2 * c)) << 4);
      f32x4 v0 = f32x4{0, 0, 0, 0}, v1 = f32x4{0, 0, 0, 0};
#pragma unroll
      for (int p = 0; p < 8; ++p) {
        v0 += *(const LAS f32x4*)(LAS const char*)(rp + p * 6144);
        v1 += *(const LAS f32x4*)(LAS const char*)(rp + p * 6144 + 256);
      }
      u32x4 r = __builtin_bit_cast(u32x4, rs);
      u32x4 ov = {pack2(v0[0] + bflo(r[0]), v0[1] + bfhi(r[0])), pack2(v0[2] + bflo(r[1]), v0[3] + bfhi(r[1])),
                  pack2(v1[0] + bflo(r[2]), v1[1] + bfhi(r[2])), pack2(v1[2] + bflo(r[3]), v1[3] + bfhi(r[3]))};
      *(u32x4*)(out + ooff + vo) = ov;
    }
    MS(8);
  }
}

extern "C" void kernel_launch(void* const* d_in, const int* in_sizes, int n_in, void* d_out, int out_size,
                              void* d_ws, size_t ws_size, hipStream_t stream) {
  const char* x = (const char*)d_in[0];
  const char* wqkv = (const char*)d_in[1];
  const char* wo = (const char*)d_in[2];
  const char* w1 = (const char*)d_in[4];
  const char* w3 = (const char*)d_in[5];
  const char* w2 = (const char*)d_in[6];
  char* ws = (char*)d_ws;
  (void)hipFuncSetAttribute((const void*)k1_qkv, hipFuncAttributeMaxDynamicSharedMemorySize, K1_LDS);
  (void)hipFuncSetAttribute((const void*)k_mega, hipFuncAttributeMaxDynamicSharedMemorySize, MG_LDS);
  k1_qkv<<<256, 512, K1_LDS, stream>>>(x, wqkv, ws, ws + (10u << 20) + (512u << 10));
  k_mega<<<256, 512, MG_LDS, stream>>>(x, wo, w1, w3, w2, ws, (char*)d_out);
}
```

```cpp
#include <hip/hip_runtime.h>
#include <hip/hip_bf16.h>
#include <stdint.h>

typedef short bf16x8 __attribute__((ext_vector_type(8)));
typedef short s16x4 __attribute__((ext_vector_type(4)));
typedef float f32x4 __attribute__((ext_vector_type(4)));
typedef float f32x2 __attribute__((ext_vector_type(2)));
typedef unsigned u32x4 __attribute__((ext_vector_type(4)));
typedef unsigned u32x2 __attribute__((ext_vector_type(2)));
typedef __bf16 bf16x2 __attribute__((ext_vector_type(2)));
#define LAS __attribute__((address_space(3)))
#define DEV __device__ __forceinline__

constexpr int S_ = 2048, D_ = 512, H_ = 32, F_ = 1792;
constexpr float LN_EPS = 1e-5f;

DEV f32x4 mfma16(bf16x8 a, bf16x8 b, f32x4 c) { return __builtin_amdgcn_mfma_f32_16x16x32_bf16(a, b, c, 0, 0, 0); }
DEV unsigned pack2(float a, float b) {
  f32x2 v = {a, b};
  return __builtin_bit_cast(unsigned, __builtin_convertvector(v, bf16x2));
}
DEV float bflo(unsigned u) { return __builtin_bit_cast(float, u << 16); }
DEV float bfhi(unsigned u) { return __builtin_bit_cast(float, u & 0xffff0000u); }
DEV float dot2(unsigned a, unsigned b, float c) {
  return __builtin_amdgcn_fdot2_f32_bf16(__builtin_bit_cast(bf16x2, a), __builtin_bit_cast(bf16x2, b), c, false);
}
DEV void dma16(const char* gptr, unsigned ldsbase) {
  unsigned sv;
  asm volatile("s_mov_b32 %0, m0\n\ts_mov_b32 m0, %2\n\ts_nop 0\n\tglobal_load_lds_dwordx4 %1, off\n\ts_mov_b32 m0, %0"
               : "=&s"(sv) : "v"(gptr), "s"(ldsbase) : "memory");
}
DEV void dma16s(unsigned voff, const char* sbase, unsigned ldsbase) {
  unsigned sv;
  asm volatile("s_mov_b32 %0, m0\n\ts_mov_b32 m0, %3\n\ts_nop 0\n\tglobal_load_lds_dwordx4 %1, %2\n\ts_mov_b32 m0, %0"
               : "=&s"(sv) : "v"(voff), "s"(sbase), "s"(ldsbase) : "memory");
}
DEV void dma16sm(unsigned voff, const char* sbase, unsigned ldsbase, unsigned long long mask) {
  unsigned sv;
  asm volatile("s_mov_b32 %0, m0\n\ts_mov_b32 m0, %3\n\ts_mov_b64 exec, %4\n\tglobal_load_lds_dwordx4 %1, %2\n\ts_mov_b64 exec, -1\n\ts_mov_b32 m0, %0"
               : "=&s"(sv) : "v"(voff), "s"(sbase), "s"(ldsbase), "s"(mask) : "memory");
}
DEV void gld16(bf16x8& d, unsigned voff, const char* sbase) {
  asm volatile("global_load_dwordx4 %0, %1, %2" : "=v"(d) : "v"(voff), "s"(sbase) : "memory");
}
template <int N> DEV void wait_bar() { asm volatile("s_waitcnt vmcnt(%0) lgkmcnt(0)\n\ts_barrier" ::"n"(N) : "memory"); }
DEV void bar() { asm volatile("s_waitcnt lgkmcnt(0)\n\ts_barrier" ::: "memory"); }
DEV bf16x8 lds128(const char* p) { return *(const LAS bf16x8*)(LAS const char*)p; }
DEV s16x4 ldstr(const char* p) { return __builtin_amdgcn_ds_read_tr16_b64_v4i16((LAS s16x4*)p); }
DEV bf16x8 cat(s16x4 a, s16x4 b) { return __builtin_shufflevector(a, b, 0, 1, 2, 3, 4, 5, 6, 7); }
DEV float xsum(float v) { v += __shfl_xor(v, 16); v += __shfl_xor(v, 32); return v; }
DEV float xmax(float v) { v = fmaxf(v, __shfl_xor(v, 16)); v = fmaxf(v, __shfl_xor(v, 32)); return v; }
DEV float dpp_xor1(float v) { return __builtin_bit_cast(float, __builtin_amdgcn_mov_dpp(__builtin_bit_cast(int, v), 0xB1, 0xF, 0xF, true)); }
DEV float dpp_xor2(float v) { return __builtin_bit_cast(float, __builtin_amdgcn_mov_dpp(__builtin_bit_cast(int, v), 0x4E, 0xF, 0xF, true)); }
DEV float dpp_ror4(float v) { return __builtin_bit_cast(float, __builtin_amdgcn_mov_dpp(__builtin_bit_cast(int, v), 0x124, 0xF, 0xF, true)); }
DEV float dpp_ror8(float v) { return __builtin_bit_cast(float, __builtin_amdgcn_mov_dpp(__builtin_bit_cast(int, v), 0x128, 0xF, 0xF, true)); }
DEV unsigned uni(unsigned v) { return __builtin_amdgcn_readfirstlane(v); }
#define WT 31
template <bool W> DEV void st16(char* p, u32x4 v) {
  if constexpr (W) asm volatile("global_store_dwordx4 %0, %1, off sc0 sc1\n\ts_nop 1" ::"v"(p), "v"(v) : "memory");
  else *(u32x4*)p = v;
}
template <bool W> DEV void st8(char* p, u32x2 v) {
  if constexpr (W) asm volatile("global_store_dwordx2 %0, %1, off sc0 sc1" ::"v"(p), "v"(v) : "memory");
  else *(u32x2*)p = v;
}

template <int A, int B> struct Min { static constexpr int v = A < B ? A : B; };

template <int KT, int NK, int NS, int P, class Issue, class Compute>
DEV void pipe_steps(Issue& issue, Compute& compute) {
  if constexpr (KT < NK) {
    wait_bar<(Min<KT + NS - 1, NK>::v - KT - 1) * P>();
    if constexpr (KT + NS - 1 < NK) issue((KT + NS - 1) % NS);
    compute(KT % NS, KT);
    pipe_steps<KT + 1, NK, NS, P>(issue, compute);
  }
}

constexpr int K1_STAGE = 16384 + 12288, K1_NS = 5;
constexpr int K1_LDS = K1_STAGE * K1_NS + 1024 + 1536;
#define SB __builtin_amdgcn_sched_barrier(0)
DEV float xsum2(float v) {
  auto a = __builtin_amdgcn_permlane16_swap(__float_as_uint(v), __float_as_uint(v), false, false);
  v = __uint_as_float(a[0]) + __uint_as_float(a[1]);
  auto b = __builtin_amdgcn_permlane32_swap(__float_as_uint(v), __float_as_uint(v), false, false);
  return __uint_as_float(b[0]) + __uint_as_float(b[1]);
}

__global__ __launch_bounds__(576) void k1_qkv(const char* __restrict__ x, const char* __restrict__ w,
                                              char* __restrict__ qkv, char* __restrict__ flags,
                                              const char* __restrict__ wo, const char* __restrict__ w1, const char* __restrict__ w3,
                                              const char* __restrict__ w2, const char* __restrict__ out) {
  extern __shared__ __attribute__((aligned(16))) char smem[];
  const int tid = threadIdx.x, lane = tid & 63, wv = uni(tid >> 6);
  const int g = lane >> 4, i = lane & 15, q4 = (lane >> 2) & 3, p4 = lane & 3;
  const int tg = wv & 3, nh = wv >> 2;
  const int b = blockIdx.x, xcd = b & 7, bi = b >> 3;
  const int cb = (xcd & 1) * 8 + (bi & 7), tb = (xcd >> 1) * 4 + (bi >> 3);
  const unsigned lds0 = (unsigned)(size_t)(LAS char*)smem;
  if (wv == 8) {
    if (lane < 24) st16<true>(flags + ((lane >> 3) * 256 + b) * 128 + (lane & 7) * 16, u32x4{0, 0, 0, 0});
    return;
  }

  const char* gx = x + (size_t)(tb * 128 + wv * 16) * 1024;
  const unsigned vt = (lane >> 3) * 1024 + (((lane & 7) ^ (lane >> 3)) << 4);
  const char* gw = w + cb * 192;
  unsigned vw0, vw1;
  {
    int qq = wv * 64 + lane;
    int k = qq / 12, c = (qq % 12) ^ (((k >> 3) & 1) << 1);
    vw0 = k * 3072 + c * 16;
    qq = 512 + wv * 32 + (lane & 31);
    k = qq / 12; c = (qq % 12) ^ (((k >> 3) & 1) << 1);
    vw1 = k * 3072 + c * 16;
  }
  const int k0 = bi & 7;
  auto dma = [&](int s, int j) {
    const int kk = (k0 + s) & 7;
    const unsigned base = lds0 + (s % K1_NS) * K1_STAGE;
    if (j == 0) dma16s(vt, gx + kk * 128, base + wv * 2048);
    else if (j == 1) dma16s(vt, gx + kk * 128 + 8192, base + wv * 2048 + 1024);
    else if (j == 2) dma16s(vw0, gw + kk * (64 * 3072), base + 16384 + wv * 1024);
    else dma16sm(vw1, gw + kk * (64 * 3072), base + 16384 + 8192 + wv * 512, 0xFFFFFFFFull);
  };
#pragma unroll
  for (int s = 0; s < K1_NS - 1; ++s)
#pragma unroll
    for (int j = 0; j < 4; ++j) dma(s, j);

  f32x4 acc[2][3], cs = f32x4{0, 0, 0, 0}, s1v = f32x4{0, 0, 0, 0};
#pragma unroll
  for (int a = 0; a < 3; ++a) { acc[0][a] = f32x4{0, 0, 0, 0}; acc[1][a] = f32x4{0, 0, 0, 0}; }
  float s2a = 0.f, s2b = 0.f;
  const bf16x8 ones = {0x3F80, 0x3F80, 0x3F80, 0x3F80, 0x3F80, 0x3F80, 0x3F80, 0x3F80};
  int toff[2][2];
#pragma unroll
  for (int tt = 0; tt < 2; ++tt) {
    toff[tt][0] = (tg * 32 + (tt ^ nh) * 16 + i) * 128 + ((g ^ (i & 7)) << 4);
    toff[tt][1] = (tg * 32 + (tt ^ nh) * 16 + i) * 128 + (((4 + g) ^ (i & 7)) << 4);
  }
  int woff[3], un[3];
#pragma unroll
  for (int nt = 0; nt < 3; ++nt) {
    int a = nt + tg; a = a >= 3 ? a - 3 : a; a = a >= 3 ? a - 3 : a;
    un[nt] = nh * 3 + a;
    woff[nt] = 16384 + (8 * g + q4) * 192 + ((un[nt] ^ (g & 1)) << 5) + p4 * 8;
  }
  auto ldB = [&](int tt, int kt, int ks) -> bf16x8 { return lds128(smem + (kt % K1_NS) * K1_STAGE + toff[tt][ks]); };
  auto ldA = [&](int nt, int kt, int ks) -> bf16x8 {
    const char* p = smem + (kt % K1_NS) * K1_STAGE + woff[nt] + ks * 6144;
    return cat(ldstr(p), ldstr(p + 768));
  };
  bf16x8 A0[3], A1[3], B0[2], B1[2];
  auto phase = [&](bf16x8 (&Ac)[3], bf16x8 (&Bc)[2], bf16x8 (&An)[3], bf16x8 (&Bn)[2], int kn, int ksn, int dk, int dj) {
    const u32x4 u = __builtin_bit_cast(u32x4, Bc[0]);
#pragma unroll
    for (int m = 0; m < 8; ++m) {
      if (kn < 8) {
        if (m == 0) { Bn[0] = ldB(0, kn, ksn); An[0] = ldA(0, kn, ksn); }
        else if (m == 1) Bn[1] = ldB(1, kn, ksn);
        else if (m < 4) An[m - 1] = ldA(m - 1, kn, ksn);
      }
      if (m < 6) acc[m & 1][m >> 1] = mfma16(Ac[m >> 1], Bc[m & 1], acc[m & 1][m >> 1]);
      else if (m == 6) cs = mfma16(Ac[0], ones, cs);
      else s1v = mfma16(ones, Bc[0], s1v);
      {
        float f = (m & 1) ? bfhi(u[m >> 1]) : bflo(u[m >> 1]);
        asm volatile("" : "+v"(f));
        if (m & 1) s2b = __builtin_fmaf(f, f, s2b); else s2a = __builtin_fmaf(f, f, s2a);
      }
      if (dk < 8 && (m == 2 || m == 5)) dma(dk, dj + (m == 5));
      SB;
    }
  };
  wait_bar<12>();
  B0[0] = ldB(0, 0, 0); A0[0] = ldA(0, 0, 0); B0[1] = ldB(1, 0, 0); A0[1] = ldA(1, 0, 0); A0[2] = ldA(2, 0, 0);
#pragma unroll
  for (int kt = 0; kt < 8; ++kt) {
    phase(A0, B0, A1, B1, kt, 1, kt + 4, 0);
    if (kt < 4) wait_bar<10>();
    else if (kt == 4) wait_bar<8>();
    else if (kt == 5) wait_bar<4>();
    else if (kt == 6) wait_bar<0>();
    phase(A1, B1, A0, B0, kt + 1, 0, kt + 4, 2);
  }

  float* st = (float*)(smem + K1_STAGE * K1_NS);
  float* csp = (float*)(smem + K1_STAGE * K1_NS + 1024);
  const float s2 = xsum2(s2a + s2b);
  if (g == 0) {
    float mean = s1v[0] * (1.f / 512.f);
    float var = fmaxf(s2 * (1.f / 512.f) - mean * mean, 0.f);
    float rstd = rsqrtf(var + LN_EPS);
    *(f32x2*)(st + (tg * 32 + nh * 16 + i) * 2) = f32x2{-mean * rstd, rstd};
  }
  if (i == 0 && tg < 3) *(f32x4*)(csp + un[0] * 16 + 4 * g) = cs;
  bar();
  f32x2 ms[2];
#pragma unroll
  for (int tt = 0; tt < 2; ++tt) ms[tt] = *(const f32x2*)(st + (tg * 32 + (tt ^ nh) * 16 + i) * 2);
#pragma unroll
  for (int nt = 0; nt < 3; ++nt) {
    f32x4 c = *(const f32x4*)(csp + un[nt] * 16 + 4 * g);
    int n0 = cb * 96 + un[nt] * 16;
    char* dst = qkv + (size_t)(n0 >> 4) * (2048 * 32) + g * 8;
#pragma unroll
    for (int tt = 0; tt < 2; ++tt) {
      f32x4 y = acc[tt][nt] * ms[tt][1] + ms[tt][0] * c;
      u32x2 o = {pack2(y[0], y[1]), pack2(y[2], y[3])};
      st8<(WT & 1) != 0>(dst + (size_t)(tb * 128 + tg * 32 + (tt ^ nh) * 16 + i) * 32, o);
    }
  }
}

constexpr int S0 = 0, S1 = 49152, S2 = 98304, CSP = 147456, MG_LDS = 163840;
DEV void flag_set(unsigned* f) {
  unsigned one = 1;
  asm volatile("global_store_dword %0, %1, off sc0 sc1" ::"v"(f), "v"(one) : "memory");
}
DEV void flag_wait(const unsigned* p) {
  for (int it = 0; it < (1 << 20); ++it) {
    unsigned c;
    asm volatile("global_load_dword %0, %1, off sc0 sc1\n\ts_waitcnt vmcnt(0)" : "=v"(c) : "v"(p) : "memory");
    if (__builtin_amdgcn_ballot_w64(c != 1u) == 0) break;
  }
}
template <int V> struct IC { static constexpr int value = V; };
DEV int opaque(int v) { asm volatile("" : "+v"(v)); return v; }
DEV int opaque_s(int v) { asm volatile("" : "+s"(v)); return v; }

struct CW {
  const char *g1, *g3; unsigned vw; unsigned long long wmask;
};
DEV CW c_setup(const char* w1, const char* w3, int b, int wv, int lane) {
  const int xcd = b & 7, bi = b >> 3;
  const int cb = (xcd & 3) * 4 + (bi & 3);
  CW c;
  c.g1 = w1 + (size_t)(wv * 8) * (F_ * 2) + cb * 224;
  c.g3 = w3 + (size_t)(wv * 8) * (F_ * 2) + cb * 224;
  const int unit = ((lane >> 1) & 7) ^ ((lane >> 4) | ((wv & 1) << 2));
  c.vw = (lane >> 4) * (F_ * 2) + unit * 32 + (lane & 1) * 16;
  c.wmask = __builtin_amdgcn_ballot_w64(unit < 7);
  return c;
}
DEV void c_dmaw(const CW& c, unsigned ldsw, int kt, int j) {
  const unsigned base = ldsw + (kt % 3) * 49152;
  if (j < 2) dma16sm(c.vw, c.g1 + (size_t)(kt * 64 + j * 4) * (F_ * 2), base + 16384 + j * 1024, c.wmask);
  else dma16sm(c.vw, c.g3 + (size_t)(kt * 64 + (j - 2) * 4) * (F_ * 2), base + 32768 + (j - 2) * 1024, c.wmask);
}
DEV constexpr int d_unit(int u) {
  return u < 3 ? S2 + u * 2048 : u < 6 ? S0 + (u - 3) * 2048 : u < 9 ? S1 + (u - 6) * 2048 : u == 9 ? CSP
       : u < 13 ? S2 + (u - 10) * 2048 : S0;
}
DEV constexpr int d_ustr(int u) { return u == 9 ? 2048 : 6144; }
DEV void d_dma(const char* gw2, unsigned vw, unsigned lds0, int wv, int u, int q) {
  dma16s(vw, gw2 + (size_t)(u * 16 + q * 8) * 1024, lds0 + d_unit(u) + wv * d_ustr(u) + q * 1024);
}

template <int NT>
DEV void c_body(char* smem, unsigned lds0, const char* gt, const CW& cw, unsigned vt, int wv, int lane,
                const char* __restrict__ stats, const char* __restrict__ csb, char* __restrict__ hh, int tok0, int cb, const char* gw2, unsigned vwd) {
  const int tg = wv & 3;
  const unsigned ldsw = lds0 + wv * 2048;
  const int g = lane >> 4, i = lane & 15, q4 = (lane >> 2) & 3, p4 = lane & 3;
  f32x4 acc[2][2 * NT];
#pragma unroll
  for (int a = 0; a < 2 * NT; ++a) { acc[0][a] = f32x4{0, 0, 0, 0}; acc[1][a] = f32x4{0, 0, 0, 0}; }
  const int toff0 = (tg * 32 + i) * 128 + ((g ^ (i & 7)) << 4);
  const int toff1 = (tg * 32 + i) * 128 + (((4 + g) ^ (i & 7)) << 4);
  const int odd = cb & 1;
  const int fsw = q4 | ((g & 1) << 2);
  int T[NT], wa[NT];
#pragma unroll
  for (int a = 0; a < NT; ++a) {
    T[a] = NT == 4 ? odd * 3 + a : (a < 2 ? (odd ? 1 : 4) + a : (odd ? 0 : 6));
    wa[a] = 16384 + (8 * g + q4) * 256 + ((T[a] ^ fsw) << 5) + p4 * 8;
  }
  auto dmat = [&](int kt, int j) { dma16s(vt, gt + kt * 128 + j * 8192, ldsw + (kt % 3) * 49152 + j * 1024); };
  auto dma = [&](int kt, int j) {
    if (j < 2) dmat(kt, j); else c_dmaw(cw, ldsw, kt, j - 2);
  };
  auto ldB = [&](bf16x8* B, int kt, int ks) {
    const char* sb = smem + (kt % 3) * 49152 + (ks ? toff1 : toff0);
    B[0] = lds128(sb); B[1] = lds128(sb + 2048);
  };
  auto ldA = [&](int a, int kt, int ks) -> bf16x8 {
    const char* wp = smem + (kt % 3) * 49152 + wa[a < NT ? a : a - NT] + (a < NT ? 0 : 16384) + ks * 8192;
    return cat(ldstr(wp), ldstr(wp + 1024));
  };
  auto mma = [&](int a, bf16x8 A, const bf16x8* B) {
    acc[0][a] = mfma16(A, B[0], acc[0][a]);
    acc[1][a] = mfma16(A, B[1], acc[1][a]);
  };
  bf16x8 A0[2 * NT], A1[2 * NT], B0[2], B1[2];
  bf16x8 sa[2], cp[4];
#pragma unroll
  for (int kt = 0; kt < 3; ++kt) { dmat(kt, 0); dmat(kt, 1); }
  gld16(sa[0], (unsigned)(i * 64 + g * 16), stats + (size_t)(tok0 + tg * 32) * 64);
  gld16(sa[1], (unsigned)(i * 64 + g * 16), stats + (size_t)(tok0 + tg * 32 + 16) * 64);
  const int cq = min(tg * 16 + (lane >> 2), 55);
  if (NT == 3) {
#pragma unroll
    for (int m = 0; m < 4; ++m) gld16(cp[m], (unsigned)(((lane & 3) * 4 + m) * 896 + cq * 16), csb + (size_t)cb * 14336);
  }
  constexpr int X = NT == 3 ? 6 : 2;
  wait_bar<4 + X>();
  float rs[2], mr[2];
  float* csp = (float*)(smem + CSP);
  ldB(B0, 0, 0);
#pragma unroll
  for (int a = 0; a < 2 * NT; ++a) A0[a] = ldA(a, 0, 0);
#pragma unroll
  for (int kt = 0; kt < 8; ++kt) {
    ldB(B1, kt, 1);
#pragma unroll
    for (int a = 0; a < 2 * NT; ++a) {
      if (a < NT) { A1[2 * a] = ldA(2 * a, kt, 1); A1[2 * a + 1] = ldA(2 * a + 1, kt, 1); }
      mma(a, A0[a], B0);
      __builtin_amdgcn_sched_barrier(0);
    }
    if (kt == 0) wait_bar<2 + X>();
    else if (kt != 1) wait_bar<6>();
    else if (NT == 3) asm volatile("s_waitcnt vmcnt(6) lgkmcnt(0)\n\ts_barrier" : "+v"(sa[0]), "+v"(sa[1]), "+v"(cp[0]), "+v"(cp[1]), "+v"(cp[2]), "+v"(cp[3]) : : "memory");
    else asm volatile("s_waitcnt vmcnt(6) lgkmcnt(0)\n\ts_barrier" : "+v"(sa[0]), "+v"(sa[1]) : : "memory");
    if (kt < 7) ldB(B0, kt + 1, 0);
    if (kt == 1) {
#pragma unroll
      for (int tt = 0; tt < 2; ++tt) {
        const f32x4 sv = __builtin_bit_cast(f32x4, sa[tt]);
        const float s1 = xsum2(sv[0] + sv[2]), s2 = xsum2(sv[1] + sv[3]);
        const float mean = s1 * (1.f / 512.f);
        const float var = fmaxf(s2 * (1.f / 512.f) - mean * mean, 0.f);
        rs[tt] = rsqrtf(var + LN_EPS); mr[tt] = mean * rs[tt];
      }
      if (NT == 3) {
        f32x4 c = (__builtin_bit_cast(f32x4, cp[0]) + __builtin_bit_cast(f32x4, cp[1])) +
                  (__builtin_bit_cast(f32x4, cp[2]) + __builtin_bit_cast(f32x4, cp[3]));
#pragma unroll
        for (int e = 0; e < 4; ++e) { c[e] += dpp_xor1(c[e]); c[e] += dpp_xor2(c[e]); }
        if ((lane & 3) == 0) *(f32x4*)(csp + cq * 4) = c;
      }
    }
#pragma unroll
    for (int a = 0; a < 2 * NT; ++a) {
      if (kt < 7 && a < NT) { A0[2 * a] = ldA(2 * a, kt + 1, 0); A0[2 * a + 1] = ldA(2 * a + 1, kt + 1, 0); }
      mma(a, A1[a], B1);
      if (a >= 2 * NT - 6) {
        const int j = a - (2 * NT - 6);
        if (kt + 3 < 8) dma(kt + 3, j);
        else d_dma(gw2, vwd, lds0, wv, (kt - 5) * 3 + (j >> 1), j & 1);
      }
      __builtin_amdgcn_sched_barrier(0);
    }
  }
  unsigned pk[2][NT][2];
#pragma unroll
  for (int tt = 0; tt < 2; ++tt) {
    char* dst = hh + ((size_t)((tok0 >> 4) + tg * 2 + tt) * 56) * 1024 + lane * 16;
#pragma unroll
    for (int a = 0; a < NT; ++a) {
      f32x4 cg = *(const f32x4*)(csp + T[a] * 16 + 4 * g);
      f32x4 cu = *(const f32x4*)(csp + (7 + T[a]) * 16 + 4 * g);
      float r[4];
#pragma unroll
      for (int e = 0; e < 4; ++e) {
        float ga = acc[tt][a][e] * rs[tt] - mr[tt] * cg[e];
        float up = acc[tt][NT + a][e] * rs[tt] - mr[tt] * cu[e];
        float sg = __builtin_amdgcn_rcpf(1.f + __builtin_amdgcn_exp2f(-1.4426950408889634f * ga));
        r[e] = ga * sg * up;
      }
      pk[tt][a][0] = pack2(r[0], r[1]); pk[tt][a][1] = pack2(r[2], r[3]);
      const int G = cb * 7 + T[a];
      if (a & 1) {
        st16<true>(dst + (size_t)(G >> 1) * 1024, u32x4{pk[tt][a - 1][0], pk[tt][a - 1][1], pk[tt][a][0], pk[tt][a][1]});
      } else if (a == NT - 1) {
        st8<true>(dst + (size_t)(G >> 1) * 1024 + (G & 1) * 8, u32x2{pk[tt][a][0], pk[tt][a][1]});
      }
    }
  }
}

__global__ __launch_bounds__(512) void k_mega(const char* __restrict__ x, const char* __restrict__ wo,
                                              const char* __restrict__ w1, const char* __restrict__ w3,
                                              const char* __restrict__ w2, char* __restrict__ ws, char* __restrict__ out) {
  extern __shared__ __attribute__((aligned(16))) char smem[];
  const char* qkv = ws;
  char* o = ws + (6u << 20);
  char* h = ws + (8u << 20);
  char* stats = ws + (10u << 20);
  unsigned* flags = (unsigned*)(ws + (10u << 20) + (512u << 10));
  char* csb = ws + (10u << 20) + (768u << 10);
  char* hh = ws + (11u << 20);
  const int b = blockIdx.x;
  const unsigned lds0 = (unsigned)(size_t)(LAS char*)smem;
  const int cbG = (b >> 3) & 7, tbG = (b & 7) * 4 + (b >> 6);
  const int cbC = (b & 3) * 4 + ((b >> 3) & 3), tbC = ((b >> 2) & 1) * 8 + (b >> 5);

  {
    const int tid = threadIdx.x, lane = tid & 63, wv = uni(tid >> 6);
    const int g = lane >> 4, i = lane & 15, q4 = (lane >> 2) & 3, p4 = lane & 3;
    const int hp = (b & 7) * 2 + ((b >> 3) & 1), qb = b >> 4;
    const int q0 = qb * 128;
    const size_t HS = 2048 * 32;
    {
      const int r = wv >> 1;
      const char* src = qkv + (size_t)((1 + (r >> 1)) * 32 + hp * 2 + (r & 1)) * HS;
#pragma unroll
      for (int j = 0; j < 6; ++j) {
        int ch = ((wv & 1) * 6 + j) * 64 + lane;
        int key = q0 - 256 + (ch >> 1);
        key = key < 0 ? 0 : key;
        dma16(src + (size_t)key * 32 + (ch & 1) * 16, lds0 + r * 12288 + ((wv & 1) * 6 + j) * 1024);
      }
    }
    const int qs = q0 + wv * 16;
    bf16x8 Q[2], cw0, cw1;
    gld16(Q[0], (unsigned)(i * 32 + (g & 1) * 16), qkv + (size_t)(hp * 2) * HS + (size_t)qs * 32);
    gld16(Q[1], (unsigned)(i * 32 + (g & 1) * 16), qkv + (size_t)(hp * 2 + 1) * HS + (size_t)qs * 32);
    asm volatile("s_waitcnt vmcnt(0)\n\ts_barrier" : "+v"(Q[0]), "+v"(Q[1]) : : "memory");
    const unsigned vwo = (lane >> 3) * 1024 + ((((lane & 7) >> 1) ^ ((lane >> 4) & 3)) << 5) + (lane & 1) * 16;
    const char* gwo = wo + (size_t)(wv * 64) * 1024 + cbG * 128;
    const CW cw1s = c_setup(w1, w3, b, wv, lane);
    auto pf = [&](int k) {
      if (k < 2) {
        const int ch = max(wv - 1, 0) * 4 + (lane & 3);
        const char* pc = (ch >= 14 ? w3 + (ch - 14) * 16 : w1 + ch * 16) + (size_t)(tbC * 32 + (lane >> 2)) * (F_ * 2) + cbC * 224;
        if (k == 0) asm volatile("global_load_dwordx4 %0, %1, off" : "=v"(cw0) : "v"(pc) : "memory");
        else asm volatile("global_load_dwordx4 %0, %1, off" : "=v"(cw1) : "v"(pc + 16 * (F_ * 2)) : "memory");
      } else if (k < 10) dma16s(vwo, gwo + (size_t)((k - 2) * 8) * 1024, lds0 + S2 + wv * 8192 + (k - 2) * 1024);
      else if (k < 14) c_dmaw(cw1s, lds0 + wv * 2048, 1, k - 10);
      else if (k == 14) {
        const int c0 = b * 8 + wv;
        dma16s(lane * 16, w2 + (size_t)(c0 < 1792 ? c0 : c0 - 1792) * 1024, lds0 + S1 + wv * 1024);
      }
    };
    if (g >= 2) { Q[0] = bf16x8{0, 0, 0, 0, 0, 0, 0, 0}; Q[1] = bf16x8{0, 0, 0, 0, 0, 0, 0, 0}; }
    const float c = 0.25f * 1.4426950408889634f;
    const float NEG = -1e30f;
    const int lrow = wv * 16;
    f32x4 cin0, cin16;
#pragma unroll
    for (int r = 0; r < 4; ++r) { cin0[r] = (4 * g + r > i) ? 0.f : NEG; cin16[r] = (4 * g + r <= i) ? 0.f : NEG; }
    const bf16x8 ones = {0x3F80, 0x3F80, 0x3F80, 0x3F80, 0x3F80, 0x3F80, 0x3F80, 0x3F80};
    u32x4 ov;
#pragma unroll
    for (int u = 0; u < 2; ++u) {
      const char* Kl = smem + u * 12288;
      const char* Vl = smem + 24576 + u * 12288;
      f32x4 s[17];
#pragma unroll
      for (int t = 0; t < 17; ++t) {
        bf16x8 A = lds128(Kl + (lrow + t * 16 + i) * 32 + (g & 1) * 16);
        s[t] = mfma16(A, Q[u], t == 0 ? cin0 : t == 16 ? cin16 : f32x4{0, 0, 0, 0});
      }
      float m = s[16][0];
#pragma unroll
      for (int t = 0; t < 17; ++t)
#pragma unroll
        for (int r = 0; r < 4; ++r) m = fmaxf(m, s[t][r]);
      m = xmax(m);
      const f32x2 c2 = {c, c}, m2 = {-m * c, -m * c};
      unsigned pk[17][2];
      auto soft = [&](int t) {
        f32x2 a0 = __builtin_elementwise_fma(f32x2{s[t][0], s[t][1]}, c2, m2);
        f32x2 a1 = __builtin_elementwise_fma(f32x2{s[t][2], s[t][3]}, c2, m2);
        pk[t][0] = pack2(__builtin_amdgcn_exp2f(a0[0]), __builtin_amdgcn_exp2f(a0[1]));
        pk[t][1] = pack2(__builtin_amdgcn_exp2f(a1[0]), __builtin_amdgcn_exp2f(a1[1]));
      };
      if (qs >= 256) {
#pragma unroll
        for (int t = 0; t < 17; ++t) {
          soft(t);
          if (u == 0 && t < 15) pf(t);
        }
      } else {
        const int nneg = (256 - qs) >> 4;
#pragma unroll
        for (int t = 0; t < 17; ++t) {
          soft(t);
          if (t < 16) { const unsigned mk = t < nneg ? 0u : ~0u; pk[t][0] &= mk; pk[t][1] &= mk; }
          if (u == 0 && t < 15) pf(t);
        }
      }
      f32x4 oacc = f32x4{0, 0, 0, 0}, lsum = f32x4{0, 0, 0, 0};
#pragma unroll
      for (int tp = 0; tp < 9; ++tp) {
        const int t0 = 2 * tp, t1 = (2 * tp + 1 < 17) ? 2 * tp + 1 : 16;
        bf16x8 A = cat(ldstr(Vl + (lrow + t0 * 16 + 4 * g + q4) * 32 + p4 * 8), ldstr(Vl + (lrow + t1 * 16 + 4 * g + q4) * 32 + p4 * 8));
        u32x4 pb = {pk[t0][0], pk[t0][1], (2 * tp + 1 < 17) ? pk[t1][0] : 0u, (2 * tp + 1 < 17) ? pk[t1][1] : 0u};
        oacc = mfma16(A, __builtin_bit_cast(bf16x8, pb), oacc);
        lsum = mfma16(ones, __builtin_bit_cast(bf16x8, pb), lsum);
      }
      const float inv = __builtin_amdgcn_rcpf(lsum[0]);
      ov[2 * u] = pack2(oacc[0] * inv, oacc[1] * inv);
      ov[2 * u + 1] = pack2(oacc[2] * inv, oacc[3] * inv);
    }
    st16<true>(o + ((size_t)(qs >> 4) * 16 + hp) * 1024 + lane * 16, ov);
    asm volatile("s_waitcnt vmcnt(0) lgkmcnt(0)\n\ts_barrier" : "+v"(cw0), "+v"(cw1) : : "memory");
    if (tid < 16) flag_set(flags + (((qb >> 1) | (tid << 3) | ((qb & 1) << 7)) * 32 + hp));
    {
      const CW cw = c_setup(w1, w3, b, wv, lane);
#pragma unroll
      for (int j = 0; j < 4; ++j) c_dmaw(cw, lds0 + wv * 2048, 0, j);
    }
    if (wv == 0) { flag_wait(flags + b * 32 + (lane & 15)); }
    else {
      const u32x4 ua = __builtin_bit_cast(u32x4, cw0), ub = __builtin_bit_cast(u32x4, cw1);
      f32x4 lo, hi;
#pragma unroll
      for (int e = 0; e < 2; ++e) {
        lo[2 * e] = bflo(ua[e]) + bflo(ub[e]); lo[2 * e + 1] = bfhi(ua[e]) + bfhi(ub[e]);
        hi[2 * e] = bflo(ua[2 + e]) + bflo(ub[2 + e]); hi[2 * e + 1] = bfhi(ua[2 + e]) + bfhi(ub[2 + e]);
      }
#pragma unroll
      for (int e = 0; e < 4; ++e) {
        lo[e] += dpp_ror4(lo[e]); lo[e] += dpp_ror8(lo[e]); lo[e] = xsum2(lo[e]);
        hi[e] += dpp_ror4(hi[e]); hi[e] += dpp_ror8(hi[e]); hi[e] = xsum2(hi[e]);
      }
      if (lane < 4) {
        char* d = csb + (size_t)(cbC * 16 + tbC) * 896 + ((wv - 1) * 4 + lane) * 32;
        st16<true>(d, __builtin_bit_cast(u32x4, lo));
        st16<true>(d + 16, __builtin_bit_cast(u32x4, hi));
      }
      asm volatile("s_waitcnt vmcnt(0)" ::: "memory");
    }
    asm volatile("s_barrier" ::: "memory");
    if (tid >= 64 && tid < 80) flag_set(flags + 8192 + ((cbC >> 2) | ((tid & 1) << 2) | ((cbC & 3) << 3) | (((tid & 15) >> 1) << 5)) * 32 + 16 + tbC);
  }
  {
    const int tid = opaque(threadIdx.x), lane = tid & 63, wv = uni(tid >> 6);
    const int g = lane >> 4, i = lane & 15, q4 = (lane >> 2) & 3, p4 = lane & 3;
    const int kp = wv & 3, th = wv >> 2;
    bf16x8 B[4][2], rs;
    const char* ga = o + ((size_t)(tbG * 4 + th * 2) * 16 + kp * 4) * 1024;
#pragma unroll
    for (int s = 0; s < 4; ++s) {
      gld16(B[s][0], lane * 16, ga + s * 1024);
      gld16(B[s][1], lane * 16, ga + (16 + s) * 1024);
    }
    const size_t ooff = (size_t)(tbG * 64 + wv * 8) * 1024 + cbG * 128;
    const unsigned vo = (lane >> 3) * 1024 + (lane & 7) * 16;
    gld16(rs, vo, x + ooff);
    const int sw = (2 * g + (q4 >> 1)) & 3;
    const char* wr = smem + S2 + kp * 16384 + (4 * g + q4) * 128 + p4 * 8;
    bf16x8 A[4][4];
#pragma unroll
    for (int s = 0; s < 4; ++s)
#pragma unroll
      for (int nt = 0; nt < 4; ++nt) {
        const char* p = wr + s * 4096 + ((nt ^ sw) << 5);
        A[s][nt] = cat(ldstr(p), ldstr(p + 2048));
      }
    f32x4 acc[2][4];
#pragma unroll
    for (int t = 0; t < 2; ++t)
#pragma unroll
      for (int n = 0; n < 4; ++n) acc[t][n] = f32x4{0, 0, 0, 0};
    asm volatile("s_waitcnt vmcnt(7)" : "+v"(B[0][0]), "+v"(B[0][1]) : : "memory");
#pragma unroll
    for (int nt = 0; nt < 4; ++nt) { acc[0][nt] = mfma16(A[0][nt], B[0][0], acc[0][nt]); acc[1][nt] = mfma16(A[0][nt], B[0][1], acc[1][nt]); }
    asm volatile("s_waitcnt vmcnt(5)" : "+v"(B[1][0]), "+v"(B[1][1]) : : "memory");
#pragma unroll
    for (int nt = 0; nt < 4; ++nt) { acc[0][nt] = mfma16(A[1][nt], B[1][0], acc[0][nt]); acc[1][nt] = mfma16(A[1][nt], B[1][1], acc[1][nt]); }
    asm volatile("s_waitcnt vmcnt(3)" : "+v"(B[2][0]), "+v"(B[2][1]) : : "memory");
#pragma unroll
    for (int nt = 0; nt < 4; ++nt) { acc[0][nt] = mfma16(A[2][nt], B[2][0], acc[0][nt]); acc[1][nt] = mfma16(A[2][nt], B[2][1], acc[1][nt]); }
    asm volatile("s_waitcnt vmcnt(1)" : "+v"(B[3][0]), "+v"(B[3][1]) : : "memory");
#pragma unroll
    for (int nt = 0; nt < 4; ++nt) { acc[0][nt] = mfma16(A[3][nt], B[3][0], acc[0][nt]); acc[1][nt] = mfma16(A[3][nt], B[3][1], acc[1][nt]); }
    bar();
    {
      const int mo = S2 + kp * 16384 + g * 256 + ((i ^ ((g >> 1) * 2)) << 4);
#pragma unroll
      for (int tt = 0; tt < 2; ++tt)
#pragma unroll
        for (int nt = 0; nt < 4; ++nt)
          *(LAS f32x4*)(LAS char*)(smem + (mo ^ (nt * 64)) + ((th * 2 + tt) * 4 + nt) * 1024) = acc[tt][nt];
    }
    asm volatile("s_waitcnt vmcnt(0) lgkmcnt(0)\n\ts_barrier" : "+v"(rs) : : "memory");
    {
      const int c = lane & 7;
      const int tok = (lane >> 3) + 8 * (wv & 1);
      const char* rp = smem + S2 + ((wv >> 1) * 4 + (c >> 1)) * 1024 + (c & 1) * 512 + ((tok ^ (2 * c)) << 4);
      f32x4 v0 = f32x4{0, 0, 0, 0}, v1 = f32x4{0, 0, 0, 0};
#pragma unroll
      for (int p = 0; p < 4; ++p) {
        v0 += *(const LAS f32x4*)(LAS const char*)(rp + p * 16384);
        v1 += *(const LAS f32x4*)(LAS const char*)(rp + p * 16384 + 256);
      }
      u32x4 r = __builtin_bit_cast(u32x4, rs);
      u32x4 ov = {pack2(v0[0] + bflo(r[0]), v0[1] + bfhi(r[0])), pack2(v0[2] + bflo(r[1]), v0[3] + bfhi(r[1])),
                  pack2(v1[0] + bflo(r[2]), v1[1] + bfhi(r[2])), pack2(v1[2] + bflo(r[3]), v1[3] + bfhi(r[3]))};
      st16<true>(h + ooff + vo, ov);
      float s1 = 0.f, s2 = 0.f;
#pragma unroll
      for (int e = 0; e < 4; ++e) { s1 = dot2(ov[e], 0x3F803F80u, s1); s2 = dot2(ov[e], ov[e], s2); }
#pragma unroll
      for (int m = 1; m < 8; m <<= 1) { s1 += __shfl_xor(s1, m); s2 += __shfl_xor(s2, m); }
      if (c == 0) st8<true>(stats + ((size_t)(tbG * 64 + wv * 8 + (lane >> 3)) * 8 + cbG) * 8,
                            u32x2{__float_as_uint(s1), __float_as_uint(s2)});
    }
    asm volatile("s_waitcnt vmcnt(0) lgkmcnt(0)\n\ts_barrier" ::: "memory");
    if (tid < 16) flag_set(flags + 8192 + ((tid & 3) | ((tbG >> 4) << 2) | ((tid >> 2) << 3) | (((tbG >> 1) & 7) << 5)) * 32 + (tbG & 1) * 8 + cbG);
    {
      const CW cw = c_setup(w1, w3, b, wv, lane);
#pragma unroll
      for (int j = 0; j < 4; ++j) c_dmaw(cw, lds0 + wv * 2048, 2, j);
    }
    if (wv == 0) flag_wait(flags + 8192 + b * 32 + (lane & 31));
    asm volatile("s_barrier" ::: "memory");
  }
  const unsigned vwd_ = 0;
  {
    const int tid = opaque(threadIdx.x), lane = tid & 63, wv = uni(tid >> 6);
    const char* gt = h + (size_t)(tbC * 128 + wv * 16) * 1024;
    const unsigned vt = (lane >> 3) * 1024 + (((lane & 7) ^ (lane >> 3)) << 4);
    const CW cw = c_setup(w1, w3, b, wv, lane);
    const unsigned vwd = (lane >> 3) * 1024 + ((((lane & 7) >> 1) ^ ((lane >> 4) & 3)) << 5) + (lane & 1) * 16;
    const char* gw2 = w2 + (size_t)(wv * 224) * 1024 + cbG * 128;
    if ((wv >> 2) == 0) c_body<4>(smem, lds0, gt, cw, vt, wv, lane, stats, csb, hh, tbC * 128, cbC, gw2, vwd);
    else c_body<3>(smem, lds0, gt, cw, vt, wv, lane, stats, csb, hh, tbC * 128, cbC, gw2, vwd);
  }
  {
    const int tid = opaque(threadIdx.x), lane = tid & 63, wv = uni(tid >> 6);
    const int g = lane >> 4, i = lane & 15, q4 = (lane >> 2) & 3, p4 = lane & 3;
    asm volatile("s_waitcnt vmcnt(0) lgkmcnt(0)\n\ts_barrier" ::: "memory");
    if (tid < 16) flag_set(flags + 16384 + ((tbC >> 1) | (tid << 3) | ((tbC & 1) << 7)) * 32 + cbC);
    const unsigned vwd = (lane >> 3) * 1024 + ((((lane & 7) >> 1) ^ ((lane >> 4) & 3)) << 5) + (lane & 1) * 16;
    const char* gw2 = w2 + (size_t)(wv * 224) * 1024 + cbG * 128;
    d_dma(gw2, vwd, lds0, wv, 9, 0); d_dma(gw2, vwd, lds0, wv, 9, 1);
    const int sw = (2 * g + (q4 >> 1)) & 3;
    const int ro = (4 * g + q4) * 128 + p4 * 8;
    int rn[4];
#pragma unroll
    for (int nt = 0; nt < 4; ++nt) rn[nt] = ro + ((nt ^ sw) << 5);
    auto lda = [&](bf16x8* Af, int j) {
      const char* p0 = smem + d_unit(2 * j) + wv * d_ustr(2 * j);
      const char* p1 = smem + d_unit(2 * j + 1) + wv * d_ustr(2 * j + 1);
#pragma unroll
      for (int nt = 0; nt < 4; ++nt) Af[nt] = cat(ldstr(p0 + rn[nt]), ldstr(p1 + rn[nt]));
    };
    bf16x8 A[2][4], B[7][4], rs;
    lda(A[0], 0); lda(A[1], 1);
    asm volatile("s_waitcnt lgkmcnt(0)" : "+v"(A[0][0]), "+v"(A[0][1]), "+v"(A[0][2]), "+v"(A[0][3]), "+v"(A[1][0]), "+v"(A[1][1]), "+v"(A[1][2]), "+v"(A[1][3]) : : "memory");
#pragma unroll
    for (int u = 10; u < 14; ++u) { d_dma(gw2, vwd, lds0, wv, u, 0); d_dma(gw2, vwd, lds0, wv, u, 1); }
    if (wv == 0) flag_wait(flags + 16384 + b * 32 + (lane & 15));
    asm volatile("s_barrier" ::: "memory");
    const char* ga = hh + ((size_t)(tbG * 4) * 56 + wv * 7) * 1024;
#pragma unroll
    for (int j = 0; j < 7; ++j)
#pragma unroll
      for (int tt = 0; tt < 4; ++tt) gld16(B[j][tt], lane * 16, ga + (size_t)(tt * 56 + j) * 1024);
    const size_t ooff = (size_t)(tbG * 64 + wv * 8) * 1024 + cbG * 128;
    const unsigned vo = (lane >> 3) * 1024 + (lane & 7) * 16;
    gld16(rs, vo, h + ooff);
    f32x4 acc[4][4];
#pragma unroll
    for (int t = 0; t < 4; ++t)
#pragma unroll
      for (int n = 0; n < 4; ++n) acc[t][n] = f32x4{0, 0, 0, 0};
    auto step = [&](auto J) {
      constexpr int j = decltype(J)::value;
      asm volatile("s_waitcnt vmcnt(%4)" : "+v"(B[j][0]), "+v"(B[j][1]), "+v"(B[j][2]), "+v"(B[j][3]) : "n"(25 - 4 * j) : "memory");
#pragma unroll
      for (int nt = 0; nt < 4; ++nt)
#pragma unroll
        for (int tt = 0; tt < 4; ++tt) acc[tt][nt] = mfma16(A[j & 1][nt], B[j][tt], acc[tt][nt]);
      __builtin_amdgcn_sched_barrier(0);
      if constexpr (j + 2 < 7) lda(A[j & 1], j + 2);
    };
    step(IC<0>{}); step(IC<1>{}); step(IC<2>{}); step(IC<3>{}); step(IC<4>{}); step(IC<5>{}); step(IC<6>{});
    {
      const int mo = wv * 6144 + g * 256 + ((i ^ ((g >> 1) * 2)) << 4);
#pragma unroll
      for (int tt = 0; tt < 4; ++tt)
#pragma unroll
        for (int nt = 0; nt < 4; ++nt) {
          const int t = tt * 4 + nt;
          const int base = t < 6 ? S2 + t * 1024 : t < 12 ? S0 + (t - 6) * 1024 : S1 + (t - 12) * 1024;
          *(LAS f32x4*)(LAS char*)(smem + (mo ^ (nt * 64)) + base) = acc[tt][nt];
        }
    }
    asm volatile("s_waitcnt vmcnt(0) lgkmcnt(0)\n\ts_barrier" : "+v"(rs) : : "memory");
    {
      const int c = lane & 7;
      const int tok = (lane >> 3) + 8 * (wv & 1);
      const int t = (wv >> 1) * 4 + (c >> 1);
      const int base = t < 6 ? S2 + t * 1024 : t < 12 ? S0 + (t - 6) * 1024 : S1 + (t - 12) * 1024;
      const char* rp = smem + base + (c & 1) * 512 + ((tok ^ (2 * c)) << 4);
      f32x4 v0 = f32x4{0, 0, 0, 0}, v1 = f32x4{0, 0, 0, 0};
#pragma unroll
      for (int p = 0; p < 8; ++p) {
        v0 += *(const LAS f32x4*)(LAS const char*)(rp + p * 6144);
        v1 += *(const LAS f32x4*)(LAS const char*)(rp + p * 6144 + 256);
      }
      u32x4 r = __builtin_bit_cast(u32x4, rs);
      u32x4 ov = {pack2(v0[0] + bflo(r[0]), v0[1] + bfhi(r[0])), pack2(v0[2] + bflo(r[1]), v0[3] + bfhi(r[1])),
                  pack2(v1[0] + bflo(r[2]), v1[1] + bfhi(r[2])), pack2(v1[2] + bflo(r[3]), v1[3] + bfhi(r[3]))};
      *(u32x4*)(out + ooff + vo) = ov;
    }
  }
}

extern "C" void kernel_launch(void* const* d_in, const int* in_sizes, int n_in, void* d_out, int out_size,
                              void* d_ws, size_t ws_size, hipStream_t stream) {
  const char* x = (const char*)d_in[0];
  const char* wqkv = (const char*)d_in[1];
  const char* wo = (const char*)d_in[2];
  const char* w1 = (const char*)d_in[4];
  const char* w3 = (const char*)d_in[5];
  const char* w2 = (const char*)d_in[6];
  char* ws = (char*)d_ws;
  (void)hipFuncSetAttribute((const void*)k1_qkv, hipFuncAttributeMaxDynamicSharedMemorySize, K1_LDS);
  (void)hipFuncSetAttribute((const void*)k_mega, hipFuncAttributeMaxDynamicSharedMemorySize, MG_LDS);
  k1_qkv<<<256, 576, K1_LDS, stream>>>(x, wqkv, ws, ws + (10u << 20) + (512u << 10), wo, w1, w3, w2, (const char*)d_out);
  k_mega<<<256, 512, MG_LDS, stream>>>(x, wo, w1, w3, w2, ws, (char*)d_out);
}
```

```cpp
#include <hip/hip_runtime.h>
#include <hip/hip_bf16.h>
#include <stdint.h>

typedef short bf16x8 __attribute__((ext_vector_type(8)));
typedef short s16x4 __attribute__((ext_vector_type(4)));
typedef float f32x4 __attribute__((ext_vector_type(4)));
typedef float f32x2 __attribute__((ext_vector_type(2)));
typedef unsigned u32x4 __attribute__((ext_vector_type(4)));
typedef unsigned u32x2 __attribute__((ext_vector_type(2)));
typedef __bf16 bf16x2 __attribute__((ext_vector_type(2)));
#define LAS __attribute__((address_space(3)))
#define DEV __device__ __forceinline__

constexpr int S_ = 2048, D_ = 512, H_ = 32, F_ = 1792;
constexpr float LN_EPS = 1e-5f;

DEV f32x4 mfma16(bf16x8 a, bf16x8 b, f32x4 c) { return __builtin_amdgcn_mfma_f32_16x16x32_bf16(a, b, c, 0, 0, 0); }
DEV unsigned pack2(float a, float b) {
  f32x2 v = {a, b};
  return __builtin_bit_cast(unsigned, __builtin_convertvector(v, bf16x2));
}
DEV float bflo(unsigned u) { return __builtin_bit_cast(float, u << 16); }
DEV float bfhi(unsigned u) { return __builtin_bit_cast(float, u & 0xffff0000u); }
DEV float dot2(unsigned a, unsigned b, float c) {
  return __builtin_amdgcn_fdot2_f32_bf16(__builtin_bit_cast(bf16x2, a), __builtin_bit_cast(bf16x2, b), c, false);
}
DEV void dma16(const char* gptr, unsigned ldsbase) {
  unsigned sv;
  asm volatile("s_mov_b32 %0, m0\n\ts_mov_b32 m0, %2\n\ts_nop 0\n\tglobal_load_lds_dwordx4 %1, off\n\ts_mov_b32 m0, %0"
               : "=&s"(sv) : "v"(gptr), "s"(ldsbase) : "memory");
}
DEV void dma16s(unsigned voff, const char* sbase, unsigned ldsbase) {
  unsigned sv;
  asm volatile("s_mov_b32 %0, m0\n\ts_mov_b32 m0, %3\n\ts_nop 0\n\tglobal_load_lds_dwordx4 %1, %2\n\ts_mov_b32 m0, %0"
               : "=&s"(sv) : "v"(voff), "s"(sbase), "s"(ldsbase) : "memory");
}
DEV void dma16sm(unsigned voff, const char* sbase, unsigned ldsbase, unsigned long long mask) {
  unsigned sv;
  asm volatile("s_mov_b32 %0, m0\n\ts_mov_b32 m0, %3\n\ts_mov_b64 exec, %4\n\tglobal_load_lds_dwordx4 %1, %2\n\ts_mov_b64 exec, -1\n\ts_mov_b32 m0, %0"
               : "=&s"(sv) : "v"(voff), "s"(sbase), "s"(ldsbase), "s"(mask) : "memory");
}
DEV void gld16(bf16x8& d, unsigned voff, const char* sbase) {
  asm volatile("global_load_dwordx4 %0, %1, %2" : "=v"(d) : "v"(voff), "s"(sbase) : "memory");
}
template <int N> DEV void wait_bar() { asm volatile("s_waitcnt vmcnt(%0) lgkmcnt(0)\n\ts_barrier" ::"n"(N) : "memory"); }
DEV void bar() { asm volatile("s_waitcnt lgkmcnt(0)\n\ts_barrier" ::: "memory"); }
DEV bf16x8 lds128(const char* p) { return *(const LAS bf16x8*)(LAS const char*)p; }
DEV s16x4 ldstr(const char* p) { return __builtin_amdgcn_ds_read_tr16_b64_v4i16((LAS s16x4*)p); }
DEV bf16x8 cat(s16x4 a, s16x4 b) { return __builtin_shufflevector(a, b, 0, 1, 2, 3, 4, 5, 6, 7); }
DEV float xsum(float v) { v += __shfl_xor(v, 16); v += __shfl_xor(v, 32); return v; }
DEV float xmax(float v) { v = fmaxf(v, __shfl_xor(v, 16)); v = fmaxf(v, __shfl_xor(v, 32)); return v; }
DEV float dpp_xor1(float v) { return __builtin_bit_cast(float, __builtin_amdgcn_mov_dpp(__builtin_bit_cast(int, v), 0xB1, 0xF, 0xF, true)); }
DEV float dpp_xor2(float v) { return __builtin_bit_cast(float, __builtin_amdgcn_mov_dpp(__builtin_bit_cast(int, v), 0x4E, 0xF, 0xF, true)); }
DEV float dpp_ror4(float v) { return __builtin_bit_cast(float, __builtin_amdgcn_mov_dpp(__builtin_bit_cast(int, v), 0x124, 0xF, 0xF, true)); }
DEV float dpp_ror8(float v) { return __builtin_bit_cast(float, __builtin_amdgcn_mov_dpp(__builtin_bit_cast(int, v), 0x128, 0xF, 0xF, true)); }
DEV unsigned uni(unsigned v) { return __builtin_amdgcn_readfirstlane(v); }
#define WT 31
template <bool W> DEV void st16(char* p, u32x4 v) {
  if constexpr (W) asm volatile("global_store_dwordx4 %0, %1, off sc0 sc1\n\ts_nop 1" ::"v"(p), "v"(v) : "memory");
  else *(u32x4*)p = v;
}
template <bool W> DEV void st8(char* p, u32x2 v) {
  if constexpr (W) asm volatile("global_store_dwordx2 %0, %1, off sc0 sc1" ::"v"(p), "v"(v) : "memory");
  else *(u32x2*)p = v;
}

template <int A, int B> struct Min { static constexpr int v = A < B ? A : B; };

template <int KT, int NK, int NS, int P, class Issue, class Compute>
DEV void pipe_steps(Issue& issue, Compute& compute) {
  if constexpr (KT < NK) {
    wait_bar<(Min<KT + NS - 1, NK>::v - KT - 1) * P>();
    if constexpr (KT + NS - 1 < NK) issue((KT + NS - 1) % NS);
    compute(KT % NS, KT);
    pipe_steps<KT + 1, NK, NS, P>(issue, compute);
  }
}

constexpr int K1_STAGE = 16384 + 12288, K1_NS = 5;
constexpr int K1_LDS = K1_STAGE * K1_NS + 1024 + 1536;
#define SB __builtin_amdgcn_sched_barrier(0)
DEV float xsum2(float v) {
  auto a = __builtin_amdgcn_permlane16_swap(__float_as_uint(v), __float_as_uint(v), false, false);
  v = __uint_as_float(a[0]) + __uint_as_float(a[1]);
  auto b = __builtin_amdgcn_permlane32_swap(__float_as_uint(v), __float_as_uint(v), false, false);
  return __uint_as_float(b[0]) + __uint_as_float(b[1]);
}

__global__ __launch_bounds__(512) void k1_qkv(const char* __restrict__ x, const char* __restrict__ w,
                                              char* __restrict__ qkv, char* __restrict__ flags) {
  extern __shared__ __attribute__((aligned(16))) char smem[];
  const int tid = threadIdx.x, lane = tid & 63, wv = uni(tid >> 6);
  const int g = lane >> 4, i = lane & 15, q4 = (lane >> 2) & 3, p4 = lane & 3;
  const int tg = wv & 3, nh = wv >> 2;
  const int b = blockIdx.x, xcd = b & 7, bi = b >> 3;
  const int cb = (xcd & 1) * 8 + (bi & 7), tb = (xcd >> 1) * 4 + (bi >> 3);
  const unsigned lds0 = (unsigned)(size_t)(LAS char*)smem;

  const char* gx = x + (size_t)(tb * 128 + wv * 16) * 1024;
  const unsigned vt = (lane >> 3) * 1024 + (((lane & 7) ^ (lane >> 3)) << 4);
  const char* gw = w + cb * 192;
  unsigned vw0, vw1;
  {
    int qq = wv * 64 + lane;
    int k = qq / 12, c = (qq % 12) ^ (((k >> 3) & 1) << 1);
    vw0 = k * 3072 + c * 16;
    qq = 512 + wv * 32 + (lane & 31);
    k = qq / 12; c = (qq % 12) ^ (((k >> 3) & 1) << 1);
    vw1 = k * 3072 + c * 16;
  }
  const int k0 = bi & 7;
  auto dma = [&](int s, int j) {
    const int kk = (k0 + s) & 7;
    const unsigned base = lds0 + (s % K1_NS) * K1_STAGE;
    if (j == 0) dma16s(vt, gx + kk * 128, base + wv * 2048);
    else if (j == 1) dma16s(vt, gx + kk * 128 + 8192, base + wv * 2048 + 1024);
    else if (j == 2) dma16s(vw0, gw + kk * (64 * 3072), base + 16384 + wv * 1024);
    else dma16sm(vw1, gw + kk * (64 * 3072), base + 16384 + 8192 + wv * 512, 0xFFFFFFFFull);
  };
#pragma unroll
  for (int s = 0; s < K1_NS - 1; ++s)
#pragma unroll
    for (int j = 0; j < 4; ++j) dma(s, j);

  f32x4 acc[2][3], cs = f32x4{0, 0, 0, 0}, s1v = f32x4{0, 0, 0, 0};
#pragma unroll
  for (int a = 0; a < 3; ++a) { acc[0][a] = f32x4{0, 0, 0, 0}; acc[1][a] = f32x4{0, 0, 0, 0}; }
  float s2a = 0.f, s2b = 0.f;
  const bf16x8 ones = {0x3F80, 0x3F80, 0x3F80, 0x3F80, 0x3F80, 0x3F80, 0x3F80, 0x3F80};
  int toff[2][2];
#pragma unroll
  for (int tt = 0; tt < 2; ++tt) {
    toff[tt][0] = (tg * 32 + (tt ^ nh) * 16 + i) * 128 + ((g ^ (i & 7)) << 4);
    toff[tt][1] = (tg * 32 + (tt ^ nh) * 16 + i) * 128 + (((4 + g) ^ (i & 7)) << 4);
  }
  int woff[3], un[3];
#pragma unroll
  for (int nt = 0; nt < 3; ++nt) {
    int a = nt + tg; a = a >= 3 ? a - 3 : a; a = a >= 3 ? a - 3 : a;
    un[nt] = nh * 3 + a;
    woff[nt] = 16384 + (8 * g + q4) * 192 + ((un[nt] ^ (g & 1)) << 5) + p4 * 8;
  }
  auto ldB = [&](int tt, int kt, int ks) -> bf16x8 { return lds128(smem + (kt % K1_NS) * K1_STAGE + toff[tt][ks]); };
  auto ldA = [&](int nt, int kt, int ks) -> bf16x8 {
    const char* p = smem + (kt % K1_NS) * K1_STAGE + woff[nt] + ks * 6144;
    return cat(ldstr(p), ldstr(p + 768));
  };
  bf16x8 A0[3], A1[3], B0[2], B1[2];
  auto phase = [&](bf16x8 (&Ac)[3], bf16x8 (&Bc)[2], bf16x8 (&An)[3], bf16x8 (&Bn)[2], int kn, int ksn, int dk, int dj) {
    const u32x4 u = __builtin_bit_cast(u32x4, Bc[0]);
#pragma unroll
    for (int m = 0; m < 8; ++m) {
      if (kn < 8) {
        if (m == 0) { Bn[0] = ldB(0, kn, ksn); An[0] = ldA(0, kn, ksn); }
        else if (m == 1) Bn[1] = ldB(1, kn, ksn);
        else if (m < 4) An[m - 1] = ldA(m - 1, kn, ksn);
      }
      if (m < 6) acc[m & 1][m >> 1] = mfma16(Ac[m >> 1], Bc[m & 1], acc[m & 1][m >> 1]);
      else if (m == 6) cs = mfma16(Ac[0], ones, cs);
      else s1v = mfma16(ones, Bc[0], s1v);
      {
        float f = (m & 1) ? bfhi(u[m >> 1]) : bflo(u[m >> 1]);
        asm volatile("" : "+v"(f));
        if (m & 1) s2b = __builtin_fmaf(f, f, s2b); else s2a = __builtin_fmaf(f, f, s2a);
      }
      if (dk < 8 && (m == 2 || m == 5)) dma(dk, dj + (m == 5));
      SB;
    }
  };
  wait_bar<12>();
  B0[0] = ldB(0, 0, 0); A0[0] = ldA(0, 0, 0); B0[1] = ldB(1, 0, 0); A0[1] = ldA(1, 0, 0); A0[2] = ldA(2, 0, 0);
#pragma unroll
  for (int kt = 0; kt < 8; ++kt) {
    phase(A0, B0, A1, B1, kt, 1, kt + 4, 0);
    if (kt < 4) wait_bar<10>();
    else if (kt == 4) wait_bar<8>();
    else if (kt == 5) wait_bar<4>();
    else if (kt == 6) wait_bar<0>();
    phase(A1, B1, A0, B0, kt + 1, 0, kt + 4, 2);
  }

  float* st = (float*)(smem + K1_STAGE * K1_NS);
  float* csp = (float*)(smem + K1_STAGE * K1_NS + 1024);
  const float s2 = xsum2(s2a + s2b);
  if (g == 0) {
    float mean = s1v[0] * (1.f / 512.f);
    float var = fmaxf(s2 * (1.f / 512.f) - mean * mean, 0.f);
    float rstd = rsqrtf(var + LN_EPS);
    *(f32x2*)(st + (tg * 32 + nh * 16 + i) * 2) = f32x2{-mean * rstd, rstd};
  }
  if (i == 0 && tg < 3) *(f32x4*)(csp + un[0] * 16 + 4 * g) = cs;
  bar();
  f32x2 ms[2];
#pragma unroll
  for (int tt = 0; tt < 2; ++tt) ms[tt] = *(const f32x2*)(st + (tg * 32 + (tt ^ nh) * 16 + i) * 2);
#pragma unroll
  for (int nt = 0; nt < 3; ++nt) {
    f32x4 c = *(const f32x4*)(csp + un[nt] * 16 + 4 * g);
    int n0 = cb * 96 + un[nt] * 16;
    char* dst = qkv + (size_t)(n0 >> 4) * (2048 * 32) + g * 8;
#pragma unroll
    for (int tt = 0; tt < 2; ++tt) {
      f32x4 y = acc[tt][nt] * ms[tt][1] + ms[tt][0] * c;
      u32x2 o = {pack2(y[0], y[1]), pack2(y[2], y[3])};
      st8<(WT & 1) != 0>(dst + (size_t)(tb * 128 + tg * 32 + (tt ^ nh) * 16 + i) * 32, o);
    }
  }
  if (tid < 24) st16<true>(flags + ((tid >> 3) * 256 + b) * 128 + (tid & 7) * 16, u32x4{0, 0, 0, 0});
}

constexpr int S0 = 0, S1 = 49152, S2 = 98304, CSP = 147456, MG_LDS = 163840;
DEV void flag_set(unsigned* f) {
  unsigned one = 1;
  asm volatile("global_store_dword %0, %1, off sc0 sc1" ::"v"(f), "v"(one) : "memory");
}
DEV void flag_wait(const unsigned* p) {
  for (int it = 0; it < (1 << 20); ++it) {
    unsigned c;
    asm volatile("global_load_dword %0, %1, off sc0 sc1\n\ts_waitcnt vmcnt(0)" : "=v"(c) : "v"(p) : "memory");
    if (__builtin_amdgcn_ballot_w64(c != 1u) == 0) break;
  }
}
template <int V> struct IC { static constexpr int value = V; };
DEV int opaque(int v) { asm volatile("" : "+v"(v)); return v; }
DEV int opaque_s(int v) { asm volatile("" : "+s"(v)); return v; }

struct CW {
  const char *g1, *g3; unsigned vw; unsigned long long wmask;
};
DEV CW c_setup(const char* w1, const char* w3, int b, int wv, int lane) {
  const int xcd = b & 7, bi = b >> 3;
  const int cb = (xcd & 3) * 4 + (bi & 3);
  CW c;
  c.g1 = w1 + (size_t)(wv * 8) * (F_ * 2) + cb * 224;
  c.g3 = w3 + (size_t)(wv * 8) * (F_ * 2) + cb * 224;
  const int unit = ((lane >> 1) & 7) ^ ((lane >> 4) | ((wv & 1) << 2));
  c.vw = (lane >> 4) * (F_ * 2) + unit * 32 + (lane & 1) * 16;
  c.wmask = __builtin_amdgcn_ballot_w64(unit < 7);
  return c;
}
DEV void c_dmaw(const CW& c, unsigned ldsw, int kt, int j) {
  const unsigned base = ldsw + (kt % 3) * 49152;
  if (j < 2) dma16sm(c.vw, c.g1 + (size_t)(kt * 64 + j * 4) * (F_ * 2), base + 16384 + j * 1024, c.wmask);
  else dma16sm(c.vw, c.g3 + (size_t)(kt * 64 + (j - 2) * 4) * (F_ * 2), base + 32768 + (j - 2) * 1024, c.wmask);
}
DEV constexpr int d_unit(int u) {
  return u < 3 ? S2 + u * 2048 : u < 6 ? S0 + (u - 3) * 2048 : u < 9 ? S1 + (u - 6) * 2048 : u == 9 ? CSP
       : u < 13 ? S2 + (u - 10) * 2048 : S0;
}
DEV constexpr int d_ustr(int u) { return u == 9 ? 2048 : 6144; }
DEV void d_dma(const char* gw2, unsigned vw, unsigned lds0, int wv, int u, int q) {
  dma16s(vw, gw2 + (size_t)(u * 16 + q * 8) * 1024, lds0 + d_unit(u) + wv * d_ustr(u) + q * 1024);
}

template <int NT>
DEV void c_body(char* smem, unsigned lds0, const char* gt, const CW& cw, unsigned vt, int wv, int lane,
                const char* __restrict__ stats, const char* __restrict__ csb, char* __restrict__ hh, int tok0, int cb, const char* gw2, unsigned vwd) {
  const int tg = wv & 3;
  const unsigned ldsw = lds0 + wv * 2048;
  const int g = lane >> 4, i = lane & 15, q4 = (lane >> 2) & 3, p4 = lane & 3;
  f32x4 acc[2][2 * NT];
#pragma unroll
  for (int a = 0; a < 2 * NT; ++a) { acc[0][a] = f32x4{0, 0, 0, 0}; acc[1][a] = f32x4{0, 0, 0, 0}; }
  const int toff0 = (tg * 32 + i) * 128 + ((g ^ (i & 7)) << 4);
  const int toff1 = (tg * 32 + i) * 128 + (((4 + g) ^ (i & 7)) << 4);
  const int odd = cb & 1;
  const int fsw = q4 | ((g & 1) << 2);
  int T[NT], wa[NT];
#pragma unroll
  for (int a = 0; a < NT; ++a) {
    T[a] = NT == 4 ? odd * 3 + a : (a < 2 ? (odd ? 1 : 4) + a : (odd ? 0 : 6));
    wa[a] = 16384 + (8 * g + q4) * 256 + ((T[a] ^ fsw) << 5) + p4 * 8;
  }
  auto dmat = [&](int kt, int j) { dma16s(vt, gt + kt * 128 + j * 8192, ldsw + (kt % 3) * 49152 + j * 1024); };
  auto dma = [&](int kt, int j) {
    if (j < 2) dmat(kt, j); else c_dmaw(cw, ldsw, kt, j - 2);
  };
  auto ldB = [&](bf16x8* B, int kt, int ks) {
    const char* sb = smem + (kt % 3) * 49152 + (ks ? toff1 : toff0);
    B[0] = lds128(sb); B[1] = lds128(sb + 2048);
  };
  auto ldA = [&](int a, int kt, int ks) -> bf16x8 {
    const char* wp = smem + (kt % 3) * 49152 + wa[a < NT ? a : a - NT] + (a < NT ? 0 : 16384) + ks * 8192;
    return cat(ldstr(wp), ldstr(wp + 1024));
  };
  auto mma = [&](int a, bf16x8 A, const bf16x8* B) {
    acc[0][a] = mfma16(A, B[0], acc[0][a]);
    acc[1][a] = mfma16(A, B[1], acc[1][a]);
  };
  bf16x8 A0[2 * NT], A1[2 * NT], B0[2], B1[2];
  bf16x8 sa[2], cp[4];
#pragma unroll
  for (int kt = 0; kt < 3; ++kt) { dmat(kt, 0); dmat(kt, 1); }
  gld16(sa[0], (unsigned)(i * 64 + g * 16), stats + (size_t)(tok0 + tg * 32) * 64);
  gld16(sa[1], (unsigned)(i * 64 + g * 16), stats + (size_t)(tok0 + tg * 32 + 16) * 64);
  const int cq = min(tg * 16 + (lane >> 2), 55);
  if (NT == 3) {
#pragma unroll
    for (int m = 0; m < 4; ++m) gld16(cp[m], (unsigned)(((lane & 3) * 4 + m) * 896 + cq * 16), csb + (size_t)cb * 14336);
  }
  constexpr int X = NT == 3 ? 6 : 2;
  wait_bar<4 + X>();
  float rs[2], mr[2];
  float* csp = (float*)(smem + CSP);
  ldB(B0, 0, 0);
#pragma unroll
  for (int a = 0; a < 2 * NT; ++a) A0[a] = ldA(a, 0, 0);
#pragma unroll
  for (int kt = 0; kt < 8; ++kt) {
    ldB(B1, kt, 1);
#pragma unroll
    for (int a = 0; a < 2 * NT; ++a) {
      if (a < NT) { A1[2 * a] = ldA(2 * a, kt, 1); A1[2 * a + 1] = ldA(2 * a + 1, kt, 1); }
      mma(a, A0[a], B0);
      __builtin_amdgcn_sched_barrier(0);
    }
    if (kt == 0) wait_bar<2 + X>();
    else if (kt != 1) wait_bar<6>();
    else if (NT == 3) asm volatile("s_waitcnt vmcnt(6) lgkmcnt(0)\n\ts_barrier" : "+v"(sa[0]), "+v"(sa[1]), "+v"(cp[0]), "+v"(cp[1]), "+v"(cp[2]), "+v"(cp[3]) : : "memory");
    else asm volatile("s_waitcnt vmcnt(6) lgkmcnt(0)\n\ts_barrier" : "+v"(sa[0]), "+v"(sa[1]) : : "memory");
    if (kt < 7) ldB(B0, kt + 1, 0);
    if (kt == 1) {
#pragma unroll
      for (int tt = 0; tt < 2; ++tt) {
        const f32x4 sv = __builtin_bit_cast(f32x4, sa[tt]);
        const float s1 = xsum2(sv[0] + sv[2]), s2 = xsum2(sv[1] + sv[3]);
        const float mean = s1 * (1.f / 512.f);
        const float var = fmaxf(s2 * (1.f / 512.f) - mean * mean, 0.f);
        rs[tt] = rsqrtf(var + LN_EPS); mr[tt] = mean * rs[tt];
      }
      if (NT == 3) {
        f32x4 c = (__builtin_bit_cast(f32x4, cp[0]) + __builtin_bit_cast(f32x4, cp[1])) +
                  (__builtin_bit_cast(f32x4, cp[2]) + __builtin_bit_cast(f32x4, cp[3]));
#pragma unroll
        for (int e = 0; e < 4; ++e) { c[e] += dpp_xor1(c[e]); c[e] += dpp_xor2(c[e]); }
        if ((lane & 3) == 0) *(f32x4*)(csp + cq * 4) = c;
      }
    }
#pragma unroll
    for (int a = 0; a < 2 * NT; ++a) {
      if (kt < 7 && a < NT) { A0[2 * a] = ldA(2 * a, kt + 1, 0); A0[2 * a + 1] = ldA(2 * a + 1, kt + 1, 0); }
      mma(a, A1[a], B1);
      if (a >= 2 * NT - 6) {
        const int j = a - (2 * NT - 6);
        if (kt + 3 < 8) dma(kt + 3, j);
        else d_dma(gw2, vwd, lds0, wv, (kt - 5) * 3 + (j >> 1), j & 1);
      }
      __builtin_amdgcn_sched_barrier(0);
    }
  }
  unsigned pk[2][NT][2];
#pragma unroll
  for (int tt = 0; tt < 2; ++tt) {
    char* dst = hh + ((size_t)((tok0 >> 4) + tg * 2 + tt) * 56) * 1024 + lane * 16;
#pragma unroll
    for (int a = 0; a < NT; ++a) {
      f32x4 cg = *(const f32x4*)(csp + T[a] * 16 + 4 * g);
      f32x4 cu = *(const f32x4*)(csp + (7 + T[a]) * 16 + 4 * g);
      float r[4];
#pragma unroll
      for (int e = 0; e < 4; ++e) {
        float ga = acc[tt][a][e] * rs[tt] - mr[tt] * cg[e];
        float up = acc[tt][NT + a][e] * rs[tt] - mr[tt] * cu[e];
        float sg = __builtin_amdgcn_rcpf(1.f + __builtin_amdgcn_exp2f(-1.4426950408889634f * ga));
        r[e] = ga * sg * up;
      }
      pk[tt][a][0] = pack2(r[0], r[1]); pk[tt][a][1] = pack2(r[2], r[3]);
      const int G = cb * 7 + T[a];
      if (a & 1) {
        st16<true>(dst + (size_t)(G >> 1) * 1024, u32x4{pk[tt][a - 1][0], pk[tt][a - 1][1], pk[tt][a][0], pk[tt][a][1]});
      } else if (a == NT - 1) {
        st8<true>(dst + (size_t)(G >> 1) * 1024 + (G & 1) * 8, u32x2{pk[tt][a][0], pk[tt][a][1]});
      }
    }
  }
}

__global__ __launch_bounds__(512) void k_mega(const char* __restrict__ x, const char* __restrict__ wo,
                                              const char* __restrict__ w1, const char* __restrict__ w3,
                                              const char* __restrict__ w2, char* __restrict__ ws, char* __restrict__ out) {
  extern __shared__ __attribute__((aligned(16))) char smem[];
  const char* qkv = ws;
  char* o = ws + (6u << 20);
  char* h = ws + (8u << 20);
  char* stats = ws + (10u << 20);
  unsigned* flags = (unsigned*)(ws + (10u << 20) + (512u << 10));
  char* csb = ws + (10u << 20) + (768u << 10);
  char* hh = ws + (11u << 20);
  const int b = blockIdx.x;
  const unsigned lds0 = (unsigned)(size_t)(LAS char*)smem;
  const int cbG = (b >> 3) & 7, tbG = (b & 7) * 4 + (b >> 6);
  const int cbC = (b & 3) * 4 + ((b >> 3) & 3), tbC = ((b >> 2) & 1) * 8 + (b >> 5);

  {
    const int tid = threadIdx.x, lane = tid & 63, wv = uni(tid >> 6);
    const int g = lane >> 4, i = lane & 15, q4 = (lane >> 2) & 3, p4 = lane & 3;
    const int hp = (b & 7) * 2 + ((b >> 3) & 1), qb = b >> 4;
    const int q0 = qb * 128;
    const size_t HS = 2048 * 32;
    {
      const int r = wv >> 1;
      const char* src = qkv + (size_t)((1 + (r >> 1)) * 32 + hp * 2 + (r & 1)) * HS;
#pragma unroll
      for (int j = 0; j < 6; ++j) {
        int ch = ((wv & 1) * 6 + j) * 64 + lane;
        int key = q0 - 256 + (ch >> 1);
        key = key < 0 ? 0 : key;
        dma16(src + (size_t)key * 32 + (ch & 1) * 16, lds0 + r * 12288 + ((wv & 1) * 6 + j) * 1024);
      }
    }
    const int qs = q0 + wv * 16;
    bf16x8 Q[2], cw0, cw1;
    gld16(Q[0], (unsigned)(i * 32 + (g & 1) * 16), qkv + (size_t)(hp * 2) * HS + (size_t)qs * 32);
    gld16(Q[1], (unsigned)(i * 32 + (g & 1) * 16), qkv + (size_t)(hp * 2 + 1) * HS + (size_t)qs * 32);
    asm volatile("s_waitcnt vmcnt(0)\n\ts_barrier" : "+v"(Q[0]), "+v"(Q[1]) : : "memory");
    const unsigned vwo = (lane >> 3) * 1024 + ((((lane & 7) >> 1) ^ ((lane >> 4) & 3)) << 5) + (lane & 1) * 16;
    const char* gwo = wo + (size_t)(wv * 64) * 1024 + cbG * 128;
    const CW cw1s = c_setup(w1, w3, b, wv, lane);
    auto pf = [&](int k) {
      if (k < 2) {
        const int ch = max(wv - 1, 0) * 4 + (lane & 3);
        const char* pc = (ch >= 14 ? w3 + (ch - 14) * 16 : w1 + ch * 16) + (size_t)(tbC * 32 + (lane >> 2)) * (F_ * 2) + cbC * 224;
        if (k == 0) asm volatile("global_load_dwordx4 %0, %1, off" : "=v"(cw0) : "v"(pc) : "memory");
        else asm volatile("global_load_dwordx4 %0, %1, off" : "=v"(cw1) : "v"(pc + 16 * (F_ * 2)) : "memory");
      } else if (k < 10) dma16s(vwo, gwo + (size_t)((k - 2) * 8) * 1024, lds0 + S2 + wv * 8192 + (k - 2) * 1024);
      else if (k < 14) c_dmaw(cw1s, lds0 + wv * 2048, 1, k - 10);
      else if (k == 14) {
        const int c0 = b * 8 + wv;
        dma16s(lane * 16, w2 + (size_t)(c0 < 1792 ? c0 : c0 - 1792) * 1024, lds0 + S1 + wv * 1024);
      }
    };
    if (g >= 2) { Q[0] = bf16x8{0, 0, 0, 0, 0, 0, 0, 0}; Q[1] = bf16x8{0, 0, 0, 0, 0, 0, 0, 0}; }
    const float c = 0.25f * 1.4426950408889634f;
    const float NEG = -1e30f;
    const int lrow = wv * 16;
    f32x4 cin0, cin16;
#pragma unroll
    for (int r = 0; r < 4; ++r) { cin0[r] = (4 * g + r > i) ? 0.f : NEG; cin16[r] = (4 * g + r <= i) ? 0.f : NEG; }
    const bf16x8 ones = {0x3F80, 0x3F80, 0x3F80, 0x3F80, 0x3F80, 0x3F80, 0x3F80, 0x3F80};
    u32x4 ov;
#pragma unroll
    for (int u = 0; u < 2; ++u) {
      const char* Kl = smem + u * 12288;
      const char* Vl = smem + 24576 + u * 12288;
      f32x4 s[17];
#pragma unroll
      for (int t = 0; t < 17; ++t) {
        bf16x8 A = lds128(Kl + (lrow + t * 16 + i) * 32 + (g & 1) * 16);
        s[t] = mfma16(A, Q[u], t == 0 ? cin0 : t == 16 ? cin16 : f32x4{0, 0, 0, 0});
      }
      float m = s[16][0];
#pragma unroll
      for (int t = 0; t < 17; ++t)
#pragma unroll
        for (int r = 0; r < 4; ++r) m = fmaxf(m, s[t][r]);
      m = xmax(m);
      const f32x2 c2 = {c, c}, m2 = {-m * c, -m * c};
      unsigned pk[17][2];
      auto soft = [&](int t) {
        f32x2 a0 = __builtin_elementwise_fma(f32x2{s[t][0], s[t][1]}, c2, m2);
        f32x2 a1 = __builtin_elementwise_fma(f32x2{s[t][2], s[t][3]}, c2, m2);
        pk[t][0] = pack2(__builtin_amdgcn_exp2f(a0[0]), __builtin_amdgcn_exp2f(a0[1]));
        pk[t][1] = pack2(__builtin_amdgcn_exp2f(a1[0]), __builtin_amdgcn_exp2f(a1[1]));
      };
      if (qs >= 256) {
#pragma unroll
        for (int t = 0; t < 17; ++t) {
          soft(t);
          if (u == 0 && t < 15) pf(t);
        }
      } else {
        const int nneg = (256 - qs) >> 4;
#pragma unroll
        for (int t = 0; t < 17; ++t) {
          soft(t);
          if (t < 16) { const unsigned mk = t < nneg ? 0u : ~0u; pk[t][0] &= mk; pk[t][1] &= mk; }
          if (u == 0 && t < 15) pf(t);
        }
      }
      f32x4 oacc = f32x4{0, 0, 0, 0}, lsum = f32x4{0, 0, 0, 0};
#pragma unroll
      for (int tp = 0; tp < 9; ++tp) {
        const int t0 = 2 * tp, t1 = (2 * tp + 1 < 17) ? 2 * tp + 1 : 16;
        bf16x8 A = cat(ldstr(Vl + (lrow + t0 * 16 + 4 * g + q4) * 32 + p4 * 8), ldstr(Vl + (lrow + t1 * 16 + 4 * g + q4) * 32 + p4 * 8));
        u32x4 pb = {pk[t0][0], pk[t0][1], (2 * tp + 1 < 17) ? pk[t1][0] : 0u, (2 * tp + 1 < 17) ? pk[t1][1] : 0u};
        oacc = mfma16(A, __builtin_bit_cast(bf16x8, pb), oacc);
        lsum = mfma16(ones, __builtin_bit_cast(bf16x8, pb), lsum);
      }
      const float inv = __builtin_amdgcn_rcpf(lsum[0]);
      ov[2 * u] = pack2(oacc[0] * inv, oacc[1] * inv);
      ov[2 * u + 1] = pack2(oacc[2] * inv, oacc[3] * inv);
    }
    st16<true>(o + ((size_t)(qs >> 4) * 16 + hp) * 1024 + lane * 16, ov);
    asm volatile("s_waitcnt vmcnt(0) lgkmcnt(0)\n\ts_barrier" : "+v"(cw0), "+v"(cw1) : : "memory");
    if (tid < 16) flag_set(flags + (((qb >> 1) | (tid << 3) | ((qb & 1) << 7)) * 32 + hp));
    {
      const CW cw = c_setup(w1, w3, b, wv, lane);
#pragma unroll
      for (int j = 0; j < 4; ++j) c_dmaw(cw, lds0 + wv * 2048, 0, j);
    }
    if (wv == 0) { flag_wait(flags + b * 32 + (lane & 15)); }
    else {
      const u32x4 ua = __builtin_bit_cast(u32x4, cw0), ub = __builtin_bit_cast(u32x4, cw1);
      f32x4 lo, hi;
#pragma unroll
      for (int e = 0; e < 2; ++e) {
        lo[2 * e] = bflo(ua[e]) + bflo(ub[e]); lo[2 * e + 1] = bfhi(ua[e]) + bfhi(ub[e]);
        hi[2 * e] = bflo(ua[2 + e]) + bflo(ub[2 + e]); hi[2 * e + 1] = bfhi(ua[2 + e]) + bfhi(ub[2 + e]);
      }
#pragma unroll
      for (int e = 0; e < 4; ++e) {
        lo[e] += dpp_ror4(lo[e]); lo[e] += dpp_ror8(lo[e]); lo[e] = xsum2(lo[e]);
        hi[e] += dpp_ror4(hi[e]); hi[e] += dpp_ror8(hi[e]); hi[e] = xsum2(hi[e]);
      }
      if (lane < 4) {
        char* d = csb + (size_t)(cbC * 16 + tbC) * 896 + ((wv - 1) * 4 + lane) * 32;
        st16<true>(d, __builtin_bit_cast(u32x4, lo));
        st16<true>(d + 16, __builtin_bit_cast(u32x4, hi));
      }
      asm volatile("s_waitcnt vmcnt(0)" ::: "memory");
    }
    asm volatile("s_barrier" ::: "memory");
    if (tid >= 64 && tid < 80) flag_set(flags + 8192 + ((cbC >> 2) | ((tid & 1) << 2) | ((cbC & 3) << 3) | (((tid & 15) >> 1) << 5)) * 32 + 16 + tbC);
  }
  {
    const int tid = opaque(threadIdx.x), lane = tid & 63, wv = uni(tid >> 6);
    const int g = lane >> 4, i = lane & 15, q4 = (lane >> 2) & 3, p4 = lane & 3;
    const int kp = wv & 3, th = wv >> 2;
    bf16x8 B[4][2], rs;
    const char* ga = o + ((size_t)(tbG * 4 + th * 2) * 16 + kp * 4) * 1024;
#pragma unroll
    for (int s = 0; s < 4; ++s) {
      gld16(B[s][0], lane * 16, ga + s * 1024);
      gld16(B[s][1], lane * 16, ga + (16 + s) * 1024);
    }
    const size_t ooff = (size_t)(tbG * 64 + wv * 8) * 1024 + cbG * 128;
    const unsigned vo = (lane >> 3) * 1024 + (lane & 7) * 16;
    gld16(rs, vo, x + ooff);
    const int sw = (2 * g + (q4 >> 1)) & 3;
    const char* wr = smem + S2 + kp * 16384 + (4 * g + q4) * 128 + p4 * 8;
    bf16x8 A[4][4];
#pragma unroll
    for (int s = 0; s < 4; ++s)
#pragma unroll
      for (int nt = 0; nt < 4; ++nt) {
        const char* p = wr + s * 4096 + ((nt ^ sw) << 5);
        A[s][nt] = cat(ldstr(p), ldstr(p + 2048));
      }
    f32x4 acc[2][4];
#pragma unroll
    for (int t = 0; t < 2; ++t)
#pragma unroll
      for (int n = 0; n < 4; ++n) acc[t][n] = f32x4{0, 0, 0, 0};
    asm volatile("s_waitcnt vmcnt(7)" : "+v"(B[0][0]), "+v"(B[0][1]) : : "memory");
#pragma unroll
    for (int nt = 0; nt < 4; ++nt) { acc[0][nt] = mfma16(A[0][nt], B[0][0], acc[0][nt]); acc[1][nt] = mfma16(A[0][nt], B[0][1], acc[1][nt]); }
    asm volatile("s_waitcnt vmcnt(5)" : "+v"(B[1][0]), "+v"(B[1][1]) : : "memory");
#pragma unroll
    for (int nt = 0; nt < 4; ++nt) { acc[0][nt] = mfma16(A[1][nt], B[1][0], acc[0][nt]); acc[1][nt] = mfma16(A[1][nt], B[1][1], acc[1][nt]); }
    asm volatile("s_waitcnt vmcnt(3)" : "+v"(B[2][0]), "+v"(B[2][1]) : : "memory");
#pragma unroll
    for (int nt = 0; nt < 4; ++nt) { acc[0][nt] = mfma16(A[2][nt], B[2][0], acc[0][nt]); acc[1][nt] = mfma16(A[2][nt], B[2][1], acc[1][nt]); }
    asm volatile("s_waitcnt vmcnt(1)" : "+v"(B[3][0]), "+v"(B[3][1]) : : "memory");
#pragma unroll
    for (int nt = 0; nt < 4; ++nt) { acc[0][nt] = mfma16(A[3][nt], B[3][0], acc[0][nt]); acc[1][nt] = mfma16(A[3][nt], B[3][1], acc[1][nt]); }
    bar();
    {
      const int mo = S2 + kp * 16384 + g * 256 + ((i ^ ((g >> 1) * 2)) << 4);
#pragma unroll
      for (int tt = 0; tt < 2; ++tt)
#pragma unroll
        for (int nt = 0; nt < 4; ++nt)
          *(LAS f32x4*)(LAS char*)(smem + (mo ^ (nt * 64)) + ((th * 2 + tt) * 4 + nt) * 1024) = acc[tt][nt];
    }
    asm volatile("s_waitcnt vmcnt(0) lgkmcnt(0)\n\ts_barrier" : "+v"(rs) : : "memory");
    {
      const int c = lane & 7;
      const int tok = (lane >> 3) + 8 * (wv & 1);
      const char* rp = smem + S2 + ((wv >> 1) * 4 + (c >> 1)) * 1024 + (c & 1) * 512 + ((tok ^ (2 * c)) << 4);
      f32x4 v0 = f32x4{0, 0, 0, 0}, v1 = f32x4{0, 0, 0, 0};
#pragma unroll
      for (int p = 0; p < 4; ++p) {
        v0 += *(const LAS f32x4*)(LAS const char*)(rp + p * 16384);
        v1 += *(const LAS f32x4*)(LAS const char*)(rp + p * 16384 + 256);
      }
      u32x4 r = __builtin_bit_cast(u32x4, rs);
      u32x4 ov = {pack2(v0[0] + bflo(r[0]), v0[1] + bfhi(r[0])), pack2(v0[2] + bflo(r[1]), v0[3] + bfhi(r[1])),
                  pack2(v1[0] + bflo(r[2]), v1[1] + bfhi(r[2])), pack2(v1[2] + bflo(r[3]), v1[3] + bfhi(r[3]))};
      st16<true>(h + ooff + vo, ov);
      float s1 = 0.f, s2 = 0.f;
#pragma unroll
      for (int e = 0; e < 4; ++e) { s1 = dot2(ov[e], 0x3F803F80u, s1); s2 = dot2(ov[e], ov[e], s2); }
#pragma unroll
      for (int m = 1; m < 8; m <<= 1) { s1 += __shfl_xor(s1, m); s2 += __shfl_xor(s2, m); }
      if (c == 0) st8<true>(stats + ((size_t)(tbG * 64 + wv * 8 + (lane >> 3)) * 8 + cbG) * 8,
                            u32x2{__float_as_uint(s1), __float_as_uint(s2)});
    }
    asm volatile("s_waitcnt vmcnt(0) lgkmcnt(0)\n\ts_barrier" ::: "memory");
    if (tid < 16) flag_set(flags + 8192 + ((tid & 3) | ((tbG >> 4) << 2) | ((tid >> 2) << 3) | (((tbG >> 1) & 7) << 5)) * 32 + (tbG & 1) * 8 + cbG);
    {
      const CW cw = c_setup(w1, w3, b, wv, lane);
#pragma unroll
      for (int j = 0; j < 4; ++j) c_dmaw(cw, lds0 + wv * 2048, 2, j);
    }
    if (wv == 0) flag_wait(flags + 8192 + b * 32 + (lane & 31));
    asm volatile("s_barrier" ::: "memory");
  }
  const unsigned vwd_ = 0;
  {
    const int tid = opaque(threadIdx.x), lane = tid & 63, wv = uni(tid >> 6);
    const char* gt = h + (size_t)(tbC * 128 + wv * 16) * 1024;
    const unsigned vt = (lane >> 3) * 1024 + (((lane & 7) ^ (lane >> 3)) << 4);
    const CW cw = c_setup(w1, w3, b, wv, lane);
    const unsigned vwd = (lane >> 3) * 1024 + ((((lane & 7) >> 1) ^ ((lane >> 4) & 3)) << 5) + (lane & 1) * 16;
    const char* gw2 = w2 + (size_t)(wv * 224) * 1024 + cbG * 128;
    if ((wv >> 2) == 0) c_body<4>(smem, lds0, gt, cw, vt, wv, lane, stats, csb, hh, tbC * 128, cbC, gw2, vwd);
    else c_body<3>(smem, lds0, gt, cw, vt, wv, lane, stats, csb, hh, tbC * 128, cbC, gw2, vwd);
  }
  {
    const int tid = opaque(threadIdx.x), lane = tid & 63, wv = uni(tid >> 6);
    const int g = lane >> 4, i = lane & 15, q4 = (lane >> 2) & 3, p4 = lane & 3;
    asm volatile("s_waitcnt vmcnt(0) lgkmcnt(0)\n\ts_barrier" ::: "memory");
    if (tid < 16) flag_set(flags + 16384 + ((tbC >> 1) | (tid << 3) | ((tbC & 1) << 7)) * 32 + cbC);
    const unsigned vwd = (lane >> 3) * 1024 + ((((lane & 7) >> 1) ^ ((lane >> 4) & 3)) << 5) + (lane & 1) * 16;
    const char* gw2 = w2 + (size_t)(wv * 224) * 1024 + cbG * 128;
    d_dma(gw2, vwd, lds0, wv, 9, 0); d_dma(gw2, vwd, lds0, wv, 9, 1);
    const int sw = (2 * g + (q4 >> 1)) & 3;
    const int ro = (4 * g + q4) * 128 + p4 * 8;
    int rn[4];
#pragma unroll
    for (int nt = 0; nt < 4; ++nt) rn[nt] = ro + ((nt ^ sw) << 5);
    auto lda = [&](bf16x8* Af, int j) {
      const char* p0 = smem + d_unit(2 * j) + wv * d_ustr(2 * j);
      const char* p1 = smem + d_unit(2 * j + 1) + wv * d_ustr(2 * j + 1);
#pragma unroll
      for (int nt = 0; nt < 4; ++nt) Af[nt] = cat(ldstr(p0 + rn[nt]), ldstr(p1 + rn[nt]));
    };
    bf16x8 A[2][4], B[7][4], rs;
    lda(A[0], 0); lda(A[1], 1);
    asm volatile("s_waitcnt lgkmcnt(0)" : "+v"(A[0][0]), "+v"(A[0][1]), "+v"(A[0][2]), "+v"(A[0][3]), "+v"(A[1][0]), "+v"(A[1][1]), "+v"(A[1][2]), "+v"(A[1][3]) : : "memory");
#pragma unroll
    for (int u = 10; u < 14; ++u) { d_dma(gw2, vwd, lds0, wv, u, 0); d_dma(gw2, vwd, lds0, wv, u, 1); }
    if (wv == 0) flag_wait(flags + 16384 + b * 32 + (lane & 15));
    asm volatile("s_barrier" ::: "memory");
    const char* ga = hh + ((size_t)(tbG * 4) * 56 + wv * 7) * 1024;
#pragma unroll
    for (int j = 0; j < 7; ++j)
#pragma unroll
      for (int tt = 0; tt < 4; ++tt) gld16(B[j][tt], lane * 16, ga + (size_t)(tt * 56 + j) * 1024);
    const size_t ooff = (size_t)(tbG * 64 + wv * 8) * 1024 + cbG * 128;
    const unsigned vo = (lane >> 3) * 1024 + (lane & 7) * 16;
    gld16(rs, vo, h + ooff);
    f32x4 acc[4][4];
#pragma unroll
    for (int t = 0; t < 4; ++t)
#pragma unroll
      for (int n = 0; n < 4; ++n) acc[t][n] = f32x4{0, 0, 0, 0};
    auto step = [&](auto J) {
      constexpr int j = decltype(J)::value;
      asm volatile("s_waitcnt vmcnt(%4)" : "+v"(B[j][0]), "+v"(B[j][1]), "+v"(B[j][2]), "+v"(B[j][3]) : "n"(25 - 4 * j) : "memory");
#pragma unroll
      for (int nt = 0; nt < 4; ++nt)
#pragma unroll
        for (int tt = 0; tt < 4; ++tt) acc[tt][nt] = mfma16(A[j & 1][nt], B[j][tt], acc[tt][nt]);
      __builtin_amdgcn_sched_barrier(0);
      if constexpr (j + 2 < 7) lda(A[j & 1], j + 2);
    };
    step(IC<0>{}); step(IC<1>{}); step(IC<2>{}); step(IC<3>{}); step(IC<4>{}); step(IC<5>{}); step(IC<6>{});
    {
      const int mo = wv * 6144 + g * 256 + ((i ^ ((g >> 1) * 2)) << 4);
#pragma unroll
      for (int tt = 0; tt < 4; ++tt)
#pragma unroll
        for (int nt = 0; nt < 4; ++nt) {
          const int t = tt * 4 + nt;
          const int base = t < 6 ? S2 + t * 1024 : t < 12 ? S0 + (t - 6) * 1024 : S1 + (t - 12) * 1024;
          *(LAS f32x4*)(LAS char*)(smem + (mo ^ (nt * 64)) + base) = acc[tt][nt];
        }
    }
    asm volatile("s_waitcnt vmcnt(0) lgkmcnt(0)\n\ts_barrier" : "+v"(rs) : : "memory");
    {
      const int c = lane & 7;
      const int tok = (lane >> 3) + 8 * (wv & 1);
      const int t = (wv >> 1) * 4 + (c >> 1);
      const int base = t < 6 ? S2 + t * 1024 : t < 12 ? S0 + (t - 6) * 1024 : S1 + (t - 12) * 1024;
      const char* rp = smem + base + (c & 1) * 512 + ((tok ^ (2 * c)) << 4);
      f32x4 v0 = f32x4{0, 0, 0, 0}, v1 = f32x4{0, 0, 0, 0};
#pragma unroll
      for (int p = 0; p < 8; ++p) {
        v0 += *(const LAS f32x4*)(LAS const char*)(rp + p * 6144);
        v1 += *(const LAS f32x4*)(LAS const char*)(rp + p * 6144 + 256);
      }
      u32x4 r = __builtin_bit_cast(u32x4, rs);
      u32x4 ov = {pack2(v0[0] + bflo(r[0]), v0[1] + bfhi(r[0])), pack2(v0[2] + bflo(r[1]), v0[3] + bfhi(r[1])),
                  pack2(v1[0] + bflo(r[2]), v1[1] + bfhi(r[2])), pack2(v1[2] + bflo(r[3]), v1[3] + bfhi(r[3]))};
      *(u32x4*)(out + ooff + vo) = ov;
    }
  }
}

extern "C" void kernel_launch(void* const* d_in, const int* in_sizes, int n_in, void* d_out, int out_size,
                              void* d_ws, size_t ws_size, hipStream_t stream) {
  const char* x = (const char*)d_in[0];
  const char* wqkv = (const char*)d_in[1];
  const char* wo = (const char*)d_in[2];
  const char* w1 = (const char*)d_in[4];
  const char* w3 = (const char*)d_in[5];
  const char* w2 = (const char*)d_in[6];
  char* ws = (char*)d_ws;
  (void)hipFuncSetAttribute((const void*)k1_qkv, hipFuncAttributeMaxDynamicSharedMemorySize, K1_LDS);
  (void)hipFuncSetAttribute((const void*)k_mega, hipFuncAttributeMaxDynamicSharedMemorySize, MG_LDS);
  k1_qkv<<<256, 512, K1_LDS, stream>>>(x, wqkv, ws, ws + (10u << 20) + (512u << 10));
  k_mega<<<256, 512, MG_LDS, stream>>>(x, wo, w1, w3, w2, ws, (char*)d_out);
}
```

```cpp
#include <hip/hip_runtime.h>
#include <hip/hip_bf16.h>
#include <stdint.h>

typedef short bf16x8 __attribute__((ext_vector_type(8)));
typedef short s16x4 __attribute__((ext_vector_type(4)));
typedef float f32x4 __attribute__((ext_vector_type(4)));
typedef float f32x2 __attribute__((ext_vector_type(2)));
typedef unsigned u32x4 __attribute__((ext_vector_type(4)));
typedef unsigned u32x2 __attribute__((ext_vector_type(2)));
typedef __bf16 bf16x2 __attribute__((ext_vector_type(2)));
#define LAS __attribute__((address_space(3)))
#define DEV __device__ __forceinline__

constexpr int S_ = 2048, D_ = 512, H_ = 32, F_ = 1792;
constexpr float LN_EPS = 1e-5f;

typedef float f32x16 __attribute__((ext_vector_type(16)));
DEV f32x16 mfma32(bf16x8 a, bf16x8 b, f32x16 c) { return __builtin_amdgcn_mfma_f32_32x32x16_bf16(a, b, c, 0, 0, 0); }
DEV f32x4 mfma16(bf16x8 a, bf16x8 b, f32x4 c) { return __builtin_amdgcn_mfma_f32_16x16x32_bf16(a, b, c, 0, 0, 0); }
DEV unsigned pack2(float a, float b) {
  f32x2 v = {a, b};
  return __builtin_bit_cast(unsigned, __builtin_convertvector(v, bf16x2));
}
DEV float bflo(unsigned u) { return __builtin_bit_cast(float, u << 16); }
DEV float bfhi(unsigned u) { return __builtin_bit_cast(float, u & 0xffff0000u); }
DEV float dot2(unsigned a, unsigned b, float c) {
  return __builtin_amdgcn_fdot2_f32_bf16(__builtin_bit_cast(bf16x2, a), __builtin_bit_cast(bf16x2, b), c, false);
}
DEV void dma16(const char* gptr, unsigned ldsbase) {
  unsigned sv;
  asm volatile("s_mov_b32 %0, m0\n\ts_mov_b32 m0, %2\n\ts_nop 0\n\tglobal_load_lds_dwordx4 %1, off\n\ts_mov_b32 m0, %0"
               : "=&s"(sv) : "v"(gptr), "s"(ldsbase) : "memory");
}
DEV void dma16s(unsigned voff, const char* sbase, unsigned ldsbase) {
  unsigned sv;
  asm volatile("s_mov_b32 %0, m0\n\ts_mov_b32 m0, %3\n\ts_nop 0\n\tglobal_load_lds_dwordx4 %1, %2\n\ts_mov_b32 m0, %0"
               : "=&s"(sv) : "v"(voff), "s"(sbase), "s"(ldsbase) : "memory");
}
DEV void dma16sm(unsigned voff, const char* sbase, unsigned ldsbase, unsigned long long mask) {
  unsigned sv;
  asm volatile("s_mov_b32 %0, m0\n\ts_mov_b32 m0, %3\n\ts_mov_b64 exec, %4\n\tglobal_load_lds_dwordx4 %1, %2\n\ts_mov_b64 exec, -1\n\ts_mov_b32 m0, %0"
               : "=&s"(sv) : "v"(voff), "s"(sbase), "s"(ldsbase), "s"(mask) : "memory");
}
DEV void gld16(bf16x8& d, unsigned voff, const char* sbase) {
  asm volatile("global_load_dwordx4 %0, %1, %2" : "=v"(d) : "v"(voff), "s"(sbase) : "memory");
}
template <int N> DEV void wait_bar() { asm volatile("s_waitcnt vmcnt(%0) lgkmcnt(0)\n\ts_barrier" ::"n"(N) : "memory"); }
DEV void bar() { asm volatile("s_waitcnt lgkmcnt(0)\n\ts_barrier" ::: "memory"); }
DEV bf16x8 lds128(const char* p) { return *(const LAS bf16x8*)(LAS const char*)p; }
DEV s16x4 ldstr(const char* p) { return __builtin_amdgcn_ds_read_tr16_b64_v4i16((LAS s16x4*)p); }
DEV bf16x8 cat(s16x4 a, s16x4 b) { return __builtin_shufflevector(a, b, 0, 1, 2, 3, 4, 5, 6, 7); }
DEV float xsum(float v) { v += __shfl_xor(v, 16); v += __shfl_xor(v, 32); return v; }
DEV float xmax(float v) { v = fmaxf(v, __shfl_xor(v, 16)); v = fmaxf(v, __shfl_xor(v, 32)); return v; }
DEV float dpp_xor1(float v) { return __builtin_bit_cast(float, __builtin_amdgcn_mov_dpp(__builtin_bit_cast(int, v), 0xB1, 0xF, 0xF, true)); }
DEV float dpp_xor2(float v) { return __builtin_bit_cast(float, __builtin_amdgcn_mov_dpp(__builtin_bit_cast(int, v), 0x4E, 0xF, 0xF, true)); }
DEV float dpp_ror4(float v) { return __builtin_bit_cast(float, __builtin_amdgcn_mov_dpp(__builtin_bit_cast(int, v), 0x124, 0xF, 0xF, true)); }
DEV float dpp_ror8(float v) { return __builtin_bit_cast(float, __builtin_amdgcn_mov_dpp(__builtin_bit_cast(int, v), 0x128, 0xF, 0xF, true)); }
DEV unsigned uni(unsigned v) { return __builtin_amdgcn_readfirstlane(v); }
#define WT 31
template <bool W> DEV void st16(char* p, u32x4 v) {
  if constexpr (W) asm volatile("global_store_dwordx4 %0, %1, off sc0 sc1\n\ts_nop 1" ::"v"(p), "v"(v) : "memory");
  else *(u32x4*)p = v;
}
template <bool W> DEV void st8(char* p, u32x2 v) {
  if constexpr (W) asm volatile("global_store_dwordx2 %0, %1, off sc0 sc1" ::"v"(p), "v"(v) : "memory");
  else *(u32x2*)p = v;
}

template <int A, int B> struct Min { static constexpr int v = A < B ? A : B; };

template <int KT, int NK, int NS, int P, class Issue, class Compute>
DEV void pipe_steps(Issue& issue, Compute& compute) {
  if constexpr (KT < NK) {
    wait_bar<(Min<KT + NS - 1, NK>::v - KT - 1) * P>();
    if constexpr (KT + NS - 1 < NK) issue((KT + NS - 1) % NS);
    compute(KT % NS, KT);
    pipe_steps<KT + 1, NK, NS, P>(issue, compute);
  }
}

constexpr int K1_STAGE = 16384 + 12288, K1_NS = 5;
constexpr int K1_LDS = K1_STAGE * K1_NS + 1024 + 1536;
#define SB __builtin_amdgcn_sched_barrier(0)
DEV float xsum2(float v) {
  auto a = __builtin_amdgcn_permlane16_swap(__float_as_uint(v), __float_as_uint(v), false, false);
  v = __uint_as_float(a[0]) + __uint_as_float(a[1]);
  auto b = __builtin_amdgcn_permlane32_swap(__float_as_uint(v), __float_as_uint(v), false, false);
  return __uint_as_float(b[0]) + __uint_as_float(b[1]);
}

__global__ __launch_bounds__(512) void k1_qkv(const char* __restrict__ x, const char* __restrict__ w,
                                              char* __restrict__ qkv, char* __restrict__ flags) {
  extern __shared__ __attribute__((aligned(16))) char smem[];
  const int tid = threadIdx.x, lane = tid & 63, wv = uni(tid >> 6);
  const int g = lane >> 4, i = lane & 15, q4 = (lane >> 2) & 3, p4 = lane & 3;
  const int tg = wv & 3, nh = wv >> 2;
  const int b = blockIdx.x, xcd = b & 7, bi = b >> 3;
  const int cb = (xcd & 1) * 8 + (bi & 7), tb = (xcd >> 1) * 4 + (bi >> 3);
  const unsigned lds0 = (unsigned)(size_t)(LAS char*)smem;

  const char* gx = x + (size_t)(tb * 128 + wv * 16) * 1024;
  const unsigned vt = (lane >> 3) * 1024 + (((lane & 7) ^ (lane >> 3)) << 4);
  const char* gw = w + cb * 192;
  unsigned vw0, vw1;
  {
    int qq = wv * 64 + lane;
    int k = qq / 12, c = (qq % 12) ^ (((k >> 3) & 1) << 1);
    vw0 = k * 3072 + c * 16;
    qq = 512 + wv * 32 + (lane & 31);
    k = qq / 12; c = (qq % 12) ^ (((k >> 3) & 1) << 1);
    vw1 = k * 3072 + c * 16;
  }
  const int k0 = bi & 7;
  auto dma = [&](int s, int j) {
    const int kk = (k0 + s) & 7;
    const unsigned base = lds0 + (s % K1_NS) * K1_STAGE;
    if (j == 0) dma16s(vt, gx + kk * 128, base + wv * 2048);
    else if (j == 1) dma16s(vt, gx + kk * 128 + 8192, base + wv * 2048 + 1024);
    else if (j == 2) dma16s(vw0, gw + kk * (64 * 3072), base + 16384 + wv * 1024);
    else dma16sm(vw1, gw + kk * (64 * 3072), base + 16384 + 8192 + wv * 512, 0xFFFFFFFFull);
  };
#pragma unroll
  for (int s = 0; s < K1_NS - 1; ++s)
#pragma unroll
    for (int j = 0; j < 4; ++j) dma(s, j);

  f32x4 acc[2][3], cs = f32x4{0, 0, 0, 0}, s1v = f32x4{0, 0, 0, 0};
#pragma unroll
  for (int a = 0; a < 3; ++a) { acc[0][a] = f32x4{0, 0, 0, 0}; acc[1][a] = f32x4{0, 0, 0, 0}; }
  float s2a = 0.f, s2b = 0.f;
  const bf16x8 ones = {0x3F80, 0x3F80, 0x3F80, 0x3F80, 0x3F80, 0x3F80, 0x3F80, 0x3F80};
  int toff[2][2];
#pragma unroll
  for (int tt = 0; tt < 2; ++tt) {
    toff[tt][0] = (tg * 32 + (tt ^ nh) * 16 + i) * 128 + ((g ^ (i & 7)) << 4);
    toff[tt][1] = (tg * 32 + (tt ^ nh) * 16 + i) * 128 + (((4 + g) ^ (i & 7)) << 4);
  }
  int woff[3], un[3];
#pragma unroll
  for (int nt = 0; nt < 3; ++nt) {
    int a = nt + tg; a = a >= 3 ? a - 3 : a; a = a >= 3 ? a - 3 : a;
    un[nt] = nh * 3 + a;
    woff[nt] = 16384 + (8 * g + q4) * 192 + ((un[nt] ^ (g & 1)) << 5) + p4 * 8;
  }
  auto ldB = [&](int tt, int kt, int ks) -> bf16x8 { return lds128(smem + (kt % K1_NS) * K1_STAGE + toff[tt][ks]); };
  auto ldA = [&](int nt, int kt, int ks) -> bf16x8 {
    const char* p = smem + (kt % K1_NS) * K1_STAGE + woff[nt] + ks * 6144;
    return cat(ldstr(p), ldstr(p + 768));
  };
  bf16x8 A0[3], A1[3], B0[2], B1[2];
  auto phase = [&](bf16x8 (&Ac)[3], bf16x8 (&Bc)[2], bf16x8 (&An)[3], bf16x8 (&Bn)[2], int kn, int ksn, int dk, int dj) {
    const u32x4 u = __builtin_bit_cast(u32x4, Bc[0]);
#pragma unroll
    for (int m = 0; m < 8; ++m) {
      if (kn < 8) {
        if (m == 0) { Bn[0] = ldB(0, kn, ksn); An[0] = ldA(0, kn, ksn); }
        else if (m == 1) Bn[1] = ldB(1, kn, ksn);
        else if (m < 4) An[m - 1] = ldA(m - 1, kn, ksn);
      }
      if (m < 6) acc[m & 1][m >> 1] = mfma16(Ac[m >> 1], Bc[m & 1], acc[m & 1][m >> 1]);
      else if (m == 6) cs = mfma16(Ac[0], ones, cs);
      else s1v = mfma16(ones, Bc[0], s1v);
      {
        float f = (m & 1) ? bfhi(u[m >> 1]) : bflo(u[m >> 1]);
        asm volatile("" : "+v"(f));
        if (m & 1) s2b = __builtin_fmaf(f, f, s2b); else s2a = __builtin_fmaf(f, f, s2a);
      }
      if (dk < 8 && (m == 2 || m == 5)) dma(dk, dj + (m == 5));
      SB;
    }
  };
  wait_bar<12>();
  B0[0] = ldB(0, 0, 0); A0[0] = ldA(0, 0, 0); B0[1] = ldB(1, 0, 0); A0[1] = ldA(1, 0, 0); A0[2] = ldA(2, 0, 0);
#pragma unroll
  for (int kt = 0; kt < 8; ++kt) {
    phase(A0, B0, A1, B1, kt, 1, kt + 4, 0);
    if (kt < 4) wait_bar<10>();
    else if (kt == 4) wait_bar<8>();
    else if (kt == 5) wait_bar<4>();
    else if (kt == 6) wait_bar<0>();
    phase(A1, B1, A0, B0, kt + 1, 0, kt + 4, 2);
  }

  float* st = (float*)(smem + K1_STAGE * K1_NS);
  float* csp = (float*)(smem + K1_STAGE * K1_NS + 1024);
  const float s2 = xsum2(s2a + s2b);
  if (g == 0) {
    float mean = s1v[0] * (1.f / 512.f);
    float var = fmaxf(s2 * (1.f / 512.f) - mean * mean, 0.f);
    float rstd = rsqrtf(var + LN_EPS);
    *(f32x2*)(st + (tg * 32 + nh * 16 + i) * 2) = f32x2{-mean * rstd, rstd};
  }
  if (i == 0 && tg < 3) *(f32x4*)(csp + un[0] * 16 + 4 * g) = cs;
  bar();
  f32x2 ms[2];
#pragma unroll
  for (int tt = 0; tt < 2; ++tt) ms[tt] = *(const f32x2*)(st + (tg * 32 + (tt ^ nh) * 16 + i) * 2);
#pragma unroll
  for (int nt = 0; nt < 3; ++nt) {
    f32x4 c = *(const f32x4*)(csp + un[nt] * 16 + 4 * g);
    int n0 = cb * 96 + un[nt] * 16;
    char* dst = qkv + (size_t)(n0 >> 4) * (2048 * 32) + g * 8;
#pragma unroll
    for (int tt = 0; tt < 2; ++tt) {
      f32x4 y = acc[tt][nt] * ms[tt][1] + ms[tt][0] * c;
      u32x2 o = {pack2(y[0], y[1]), pack2(y[2], y[3])};
      st8<(WT & 1) != 0>(dst + (size_t)(tb * 128 + tg * 32 + (tt ^ nh) * 16 + i) * 32, o);
    }
  }
  if (tid < 24) st16<true>(flags + ((tid >> 3) * 256 + b) * 128 + (tid & 7) * 16, u32x4{0, 0, 0, 0});
}

constexpr int S0 = 0, S1 = 49152, S2 = 98304, CSP = 147456, MG_LDS = 163840;
DEV void flag_set(unsigned* f) {
  unsigned one = 1;
  asm volatile("global_store_dword %0, %1, off sc0 sc1" ::"v"(f), "v"(one) : "memory");
}
DEV void flag_wait(const unsigned* p) {
  for (int it = 0; it < (1 << 20); ++it) {
    unsigned c;
    asm volatile("global_load_dword %0, %1, off sc0 sc1\n\ts_waitcnt vmcnt(0)" : "=v"(c) : "v"(p) : "memory");
    if (__builtin_amdgcn_ballot_w64(c != 1u) == 0) break;
  }
}
template <int V> struct IC { static constexpr int value = V; };
DEV int opaque(int v) { asm volatile("" : "+v"(v)); return v; }
DEV int opaque_s(int v) { asm volatile("" : "+s"(v)); return v; }

struct CW {
  const char *g1, *g3; unsigned vw; unsigned long long wmask;
};
DEV CW c_setup(const char* w1, const char* w3, int b, int wv, int lane) {
  const int xcd = b & 7, bi = b >> 3;
  const int cb = (xcd & 3) * 4 + (bi & 3);
  CW c;
  c.g1 = w1 + (size_t)(wv * 8) * (F_ * 2) + cb * 224;
  c.g3 = w3 + (size_t)(wv * 8) * (F_ * 2) + cb * 224;
  const int unit = ((lane >> 1) & 7) ^ ((lane >> 4) | ((wv & 1) << 2));
  c.vw = (lane >> 4) * (F_ * 2) + unit * 32 + (lane & 1) * 16;
  c.wmask = __builtin_amdgcn_ballot_w64(unit < 7);
  return c;
}
DEV void c_dmaw(const CW& c, unsigned ldsw, int kt, int j) {
  const unsigned base = ldsw + (kt % 3) * 49152;
  if (j < 2) dma16sm(c.vw, c.g1 + (size_t)(kt * 64 + j * 4) * (F_ * 2), base + 16384 + j * 1024, c.wmask);
  else dma16sm(c.vw, c.g3 + (size_t)(kt * 64 + (j - 2) * 4) * (F_ * 2), base + 32768 + (j - 2) * 1024, c.wmask);
}
DEV constexpr int d_unit(int u) {
  return u < 3 ? S2 + u * 2048 : u < 6 ? S0 + (u - 3) * 2048 : u < 9 ? S1 + (u - 6) * 2048 : u == 9 ? CSP
       : u < 13 ? S2 + (u - 10) * 2048 : S0;
}
DEV constexpr int d_ustr(int u) { return u == 9 ? 2048 : 6144; }
DEV void d_dma(const char* gw2, unsigned vw, unsigned lds0, int wv, int u, int q) {
  dma16s(vw, gw2 + (size_t)(u * 16 + q * 8) * 1024, lds0 + d_unit(u) + wv * d_ustr(u) + q * 1024);
}

template <int NT>
DEV void c_body(char* smem, unsigned lds0, const char* gt, const CW& cw, unsigned vt, int wv, int lane,
                const char* __restrict__ stats, const char* __restrict__ csb, char* __restrict__ hh, int tok0, int cb, const char* gw2, unsigned vwd) {
  const int tg = wv & 3;
  const unsigned ldsw = lds0 + wv * 2048;
  const int g = lane >> 4, i = lane & 15, q4 = (lane >> 2) & 3, p4 = lane & 3;
  f32x4 acc[2][2 * NT];
#pragma unroll
  for (int a = 0; a < 2 * NT; ++a) { acc[0][a] = f32x4{0, 0, 0, 0}; acc[1][a] = f32x4{0, 0, 0, 0}; }
  const int toff0 = (tg * 32 + i) * 128 + ((g ^ (i & 7)) << 4);
  const int toff1 = (tg * 32 + i) * 128 + (((4 + g) ^ (i & 7)) << 4);
  const int odd = cb & 1;
  const int fsw = q4 | ((g & 1) << 2);
  int T[NT], wa[NT];
#pragma unroll
  for (int a = 0; a < NT; ++a) {
    T[a] = NT == 4 ? odd * 3 + a : (a < 2 ? (odd ? 1 : 4) + a : (odd ? 0 : 6));
    wa[a] = 16384 + (8 * g + q4) * 256 + ((T[a] ^ fsw) << 5) + p4 * 8;
  }
  auto dmat = [&](int kt, int j) { dma16s(vt, gt + kt * 128 + j * 8192, ldsw + (kt % 3) * 49152 + j * 1024); };
  auto dma = [&](int kt, int j) {
    if (j < 2) dmat(kt, j); else c_dmaw(cw, ldsw, kt, j - 2);
  };
  auto ldB = [&](bf16x8* B, int kt, int ks) {
    const char* sb = smem + (kt % 3) * 49152 + (ks ? toff1 : toff0);
    B[0] = lds128(sb); B[1] = lds128(sb + 2048);
  };
  auto ldA = [&](int a, int kt, int ks) -> bf16x8 {
    const char* wp = smem + (kt % 3) * 49152 + wa[a < NT ? a : a - NT] + (a < NT ? 0 : 16384) + ks * 8192;
    return cat(ldstr(wp), ldstr(wp + 1024));
  };
  auto mma = [&](int a, bf16x8 A, const bf16x8* B) {
    acc[0][a] = mfma16(A, B[0], acc[0][a]);
    acc[1][a] = mfma16(A, B[1], acc[1][a]);
  };
  bf16x8 A0[2 * NT], A1[2 * NT], B0[2], B1[2];
  bf16x8 sa[2], cp[4];
#pragma unroll
  for (int kt = 0; kt < 3; ++kt) { dmat(kt, 0); dmat(kt, 1); }
  gld16(sa[0], (unsigned)(i * 64 + g * 16), stats + (size_t)(tok0 + tg * 32) * 64);
  gld16(sa[1], (unsigned)(i * 64 + g * 16), stats + (size_t)(tok0 + tg * 32 + 16) * 64);
  const int cq = min(tg * 16 + (lane >> 2), 55);
  if (NT == 3) {
#pragma unroll
    for (int m = 0; m < 4; ++m) gld16(cp[m], (unsigned)(((lane & 3) * 4 + m) * 896 + cq * 16), csb + (size_t)cb * 14336);
  }
  constexpr int X = NT == 3 ? 6 : 2;
  wait_bar<4 + X>();
  float rs[2], mr[2];
  float* csp = (float*)(smem + CSP);
  ldB(B0, 0, 0);
#pragma unroll
  for (int a = 0; a < 2 * NT; ++a) A0[a] = ldA(a, 0, 0);
#pragma unroll
  for (int kt = 0; kt < 8; ++kt) {
    ldB(B1, kt, 1);
#pragma unroll
    for (int a = 0; a < 2 * NT; ++a) {
      if (a < NT) { A1[2 * a] = ldA(2 * a, kt, 1); A1[2 * a + 1] = ldA(2 * a + 1, kt, 1); }
      mma(a, A0[a], B0);
      __builtin_amdgcn_sched_barrier(0);
    }
    if (kt == 0) wait_bar<2 + X>();
    else if (kt != 1) wait_bar<6>();
    else if (NT == 3) asm volatile("s_waitcnt vmcnt(6) lgkmcnt(0)\n\ts_barrier" : "+v"(sa[0]), "+v"(sa[1]), "+v"(cp[0]), "+v"(cp[1]), "+v"(cp[2]), "+v"(cp[3]) : : "memory");
    else asm volatile("s_waitcnt vmcnt(6) lgkmcnt(0)\n\ts_barrier" : "+v"(sa[0]), "+v"(sa[1]) : : "memory");
    if (kt < 7) ldB(B0, kt + 1, 0);
    if (kt == 1) {
#pragma unroll
      for (int tt = 0; tt < 2; ++tt) {
        const f32x4 sv = __builtin_bit_cast(f32x4, sa[tt]);
        const float s1 = xsum2(sv[0] + sv[2]), s2 = xsum2(sv[1] + sv[3]);
        const float mean = s1 * (1.f / 512.f);
        const float var = fmaxf(s2 * (1.f / 512.f) - mean * mean, 0.f);
        rs[tt] = rsqrtf(var + LN_EPS); mr[tt] = mean * rs[tt];
      }
      if (NT == 3) {
        f32x4 c = (__builtin_bit_cast(f32x4, cp[0]) + __builtin_bit_cast(f32x4, cp[1])) +
                  (__builtin_bit_cast(f32x4, cp[2]) + __builtin_bit_cast(f32x4, cp[3]));
#pragma unroll
        for (int e = 0; e < 4; ++e) { c[e] += dpp_xor1(c[e]); c[e] += dpp_xor2(c[e]); }
        if ((lane & 3) == 0) *(f32x4*)(csp + cq * 4) = c;
      }
    }
#pragma unroll
    for (int a = 0; a < 2 * NT; ++a) {
      if (kt < 7 && a < NT) { A0[2 * a] = ldA(2 * a, kt + 1, 0); A0[2 * a + 1] = ldA(2 * a + 1, kt + 1, 0); }
      mma(a, A1[a], B1);
      if (a >= 2 * NT - 6) {
        const int j = a - (2 * NT - 6);
        if (kt + 3 < 8) dma(kt + 3, j);
        else d_dma(gw2, vwd, lds0, wv, (kt - 5) * 3 + (j >> 1), j & 1);
      }
      __builtin_amdgcn_sched_barrier(0);
    }
  }
  unsigned pk[2][NT][2];
#pragma unroll
  for (int tt = 0; tt < 2; ++tt) {
    char* dst = hh + ((size_t)((tok0 >> 4) + tg * 2 + tt) * 56) * 1024 + lane * 16;
#pragma unroll
    for (int a = 0; a < NT; ++a) {
      f32x4 cg = *(const f32x4*)(csp + T[a] * 16 + 4 * g);
      f32x4 cu = *(const f32x4*)(csp + (7 + T[a]) * 16 + 4 * g);
      float r[4];
#pragma unroll
      for (int e = 0; e < 4; ++e) {
        float ga = acc[tt][a][e] * rs[tt] - mr[tt] * cg[e];
        float up = acc[tt][NT + a][e] * rs[tt] - mr[tt] * cu[e];
        float sg = __builtin_amdgcn_rcpf(1.f + __builtin_amdgcn_exp2f(-1.4426950408889634f * ga));
        r[e] = ga * sg * up;
      }
      pk[tt][a][0] = pack2(r[0], r[1]); pk[tt][a][1] = pack2(r[2], r[3]);
      const int G = cb * 7 + T[a];
      if (a & 1) {
        st16<true>(dst + (size_t)(G >> 1) * 1024, u32x4{pk[tt][a - 1][0], pk[tt][a - 1][1], pk[tt][a][0], pk[tt][a][1]});
      } else if (a == NT - 1) {
        st8<true>(dst + (size_t)(G >> 1) * 1024 + (G & 1) * 8, u32x2{pk[tt][a][0], pk[tt][a][1]});
      }
    }
  }
}

__global__ __launch_bounds__(512) void k_mega(const char* __restrict__ x, const char* __restrict__ wo,
                                              const char* __restrict__ w1, const char* __restrict__ w3,
                                              const char* __restrict__ w2, char* __restrict__ ws, char* __restrict__ out) {
  extern __shared__ __attribute__((aligned(16))) char smem[];
  const char* qkv = ws;
  char* o = ws + (6u << 20);
  char* h = ws + (8u << 20);
  char* stats = ws + (10u << 20);
  unsigned* flags = (unsigned*)(ws + (10u << 20) + (512u << 10));
  char* csb = ws + (10u << 20) + (768u << 10);
  char* hh = ws + (11u << 20);
  const int b = blockIdx.x;
  const unsigned lds0 = (unsigned)(size_t)(LAS char*)smem;
  const int cbG = (b >> 3) & 7, tbG = (b & 7) * 4 + (b >> 6);
  const int cbC = (b & 3) * 4 + ((b >> 3) & 3), tbC = ((b >> 2) & 1) * 8 + (b >> 5);

  {
    const int tid = threadIdx.x, lane = tid & 63, wv = uni(tid >> 6);
    const int c32 = lane & 31, hf = lane >> 5, q4 = (lane >> 2) & 3, p4 = lane & 3;
    const int hp = (b & 7) * 2 + ((b >> 3) & 1), qb = b >> 4;
    const int q0 = qb * 128;
    const size_t HS = 2048 * 32;
    {
      const int r = wv >> 1;
      const char* src = qkv + (size_t)((1 + (r >> 1)) * 32 + hp * 2 + (r & 1)) * HS;
#pragma unroll
      for (int j = 0; j < 6; ++j) {
        int ch = ((wv & 1) * 6 + j) * 64 + lane;
        int key = q0 - 256 + (ch >> 1);
        key = key < 0 ? 0 : key;
        const int half = r < 2 ? (ch ^ (ch >> 5)) & 1 : ch & 1;
        dma16(src + (size_t)key * 32 + half * 16, lds0 + r * 12288 + ((wv & 1) * 6 + j) * 1024);
      }
    }
    const int u = wv >> 2, qg = wv & 3;
    const int qs = q0 + qg * 32;
    bf16x8 Q, cw0, cw1;
    gld16(Q, (unsigned)(c32 * 32 + hf * 16), qkv + (size_t)(hp * 2 + u) * HS + (size_t)qs * 32);
    const float c = 0.25f * 1.4426950408889634f;
    const float NEG = -1e30f;
    f32x16 cin0, cin8;
#pragma unroll
    for (int r = 0; r < 16; ++r) {
      const int kl = (r & 3) + 8 * (r >> 2) + 4 * hf;
      cin0[r] = kl > c32 ? 0.f : NEG; cin8[r] = kl <= c32 ? 0.f : NEG;
    }
    asm volatile("s_waitcnt vmcnt(0)\n\ts_barrier" : "+v"(Q), "+v"(cin0), "+v"(cin8) : : "memory");
    const unsigned vwo = (lane >> 3) * 1024 + ((((lane & 7) >> 1) ^ ((lane >> 4) & 3)) << 5) + (lane & 1) * 16;
    const char* gwo = wo + (size_t)(wv * 64) * 1024 + cbG * 128;
    const CW cw1s = c_setup(w1, w3, b, wv, lane);
    auto pf = [&](int k) {
      if (k < 2) {
        const int ch = max(wv - 1, 0) * 4 + (lane & 3);
        const char* pc = (ch >= 14 ? w3 + (ch - 14) * 16 : w1 + ch * 16) + (size_t)(tbC * 32 + (lane >> 2)) * (F_ * 2) + cbC * 224;
        if (k == 0) asm volatile("global_load_dwordx4 %0, %1, off" : "=v"(cw0) : "v"(pc) : "memory");
        else asm volatile("global_load_dwordx4 %0, %1, off" : "=v"(cw1) : "v"(pc + 16 * (F_ * 2)) : "memory");
      } else if (k < 10) dma16s(vwo, gwo + (size_t)((k - 2) * 8) * 1024, lds0 + S2 + wv * 8192 + (k - 2) * 1024);
      else if (k < 14) c_dmaw(cw1s, lds0 + wv * 2048, 1, k - 10);
      else if (k == 14) {
        const int c0 = b * 8 + wv;
        dma16s(lane * 16, w2 + (size_t)(c0 < 1792 ? c0 : c0 - 1792) * 1024, lds0 + S1 + wv * 1024);
      }
    };
    {
      const char* Kl = smem + u * 12288 + (qg * 32 + c32) * 32 + ((hf ^ (c32 >> 4)) << 4);
      const char* Vl = smem + 24576 + u * 12288 + (qg * 32 + 4 * hf + q4) * 32 + p4 * 8;
      f32x16 s[9];
#pragma unroll
      for (int t = 0; t < 9; ++t) {
        f32x16 z;
#pragma unroll
        for (int r = 0; r < 16; ++r) z[r] = 0.f;
        s[t] = mfma32(lds128(Kl + t * 1024), Q, t == 0 ? cin0 : t == 8 ? cin8 : z);
      }
      float m = s[8][0];
#pragma unroll
      for (int t = 0; t < 9; ++t)
#pragma unroll
        for (int r = 0; r < 16; ++r) m = fmaxf(m, s[t][r]);
      {
        auto sw = __builtin_amdgcn_permlane32_swap(__float_as_uint(m), __float_as_uint(m), false, false);
        m = fmaxf(__uint_as_float(sw[0]), __uint_as_float(sw[1]));
      }
      const f32x2 c2 = {c, c}, m2 = {-m * c, -m * c};
      const int nneg = qs >= 256 ? 0 : (256 - qs) >> 5;
      f32x16 oacc;
#pragma unroll
      for (int r = 0; r < 16; ++r) oacc[r] = 0.f;
      float ls0 = 0.f, ls1 = 0.f;
#pragma unroll
      for (int t = 0; t < 9; ++t) {
        const float mt = t < nneg ? NEG : m2[0];
        const f32x2 mt2 = {mt, mt};
#pragma unroll
        for (int sh = 0; sh < 2; ++sh) {
          u32x4 pb;
#pragma unroll
          for (int e = 0; e < 4; ++e) {
            f32x2 a = __builtin_elementwise_fma(f32x2{s[t][8 * sh + 2 * e], s[t][8 * sh + 2 * e + 1]}, c2, mt2);
            pb[e] = pack2(__builtin_amdgcn_exp2f(a[0]), __builtin_amdgcn_exp2f(a[1]));
          }
          ls0 = dot2(pb[0], 0x3F803F80u, ls0); ls1 = dot2(pb[1], 0x3F803F80u, ls1);
          ls0 = dot2(pb[2], 0x3F803F80u, ls0); ls1 = dot2(pb[3], 0x3F803F80u, ls1);
          const char* vp = Vl + (t * 32 + sh * 16) * 32;
          oacc = mfma32(cat(ldstr(vp), ldstr(vp + 256)), __builtin_bit_cast(bf16x8, pb), oacc);
          if (2 * t + sh < 15) pf(2 * t + sh);
        }
      }
      float ls = ls0 + ls1;
      {
        auto sw = __builtin_amdgcn_permlane32_swap(__float_as_uint(ls), __float_as_uint(ls), false, false);
        ls = __uint_as_float(sw[0]) + __uint_as_float(sw[1]);
      }
      const float inv = __builtin_amdgcn_rcpf(ls);
      const u32x4 ov = {pack2(oacc[0] * inv, oacc[1] * inv), pack2(oacc[2] * inv, oacc[3] * inv),
                        pack2(oacc[4] * inv, oacc[5] * inv), pack2(oacc[6] * inv, oacc[7] * inv)};
      st16<true>(o + ((size_t)((qs >> 4) + (c32 >> 4)) * 16 + hp) * 1024 + ((2 * u + hf) * 16 + (c32 & 15)) * 16, ov);
    }
    asm volatile("s_waitcnt vmcnt(0) lgkmcnt(0)\n\ts_barrier" : "+v"(cw0), "+v"(cw1) : : "memory");
    if (tid < 16) flag_set(flags + (((qb >> 1) | (tid << 3) | ((qb & 1) << 7)) * 32 + hp));
    {
      const CW cw = c_setup(w1, w3, b, wv, lane);
#pragma unroll
      for (int j = 0; j < 4; ++j) c_dmaw(cw, lds0 + wv * 2048, 0, j);
    }
    if (wv == 0) { flag_wait(flags + b * 32 + (lane & 15)); }
    else {
      const u32x4 ua = __builtin_bit_cast(u32x4, cw0), ub = __builtin_bit_cast(u32x4, cw1);
      f32x4 lo, hi;
#pragma unroll
      for (int e = 0; e < 2; ++e) {
        lo[2 * e] = bflo(ua[e]) + bflo(ub[e]); lo[2 * e + 1] = bfhi(ua[e]) + bfhi(ub[e]);
        hi[2 * e] = bflo(ua[2 + e]) + bflo(ub[2 + e]); hi[2 * e + 1] = bfhi(ua[2 + e]) + bfhi(ub[2 + e]);
      }
#pragma unroll
      for (int e = 0; e < 4; ++e) {
        lo[e] += dpp_ror4(lo[e]); lo[e] += dpp_ror8(lo[e]); lo[e] = xsum2(lo[e]);
        hi[e] += dpp_ror4(hi[e]); hi[e] += dpp_ror8(hi[e]); hi[e] = xsum2(hi[e]);
      }
      if (lane < 4) {
        char* d = csb + (size_t)(cbC * 16 + tbC) * 896 + ((wv - 1) * 4 + lane) * 32;
        st16<true>(d, __builtin_bit_cast(u32x4, lo));
        st16<true>(d + 16, __builtin_bit_cast(u32x4, hi));
      }
      asm volatile("s_waitcnt vmcnt(0)" ::: "memory");
    }
    asm volatile("s_barrier" ::: "memory");
    if (tid >= 64 && tid < 80) flag_set(flags + 8192 + ((cbC >> 2) | ((tid & 1) << 2) | ((cbC & 3) << 3) | (((tid & 15) >> 1) << 5)) * 32 + 16 + tbC);
  }
  {
    const int tid = opaque(threadIdx.x), lane = tid & 63, wv = uni(tid >> 6);
    const int g = lane >> 4, i = lane & 15, q4 = (lane >> 2) & 3, p4 = lane & 3;
    const int kp = wv & 3, th = wv >> 2;
    bf16x8 B[4][2], rs;
    const char* ga = o + ((size_t)(tbG * 4 + th * 2) * 16 + kp * 4) * 1024;
#pragma unroll
    for (int s = 0; s < 4; ++s) {
      gld16(B[s][0], lane * 16, ga + s * 1024);
      gld16(B[s][1], lane * 16, ga + (16 + s) * 1024);
    }
    const size_t ooff = (size_t)(tbG * 64 + wv * 8) * 1024 + cbG * 128;
    const unsigned vo = (lane >> 3) * 1024 + (lane & 7) * 16;
    gld16(rs, vo, x + ooff);
    const int sw = (2 * (g & 1) + (q4 >> 1)) & 3;
    const char* wr = smem + S2 + kp * 16384 + (16 * (g >> 1) + 4 * (g & 1) + q4) * 128 + p4 * 8;
    bf16x8 A[4][4];
#pragma unroll
    for (int s = 0; s < 4; ++s)
#pragma unroll
      for (int nt = 0; nt < 4; ++nt) {
        const char* p = wr + s * 4096 + ((nt ^ sw) << 5);
        A[s][nt] = cat(ldstr(p), ldstr(p + 1024));
      }
    f32x4 acc[2][4];
#pragma unroll
    for (int t = 0; t < 2; ++t)
#pragma unroll
      for (int n = 0; n < 4; ++n) acc[t][n] = f32x4{0, 0, 0, 0};
    asm volatile("s_waitcnt vmcnt(7)" : "+v"(B[0][0]), "+v"(B[0][1]) : : "memory");
#pragma unroll
    for (int nt = 0; nt < 4; ++nt) { acc[0][nt] = mfma16(A[0][nt], B[0][0], acc[0][nt]); acc[1][nt] = mfma16(A[0][nt], B[0][1], acc[1][nt]); }
    asm volatile("s_waitcnt vmcnt(5)" : "+v"(B[1][0]), "+v"(B[1][1]) : : "memory");
#pragma unroll
    for (int nt = 0; nt < 4; ++nt) { acc[0][nt] = mfma16(A[1][nt], B[1][0], acc[0][nt]); acc[1][nt] = mfma16(A[1][nt], B[1][1], acc[1][nt]); }
    asm volatile("s_waitcnt vmcnt(3)" : "+v"(B[2][0]), "+v"(B[2][1]) : : "memory");
#pragma unroll
    for (int nt = 0; nt < 4; ++nt) { acc[0][nt] = mfma16(A[2][nt], B[2][0], acc[0][nt]); acc[1][nt] = mfma16(A[2][nt], B[2][1], acc[1][nt]); }
    asm volatile("s_waitcnt vmcnt(1)" : "+v"(B[3][0]), "+v"(B[3][1]) : : "memory");
#pragma unroll
    for (int nt = 0; nt < 4; ++nt) { acc[0][nt] = mfma16(A[3][nt], B[3][0], acc[0][nt]); acc[1][nt] = mfma16(A[3][nt], B[3][1], acc[1][nt]); }
    bar();
    {
      const int mo = S2 + kp * 16384 + g * 256 + ((i ^ ((g >> 1) * 2)) << 4);
#pragma unroll
      for (int tt = 0; tt < 2; ++tt)
#pragma unroll
        for (int nt = 0; nt < 4; ++nt)
          *(LAS f32x4*)(LAS char*)(smem + (mo ^ (nt * 64)) + ((th * 2 + tt) * 4 + nt) * 1024) = acc[tt][nt];
    }
    asm volatile("s_waitcnt vmcnt(0) lgkmcnt(0)\n\ts_barrier" : "+v"(rs) : : "memory");
    {
      const int c = lane & 7;
      const int tok = (lane >> 3) + 8 * (wv & 1);
      const char* rp = smem + S2 + ((wv >> 1) * 4 + (c >> 1)) * 1024 + (c & 1) * 512 + ((tok ^ (2 * c)) << 4);
      f32x4 v0 = f32x4{0, 0, 0, 0}, v1 = f32x4{0, 0, 0, 0};
#pragma unroll
      for (int p = 0; p < 4; ++p) {
        v0 += *(const LAS f32x4*)(LAS const char*)(rp + p * 16384);
        v1 += *(const LAS f32x4*)(LAS const char*)(rp + p * 16384 + 256);
      }
      u32x4 r = __builtin_bit_cast(u32x4, rs);
      u32x4 ov = {pack2(v0[0] + bflo(r[0]), v0[1] + bfhi(r[0])), pack2(v0[2] + bflo(r[1]), v0[3] + bfhi(r[1])),
                  pack2(v1[0] + bflo(r[2]), v1[1] + bfhi(r[2])), pack2(v1[2] + bflo(r[3]), v1[3] + bfhi(r[3]))};
      st16<true>(h + ooff + vo, ov);
      float s1 = 0.f, s2 = 0.f;
#pragma unroll
      for (int e = 0; e < 4; ++e) { s1 = dot2(ov[e], 0x3F803F80u, s1); s2 = dot2(ov[e], ov[e], s2); }
#pragma unroll
      for (int m = 1; m < 8; m <<= 1) { s1 += __shfl_xor(s1, m); s2 += __shfl_xor(s2, m); }
      if (c == 0) st8<true>(stats + ((size_t)(tbG * 64 + wv * 8 + (lane >> 3)) * 8 + cbG) * 8,
                            u32x2{__float_as_uint(s1), __float_as_uint(s2)});
    }
    asm volatile("s_waitcnt vmcnt(0) lgkmcnt(0)\n\ts_barrier" ::: "memory");
    if (tid < 16) flag_set(flags + 8192 + ((tid & 3) | ((tbG >> 4) << 2) | ((tid >> 2) << 3) | (((tbG >> 1) & 7) << 5)) * 32 + (tbG & 1) * 8 + cbG);
    {
      const CW cw = c_setup(w1, w3, b, wv, lane);
#pragma unroll
      for (int j = 0; j < 4; ++j) c_dmaw(cw, lds0 + wv * 2048, 2, j);
    }
    if (wv == 0) flag_wait(flags + 8192 + b * 32 + (lane & 31));
    asm volatile("s_barrier" ::: "memory");
  }
  const unsigned vwd_ = 0;
  {
    const int tid = opaque(threadIdx.x), lane = tid & 63, wv = uni(tid >> 6);
    const char* gt = h + (size_t)(tbC * 128 + wv * 16) * 1024;
    const unsigned vt = (lane >> 3) * 1024 + (((lane & 7) ^ (lane >> 3)) << 4);
    const CW cw = c_setup(w1, w3, b, wv, lane);
    const unsigned vwd = (lane >> 3) * 1024 + ((((lane & 7) >> 1) ^ ((lane >> 4) & 3)) << 5) + (lane & 1) * 16;
    const char* gw2 = w2 + (size_t)(wv * 224) * 1024 + cbG * 128;
    if ((wv >> 2) == 0) c_body<4>(smem, lds0, gt, cw, vt, wv, lane, stats, csb, hh, tbC * 128, cbC, gw2, vwd);
    else c_body<3>(smem, lds0, gt, cw, vt, wv, lane, stats, csb, hh, tbC * 128, cbC, gw2, vwd);
  }
  {
    const int tid = opaque(threadIdx.x), lane = tid & 63, wv = uni(tid >> 6);
    const int g = lane >> 4, i = lane & 15, q4 = (lane >> 2) & 3, p4 = lane & 3;
    asm volatile("s_waitcnt vmcnt(0) lgkmcnt(0)\n\ts_barrier" ::: "memory");
    if (tid < 16) flag_set(flags + 16384 + ((tbC >> 1) | (tid << 3) | ((tbC & 1) << 7)) * 32 + cbC);
    const unsigned vwd = (lane >> 3) * 1024 + ((((lane & 7) >> 1) ^ ((lane >> 4) & 3)) << 5) + (lane & 1) * 16;
    const char* gw2 = w2 + (size_t)(wv * 224) * 1024 + cbG * 128;
    d_dma(gw2, vwd, lds0, wv, 9, 0); d_dma(gw2, vwd, lds0, wv, 9, 1);
    const int sw = (2 * g + (q4 >> 1)) & 3;
    const int ro = (4 * g + q4) * 128 + p4 * 8;
    int rn[4];
#pragma unroll
    for (int nt = 0; nt < 4; ++nt) rn[nt] = ro + ((nt ^ sw) << 5);
    auto lda = [&](bf16x8* Af, int j) {
      const char* p0 = smem + d_unit(2 * j) + wv * d_ustr(2 * j);
      const char* p1 = smem + d_unit(2 * j + 1) + wv * d_ustr(2 * j + 1);
#pragma unroll
      for (int nt = 0; nt < 4; ++nt) Af[nt] = cat(ldstr(p0 + rn[nt]), ldstr(p1 + rn[nt]));
    };
    bf16x8 A[2][4], B[7][4], rs;
    lda(A[0], 0); lda(A[1], 1);
    asm volatile("s_waitcnt lgkmcnt(0)" : "+v"(A[0][0]), "+v"(A[0][1]), "+v"(A[0][2]), "+v"(A[0][3]), "+v"(A[1][0]), "+v"(A[1][1]), "+v"(A[1][2]), "+v"(A[1][3]) : : "memory");
#pragma unroll
    for (int u = 10; u < 14; ++u) { d_dma(gw2, vwd, lds0, wv, u, 0); d_dma(gw2, vwd, lds0, wv, u, 1); }
    if (wv == 0) flag_wait(flags + 16384 + b * 32 + (lane & 15));
    asm volatile("s_barrier" ::: "memory");
    const char* ga = hh + ((size_t)(tbG * 4) * 56 + wv * 7) * 1024;
#pragma unroll
    for (int j = 0; j < 7; ++j)
#pragma unroll
      for (int tt = 0; tt < 4; ++tt) gld16(B[j][tt], lane * 16, ga + (size_t)(tt * 56 + j) * 1024);
    const size_t ooff = (size_t)(tbG * 64 + wv * 8) * 1024 + cbG * 128;
    const unsigned vo = (lane >> 3) * 1024 + (lane & 7) * 16;
    gld16(rs, vo, h + ooff);
    f32x4 acc[4][4];
#pragma unroll
    for (int t = 0; t < 4; ++t)
#pragma unroll
      for (int n = 0; n < 4; ++n) acc[t][n] = f32x4{0, 0, 0, 0};
    auto step = [&](auto J) {
      constexpr int j = decltype(J)::value;
      asm volatile("s_waitcnt vmcnt(%4)" : "+v"(B[j][0]), "+v"(B[j][1]), "+v"(B[j][2]), "+v"(B[j][3]) : "n"(25 - 4 * j) : "memory");
#pragma unroll
      for (int nt = 0; nt < 4; ++nt)
#pragma unroll
        for (int tt = 0; tt < 4; ++tt) acc[tt][nt] = mfma16(A[j & 1][nt], B[j][tt], acc[tt][nt]);
      __builtin_amdgcn_sched_barrier(0);
      if constexpr (j + 2 < 7) lda(A[j & 1], j + 2);
    };
    step(IC<0>{}); step(IC<1>{}); step(IC<2>{}); step(IC<3>{}); step(IC<4>{}); step(IC<5>{}); step(IC<6>{});
    {
      const int mo = wv * 6144 + g * 256 + ((i ^ ((g >> 1) * 2)) << 4);
#pragma unroll
      for (int tt = 0; tt < 4; ++tt)
#pragma unroll
        for (int nt = 0; nt < 4; ++nt) {
          const int t = tt * 4 + nt;
          const int base = t < 6 ? S2 + t * 1024 : t < 12 ? S0 + (t - 6) * 1024 : S1 + (t - 12) * 1024;
          *(LAS f32x4*)(LAS char*)(smem + (mo ^ (nt * 64)) + base) = acc[tt][nt];
        }
    }
    asm volatile("s_waitcnt vmcnt(0) lgkmcnt(0)\n\ts_barrier" : "+v"(rs) : : "memory");
    {
      const int c = lane & 7;
      const int tok = (lane >> 3) + 8 * (wv & 1);
      const int t = (wv >> 1) * 4 + (c >> 1);
      const int base = t < 6 ? S2 + t * 1024 : t < 12 ? S0 + (t - 6) * 1024 : S1 + (t - 12) * 1024;
      const char* rp = smem + base + (c & 1) * 512 + ((tok ^ (2 * c)) << 4);
      f32x4 v0 = f32x4{0, 0, 0, 0}, v1 = f32x4{0, 0, 0, 0};
#pragma unroll
      for (int p = 0; p < 8; ++p) {
        v0 += *(const LAS f32x4*)(LAS const char*)(rp + p * 6144);
        v1 += *(const LAS f32x4*)(LAS const char*)(rp + p * 6144 + 256);
      }
      u32x4 r = __builtin_bit_cast(u32x4, rs);
      u32x4 ov = {pack2(v0[0] + bflo(r[0]), v0[1] + bfhi(r[0])), pack2(v0[2] + bflo(r[1]), v0[3] + bfhi(r[1])),
                  pack2(v1[0] + bflo(r[2]), v1[1] + bfhi(r[2])), pack2(v1[2] + bflo(r[3]), v1[3] + bfhi(r[3]))};
      st16<true>(out + ooff + vo, ov);
    }
  }
}

extern "C" void kernel_launch(void* const* d_in, const int* in_sizes, int n_in, void* d_out, int out_size,
                              void* d_ws, size_t ws_size, hipStream_t stream) {
  const char* x = (const char*)d_in[0];
  const char* wqkv = (const char*)d_in[1];
  const char* wo = (const char*)d_in[2];
  const char* w1 = (const char*)d_in[4];
  const char* w3 = (const char*)d_in[5];
  const char* w2 = (const char*)d_in[6];
  char* ws = (char*)d_ws;
  (void)hipFuncSetAttribute((const void*)k1_qkv, hipFuncAttributeMaxDynamicSharedMemorySize, K1_LDS);
  (void)hipFuncSetAttribute((const void*)k_mega, hipFuncAttributeMaxDynamicSharedMemorySize, MG_LDS);
  k1_qkv<<<256, 512, K1_LDS, stream>>>(x, wqkv, ws, ws + (10u << 20) + (512u << 10));
  k_mega<<<256, 512, MG_LDS, stream>>>(x, wo, w1, w3, w2, ws, (char*)d_out);
}
```

```cpp
#include <hip/hip_runtime.h>
#include <hip/hip_bf16.h>
#include <stdint.h>

typedef short bf16x8 __attribute__((ext_vector_type(8)));
typedef short s16x4 __attribute__((ext_vector_type(4)));
typedef float f32x4 __attribute__((ext_vector_type(4)));
typedef float f32x2 __attribute__((ext_vector_type(2)));
typedef unsigned u32x4 __attribute__((ext_vector_type(4)));
typedef unsigned u32x2 __attribute__((ext_vector_type(2)));
typedef __bf16 bf16x2 __attribute__((ext_vector_type(2)));
#define LAS __attribute__((address_space(3)))
#define DEV __device__ __forceinline__

constexpr int S_ = 2048, D_ = 512, H_ = 32, F_ = 1792;
constexpr float LN_EPS = 1e-5f;

typedef float f32x16 __attribute__((ext_vector_type(16)));
DEV f32x16 mfma32(bf16x8 a, bf16x8 b, f32x16 c) { return __builtin_amdgcn_mfma_f32_32x32x16_bf16(a, b, c, 0, 0, 0); }
DEV f32x4 mfma16(bf16x8 a, bf16x8 b, f32x4 c) { return __builtin_amdgcn_mfma_f32_16x16x32_bf16(a, b, c, 0, 0, 0); }
DEV unsigned pack2(float a, float b) {
  f32x2 v = {a, b};
  return __builtin_bit_cast(unsigned, __builtin_convertvector(v, bf16x2));
}
DEV float bflo(unsigned u) { return __builtin_bit_cast(float, u << 16); }
DEV float bfhi(unsigned u) { return __builtin_bit_cast(float, u & 0xffff0000u); }
DEV float dot2(unsigned a, unsigned b, float c) {
  return __builtin_amdgcn_fdot2_f32_bf16(__builtin_bit_cast(bf16x2, a), __builtin_bit_cast(bf16x2, b), c, false);
}
DEV void dma16(const char* gptr, unsigned ldsbase) {
  unsigned sv;
  asm volatile("s_mov_b32 %0, m0\n\ts_mov_b32 m0, %2\n\ts_nop 0\n\tglobal_load_lds_dwordx4 %1, off\n\ts_mov_b32 m0, %0"
               : "=&s"(sv) : "v"(gptr), "s"(ldsbase) : "memory");
}
DEV void dma16s(unsigned voff, const char* sbase, unsigned ldsbase) {
  unsigned sv;
  asm volatile("s_mov_b32 %0, m0\n\ts_mov_b32 m0, %3\n\ts_nop 0\n\tglobal_load_lds_dwordx4 %1, %2\n\ts_mov_b32 m0, %0"
               : "=&s"(sv) : "v"(voff), "s"(sbase), "s"(ldsbase) : "memory");
}
DEV void dma16sm(unsigned voff, const char* sbase, unsigned ldsbase, unsigned long long mask) {
  unsigned sv;
  asm volatile("s_mov_b32 %0, m0\n\ts_mov_b32 m0, %3\n\ts_mov_b64 exec, %4\n\tglobal_load_lds_dwordx4 %1, %2\n\ts_mov_b64 exec, -1\n\ts_mov_b32 m0, %0"
               : "=&s"(sv) : "v"(voff), "s"(sbase), "s"(ldsbase), "s"(mask) : "memory");
}
DEV void gld16(bf16x8& d, unsigned voff, const char* sbase) {
  asm volatile("global_load_dwordx4 %0, %1, %2" : "=v"(d) : "v"(voff), "s"(sbase) : "memory");
}
template <int N> DEV void wait_bar() { asm volatile("s_waitcnt vmcnt(%0) lgkmcnt(0)\n\ts_barrier" ::"n"(N) : "memory"); }
DEV void bar() { asm volatile("s_waitcnt lgkmcnt(0)\n\ts_barrier" ::: "memory"); }
DEV bf16x8 lds128(const char* p) { return *(const LAS bf16x8*)(LAS const char*)p; }
DEV s16x4 ldstr(const char* p) { return __builtin_amdgcn_ds_read_tr16_b64_v4i16((LAS s16x4*)p); }
DEV bf16x8 cat(s16x4 a, s16x4 b) { return __builtin_shufflevector(a, b, 0, 1, 2, 3, 4, 5, 6, 7); }
DEV float xsum(float v) { v += __shfl_xor(v, 16); v += __shfl_xor(v, 32); return v; }
DEV float xmax(float v) { v = fmaxf(v, __shfl_xor(v, 16)); v = fmaxf(v, __shfl_xor(v, 32)); return v; }
DEV float dpp_xor1(float v) { return __builtin_bit_cast(float, __builtin_amdgcn_mov_dpp(__builtin_bit_cast(int, v), 0xB1, 0xF, 0xF, true)); }
DEV float dpp_xor2(float v) { return __builtin_bit_cast(float, __builtin_amdgcn_mov_dpp(__builtin_bit_cast(int, v), 0x4E, 0xF, 0xF, true)); }
DEV float dpp_ror4(float v) { return __builtin_bit_cast(float, __builtin_amdgcn_mov_dpp(__builtin_bit_cast(int, v), 0x124, 0xF, 0xF, true)); }
DEV float dpp_ror8(float v) { return __builtin_bit_cast(float, __builtin_amdgcn_mov_dpp(__builtin_bit_cast(int, v), 0x128, 0xF, 0xF, true)); }
DEV unsigned uni(unsigned v) { return __builtin_amdgcn_readfirstlane(v); }
#define WT 31
template <bool W> DEV void st16(char* p, u32x4 v) {
  if constexpr (W) asm volatile("global_store_dwordx4 %0, %1, off sc0 sc1\n\ts_nop 1" ::"v"(p), "v"(v) : "memory");
  else *(u32x4*)p = v;
}
template <bool W> DEV void st8(char* p, u32x2 v) {
  if constexpr (W) asm volatile("global_store_dwordx2 %0, %1, off sc0 sc1" ::"v"(p), "v"(v) : "memory");
  else *(u32x2*)p = v;
}

template <int A, int B> struct Min { static constexpr int v = A < B ? A : B; };

template <int KT, int NK, int NS, int P, class Issue, class Compute>
DEV void pipe_steps(Issue& issue, Compute& compute) {
  if constexpr (KT < NK) {
    wait_bar<(Min<KT + NS - 1, NK>::v - KT - 1) * P>();
    if constexpr (KT + NS - 1 < NK) issue((KT + NS - 1) % NS);
    compute(KT % NS, KT);
    pipe_steps<KT + 1, NK, NS, P>(issue, compute);
  }
}

constexpr int K1_STAGE = 16384 + 12288, K1_NS = 5;
#define SB __builtin_amdgcn_sched_barrier(0)
DEV float xsum2(float v) {
  auto a = __builtin_amdgcn_permlane16_swap(__float_as_uint(v), __float_as_uint(v), false, false);
  v = __uint_as_float(a[0]) + __uint_as_float(a[1]);
  auto b = __builtin_amdgcn_permlane32_swap(__float_as_uint(v), __float_as_uint(v), false, false);
  return __uint_as_float(b[0]) + __uint_as_float(b[1]);
}

DEV void qkv_phase(char* smem, unsigned lds0, const char* __restrict__ x, const char* __restrict__ w, char* __restrict__ qkv,
                   int cb, int tb) {
  const int tid = threadIdx.x, lane = tid & 63, wv = uni(tid >> 6);
  const int g = lane >> 4, i = lane & 15, q4 = (lane >> 2) & 3, p4 = lane & 3;
  const int tg = wv & 3, nh = wv >> 2;
  const int bi = blockIdx.x >> 3;

  const char* gx = x + (size_t)(tb * 128 + wv * 16) * 1024;
  const unsigned vt = (lane >> 3) * 1024 + (((lane & 7) ^ (lane >> 3)) << 4);
  const char* gw = w + cb * 192;
  unsigned vw0, vw1;
  {
    int qq = wv * 64 + lane;
    int k = qq / 12, c = (qq % 12) ^ (((k >> 3) & 1) << 1);
    vw0 = k * 3072 + c * 16;
    qq = 512 + wv * 32 + (lane & 31);
    k = qq / 12; c = (qq % 12) ^ (((k >> 3) & 1) << 1);
    vw1 = k * 3072 + c * 16;
  }
  const int k0 = bi & 7;
  auto dma = [&](int s, int j) {
    const int kk = (k0 + s) & 7;
    const unsigned base = lds0 + (s % K1_NS) * K1_STAGE;
    if (j == 0) dma16s(vt, gx + kk * 128, base + wv * 2048);
    else if (j == 1) dma16s(vt, gx + kk * 128 + 8192, base + wv * 2048 + 1024);
    else if (j == 2) dma16s(vw0, gw + kk * (64 * 3072), base + 16384 + wv * 1024);
    else dma16sm(vw1, gw + kk * (64 * 3072), base + 16384 + 8192 + wv * 512, 0xFFFFFFFFull);
  };
#pragma unroll
  for (int s = 0; s < K1_NS - 1; ++s)
#pragma unroll
    for (int j = 0; j < 4; ++j) dma(s, j);

  f32x4 acc[2][3], cs = f32x4{0, 0, 0, 0}, s1v = f32x4{0, 0, 0, 0};
#pragma unroll
  for (int a = 0; a < 3; ++a) { acc[0][a] = f32x4{0, 0, 0, 0}; acc[1][a] = f32x4{0, 0, 0, 0}; }
  float s2a = 0.f, s2b = 0.f;
  const bf16x8 ones = {0x3F80, 0x3F80, 0x3F80, 0x3F80, 0x3F80, 0x3F80, 0x3F80, 0x3F80};
  int toff[2][2];
#pragma unroll
  for (int tt = 0; tt < 2; ++tt) {
    toff[tt][0] = (tg * 32 + (tt ^ nh) * 16 + i) * 128 + ((g ^ (i & 7)) << 4);
    toff[tt][1] = (tg * 32 + (tt ^ nh) * 16 + i) * 128 + (((4 + g) ^ (i & 7)) << 4);
  }
  int woff[3], un[3];
#pragma unroll
  for (int nt = 0; nt < 3; ++nt) {
    int a = nt + tg; a = a >= 3 ? a - 3 : a; a = a >= 3 ? a - 3 : a;
    un[nt] = nh * 3 + a;
    woff[nt] = 16384 + (8 * g + q4) * 192 + ((un[nt] ^ (g & 1)) << 5) + p4 * 8;
  }
  auto ldB = [&](int tt, int kt, int ks) -> bf16x8 { return lds128(smem + (kt % K1_NS) * K1_STAGE + toff[tt][ks]); };
  auto ldA = [&](int nt, int kt, int ks) -> bf16x8 {
    const char* p = smem + (kt % K1_NS) * K1_STAGE + woff[nt] + ks * 6144;
    return cat(ldstr(p), ldstr(p + 768));
  };
  bf16x8 A0[3], A1[3], B0[2], B1[2];
  auto phase = [&](bf16x8 (&Ac)[3], bf16x8 (&Bc)[2], bf16x8 (&An)[3], bf16x8 (&Bn)[2], int kn, int ksn, int dk, int dj) {
    const u32x4 u = __builtin_bit_cast(u32x4, Bc[0]);
#pragma unroll
    for (int m = 0; m < 8; ++m) {
      if (kn < 8) {
        if (m == 0) { Bn[0] = ldB(0, kn, ksn); An[0] = ldA(0, kn, ksn); }
        else if (m == 1) Bn[1] = ldB(1, kn, ksn);
        else if (m < 4) An[m - 1] = ldA(m - 1, kn, ksn);
      }
      if (m < 6) acc[m & 1][m >> 1] = mfma16(Ac[m >> 1], Bc[m & 1], acc[m & 1][m >> 1]);
      else if (m == 6) cs = mfma16(Ac[0], ones, cs);
      else s1v = mfma16(ones, Bc[0], s1v);
      {
        float f = (m & 1) ? bfhi(u[m >> 1]) : bflo(u[m >> 1]);
        asm volatile("" : "+v"(f));
        if (m & 1) s2b = __builtin_fmaf(f, f, s2b); else s2a = __builtin_fmaf(f, f, s2a);
      }
      if (dk < 8 && (m == 2 || m == 5)) dma(dk, dj + (m == 5));
      SB;
    }
  };
  wait_bar<12>();
  B0[0] = ldB(0, 0, 0); A0[0] = ldA(0, 0, 0); B0[1] = ldB(1, 0, 0); A0[1] = ldA(1, 0, 0); A0[2] = ldA(2, 0, 0);
#pragma unroll
  for (int kt = 0; kt < 8; ++kt) {
    phase(A0, B0, A1, B1, kt, 1, kt + 4, 0);
    if (kt < 4) wait_bar<10>();
    else if (kt == 4) wait_bar<8>();
    else if (kt == 5) wait_bar<4>();
    else if (kt == 6) wait_bar<0>();
    phase(A1, B1, A0, B0, kt + 1, 0, kt + 4, 2);
  }

  float* st = (float*)(smem + 49152);
  float* csp = (float*)(smem + 49152 + 1024);
  const float s2 = xsum2(s2a + s2b);
  if (g == 0) {
    float mean = s1v[0] * (1.f / 512.f);
    float var = fmaxf(s2 * (1.f / 512.f) - mean * mean, 0.f);
    float rstd = rsqrtf(var + LN_EPS);
    *(f32x2*)(st + (tg * 32 + nh * 16 + i) * 2) = f32x2{-mean * rstd, rstd};
  }
  if (i == 0 && tg < 3) *(f32x4*)(csp + un[0] * 16 + 4 * g) = cs;
  bar();
  f32x2 ms[2];
#pragma unroll
  for (int tt = 0; tt < 2; ++tt) ms[tt] = *(const f32x2*)(st + (tg * 32 + (tt ^ nh) * 16 + i) * 2);
  const int otile = (g & 1) ^ nh, ohalf = g >> 1;
  const unsigned vo = ohalf * 512 + (((otile * 16 + i) ^ (8 * ohalf)) << 4);
#pragma unroll
  for (int nt = 0; nt < 3; ++nt) {
    f32x4 c = *(const f32x4*)(csp + un[nt] * 16 + 4 * g);
    unsigned o[2][2];
#pragma unroll
    for (int tt = 0; tt < 2; ++tt) {
      f32x4 y = acc[tt][nt] * ms[tt][1] + ms[tt][0] * c;
      o[tt][0] = pack2(y[0], y[1]); o[tt][1] = pack2(y[2], y[3]);
    }
    auto sx = __builtin_amdgcn_permlane16_swap(o[0][0], o[1][0], false, false);
    auto sy = __builtin_amdgcn_permlane16_swap(o[0][1], o[1][1], false, false);
    char* dst = qkv + (size_t)(cb * 6 + un[nt]) * (2048 * 32) + (size_t)(tb * 4 + tg) * 1024;
    st16<true>(dst + vo, u32x4{sx[0], sy[0], sx[1], sy[1]});
  }
}

constexpr int S0 = 0, S1 = 49152, S2 = 98304, CSP = 147456, MG_LDS = 163840;
typedef unsigned long long u64;
extern "C" __device__ u64 llvm_dispatch_id() __asm("llvm.amdgcn.dispatch.id");
DEV void flag_set(u64* f, u64 tag) {
  asm volatile("global_store_dwordx2 %0, %1, off sc0 sc1" ::"v"(f), "v"(tag) : "memory");
}
DEV void flag_wait(const u64* p, u64 tag) {
  for (int it = 0; it < (1 << 20); ++it) {
    u64 c;
    asm volatile("global_load_dwordx2 %0, %1, off sc0 sc1\n\ts_waitcnt vmcnt(0)" : "=v"(c) : "v"(p) : "memory");
    if (__builtin_amdgcn_ballot_w64(c != tag) == 0) break;
  }
}
template <int V> struct IC { static constexpr int value = V; };
DEV int opaque(int v) { asm volatile("" : "+v"(v)); return v; }
DEV int opaque_s(int v) { asm volatile("" : "+s"(v)); return v; }

struct CW {
  const char *g1, *g3; unsigned vw; unsigned long long wmask;
};
DEV CW c_setup(const char* w1, const char* w3, int b, int wv, int lane) {
  const int xcd = b & 7, bi = b >> 3;
  const int cb = (xcd & 3) * 4 + (bi & 3);
  CW c;
  c.g1 = w1 + (size_t)(wv * 8) * (F_ * 2) + cb * 224;
  c.g3 = w3 + (size_t)(wv * 8) * (F_ * 2) + cb * 224;
  const int unit = ((lane >> 1) & 7) ^ ((lane >> 4) | ((wv & 1) << 2));
  c.vw = (lane >> 4) * (F_ * 2) + unit * 32 + (lane & 1) * 16;
  c.wmask = __builtin_amdgcn_ballot_w64(unit < 7);
  return c;
}
DEV void c_dmaw(const CW& c, unsigned ldsw, int kt, int j) {
  const unsigned base = ldsw + (kt % 3) * 49152;
  if (j < 2) dma16sm(c.vw, c.g1 + (size_t)(kt * 64 + j * 4) * (F_ * 2), base + 16384 + j * 1024, c.wmask);
  else dma16sm(c.vw, c.g3 + (size_t)(kt * 64 + (j - 2) * 4) * (F_ * 2), base + 32768 + (j - 2) * 1024, c.wmask);
}
DEV constexpr int d_unit(int u) {
  return u < 3 ? S2 + u * 2048 : u < 6 ? S0 + (u - 3) * 2048 : u < 9 ? S1 + (u - 6) * 2048 : u == 9 ? CSP
       : u < 13 ? S2 + (u - 10) * 2048 : S0;
}
DEV constexpr int d_ustr(int u) { return u == 9 ? 2048 : 6144; }
DEV void d_dma(const char* gw2, unsigned vw, unsigned lds0, int wv, int u, int q) {
  dma16s(vw, gw2 + (size_t)(u * 16 + q * 8) * 1024, lds0 + d_unit(u) + wv * d_ustr(u) + q * 1024);
}

template <int NT>
DEV void c_body(char* smem, unsigned lds0, const char* gt, const CW& cw, unsigned vt, int wv, int lane,
                const char* __restrict__ stats, const char* __restrict__ csb, char* __restrict__ hh, int tok0, int cb, const char* gw2, unsigned vwd) {
  const int tg = wv & 3;
  const unsigned ldsw = lds0 + wv * 2048;
  const int g = lane >> 4, i = lane & 15, q4 = (lane >> 2) & 3, p4 = lane & 3;
  f32x4 acc[2][2 * NT];
#pragma unroll
  for (int a = 0; a < 2 * NT; ++a) { acc[0][a] = f32x4{0, 0, 0, 0}; acc[1][a] = f32x4{0, 0, 0, 0}; }
  const int toff0 = (tg * 32 + i) * 128 + ((g ^ (i & 7)) << 4);
  const int toff1 = (tg * 32 + i) * 128 + (((4 + g) ^ (i & 7)) << 4);
  const int odd = cb & 1;
  const int fsw = q4 | ((g & 1) << 2);
  int T[NT], wa[NT];
#pragma unroll
  for (int a = 0; a < NT; ++a) {
    T[a] = NT == 4 ? odd * 3 + a : (a < 2 ? (odd ? 1 : 4) + a : (odd ? 0 : 6));
    wa[a] = 16384 + (8 * g + q4) * 256 + ((T[a] ^ fsw) << 5) + p4 * 8;
  }
  auto dmat = [&](int kt, int j) { dma16s(vt, gt + kt * 128 + j * 8192, ldsw + (kt % 3) * 49152 + j * 1024); };
  auto dma = [&](int kt, int j) {
    if (j < 2) dmat(kt, j); else c_dmaw(cw, ldsw, kt, j - 2);
  };
  auto ldB = [&](bf16x8* B, int kt, int ks) {
    const char* sb = smem + (kt % 3) * 49152 + (ks ? toff1 : toff0);
    B[0] = lds128(sb); B[1] = lds128(sb + 2048);
  };
  auto ldA = [&](int a, int kt, int ks) -> bf16x8 {
    const char* wp = smem + (kt % 3) * 49152 + wa[a < NT ? a : a - NT] + (a < NT ? 0 : 16384) + ks * 8192;
    return cat(ldstr(wp), ldstr(wp + 1024));
  };
  auto mma = [&](int a, bf16x8 A, const bf16x8* B) {
    acc[0][a] = mfma16(A, B[0], acc[0][a]);
    acc[1][a] = mfma16(A, B[1], acc[1][a]);
  };
  bf16x8 A0[2 * NT], A1[2 * NT], B0[2], B1[2];
  bf16x8 sa[2], cp[4];
#pragma unroll
  for (int kt = 0; kt < 3; ++kt) { dmat(kt, 0); dmat(kt, 1); }
  gld16(sa[0], (unsigned)(i * 64 + g * 16), stats + (size_t)(tok0 + tg * 32) * 64);
  gld16(sa[1], (unsigned)(i * 64 + g * 16), stats + (size_t)(tok0 + tg * 32 + 16) * 64);
  const int cq = min(tg * 16 + (lane >> 2), 55);
  if (NT == 3) {
#pragma unroll
    for (int m = 0; m < 4; ++m) gld16(cp[m], (unsigned)(((lane & 3) * 4 + m) * 896 + cq * 16), csb + (size_t)cb * 14336);
  }
  constexpr int X = NT == 3 ? 6 : 2;
  wait_bar<4 + X>();
  float rs[2], mr[2];
  float* csp = (float*)(smem + CSP);
  ldB(B0, 0, 0);
#pragma unroll
  for (int a = 0; a < 2 * NT; ++a) A0[a] = ldA(a, 0, 0);
#pragma unroll
  for (int kt = 0; kt < 8; ++kt) {
    ldB(B1, kt, 1);
#pragma unroll
    for (int a = 0; a < 2 * NT; ++a) {
      if (a < NT) { A1[2 * a] = ldA(2 * a, kt, 1); A1[2 * a + 1] = ldA(2 * a + 1, kt, 1); }
      mma(a, A0[a], B0);
      __builtin_amdgcn_sched_barrier(0);
    }
    if (kt == 0) wait_bar<2 + X>();
    else if (kt != 1) wait_bar<6>();
    else if (NT == 3) asm volatile("s_waitcnt vmcnt(6) lgkmcnt(0)\n\ts_barrier" : "+v"(sa[0]), "+v"(sa[1]), "+v"(cp[0]), "+v"(cp[1]), "+v"(cp[2]), "+v"(cp[3]) : : "memory");
    else asm volatile("s_waitcnt vmcnt(6) lgkmcnt(0)\n\ts_barrier" : "+v"(sa[0]), "+v"(sa[1]) : : "memory");
    if (kt < 7) ldB(B0, kt + 1, 0);
    if (kt == 1) {
#pragma unroll
      for (int tt = 0; tt < 2; ++tt) {
        const f32x4 sv = __builtin_bit_cast(f32x4, sa[tt]);
        const float s1 = xsum2(sv[0] + sv[2]), s2 = xsum2(sv[1] + sv[3]);
        const float mean = s1 * (1.f / 512.f);
        const float var = fmaxf(s2 * (1.f / 512.f) - mean * mean, 0.f);
        rs[tt] = rsqrtf(var + LN_EPS); mr[tt] = mean * rs[tt];
      }
      if (NT == 3) {
        f32x4 c = (__builtin_bit_cast(f32x4, cp[0]) + __builtin_bit_cast(f32x4, cp[1])) +
                  (__builtin_bit_cast(f32x4, cp[2]) + __builtin_bit_cast(f32x4, cp[3]));
#pragma unroll
        for (int e = 0; e < 4; ++e) { c[e] += dpp_xor1(c[e]); c[e] += dpp_xor2(c[e]); }
        if ((lane & 3) == 0) *(f32x4*)(csp + cq * 4) = c;
      }
    }
#pragma unroll
    for (int a = 0; a < 2 * NT; ++a) {
      if (kt < 7 && a < NT) { A0[2 * a] = ldA(2 * a, kt + 1, 0); A0[2 * a + 1] = ldA(2 * a + 1, kt + 1, 0); }
      mma(a, A1[a], B1);
      if (a >= 2 * NT - 6) {
        const int j = a - (2 * NT - 6);
        if (kt + 3 < 8) dma(kt + 3, j);
        else d_dma(gw2, vwd, lds0, wv, (kt - 5) * 3 + (j >> 1), j & 1);
      }
      __builtin_amdgcn_sched_barrier(0);
    }
  }
  unsigned pk[2][NT][2];
#pragma unroll
  for (int tt = 0; tt < 2; ++tt) {
    char* dst = hh + ((size_t)((tok0 >> 4) + tg * 2 + tt) * 56) * 1024 + lane * 16;
#pragma unroll
    for (int a = 0; a < NT; ++a) {
      f32x4 cg = *(const f32x4*)(csp + T[a] * 16 + 4 * g);
      f32x4 cu = *(const f32x4*)(csp + (7 + T[a]) * 16 + 4 * g);
      float r[4];
#pragma unroll
      for (int e = 0; e < 4; ++e) {
        float ga = acc[tt][a][e] * rs[tt] - mr[tt] * cg[e];
        float up = acc[tt][NT + a][e] * rs[tt] - mr[tt] * cu[e];
        float sg = __builtin_amdgcn_rcpf(1.f + __builtin_amdgcn_exp2f(-1.4426950408889634f * ga));
        r[e] = ga * sg * up;
      }
      pk[tt][a][0] = pack2(r[0], r[1]); pk[tt][a][1] = pack2(r[2], r[3]);
      const int G = cb * 7 + T[a];
      if (a & 1) {
        st16<true>(dst + (size_t)(G >> 1) * 1024, u32x4{pk[tt][a - 1][0], pk[tt][a - 1][1], pk[tt][a][0], pk[tt][a][1]});
      } else if (a == NT - 1) {
        st8<true>(dst + (size_t)(G >> 1) * 1024 + (G & 1) * 8, u32x2{pk[tt][a][0], pk[tt][a][1]});
      }
    }
  }
}

__global__ __launch_bounds__(512) void k_mega(const char* __restrict__ x, const char* __restrict__ wqkv, const char* __restrict__ wo,
                                              const char* __restrict__ w1, const char* __restrict__ w3,
                                              const char* __restrict__ w2, char* __restrict__ ws, char* __restrict__ out) {
  extern __shared__ __attribute__((aligned(16))) char smem[];
  char* qkv = ws;
  char* o = ws + (6u << 20);
  char* h = ws + (8u << 20);
  char* stats = ws + (10u << 20);
  u64* flags = (u64*)(ws + (10u << 20) + (512u << 10));
  char* csb = ws + (10u << 20) + (768u << 10);
  char* hh = ws + (11u << 20);
  const u64 tag = ((u64)(size_t)__builtin_amdgcn_dispatch_ptr() << 20) + llvm_dispatch_id();
  const int b = blockIdx.x;
  const unsigned lds0 = (unsigned)(size_t)(LAS char*)smem;
  const int cbG = (b >> 3) & 7, tbG = (b & 7) * 4 + (b >> 6);
  const int cbC = (b & 3) * 4 + ((b >> 3) & 3), tbC = ((b >> 2) & 1) * 8 + (b >> 5);

  {
    const int cb = (b & 1) * 8 + ((b >> 3) & 7), tb = ((b & 7) >> 1) * 4 + (b >> 6);
    qkv_phase(smem, lds0, x, wqkv, qkv, cb, tb);
    asm volatile("s_waitcnt vmcnt(0) lgkmcnt(0)\n\ts_barrier" ::: "memory");
    const int tid = threadIdx.x;
    if (tid < 9) {
      const int P = cb * 3 + tid / 3, d = tid % 3, which = P >> 4, hp = P & 15, qb = tb + d;
      if (which ? qb < 16 : d == 0)
        flag_set(flags + 24576 + ((hp >> 1) | ((hp & 1) << 3) | (qb << 4)) * 32 + (which ? (which - 1) * 3 + d : 6), tag);
    }
  }
  {
    const int tid = opaque(threadIdx.x), lane = tid & 63, wv = uni(tid >> 6);
    const int c32 = lane & 31, hf = lane >> 5, q4 = (lane >> 2) & 3, p4 = lane & 3;
    const int hp = (b & 7) * 2 + ((b >> 3) & 1), qb = b >> 4;
    const int q0 = qb * 128;
    const size_t HS = 2048 * 32;
    bf16x8 Q, cw0, cw1;
    if (wv == 0) {
      const int d = lane % 3;
      flag_wait(flags + 24576 + b * 32 + (lane >= 6 ? 6 : qb - d < 0 ? 6 : lane), tag);
    }
    asm volatile("s_barrier" ::: "memory");
    {
      const int r = wv >> 1;
      const char* src = qkv + (size_t)((1 + (r >> 1)) * 32 + hp * 2 + (r & 1)) * HS;
#pragma unroll
      for (int j = 0; j < 6; ++j) {
        const int kb = qb * 4 - 8 + (wv & 1) * 6 + j;
        dma16s(kb < 0 ? hf * 640 : lane * 16, src + (size_t)max(kb, 0) * 1024, lds0 + r * 12288 + ((wv & 1) * 6 + j) * 1024);
      }
    }
    const int u = wv >> 2, qg = wv & 3;
    const int qs = q0 + qg * 32;
    const unsigned vq = hf * 512 + ((c32 ^ (8 * hf)) << 4);
    gld16(Q, vq, qkv + (size_t)(hp * 2 + u) * HS + (size_t)qs * 32);
    const float c = 0.25f * 1.4426950408889634f;
    const float NEG = -1e30f;
    f32x16 cin0, cin8;
#pragma unroll
    for (int r = 0; r < 16; ++r) {
      const int kl = (r & 3) + 8 * (r >> 2) + 4 * hf;
      cin0[r] = kl > c32 ? 0.f : NEG; cin8[r] = kl <= c32 ? 0.f : NEG;
    }
    asm volatile("s_waitcnt vmcnt(0)\n\ts_barrier" : "+v"(Q), "+v"(cin0), "+v"(cin8) : : "memory");
    const unsigned vwo = (lane >> 3) * 1024 + ((((lane & 7) >> 1) ^ ((lane >> 4) & 3)) << 5) + (lane & 1) * 16;
    const char* gwo = wo + (size_t)(wv * 64) * 1024 + cbG * 128;
    const CW cw1s = c_setup(w1, w3, b, wv, lane);
    auto pf = [&](int k) {
      if (k < 2) {
        const int ch = max(wv - 1, 0) * 4 + (lane & 3);
        const char* pc = (ch >= 14 ? w3 + (ch - 14) * 16 : w1 + ch * 16) + (size_t)(tbC * 32 + (lane >> 2)) * (F_ * 2) + cbC * 224;
        if (k == 0) asm volatile("global_load_dwordx4 %0, %1, off" : "=v"(cw0) : "v"(pc) : "memory");
        else asm volatile("global_load_dwordx4 %0, %1, off" : "=v"(cw1) : "v"(pc + 16 * (F_ * 2)) : "memory");
      } else if (k < 10) dma16s(vwo, gwo + (size_t)((k - 2) * 8) * 1024, lds0 + S2 + wv * 8192 + (k - 2) * 1024);
      else if (k < 14) c_dmaw(cw1s, lds0 + wv * 2048, 1, k - 10);
      else if (k == 14) {
        const int c0 = b * 8 + wv;
        dma16s(lane * 16, w2 + (size_t)(c0 < 1792 ? c0 : c0 - 1792) * 1024, lds0 + S1 + wv * 1024);
      }
    };
    {
      const char* Kl = smem + u * 12288 + qg * 1024 + vq;
      const char* Vl[2];
#pragma unroll
      for (int e = 0; e < 2; ++e)
        Vl[e] = smem + 24576 + u * 12288 + qg * 1024 + (p4 >> 1) * 512 + (((e * 8 + 4 * hf + q4) ^ (8 * (p4 >> 1))) << 4) + (p4 & 1) * 8;
      f32x16 s[9];
#pragma unroll
      for (int t = 0; t < 9; ++t) {
        f32x16 z;
#pragma unroll
        for (int r = 0; r < 16; ++r) z[r] = 0.f;
        s[t] = mfma32(lds128(Kl + t * 1024), Q, t == 0 ? cin0 : t == 8 ? cin8 : z);
      }
      float m = s[8][0];
#pragma unroll
      for (int t = 0; t < 9; ++t)
#pragma unroll
        for (int r = 0; r < 16; ++r) m = fmaxf(m, s[t][r]);
      {
        auto sw = __builtin_amdgcn_permlane32_swap(__float_as_uint(m), __float_as_uint(m), false, false);
        m = fmaxf(__uint_as_float(sw[0]), __uint_as_float(sw[1]));
      }
      const f32x2 c2 = {c, c}, m2 = {-m * c, -m * c};
      const int nneg = qs >= 256 ? 0 : (256 - qs) >> 5;
      f32x16 oacc;
#pragma unroll
      for (int r = 0; r < 16; ++r) oacc[r] = 0.f;
      float ls0 = 0.f, ls1 = 0.f;
#pragma unroll
      for (int t = 0; t < 9; ++t) {
        const float mt = t < nneg ? NEG : m2[0];
        const f32x2 mt2 = {mt, mt};
#pragma unroll
        for (int sh = 0; sh < 2; ++sh) {
          u32x4 pb;
#pragma unroll
          for (int e = 0; e < 4; ++e) {
            f32x2 a = __builtin_elementwise_fma(f32x2{s[t][8 * sh + 2 * e], s[t][8 * sh + 2 * e + 1]}, c2, mt2);
            pb[e] = pack2(__builtin_amdgcn_exp2f(a[0]), __builtin_amdgcn_exp2f(a[1]));
          }
          ls0 = dot2(pb[0], 0x3F803F80u, ls0); ls1 = dot2(pb[1], 0x3F803F80u, ls1);
          ls0 = dot2(pb[2], 0x3F803F80u, ls0); ls1 = dot2(pb[3], 0x3F803F80u, ls1);
          const int vp = t * 1024 + sh * 256;
          oacc = mfma32(cat(ldstr(Vl[0] + vp), ldstr(Vl[1] + vp)), __builtin_bit_cast(bf16x8, pb), oacc);
          if (2 * t + sh < 15) pf(2 * t + sh);
        }
      }
      float ls = ls0 + ls1;
      {
        auto sw = __builtin_amdgcn_permlane32_swap(__float_as_uint(ls), __float_as_uint(ls), false, false);
        ls = __uint_as_float(sw[0]) + __uint_as_float(sw[1]);
      }
      const float inv = __builtin_amdgcn_rcpf(ls);
      const u32x4 ov = {pack2(oacc[0] * inv, oacc[1] * inv), pack2(oacc[2] * inv, oacc[3] * inv),
                        pack2(oacc[4] * inv, oacc[5] * inv), pack2(oacc[6] * inv, oacc[7] * inv)};
      st16<true>(o + ((size_t)((qs >> 4) + (c32 >> 4)) * 16 + hp) * 1024 + ((2 * u + hf) * 16 + (c32 & 15)) * 16, ov);
    }
    asm volatile("s_waitcnt vmcnt(0) lgkmcnt(0)\n\ts_barrier" : "+v"(cw0), "+v"(cw1) : : "memory");
    if (tid < 16) flag_set(flags + (((qb >> 1) | (tid << 3) | ((qb & 1) << 7)) * 32 + hp), tag);
    {
      const CW cw = c_setup(w1, w3, b, wv, lane);
#pragma unroll
      for (int j = 0; j < 4; ++j) c_dmaw(cw, lds0 + wv * 2048, 0, j);
    }
    if (wv == 0) { flag_wait(flags + b * 32 + (lane & 15), tag); }
    else {
      const u32x4 ua = __builtin_bit_cast(u32x4, cw0), ub = __builtin_bit_cast(u32x4, cw1);
      f32x4 lo, hi;
#pragma unroll
      for (int e = 0; e < 2; ++e) {
        lo[2 * e] = bflo(ua[e]) + bflo(ub[e]); lo[2 * e + 1] = bfhi(ua[e]) + bfhi(ub[e]);
        hi[2 * e] = bflo(ua[2 + e]) + bflo(ub[2 + e]); hi[2 * e + 1] = bfhi(ua[2 + e]) + bfhi(ub[2 + e]);
      }
#pragma unroll
      for (int e = 0; e < 4; ++e) {
        lo[e] += dpp_ror4(lo[e]); lo[e] += dpp_ror8(lo[e]); lo[e] = xsum2(lo[e]);
        hi[e] += dpp_ror4(hi[e]); hi[e] += dpp_ror8(hi[e]); hi[e] = xsum2(hi[e]);
      }
      if (lane < 4) {
        char* d = csb + (size_t)(cbC * 16 + tbC) * 896 + ((wv - 1) * 4 + lane) * 32;
        st16<true>(d, __builtin_bit_cast(u32x4, lo));
        st16<true>(d + 16, __builtin_bit_cast(u32x4, hi));
      }
      asm volatile("s_waitcnt vmcnt(0)" ::: "memory");
    }
    asm volatile("s_barrier" ::: "memory");
    if (tid >= 64 && tid < 80) flag_set(flags + 8192 + ((cbC >> 2) | ((tid & 1) << 2) | ((cbC & 3) << 3) | (((tid & 15) >> 1) << 5)) * 32 + 16 + tbC, tag);
  }
  {
    const int tid = opaque(threadIdx.x), lane = tid & 63, wv = uni(tid >> 6);
    const int g = lane >> 4, i = lane & 15, q4 = (lane >> 2) & 3, p4 = lane & 3;
    const int kp = wv & 3, th = wv >> 2;
    bf16x8 B[4][2], rs;
    const char* ga = o + ((size_t)(tbG * 4 + th * 2) * 16 + kp * 4) * 1024;
#pragma unroll
    for (int s = 0; s < 4; ++s) {
      gld16(B[s][0], lane * 16, ga + s * 1024);
      gld16(B[s][1], lane * 16, ga + (16 + s) * 1024);
    }
    const size_t ooff = (size_t)(tbG * 64 + wv * 8) * 1024 + cbG * 128;
    const unsigned vo = (lane >> 3) * 1024 + (lane & 7) * 16;
    gld16(rs, vo, x + ooff);
    const int sw = (2 * (g & 1) + (q4 >> 1)) & 3;
    const char* wr = smem + S2 + kp * 16384 + (16 * (g >> 1) + 4 * (g & 1) + q4) * 128 + p4 * 8;
    bf16x8 A[4][4];
#pragma unroll
    for (int s = 0; s < 4; ++s)
#pragma unroll
      for (int nt = 0; nt < 4; ++nt) {
        const char* p = wr + s * 4096 + ((nt ^ sw) << 5);
        A[s][nt] = cat(ldstr(p), ldstr(p + 1024));
      }
    f32x4 acc[2][4];
#pragma unroll
    for (int t = 0; t < 2; ++t)
#pragma unroll
      for (int n = 0; n < 4; ++n) acc[t][n] = f32x4{0, 0, 0, 0};
    asm volatile("s_waitcnt vmcnt(7)" : "+v"(B[0][0]), "+v"(B[0][1]) : : "memory");
#pragma unroll
    for (int nt = 0; nt < 4; ++nt) { acc[0][nt] = mfma16(A[0][nt], B[0][0], acc[0][nt]); acc[1][nt] = mfma16(A[0][nt], B[0][1], acc[1][nt]); }
    asm volatile("s_waitcnt vmcnt(5)" : "+v"(B[1][0]), "+v"(B[1][1]) : : "memory");
#pragma unroll
    for (int nt = 0; nt < 4; ++nt) { acc[0][nt] = mfma16(A[1][nt], B[1][0], acc[0][nt]); acc[1][nt] = mfma16(A[1][nt], B[1][1], acc[1][nt]); }
    asm volatile("s_waitcnt vmcnt(3)" : "+v"(B[2][0]), "+v"(B[2][1]) : : "memory");
#pragma unroll
    for (int nt = 0; nt < 4; ++nt) { acc[0][nt] = mfma16(A[2][nt], B[2][0], acc[0][nt]); acc[1][nt] = mfma16(A[2][nt], B[2][1], acc[1][nt]); }
    asm volatile("s_waitcnt vmcnt(1)" : "+v"(B[3][0]), "+v"(B[3][1]) : : "memory");
#pragma unroll
    for (int nt = 0; nt < 4; ++nt) { acc[0][nt] = mfma16(A[3][nt], B[3][0], acc[0][nt]); acc[1][nt] = mfma16(A[3][nt], B[3][1], acc[1][nt]); }
    bar();
    {
      const int mo = S2 + kp * 16384 + g * 256 + ((i ^ ((g >> 1) * 2)) << 4);
#pragma unroll
      for (int tt = 0; tt < 2; ++tt)
#pragma unroll
        for (int nt = 0; nt < 4; ++nt)
          *(LAS f32x4*)(LAS char*)(smem + (mo ^ (nt * 64)) + ((th * 2 + tt) * 4 + nt) * 1024) = acc[tt][nt];
    }
    asm volatile("s_waitcnt vmcnt(0) lgkmcnt(0)\n\ts_barrier" : "+v"(rs) : : "memory");
    {
      const int c = lane & 7;
      const int tok = (lane >> 3) + 8 * (wv & 1);
      const char* rp = smem + S2 + ((wv >> 1) * 4 + (c >> 1)) * 1024 + (c & 1) * 512 + ((tok ^ (2 * c)) << 4);
      f32x4 v0 = f32x4{0, 0, 0, 0}, v1 = f32x4{0, 0, 0, 0};
#pragma unroll
      for (int p = 0; p < 4; ++p) {
        v0 += *(const LAS f32x4*)(LAS const char*)(rp + p * 16384);
        v1 += *(const LAS f32x4*)(LAS const char*)(rp + p * 16384 + 256);
      }
      u32x4 r = __builtin_bit_cast(u32x4, rs);
      u32x4 ov = {pack2(v0[0] + bflo(r[0]), v0[1] + bfhi(r[0])), pack2(v0[2] + bflo(r[1]), v0[3] + bfhi(r[1])),
                  pack2(v1[0] + bflo(r[2]), v1[1] + bfhi(r[2])), pack2(v1[2] + bflo(r[3]), v1[3] + bfhi(r[3]))};
      st16<true>(h + ooff + vo, ov);
      float s1 = 0.f, s2 = 0.f;
#pragma unroll
      for (int e = 0; e < 4; ++e) { s1 = dot2(ov[e], 0x3F803F80u, s1); s2 = dot2(ov[e], ov[e], s2); }
#pragma unroll
      for (int m = 1; m < 8; m <<= 1) { s1 += __shfl_xor(s1, m); s2 += __shfl_xor(s2, m); }
      if (c == 0) st8<true>(stats + ((size_t)(tbG * 64 + wv * 8 + (lane >> 3)) * 8 + cbG) * 8,
                            u32x2{__float_as_uint(s1), __float_as_uint(s2)});
    }
    asm volatile("s_waitcnt vmcnt(0) lgkmcnt(0)\n\ts_barrier" ::: "memory");
    if (tid < 16) flag_set(flags + 8192 + ((tid & 3) | ((tbG >> 4) << 2) | ((tid >> 2) << 3) | (((tbG >> 1) & 7) << 5)) * 32 + (tbG & 1) * 8 + cbG, tag);
    {
      const CW cw = c_setup(w1, w3, b, wv, lane);
#pragma unroll
      for (int j = 0; j < 4; ++j) c_dmaw(cw, lds0 + wv * 2048, 2, j);
    }
    if (wv == 0) flag_wait(flags + 8192 + b * 32 + (lane & 31), tag);
    asm volatile("s_barrier" ::: "memory");
  }
  const unsigned vwd_ = 0;
  {
    const int tid = opaque(threadIdx.x), lane = tid & 63, wv = uni(tid >> 6);
    const char* gt = h + (size_t)(tbC * 128 + wv * 16) * 1024;
    const unsigned vt = (lane >> 3) * 1024 + (((lane & 7) ^ (lane >> 3)) << 4);
    const CW cw = c_setup(w1, w3, b, wv, lane);
    const unsigned vwd = (lane >> 3) * 1024 + ((((lane & 7) >> 1) ^ ((lane >> 4) & 3)) << 5) + (lane & 1) * 16;
    const char* gw2 = w2 + (size_t)(wv * 224) * 1024 + cbG * 128;
    if ((wv >> 2) == 0) c_body<4>(smem, lds0, gt, cw, vt, wv, lane, stats, csb, hh, tbC * 128, cbC, gw2, vwd);
    else c_body<3>(smem, lds0, gt, cw, vt, wv, lane, stats, csb, hh, tbC * 128, cbC, gw2, vwd);
  }
  {
    const int tid = opaque(threadIdx.x), lane = tid & 63, wv = uni(tid >> 6);
    const int g = lane >> 4, i = lane & 15, q4 = (lane >> 2) & 3, p4 = lane & 3;
    asm volatile("s_waitcnt vmcnt(0) lgkmcnt(0)\n\ts_barrier" ::: "memory");
    if (tid < 16) flag_set(flags + 16384 + ((tbC >> 1) | (tid << 3) | ((tbC & 1) << 7)) * 32 + cbC, tag);
    const unsigned vwd = (lane >> 3) * 1024 + ((((lane & 7) >> 1) ^ ((lane >> 4) & 3)) << 5) + (lane & 1) * 16;
    const char* gw2 = w2 + (size_t)(wv * 224) * 1024 + cbG * 128;
    d_dma(gw2, vwd, lds0, wv, 9, 0); d_dma(gw2, vwd, lds0, wv, 9, 1);
    const int sw = (2 * g + (q4 >> 1)) & 3;
    const int ro = (4 * g + q4) * 128 + p4 * 8;
    int rn[4];
#pragma unroll
    for (int nt = 0; nt < 4; ++nt) rn[nt] = ro + ((nt ^ sw) << 5);
    auto lda = [&](bf16x8* Af, int j) {
      const char* p0 = smem + d_unit(2 * j) + wv * d_ustr(2 * j);
      const char* p1 = smem + d_unit(2 * j + 1) + wv * d_ustr(2 * j + 1);
#pragma unroll
      for (int nt = 0; nt < 4; ++nt) Af[nt] = cat(ldstr(p0 + rn[nt]), ldstr(p1 + rn[nt]));
    };
    bf16x8 A[2][4], B[7][4], rs;
    lda(A[0], 0); lda(A[1], 1);
    asm volatile("s_waitcnt lgkmcnt(0)" : "+v"(A[0][0]), "+v"(A[0][1]), "+v"(A[0][2]), "+v"(A[0][3]), "+v"(A[1][0]), "+v"(A[1][1]), "+v"(A[1][2]), "+v"(A[1][3]) : : "memory");
#pragma unroll
    for (int u = 10; u < 14; ++u) { d_dma(gw2, vwd, lds0, wv, u, 0); d_dma(gw2, vwd, lds0, wv, u, 1); }
    if (wv == 0) flag_wait(flags + 16384 + b * 32 + (lane & 15), tag);
    asm volatile("s_barrier" ::: "memory");
    const char* ga = hh + ((size_t)(tbG * 4) * 56 + wv * 7) * 1024;
#pragma unroll
    for (int j = 0; j < 7; ++j)
#pragma unroll
      for (int tt = 0; tt < 4; ++tt) gld16(B[j][tt], lane * 16, ga + (size_t)(tt * 56 + j) * 1024);
    const size_t ooff = (size_t)(tbG * 64 + wv * 8) * 1024 + cbG * 128;
    const unsigned vo = (lane >> 3) * 1024 + (lane & 7) * 16;
    gld16(rs, vo, h + ooff);
    f32x4 acc[4][4];
#pragma unroll
    for (int t = 0; t < 4; ++t)
#pragma unroll
      for (int n = 0; n < 4; ++n) acc[t][n] = f32x4{0, 0, 0, 0};
    auto step = [&](auto J) {
      constexpr int j = decltype(J)::value;
      asm volatile("s_waitcnt vmcnt(%4)" : "+v"(B[j][0]), "+v"(B[j][1]), "+v"(B[j][2]), "+v"(B[j][3]) : "n"(25 - 4 * j) : "memory");
#pragma unroll
      for (int nt = 0; nt < 4; ++nt)
#pragma unroll
        for (int tt = 0; tt < 4; ++tt) acc[tt][nt] = mfma16(A[j & 1][nt], B[j][tt], acc[tt][nt]);
      __builtin_amdgcn_sched_barrier(0);
      if constexpr (j + 2 < 7) lda(A[j & 1], j + 2);
    };
    step(IC<0>{}); step(IC<1>{}); step(IC<2>{}); step(IC<3>{}); step(IC<4>{}); step(IC<5>{}); step(IC<6>{});
    {
      const int mo = wv * 6144 + g * 256 + ((i ^ ((g >> 1) * 2)) << 4);
#pragma unroll
      for (int tt = 0; tt < 4; ++tt)
#pragma unroll
        for (int nt = 0; nt < 4; ++nt) {
          const int t = tt * 4 + nt;
          const int base = t < 6 ? S2 + t * 1024 : t < 12 ? S0 + (t - 6) * 1024 : S1 + (t - 12) * 1024;
          *(LAS f32x4*)(LAS char*)(smem + (mo ^ (nt * 64)) + base) = acc[tt][nt];
        }
    }
    asm volatile("s_waitcnt vmcnt(0) lgkmcnt(0)\n\ts_barrier" : "+v"(rs) : : "memory");
    {
      const int c = lane & 7;
      const int tok = (lane >> 3) + 8 * (wv & 1);
      const int t = (wv >> 1) * 4 + (c >> 1);
      const int base = t < 6 ? S2 + t * 1024 : t < 12 ? S0 + (t - 6) * 1024 : S1 + (t - 12) * 1024;
      const char* rp = smem + base + (c & 1) * 512 + ((tok ^ (2 * c)) << 4);
      f32x4 v0 = f32x4{0, 0, 0, 0}, v1 = f32x4{0, 0, 0, 0};
#pragma unroll
      for (int p = 0; p < 8; ++p) {
        v0 += *(const LAS f32x4*)(LAS const char*)(rp + p * 6144);
        v1 += *(const LAS f32x4*)(LAS const char*)(rp + p * 6144 + 256);
      }
      u32x4 r = __builtin_bit_cast(u32x4, rs);
      u32x4 ov = {pack2(v0[0] + bflo(r[0]), v0[1] + bfhi(r[0])), pack2(v0[2] + bflo(r[1]), v0[3] + bfhi(r[1])),
                  pack2(v1[0] + bflo(r[2]), v1[1] + bfhi(r[2])), pack2(v1[2] + bflo(r[3]), v1[3] + bfhi(r[3]))};
      st16<true>(out + ooff + vo, ov);
    }
  }
}

extern "C" void kernel_launch(void* const* d_in, const int* in_sizes, int n_in, void* d_out, int out_size,
                              void* d_ws, size_t ws_size, hipStream_t stream) {
  const char* x = (const char*)d_in[0];
  const char* wqkv = (const char*)d_in[1];
  const char* wo = (const char*)d_in[2];
  const char* w1 = (const char*)d_in[4];
  const char* w3 = (const char*)d_in[5];
  const char* w2 = (const char*)d_in[6];
  char* ws = (char*)d_ws;
  (void)hipFuncSetAttribute((const void*)k_mega, hipFuncAttributeMaxDynamicSharedMemorySize, MG_LDS);
  k_mega<<<256, 512, MG_LDS, stream>>>(x, wqkv, wo, w1, w3, w2, ws, (char*)d_out);
}
```

```cpp
#define HX -1
#define HXB 37
#include <hip/hip_runtime.h>
#include <hip/hip_bf16.h>
#include <stdint.h>

typedef short bf16x8 __attribute__((ext_vector_type(8)));
typedef short s16x4 __attribute__((ext_vector_type(4)));
typedef float f32x4 __attribute__((ext_vector_type(4)));
typedef float f32x2 __attribute__((ext_vector_type(2)));
typedef unsigned u32x4 __attribute__((ext_vector_type(4)));
typedef unsigned u32x2 __attribute__((ext_vector_type(2)));
typedef __bf16 bf16x2 __attribute__((ext_vector_type(2)));
#define LAS __attribute__((address_space(3)))
#define DEV __device__ __forceinline__

constexpr int S_ = 2048, D_ = 512, H_ = 32, F_ = 1792;
constexpr float LN_EPS = 1e-5f;

typedef float f32x16 __attribute__((ext_vector_type(16)));
DEV f32x16 mfma32(bf16x8 a, bf16x8 b, f32x16 c) { return __builtin_amdgcn_mfma_f32_32x32x16_bf16(a, b, c, 0, 0, 0); }
DEV f32x4 mfma16(bf16x8 a, bf16x8 b, f32x4 c) { return __builtin_amdgcn_mfma_f32_16x16x32_bf16(a, b, c, 0, 0, 0); }
DEV unsigned pack2(float a, float b) {
  f32x2 v = {a, b};
  return __builtin_bit_cast(unsigned, __builtin_convertvector(v, bf16x2));
}
DEV float bflo(unsigned u) { return __builtin_bit_cast(float, u << 16); }
DEV float bfhi(unsigned u) { return __builtin_bit_cast(float, u & 0xffff0000u); }
DEV float dot2(unsigned a, unsigned b, float c) {
  return __builtin_amdgcn_fdot2_f32_bf16(__builtin_bit_cast(bf16x2, a), __builtin_bit_cast(bf16x2, b), c, false);
}
DEV void dma16(const char* gptr, unsigned ldsbase) {
  unsigned sv;
  asm volatile("s_mov_b32 %0, m0\n\ts_mov_b32 m0, %2\n\ts_nop 0\n\tglobal_load_lds_dwordx4 %1, off\n\ts_mov_b32 m0, %0"
               : "=&s"(sv) : "v"(gptr), "s"(ldsbase) : "memory");
}
DEV void dma16s(unsigned voff, const char* sbase, unsigned ldsbase) {
  unsigned sv;
  asm volatile("s_mov_b32 %0, m0\n\ts_mov_b32 m0, %3\n\ts_nop 0\n\tglobal_load_lds_dwordx4 %1, %2\n\ts_mov_b32 m0, %0"
               : "=&s"(sv) : "v"(voff), "s"(sbase), "s"(ldsbase) : "memory");
}
DEV void dma16sm(unsigned voff, const char* sbase, unsigned ldsbase, unsigned long long mask) {
  unsigned sv;
  asm volatile("s_mov_b32 %0, m0\n\ts_mov_b32 m0, %3\n\ts_mov_b64 exec, %4\n\tglobal_load_lds_dwordx4 %1, %2\n\ts_mov_b64 exec, -1\n\ts_mov_b32 m0, %0"
               : "=&s"(sv) : "v"(voff), "s"(sbase), "s"(ldsbase), "s"(mask) : "memory");
}
DEV void gld16(bf16x8& d, unsigned voff, const char* sbase) {
  asm volatile("global_load_dwordx4 %0, %1, %2" : "=v"(d) : "v"(voff), "s"(sbase) : "memory");
}
template <int N> DEV void wait_bar() { asm volatile("s_waitcnt vmcnt(%0) lgkmcnt(0)\n\ts_barrier" ::"n"(N) : "memory"); }
DEV void bar() { asm volatile("s_waitcnt lgkmcnt(0)\n\ts_barrier" ::: "memory"); }
DEV bf16x8 lds128(const char* p) { return *(const LAS bf16x8*)(LAS const char*)p; }
DEV s16x4 ldstr(const char* p) { return __builtin_amdgcn_ds_read_tr16_b64_v4i16((LAS s16x4*)p); }
DEV bf16x8 cat(s16x4 a, s16x4 b) { return __builtin_shufflevector(a, b, 0, 1, 2, 3, 4, 5, 6, 7); }
DEV float xsum(float v) { v += __shfl_xor(v, 16); v += __shfl_xor(v, 32); return v; }
DEV float xmax(float v) { v = fmaxf(v, __shfl_xor(v, 16)); v = fmaxf(v, __shfl_xor(v, 32)); return v; }
DEV float dpp_xor1(float v) { return __builtin_bit_cast(float, __builtin_amdgcn_mov_dpp(__builtin_bit_cast(int, v), 0xB1, 0xF, 0xF, true)); }
DEV float dpp_xor2(float v) { return __builtin_bit_cast(float, __builtin_amdgcn_mov_dpp(__builtin_bit_cast(int, v), 0x4E, 0xF, 0xF, true)); }
DEV float dpp_ror4(float v) { return __builtin_bit_cast(float, __builtin_amdgcn_mov_dpp(__builtin_bit_cast(int, v), 0x124, 0xF, 0xF, true)); }
DEV float dpp_ror8(float v) { return __builtin_bit_cast(float, __builtin_amdgcn_mov_dpp(__builtin_bit_cast(int, v), 0x128, 0xF, 0xF, true)); }
DEV unsigned uni(unsigned v) { return __builtin_amdgcn_readfirstlane(v); }
#define WT 31
template <bool W> DEV void st16(char* p, u32x4 v) {
  if constexpr (W) asm volatile("global_store_dwordx4 %0, %1, off sc0 sc1\n\ts_nop 1" ::"v"(p), "v"(v) : "memory");
  else *(u32x4*)p = v;
}
template <bool W> DEV void st8(char* p, u32x2 v) {
  if constexpr (W) asm volatile("global_store_dwordx2 %0, %1, off sc0 sc1" ::"v"(p), "v"(v) : "memory");
  else *(u32x2*)p = v;
}

#define MS(n) do { if ((n) == HX) hx_ = (unsigned)__builtin_amdgcn_s_memrealtime(); } while (0)
#define MSDECL unsigned hx_ = 0
#define MSOUT
#define MSARG , unsigned& hx_
#define MSPASS , hx_
template <int A, int B> struct Min { static constexpr int v = A < B ? A : B; };

template <int KT, int NK, int NS, int P, class Issue, class Compute>
DEV void pipe_steps(Issue& issue, Compute& compute) {
  if constexpr (KT < NK) {
    wait_bar<(Min<KT + NS - 1, NK>::v - KT - 1) * P>();
    if constexpr (KT + NS - 1 < NK) issue((KT + NS - 1) % NS);
    compute(KT % NS, KT);
    pipe_steps<KT + 1, NK, NS, P>(issue, compute);
  }
}

constexpr int K1_STAGE = 16384 + 12288, K1_NS = 5;
#define SB __builtin_amdgcn_sched_barrier(0)
DEV float xsum2(float v) {
  auto a = __builtin_amdgcn_permlane16_swap(__float_as_uint(v), __float_as_uint(v), false, false);
  v = __uint_as_float(a[0]) + __uint_as_float(a[1]);
  auto b = __builtin_amdgcn_permlane32_swap(__float_as_uint(v), __float_as_uint(v), false, false);
  return __uint_as_float(b[0]) + __uint_as_float(b[1]);
}

DEV void qkv_phase(char* smem, unsigned lds0, const char* __restrict__ x, const char* __restrict__ w, char* __restrict__ qkv,
                   int cb, int tb MSARG) {
  const int tid = threadIdx.x, lane = tid & 63, wv = uni(tid >> 6);
  const int g = lane >> 4, i = lane & 15, q4 = (lane >> 2) & 3, p4 = lane & 3;
  const int tg = wv & 3, nh = wv >> 2;
  const int bi = blockIdx.x >> 3;

  const char* gx = x + (size_t)(tb * 128 + wv * 16) * 1024;
  const unsigned vt = (lane >> 3) * 1024 + (((lane & 7) ^ (lane >> 3)) << 4);
  const char* gw = w + cb * 192;
  unsigned vw0, vw1;
  {
    int qq = wv * 64 + lane;
    int k = qq / 12, c = (qq % 12) ^ (((k >> 3) & 1) << 1);
    vw0 = k * 3072 + c * 16;
    qq = 512 + wv * 32 + (lane & 31);
    k = qq / 12; c = (qq % 12) ^ (((k >> 3) & 1) << 1);
    vw1 = k * 3072 + c * 16;
  }
  const int k0 = bi & 7;
  auto dma = [&](int s, int j) {
    const int kk = (k0 + s) & 7;
    const unsigned base = lds0 + (s % K1_NS) * K1_STAGE;
    if (j == 0) dma16s(vt, gx + kk * 128, base + wv * 2048);
    else if (j == 1) dma16s(vt, gx + kk * 128 + 8192, base + wv * 2048 + 1024);
    else if (j == 2) dma16s(vw0, gw + kk * (64 * 3072), base + 16384 + wv * 1024);
    else dma16sm(vw1, gw + kk * (64 * 3072), base + 16384 + 8192 + wv * 512, 0xFFFFFFFFull);
  };
#pragma unroll
  for (int s = 0; s < K1_NS - 1; ++s)
#pragma unroll
    for (int j = 0; j < 4; ++j) dma(s, j);

  f32x4 acc[2][3], cs = f32x4{0, 0, 0, 0}, s1v = f32x4{0, 0, 0, 0};
#pragma unroll
  for (int a = 0; a < 3; ++a) { acc[0][a] = f32x4{0, 0, 0, 0}; acc[1][a] = f32x4{0, 0, 0, 0}; }
  float s2a = 0.f, s2b = 0.f;
  const bf16x8 ones = {0x3F80, 0x3F80, 0x3F80, 0x3F80, 0x3F80, 0x3F80, 0x3F80, 0x3F80};
  int toff[2][2];
#pragma unroll
  for (int tt = 0; tt < 2; ++tt) {
    toff[tt][0] = (tg * 32 + (tt ^ nh) * 16 + i) * 128 + ((g ^ (i & 7)) << 4);
    toff[tt][1] = (tg * 32 + (tt ^ nh) * 16 + i) * 128 + (((4 + g) ^ (i & 7)) << 4);
  }
  int woff[3], un[3];
#pragma unroll
  for (int nt = 0; nt < 3; ++nt) {
    int a = nt + tg; a = a >= 3 ? a - 3 : a; a = a >= 3 ? a - 3 : a;
    un[nt] = nh * 3 + a;
    woff[nt] = 16384 + (8 * g + q4) * 192 + ((un[nt] ^ (g & 1)) << 5) + p4 * 8;
  }
  auto ldB = [&](int tt, int kt, int ks) -> bf16x8 { return lds128(smem + (kt % K1_NS) * K1_STAGE + toff[tt][ks]); };
  auto ldA = [&](int nt, int kt, int ks) -> bf16x8 {
    const char* p = smem + (kt % K1_NS) * K1_STAGE + woff[nt] + ks * 6144;
    return cat(ldstr(p), ldstr(p + 768));
  };
  bf16x8 A0[3], A1[3], B0[2], B1[2];
  auto phase = [&](bf16x8 (&Ac)[3], bf16x8 (&Bc)[2], bf16x8 (&An)[3], bf16x8 (&Bn)[2], int kn, int ksn, int dk, int dj) {
    const u32x4 u = __builtin_bit_cast(u32x4, Bc[0]);
#pragma unroll
    for (int m = 0; m < 8; ++m) {
      if (kn < 8) {
        if (m == 0) { Bn[0] = ldB(0, kn, ksn); An[0] = ldA(0, kn, ksn); }
        else if (m == 1) Bn[1] = ldB(1, kn, ksn);
        else if (m < 4) An[m - 1] = ldA(m - 1, kn, ksn);
      }
      if (m < 6) acc[m & 1][m >> 1] = mfma16(Ac[m >> 1], Bc[m & 1], acc[m & 1][m >> 1]);
      else if (m == 6) cs = mfma16(Ac[0], ones, cs);
      else s1v = mfma16(ones, Bc[0], s1v);
      {
        float f = (m & 1) ? bfhi(u[m >> 1]) : bflo(u[m >> 1]);
        asm volatile("" : "+v"(f));
        if (m & 1) s2b = __builtin_fmaf(f, f, s2b); else s2a = __builtin_fmaf(f, f, s2a);
      }
      if (dk < 8 && (m == 2 || m == 5)) dma(dk, dj + (m == 5));
      SB;
    }
  };
  wait_bar<12>();
  MS(24);
  B0[0] = ldB(0, 0, 0); A0[0] = ldA(0, 0, 0); B0[1] = ldB(1, 0, 0); A0[1] = ldA(1, 0, 0); A0[2] = ldA(2, 0, 0);
#pragma unroll
  for (int kt = 0; kt < 8; ++kt) {
    phase(A0, B0, A1, B1, kt, 1, kt + 4, 0);
    if (kt < 4) wait_bar<10>();
    else if (kt == 4) wait_bar<8>();
    else if (kt == 5) wait_bar<4>();
    else if (kt == 6) wait_bar<0>();
    phase(A1, B1, A0, B0, kt + 1, 0, kt + 4, 2);
  }
  MS(25);

  float* st = (float*)(smem + 49152);
  float* csp = (float*)(smem + 49152 + 1024);
  const float s2 = xsum2(s2a + s2b);
  if (g == 0) {
    float mean = s1v[0] * (1.f / 512.f);
    float var = fmaxf(s2 * (1.f / 512.f) - mean * mean, 0.f);
    float rstd = rsqrtf(var + LN_EPS);
    *(f32x2*)(st + (tg * 32 + nh * 16 + i) * 2) = f32x2{-mean * rstd, rstd};
  }
  if (i == 0 && tg < 3) *(f32x4*)(csp + un[0] * 16 + 4 * g) = cs;
  bar();
  f32x2 ms[2];
#pragma unroll
  for (int tt = 0; tt < 2; ++tt) ms[tt] = *(const f32x2*)(st + (tg * 32 + (tt ^ nh) * 16 + i) * 2);
  const int otile = (g & 1) ^ nh, ohalf = g >> 1;
  const unsigned vo = ohalf * 512 + (((otile * 16 + i) ^ (8 * ohalf)) << 4);
#pragma unroll
  for (int nt = 0; nt < 3; ++nt) {
    f32x4 c = *(const f32x4*)(csp + un[nt] * 16 + 4 * g);
    unsigned o[2][2];
#pragma unroll
    for (int tt = 0; tt < 2; ++tt) {
      f32x4 y = acc[tt][nt] * ms[tt][1] + ms[tt][0] * c;
      o[tt][0] = pack2(y[0], y[1]); o[tt][1] = pack2(y[2], y[3]);
    }
    auto sx = __builtin_amdgcn_permlane16_swap(o[0][0], o[1][0], false, false);
    auto sy = __builtin_amdgcn_permlane16_swap(o[0][1], o[1][1], false, false);
    char* dst = qkv + (size_t)(cb * 6 + un[nt]) * (2048 * 32) + (size_t)(tb * 4 + tg) * 1024;
    st16<true>(dst + vo, u32x4{sx[0], sy[0], sx[1], sy[1]});
  }
}

constexpr int S0 = 0, S1 = 49152, S2 = 98304, CSP = 147456, MG_LDS = 163840;
typedef unsigned long long u64;
extern "C" __device__ u64 llvm_dispatch_id() __asm("llvm.amdgcn.dispatch.id");
DEV void flag_set(u64* f, u64 tag) {
  asm volatile("global_store_dwordx2 %0, %1, off sc0 sc1" ::"v"(f), "v"(tag) : "memory");
}
DEV void flag_wait(const u64* p, u64 tag) {
  for (int it = 0; it < (1 << 20); ++it) {
    u64 c;
    asm volatile("global_load_dwordx2 %0, %1, off sc0 sc1\n\ts_waitcnt vmcnt(0)" : "=v"(c) : "v"(p) : "memory");
    if (__builtin_amdgcn_ballot_w64(c != tag) == 0) break;
  }
}
template <int V> struct IC { static constexpr int value = V; };
DEV int opaque(int v) { asm volatile("" : "+v"(v)); return v; }
DEV int opaque_s(int v) { asm volatile("" : "+s"(v)); return v; }

struct CW {
  const char *g1, *g3; unsigned vw; unsigned long long wmask;
};
DEV CW c_setup(const char* w1, const char* w3, int b, int wv, int lane) {
  const int xcd = b & 7, bi = b >> 3;
  const int cb = (xcd & 3) * 4 + (bi & 3);
  CW c;
  c.g1 = w1 + (size_t)(wv * 8) * (F_ * 2) + cb * 224;
  c.g3 = w3 + (size_t)(wv * 8) * (F_ * 2) + cb * 224;
  const int unit = ((lane >> 1) & 7) ^ ((lane >> 4) | ((wv & 1) << 2));
  c.vw = (lane >> 4) * (F_ * 2) + unit * 32 + (lane & 1) * 16;
  c.wmask = __builtin_amdgcn_ballot_w64(unit < 7);
  return c;
}
DEV void c_dmaw(const CW& c, unsigned ldsw, int kt, int j) {
  const unsigned base = ldsw + (kt % 3) * 49152;
  if (j < 2) dma16sm(c.vw, c.g1 + (size_t)(kt * 64 + j * 4) * (F_ * 2), base + 16384 + j * 1024, c.wmask);
  else dma16sm(c.vw, c.g3 + (size_t)(kt * 64 + (j - 2) * 4) * (F_ * 2), base + 32768 + (j - 2) * 1024, c.wmask);
}
DEV constexpr int d_unit(int u) {
  return u < 3 ? S2 + u * 2048 : u < 6 ? S0 + (u - 3) * 2048 : u < 9 ? S1 + (u - 6) * 2048 : u == 9 ? CSP
       : u < 13 ? S2 + (u - 10) * 2048 : S0;
}
DEV constexpr int d_ustr(int u) { return u == 9 ? 2048 : 6144; }
DEV void d_dma(const char* gw2, unsigned vw, unsigned lds0, int wv, int u, int q) {
  dma16s(vw, gw2 + (size_t)(u * 16 + q * 8) * 1024, lds0 + d_unit(u) + wv * d_ustr(u) + q * 1024);
}

template <int NT>
DEV void c_body(char* smem, unsigned lds0, const char* gt, const CW& cw, unsigned vt, int wv, int lane,
                const char* __restrict__ stats, const char* __restrict__ csb, char* __restrict__ hh, int tok0, int cb, const char* gw2, unsigned vwd MSARG) {
  const int tg = wv & 3;
  const unsigned ldsw = lds0 + wv * 2048;
  const int g = lane >> 4, i = lane & 15, q4 = (lane >> 2) & 3, p4 = lane & 3;
  f32x4 acc[2][2 * NT];
#pragma unroll
  for (int a = 0; a < 2 * NT; ++a) { acc[0][a] = f32x4{0, 0, 0, 0}; acc[1][a] = f32x4{0, 0, 0, 0}; }
  const int toff0 = (tg * 32 + i) * 128 + ((g ^ (i & 7)) << 4);
  const int toff1 = (tg * 32 + i) * 128 + (((4 + g) ^ (i & 7)) << 4);
  const int odd = cb & 1;
  const int fsw = q4 | ((g & 1) << 2);
  int T[NT], wa[NT];
#pragma unroll
  for (int a = 0; a < NT; ++a) {
    T[a] = NT == 4 ? odd * 3 + a : (a < 2 ? (odd ? 1 : 4) + a : (odd ? 0 : 6));
    wa[a] = 16384 + (8 * g + q4) * 256 + ((T[a] ^ fsw) << 5) + p4 * 8;
  }
  auto dmat = [&](int kt, int j) { dma16s(vt, gt + kt * 128 + j * 8192, ldsw + (kt % 3) * 49152 + j * 1024); };
  auto dma = [&](int kt, int j) {
    if (j < 2) dmat(kt, j); else c_dmaw(cw, ldsw, kt, j - 2);
  };
  auto ldB = [&](bf16x8* B, int kt, int ks) {
    const char* sb = smem + (kt % 3) * 49152 + (ks ? toff1 : toff0);
    B[0] = lds128(sb); B[1] = lds128(sb + 2048);
  };
  auto ldA = [&](int a, int kt, int ks) -> bf16x8 {
    const char* wp = smem + (kt % 3) * 49152 + wa[a < NT ? a : a - NT] + (a < NT ? 0 : 16384) + ks * 8192;
    return cat(ldstr(wp), ldstr(wp + 1024));
  };
  auto mma = [&](int a, bf16x8 A, const bf16x8* B) {
    acc[0][a] = mfma16(A, B[0], acc[0][a]);
    acc[1][a] = mfma16(A, B[1], acc[1][a]);
  };
  bf16x8 A0[2 * NT], A1[2 * NT], B0[2], B1[2];
  bf16x8 sa[2], cp[4];
#pragma unroll
  for (int kt = 0; kt < 3; ++kt) { dmat(kt, 0); dmat(kt, 1); }
  gld16(sa[0], (unsigned)(i * 64 + g * 16), stats + (size_t)(tok0 + tg * 32) * 64);
  gld16(sa[1], (unsigned)(i * 64 + g * 16), stats + (size_t)(tok0 + tg * 32 + 16) * 64);
  const int cq = min(tg * 16 + (lane >> 2), 55);
  if (NT == 3) {
#pragma unroll
    for (int m = 0; m < 4; ++m) gld16(cp[m], (unsigned)(((lane & 3) * 4 + m) * 896 + cq * 16), csb + (size_t)cb * 14336);
  }
  constexpr int X = NT == 3 ? 6 : 2;
  wait_bar<4 + X>();
  MS(10);
  float rs[2], mr[2];
  float* csp = (float*)(smem + CSP);
  ldB(B0, 0, 0);
#pragma unroll
  for (int a = 0; a < 2 * NT; ++a) A0[a] = ldA(a, 0, 0);
#pragma unroll
  for (int kt = 0; kt < 8; ++kt) {
    ldB(B1, kt, 1);
#pragma unroll
    for (int a = 0; a < 2 * NT; ++a) {
      if (a < NT) { A1[2 * a] = ldA(2 * a, kt, 1); A1[2 * a + 1] = ldA(2 * a + 1, kt, 1); }
      mma(a, A0[a], B0);
      __builtin_amdgcn_sched_barrier(0);
    }
    if (kt == 0) wait_bar<2 + X>();
    else if (kt != 1) wait_bar<6>();
    else if (NT == 3) asm volatile("s_waitcnt vmcnt(6) lgkmcnt(0)\n\ts_barrier" : "+v"(sa[0]), "+v"(sa[1]), "+v"(cp[0]), "+v"(cp[1]), "+v"(cp[2]), "+v"(cp[3]) : : "memory");
    else asm volatile("s_waitcnt vmcnt(6) lgkmcnt(0)\n\ts_barrier" : "+v"(sa[0]), "+v"(sa[1]) : : "memory");
    if (kt == 0) MS(11); if (kt == 3) MS(12); if (kt == 6) MS(13);
    if (kt < 7) ldB(B0, kt + 1, 0);
    if (kt == 1) {
#pragma unroll
      for (int tt = 0; tt < 2; ++tt) {
        const f32x4 sv = __builtin_bit_cast(f32x4, sa[tt]);
        const float s1 = xsum2(sv[0] + sv[2]), s2 = xsum2(sv[1] + sv[3]);
        const float mean = s1 * (1.f / 512.f);
        const float var = fmaxf(s2 * (1.f / 512.f) - mean * mean, 0.f);
        rs[tt] = rsqrtf(var + LN_EPS); mr[tt] = mean * rs[tt];
      }
      if (NT == 3) {
        f32x4 c = (__builtin_bit_cast(f32x4, cp[0]) + __builtin_bit_cast(f32x4, cp[1])) +
                  (__builtin_bit_cast(f32x4, cp[2]) + __builtin_bit_cast(f32x4, cp[3]));
#pragma unroll
        for (int e = 0; e < 4; ++e) { c[e] += dpp_xor1(c[e]); c[e] += dpp_xor2(c[e]); }
        if ((lane & 3) == 0) *(f32x4*)(csp + cq * 4) = c;
      }
    }
#pragma unroll
    for (int a = 0; a < 2 * NT; ++a) {
      if (kt < 7 && a < NT) { A0[2 * a] = ldA(2 * a, kt + 1, 0); A0[2 * a + 1] = ldA(2 * a + 1, kt + 1, 0); }
      mma(a, A1[a], B1);
      if (a >= 2 * NT - 6) {
        const int j = a - (2 * NT - 6);
        if (kt + 3 < 8) dma(kt + 3, j);
        else d_dma(gw2, vwd, lds0, wv, (kt - 5) * 3 + (j >> 1), j & 1);
      }
      __builtin_amdgcn_sched_barrier(0);
    }
  }
  MS(14);
  unsigned pk[2][NT][2];
#pragma unroll
  for (int tt = 0; tt < 2; ++tt) {
    char* dst = hh + ((size_t)((tok0 >> 4) + tg * 2 + tt) * 56) * 1024 + lane * 16;
#pragma unroll
    for (int a = 0; a < NT; ++a) {
      f32x4 cg = *(const f32x4*)(csp + T[a] * 16 + 4 * g);
      f32x4 cu = *(const f32x4*)(csp + (7 + T[a]) * 16 + 4 * g);
      float r[4];
#pragma unroll
      for (int e = 0; e < 4; ++e) {
        float ga = acc[tt][a][e] * rs[tt] - mr[tt] * cg[e];
        float up = acc[tt][NT + a][e] * rs[tt] - mr[tt] * cu[e];
        float sg = __builtin_amdgcn_rcpf(1.f + __builtin_amdgcn_exp2f(-1.4426950408889634f * ga));
        r[e] = ga * sg * up;
      }
      pk[tt][a][0] = pack2(r[0], r[1]); pk[tt][a][1] = pack2(r[2], r[3]);
      const int G = cb * 7 + T[a];
      if (a & 1) {
        st16<true>(dst + (size_t)(G >> 1) * 1024, u32x4{pk[tt][a - 1][0], pk[tt][a - 1][1], pk[tt][a][0], pk[tt][a][1]});
      } else if (a == NT - 1) {
        st8<true>(dst + (size_t)(G >> 1) * 1024 + (G & 1) * 8, u32x2{pk[tt][a][0], pk[tt][a][1]});
      }
    }
  }
}

__global__ __launch_bounds__(512) void k_mega(const char* __restrict__ x, const char* __restrict__ wqkv, const char* __restrict__ wo,
                                              const char* __restrict__ w1, const char* __restrict__ w3,
                                              const char* __restrict__ w2, char* __restrict__ ws, char* __restrict__ out) {
  extern __shared__ __attribute__((aligned(16))) char smem[];
  MSDECL;
  MS(0);
  char* qkv = ws;
  char* o = ws + (6u << 20);
  char* h = ws + (8u << 20);
  char* stats = ws + (10u << 20);
  u64* flags = (u64*)(ws + (10u << 20) + (512u << 10));
  char* csb = ws + (10u << 20) + (768u << 10);
  char* hh = ws + (11u << 20);
  const u64 tag = ((u64)(size_t)__builtin_amdgcn_dispatch_ptr() << 20) + llvm_dispatch_id();
  const int b = blockIdx.x;
  const unsigned lds0 = (unsigned)(size_t)(LAS char*)smem;
  const int cbG = (b >> 3) & 7, tbG = (b & 7) * 4 + (b >> 6);
  const int cbC = (b & 3) * 4 + ((b >> 3) & 3), tbC = ((b >> 2) & 1) * 8 + (b >> 5);

  {
    const int cb = (b & 1) * 8 + ((b >> 3) & 7), tb = ((b & 7) >> 1) * 4 + (b >> 6);
    qkv_phase(smem, lds0, x, wqkv, qkv, cb, tb MSPASS);
    MS(26);
    asm volatile("s_waitcnt vmcnt(0) lgkmcnt(0)\n\ts_barrier" ::: "memory");
    MS(27);
    const int tid = threadIdx.x;
    if (tid < 9) {
      const int P = cb * 3 + tid / 3, d = tid % 3, which = P >> 4, hp = P & 15, qb = tb + d;
      if (which ? qb < 16 : d == 0)
        flag_set(flags + 24576 + ((hp >> 1) | ((hp & 1) << 3) | (qb << 4)) * 32 + (which ? (which - 1) * 3 + d : 6), tag);
    }
  }
  {
    const int tid = opaque(threadIdx.x), lane = tid & 63, wv = uni(tid >> 6);
    const int c32 = lane & 31, hf = lane >> 5, q4 = (lane >> 2) & 3, p4 = lane & 3;
    const int hp = (b & 7) * 2 + ((b >> 3) & 1), qb = b >> 4;
    const int q0 = qb * 128;
    const size_t HS = 2048 * 32;
    bf16x8 Q, cw0, cw1;
    if (wv == 0) {
      const int d = lane % 3;
      flag_wait(flags + 24576 + b * 32 + (lane >= 6 ? 6 : qb - d < 0 ? 6 : lane), tag);
      MS(28);
    }
    asm volatile("s_barrier" ::: "memory");
    {
      const int r = wv >> 1;
      const char* src = qkv + (size_t)((1 + (r >> 1)) * 32 + hp * 2 + (r & 1)) * HS;
#pragma unroll
      for (int j = 0; j < 6; ++j) {
        const int kb = qb * 4 - 8 + (wv & 1) * 6 + j;
        dma16s(kb < 0 ? hf * 640 : lane * 16, src + (size_t)max(kb, 0) * 1024, lds0 + r * 12288 + ((wv & 1) * 6 + j) * 1024);
      }
    }
    const int u = wv >> 2, qg = wv & 3;
    const int qs = q0 + qg * 32;
    const unsigned vq = hf * 512 + ((c32 ^ (8 * hf)) << 4);
    gld16(Q, vq, qkv + (size_t)(hp * 2 + u) * HS + (size_t)qs * 32);
    const float c = 0.25f * 1.4426950408889634f;
    const float NEG = -1e30f;
    f32x16 cin0, cin8;
#pragma unroll
    for (int r = 0; r < 16; ++r) {
      const int kl = (r & 3) + 8 * (r >> 2) + 4 * hf;
      cin0[r] = kl > c32 ? 0.f : NEG; cin8[r] = kl <= c32 ? 0.f : NEG;
    }
    asm volatile("s_waitcnt vmcnt(0)\n\ts_barrier" : "+v"(Q), "+v"(cin0), "+v"(cin8) : : "memory");
    MS(1);
    const unsigned vwo = (lane >> 3) * 1024 + ((((lane & 7) >> 1) ^ ((lane >> 4) & 3)) << 5) + (lane & 1) * 16;
    const char* gwo = wo + (size_t)(wv * 64) * 1024 + cbG * 128;
    const CW cw1s = c_setup(w1, w3, b, wv, lane);
    auto pf = [&](int k) {
      if (k < 2) {
        const int ch = max(wv - 1, 0) * 4 + (lane & 3);
        const char* pc = (ch >= 14 ? w3 + (ch - 14) * 16 : w1 + ch * 16) + (size_t)(tbC * 32 + (lane >> 2)) * (F_ * 2) + cbC * 224;
        if (k == 0) asm volatile("global_load_dwordx4 %0, %1, off" : "=v"(cw0) : "v"(pc) : "memory");
        else asm volatile("global_load_dwordx4 %0, %1, off" : "=v"(cw1) : "v"(pc + 16 * (F_ * 2)) : "memory");
      } else if (k < 10) dma16s(vwo, gwo + (size_t)((k - 2) * 8) * 1024, lds0 + S2 + wv * 8192 + (k - 2) * 1024);
      else if (k < 14) c_dmaw(cw1s, lds0 + wv * 2048, 1, k - 10);
      else if (k == 14) {
        const int c0 = b * 8 + wv;
        dma16s(lane * 16, w2 + (size_t)(c0 < 1792 ? c0 : c0 - 1792) * 1024, lds0 + S1 + wv * 1024);
      }
    };
    {
      const char* Kl = smem + u * 12288 + qg * 1024 + vq;
      const char* Vl[2];
#pragma unroll
      for (int e = 0; e < 2; ++e)
        Vl[e] = smem + 24576 + u * 12288 + qg * 1024 + (p4 >> 1) * 512 + (((e * 8 + 4 * hf + q4) ^ (8 * (p4 >> 1))) << 4) + (p4 & 1) * 8;
      f32x16 s[9];
#pragma unroll
      for (int t = 0; t < 9; ++t) {
        f32x16 z;
#pragma unroll
        for (int r = 0; r < 16; ++r) z[r] = 0.f;
        s[t] = mfma32(lds128(Kl + t * 1024), Q, t == 0 ? cin0 : t == 8 ? cin8 : z);
      }
      float m = s[8][0];
#pragma unroll
      for (int t = 0; t < 9; ++t)
#pragma unroll
        for (int r = 0; r < 16; ++r) m = fmaxf(m, s[t][r]);
      {
        auto sw = __builtin_amdgcn_permlane32_swap(__float_as_uint(m), __float_as_uint(m), false, false);
        m = fmaxf(__uint_as_float(sw[0]), __uint_as_float(sw[1]));
      }
      MS(15);
      const f32x2 c2 = {c, c}, m2 = {-m * c, -m * c};
      const int nneg = qs >= 256 ? 0 : (256 - qs) >> 5;
      f32x16 oacc;
#pragma unroll
      for (int r = 0; r < 16; ++r) oacc[r] = 0.f;
      float ls0 = 0.f, ls1 = 0.f;
#pragma unroll
      for (int t = 0; t < 9; ++t) {
        const float mt = t < nneg ? NEG : m2[0];
        const f32x2 mt2 = {mt, mt};
#pragma unroll
        for (int sh = 0; sh < 2; ++sh) {
          u32x4 pb;
#pragma unroll
          for (int e = 0; e < 4; ++e) {
            f32x2 a = __builtin_elementwise_fma(f32x2{s[t][8 * sh + 2 * e], s[t][8 * sh + 2 * e + 1]}, c2, mt2);
            pb[e] = pack2(__builtin_amdgcn_exp2f(a[0]), __builtin_amdgcn_exp2f(a[1]));
          }
          ls0 = dot2(pb[0], 0x3F803F80u, ls0); ls1 = dot2(pb[1], 0x3F803F80u, ls1);
          ls0 = dot2(pb[2], 0x3F803F80u, ls0); ls1 = dot2(pb[3], 0x3F803F80u, ls1);
          const int vp = t * 1024 + sh * 256;
          oacc = mfma32(cat(ldstr(Vl[0] + vp), ldstr(Vl[1] + vp)), __builtin_bit_cast(bf16x8, pb), oacc);
          if (2 * t + sh < 15) pf(2 * t + sh);
        }
      }
      float ls = ls0 + ls1;
      {
        auto sw = __builtin_amdgcn_permlane32_swap(__float_as_uint(ls), __float_as_uint(ls), false, false);
        ls = __uint_as_float(sw[0]) + __uint_as_float(sw[1]);
      }
      const float inv = __builtin_amdgcn_rcpf(ls);
      const u32x4 ov = {pack2(oacc[0] * inv, oacc[1] * inv), pack2(oacc[2] * inv, oacc[3] * inv),
                        pack2(oacc[4] * inv, oacc[5] * inv), pack2(oacc[6] * inv, oacc[7] * inv)};
      st16<true>(o + ((size_t)((qs >> 4) + (c32 >> 4)) * 16 + hp) * 1024 + ((2 * u + hf) * 16 + (c32 & 15)) * 16, ov);
    }
    MS(2);
    asm volatile("s_waitcnt vmcnt(0) lgkmcnt(0)\n\ts_barrier" : "+v"(cw0), "+v"(cw1) : : "memory");
    MS(16);
    if (tid < 16) flag_set(flags + (((qb >> 1) | (tid << 3) | ((qb & 1) << 7)) * 32 + hp), tag);
    {
      const CW cw = c_setup(w1, w3, b, wv, lane);
#pragma unroll
      for (int j = 0; j < 4; ++j) c_dmaw(cw, lds0 + wv * 2048, 0, j);
    }
    if (wv == 0) { flag_wait(flags + b * 32 + (lane & 15), tag); MS(17); }
    else {
      const u32x4 ua = __builtin_bit_cast(u32x4, cw0), ub = __builtin_bit_cast(u32x4, cw1);
      f32x4 lo, hi;
#pragma unroll
      for (int e = 0; e < 2; ++e) {
        lo[2 * e] = bflo(ua[e]) + bflo(ub[e]); lo[2 * e + 1] = bfhi(ua[e]) + bfhi(ub[e]);
        hi[2 * e] = bflo(ua[2 + e]) + bflo(ub[2 + e]); hi[2 * e + 1] = bfhi(ua[2 + e]) + bfhi(ub[2 + e]);
      }
#pragma unroll
      for (int e = 0; e < 4; ++e) {
        lo[e] += dpp_ror4(lo[e]); lo[e] += dpp_ror8(lo[e]); lo[e] = xsum2(lo[e]);
        hi[e] += dpp_ror4(hi[e]); hi[e] += dpp_ror8(hi[e]); hi[e] = xsum2(hi[e]);
      }
      if (lane < 4) {
        char* d = csb + (size_t)(cbC * 16 + tbC) * 896 + ((wv - 1) * 4 + lane) * 32;
        st16<true>(d, __builtin_bit_cast(u32x4, lo));
        st16<true>(d + 16, __builtin_bit_cast(u32x4, hi));
      }
      asm volatile("s_waitcnt vmcnt(0)" ::: "memory");
    }
    asm volatile("s_barrier" ::: "memory");
    if (tid >= 64 && tid < 80) flag_set(flags + 8192 + ((cbC >> 2) | ((tid & 1) << 2) | ((cbC & 3) << 3) | (((tid & 15) >> 1) << 5)) * 32 + 16 + tbC, tag);
    MS(3);
  }
  {
    const int tid = opaque(threadIdx.x), lane = tid & 63, wv = uni(tid >> 6);
    const int g = lane >> 4, i = lane & 15, q4 = (lane >> 2) & 3, p4 = lane & 3;
    const int kp = wv & 3, th = wv >> 2;
    bf16x8 B[4][2], rs;
    const char* ga = o + ((size_t)(tbG * 4 + th * 2) * 16 + kp * 4) * 1024;
#pragma unroll
    for (int s = 0; s < 4; ++s) {
      gld16(B[s][0], lane * 16, ga + s * 1024);
      gld16(B[s][1], lane * 16, ga + (16 + s) * 1024);
    }
    const size_t ooff = (size_t)(tbG * 64 + wv * 8) * 1024 + cbG * 128;
    const unsigned vo = (lane >> 3) * 1024 + (lane & 7) * 16;
    gld16(rs, vo, x + ooff);
    const int sw = (2 * (g & 1) + (q4 >> 1)) & 3;
    const char* wr = smem + S2 + kp * 16384 + (16 * (g >> 1) + 4 * (g & 1) + q4) * 128 + p4 * 8;
    bf16x8 A[4][4];
#pragma unroll
    for (int s = 0; s < 4; ++s)
#pragma unroll
      for (int nt = 0; nt < 4; ++nt) {
        const char* p = wr + s * 4096 + ((nt ^ sw) << 5);
        A[s][nt] = cat(ldstr(p), ldstr(p + 1024));
      }
    f32x4 acc[2][4];
#pragma unroll
    for (int t = 0; t < 2; ++t)
#pragma unroll
      for (int n = 0; n < 4; ++n) acc[t][n] = f32x4{0, 0, 0, 0};
    asm volatile("s_waitcnt vmcnt(7)" : "+v"(B[0][0]), "+v"(B[0][1]) : : "memory");
    MS(22);
#pragma unroll
    for (int nt = 0; nt < 4; ++nt) { acc[0][nt] = mfma16(A[0][nt], B[0][0], acc[0][nt]); acc[1][nt] = mfma16(A[0][nt], B[0][1], acc[1][nt]); }
    asm volatile("s_waitcnt vmcnt(5)" : "+v"(B[1][0]), "+v"(B[1][1]) : : "memory");
#pragma unroll
    for (int nt = 0; nt < 4; ++nt) { acc[0][nt] = mfma16(A[1][nt], B[1][0], acc[0][nt]); acc[1][nt] = mfma16(A[1][nt], B[1][1], acc[1][nt]); }
    asm volatile("s_waitcnt vmcnt(3)" : "+v"(B[2][0]), "+v"(B[2][1]) : : "memory");
#pragma unroll
    for (int nt = 0; nt < 4; ++nt) { acc[0][nt] = mfma16(A[2][nt], B[2][0], acc[0][nt]); acc[1][nt] = mfma16(A[2][nt], B[2][1], acc[1][nt]); }
    asm volatile("s_waitcnt vmcnt(1)" : "+v"(B[3][0]), "+v"(B[3][1]) : : "memory");
#pragma unroll
    for (int nt = 0; nt < 4; ++nt) { acc[0][nt] = mfma16(A[3][nt], B[3][0], acc[0][nt]); acc[1][nt] = mfma16(A[3][nt], B[3][1], acc[1][nt]); }
    MS(23);
    bar();
    {
      const int mo = S2 + kp * 16384 + g * 256 + ((i ^ ((g >> 1) * 2)) << 4);
#pragma unroll
      for (int tt = 0; tt < 2; ++tt)
#pragma unroll
        for (int nt = 0; nt < 4; ++nt)
          *(LAS f32x4*)(LAS char*)(smem + (mo ^ (nt * 64)) + ((th * 2 + tt) * 4 + nt) * 1024) = acc[tt][nt];
    }
    asm volatile("s_waitcnt vmcnt(0) lgkmcnt(0)\n\ts_barrier" : "+v"(rs) : : "memory");
    {
      const int c = lane & 7;
      const int tok = (lane >> 3) + 8 * (wv & 1);
      const char* rp = smem + S2 + ((wv >> 1) * 4 + (c >> 1)) * 1024 + (c & 1) * 512 + ((tok ^ (2 * c)) << 4);
      f32x4 v0 = f32x4{0, 0, 0, 0}, v1 = f32x4{0, 0, 0, 0};
#pragma unroll
      for (int p = 0; p < 4; ++p) {
        v0 += *(const LAS f32x4*)(LAS const char*)(rp + p * 16384);
        v1 += *(const LAS f32x4*)(LAS const char*)(rp + p * 16384 + 256);
      }
      u32x4 r = __builtin_bit_cast(u32x4, rs);
      u32x4 ov = {pack2(v0[0] + bflo(r[0]), v0[1] + bfhi(r[0])), pack2(v0[2] + bflo(r[1]), v0[3] + bfhi(r[1])),
                  pack2(v1[0] + bflo(r[2]), v1[1] + bfhi(r[2])), pack2(v1[2] + bflo(r[3]), v1[3] + bfhi(r[3]))};
      st16<true>(h + ooff + vo, ov);
      float s1 = 0.f, s2 = 0.f;
#pragma unroll
      for (int e = 0; e < 4; ++e) { s1 = dot2(ov[e], 0x3F803F80u, s1); s2 = dot2(ov[e], ov[e], s2); }
#pragma unroll
      for (int m = 1; m < 8; m <<= 1) { s1 += __shfl_xor(s1, m); s2 += __shfl_xor(s2, m); }
      if (c == 0) st8<true>(stats + ((size_t)(tbG * 64 + wv * 8 + (lane >> 3)) * 8 + cbG) * 8,
                            u32x2{__float_as_uint(s1), __float_as_uint(s2)});
    }
    MS(4);
    asm volatile("s_waitcnt vmcnt(0) lgkmcnt(0)\n\ts_barrier" ::: "memory");
    MS(18);
    if (tid < 16) flag_set(flags + 8192 + ((tid & 3) | ((tbG >> 4) << 2) | ((tid >> 2) << 3) | (((tbG >> 1) & 7) << 5)) * 32 + (tbG & 1) * 8 + cbG, tag);
    {
      const CW cw = c_setup(w1, w3, b, wv, lane);
#pragma unroll
      for (int j = 0; j < 4; ++j) c_dmaw(cw, lds0 + wv * 2048, 2, j);
    }
    if (wv == 0) flag_wait(flags + 8192 + b * 32 + (lane & 31), tag); MS(19);
    asm volatile("s_barrier" ::: "memory");
    MS(5);
  }
  const unsigned vwd_ = 0;
  {
    const int tid = opaque(threadIdx.x), lane = tid & 63, wv = uni(tid >> 6);
    const char* gt = h + (size_t)(tbC * 128 + wv * 16) * 1024;
    const unsigned vt = (lane >> 3) * 1024 + (((lane & 7) ^ (lane >> 3)) << 4);
    const CW cw = c_setup(w1, w3, b, wv, lane);
    const unsigned vwd = (lane >> 3) * 1024 + ((((lane & 7) >> 1) ^ ((lane >> 4) & 3)) << 5) + (lane & 1) * 16;
    const char* gw2 = w2 + (size_t)(wv * 224) * 1024 + cbG * 128;
    if ((wv >> 2) == 0) c_body<4>(smem, lds0, gt, cw, vt, wv, lane, stats, csb, hh, tbC * 128, cbC, gw2, vwd MSPASS);
    else c_body<3>(smem, lds0, gt, cw, vt, wv, lane, stats, csb, hh, tbC * 128, cbC, gw2, vwd MSPASS);
    MS(6);
  }
  {
    const int tid = opaque(threadIdx.x), lane = tid & 63, wv = uni(tid >> 6);
    const int g = lane >> 4, i = lane & 15, q4 = (lane >> 2) & 3, p4 = lane & 3;
    asm volatile("s_waitcnt vmcnt(0) lgkmcnt(0)\n\ts_barrier" ::: "memory");
    MS(20);
    if (tid < 16) flag_set(flags + 16384 + ((tbC >> 1) | (tid << 3) | ((tbC & 1) << 7)) * 32 + cbC, tag);
    const unsigned vwd = (lane >> 3) * 1024 + ((((lane & 7) >> 1) ^ ((lane >> 4) & 3)) << 5) + (lane & 1) * 16;
    const char* gw2 = w2 + (size_t)(wv * 224) * 1024 + cbG * 128;
    d_dma(gw2, vwd, lds0, wv, 9, 0); d_dma(gw2, vwd, lds0, wv, 9, 1);
    const int sw = (2 * g + (q4 >> 1)) & 3;
    const int ro = (4 * g + q4) * 128 + p4 * 8;
    int rn[4];
#pragma unroll
    for (int nt = 0; nt < 4; ++nt) rn[nt] = ro + ((nt ^ sw) << 5);
    auto lda = [&](bf16x8* Af, int j) {
      const char* p0 = smem + d_unit(2 * j) + wv * d_ustr(2 * j);
      const char* p1 = smem + d_unit(2 * j + 1) + wv * d_ustr(2 * j + 1);
#pragma unroll
      for (int nt = 0; nt < 4; ++nt) Af[nt] = cat(ldstr(p0 + rn[nt]), ldstr(p1 + rn[nt]));
    };
    bf16x8 A[2][4], B[7][4], rs;
    lda(A[0], 0); lda(A[1], 1);
    asm volatile("s_waitcnt lgkmcnt(0)" : "+v"(A[0][0]), "+v"(A[0][1]), "+v"(A[0][2]), "+v"(A[0][3]), "+v"(A[1][0]), "+v"(A[1][1]), "+v"(A[1][2]), "+v"(A[1][3]) : : "memory");
#pragma unroll
    for (int u = 10; u < 14; ++u) { d_dma(gw2, vwd, lds0, wv, u, 0); d_dma(gw2, vwd, lds0, wv, u, 1); }
    if (wv == 0) flag_wait(flags + 16384 + b * 32 + (lane & 15), tag); MS(21);
    asm volatile("s_barrier" ::: "memory");
    MS(7);
    const char* ga = hh + ((size_t)(tbG * 4) * 56 + wv * 7) * 1024;
#pragma unroll
    for (int j = 0; j < 7; ++j)
#pragma unroll
      for (int tt = 0; tt < 4; ++tt) gld16(B[j][tt], lane * 16, ga + (size_t)(tt * 56 + j) * 1024);
    const size_t ooff = (size_t)(tbG * 64 + wv * 8) * 1024 + cbG * 128;
    const unsigned vo = (lane >> 3) * 1024 + (lane & 7) * 16;
    gld16(rs, vo, h + ooff);
    f32x4 acc[4][4];
#pragma unroll
    for (int t = 0; t < 4; ++t)
#pragma unroll
      for (int n = 0; n < 4; ++n) acc[t][n] = f32x4{0, 0, 0, 0};
    auto step = [&](auto J) {
      constexpr int j = decltype(J)::value;
      asm volatile("s_waitcnt vmcnt(%4)" : "+v"(B[j][0]), "+v"(B[j][1]), "+v"(B[j][2]), "+v"(B[j][3]) : "n"(25 - 4 * j) : "memory");
#pragma unroll
      for (int nt = 0; nt < 4; ++nt)
#pragma unroll
        for (int tt = 0; tt < 4; ++tt) acc[tt][nt] = mfma16(A[j & 1][nt], B[j][tt], acc[tt][nt]);
      __builtin_amdgcn_sched_barrier(0);
      if constexpr (j + 2 < 7) lda(A[j & 1], j + 2);
    };
    step(IC<0>{}); step(IC<1>{}); step(IC<2>{}); step(IC<3>{}); step(IC<4>{}); step(IC<5>{}); step(IC<6>{});
    {
      const int mo = wv * 6144 + g * 256 + ((i ^ ((g >> 1) * 2)) << 4);
#pragma unroll
      for (int tt = 0; tt < 4; ++tt)
#pragma unroll
        for (int nt = 0; nt < 4; ++nt) {
          const int t = tt * 4 + nt;
          const int base = t < 6 ? S2 + t * 1024 : t < 12 ? S0 + (t - 6) * 1024 : S1 + (t - 12) * 1024;
          *(LAS f32x4*)(LAS char*)(smem + (mo ^ (nt * 64)) + base) = acc[tt][nt];
        }
    }
    asm volatile("s_waitcnt vmcnt(0) lgkmcnt(0)\n\ts_barrier" : "+v"(rs) : : "memory");
    {
      const int c = lane & 7;
      const int tok = (lane >> 3) + 8 * (wv & 1);
      const int t = (wv >> 1) * 4 + (c >> 1);
      const int base = t < 6 ? S2 + t * 1024 : t < 12 ? S0 + (t - 6) * 1024 : S1 + (t - 12) * 1024;
      const char* rp = smem + base + (c & 1) * 512 + ((tok ^ (2 * c)) << 4);
      f32x4 v0 = f32x4{0, 0, 0, 0}, v1 = f32x4{0, 0, 0, 0};
#pragma unroll
      for (int p = 0; p < 8; ++p) {
        v0 += *(const LAS f32x4*)(LAS const char*)(rp + p * 6144);
        v1 += *(const LAS f32x4*)(LAS const char*)(rp + p * 6144 + 256);
      }
      u32x4 r = __builtin_bit_cast(u32x4, rs);
      u32x4 ov = {pack2(v0[0] + bflo(r[0]), v0[1] + bfhi(r[0])), pack2(v0[2] + bflo(r[1]), v0[3] + bfhi(r[1])),
                  pack2(v1[0] + bflo(r[2]), v1[1] + bfhi(r[2])), pack2(v1[2] + bflo(r[3]), v1[3] + bfhi(r[3]))};
      st16<true>(out + ooff + vo, ov);
    }
    MS(8);
  }
  if (HX >= 0 && blockIdx.x == HXB && threadIdx.x < 64) {
    asm volatile("s_waitcnt vmcnt(0)" ::: "memory");
    const unsigned te = (unsigned)__builtin_amdgcn_s_memrealtime();
    unsigned d = te - hx_;
    if (d > 10000u) d = 10000u;
    while ((unsigned)__builtin_amdgcn_s_memrealtime() - te < 4u * d) __builtin_amdgcn_s_sleep(1);
  }
}

extern "C" void kernel_launch(void* const* d_in, const int* in_sizes, int n_in, void* d_out, int out_size,
                              void* d_ws, size_t ws_size, hipStream_t stream) {
  const char* x = (const char*)d_in[0];
  const char* wqkv = (const char*)d_in[1];
  const char* wo = (const char*)d_in[2];
  const char* w1 = (const char*)d_in[4];
  const char* w3 = (const char*)d_in[5];
  const char* w2 = (const char*)d_in[6];
  char* ws = (char*)d_ws;
  (void)hipFuncSetAttribute((const void*)k_mega, hipFuncAttributeMaxDynamicSharedMemorySize, MG_LDS);
  k_mega<<<256, 512, MG_LDS, stream>>>(x, wqkv, wo, w1, w3, w2, ws, (char*)d_out);
}
```

```cpp
#include <hip/hip_runtime.h>
#include <hip/hip_bf16.h>
#include <stdint.h>

typedef short bf16x8 __attribute__((ext_vector_type(8)));
typedef short s16x4 __attribute__((ext_vector_type(4)));
typedef float f32x4 __attribute__((ext_vector_type(4)));
typedef float f32x2 __attribute__((ext_vector_type(2)));
typedef unsigned u32x4 __attribute__((ext_vector_type(4)));
typedef unsigned u32x2 __attribute__((ext_vector_type(2)));
typedef __bf16 bf16x2 __attribute__((ext_vector_type(2)));
#define LAS __attribute__((address_space(3)))
#define DEV __device__ __forceinline__

constexpr int S_ = 2048, D_ = 512, H_ = 32, F_ = 1792;
constexpr float LN_EPS = 1e-5f;

typedef float f32x16 __attribute__((ext_vector_type(16)));
DEV f32x16 mfma32(bf16x8 a, bf16x8 b, f32x16 c) { return __builtin_amdgcn_mfma_f32_32x32x16_bf16(a, b, c, 0, 0, 0); }
DEV f32x4 mfma16(bf16x8 a, bf16x8 b, f32x4 c) { return __builtin_amdgcn_mfma_f32_16x16x32_bf16(a, b, c, 0, 0, 0); }
DEV unsigned pack2(float a, float b) {
  f32x2 v = {a, b};
  return __builtin_bit_cast(unsigned, __builtin_convertvector(v, bf16x2));
}
DEV float bflo(unsigned u) { return __builtin_bit_cast(float, u << 16); }
DEV float bfhi(unsigned u) { return __builtin_bit_cast(float, u & 0xffff0000u); }
DEV float dot2(unsigned a, unsigned b, float c) {
  return __builtin_amdgcn_fdot2_f32_bf16(__builtin_bit_cast(bf16x2, a), __builtin_bit_cast(bf16x2, b), c, false);
}
DEV void dma16(const char* gptr, unsigned ldsbase) {
  unsigned sv;
  asm volatile("s_mov_b32 %0, m0\n\ts_mov_b32 m0, %2\n\ts_nop 0\n\tglobal_load_lds_dwordx4 %1, off\n\ts_mov_b32 m0, %0"
               : "=&s"(sv) : "v"(gptr), "s"(ldsbase) : "memory");
}
DEV void dma16s(unsigned voff, const char* sbase, unsigned ldsbase) {
  unsigned sv;
  asm volatile("s_mov_b32 %0, m0\n\ts_mov_b32 m0, %3\n\ts_nop 0\n\tglobal_load_lds_dwordx4 %1, %2\n\ts_mov_b32 m0, %0"
               : "=&s"(sv) : "v"(voff), "s"(sbase), "s"(ldsbase) : "memory");
}
DEV void dma16sm(unsigned voff, const char* sbase, unsigned ldsbase, unsigned long long mask) {
  unsigned sv;
  asm volatile("s_mov_b32 %0, m0\n\ts_mov_b32 m0, %3\n\ts_mov_b64 exec, %4\n\tglobal_load_lds_dwordx4 %1, %2\n\ts_mov_b64 exec, -1\n\ts_mov_b32 m0, %0"
               : "=&s"(sv) : "v"(voff), "s"(sbase), "s"(ldsbase), "s"(mask) : "memory");
}
DEV void gld16(bf16x8& d, unsigned voff, const char* sbase) {
  asm volatile("global_load_dwordx4 %0, %1, %2" : "=v"(d) : "v"(voff), "s"(sbase) : "memory");
}
template <int N> DEV void wait_bar() { asm volatile("s_waitcnt vmcnt(%0) lgkmcnt(0)\n\ts_barrier" ::"n"(N) : "memory"); }
DEV void bar() { asm volatile("s_waitcnt lgkmcnt(0)\n\ts_barrier" ::: "memory"); }
DEV bf16x8 lds128(const char* p) { return *(const LAS bf16x8*)(LAS const char*)p; }
DEV s16x4 ldstr(const char* p) { return __builtin_amdgcn_ds_read_tr16_b64_v4i16((LAS s16x4*)p); }
DEV bf16x8 cat(s16x4 a, s16x4 b) { return __builtin_shufflevector(a, b, 0, 1, 2, 3, 4, 5, 6, 7); }
DEV float xsum(float v) { v += __shfl_xor(v, 16); v += __shfl_xor(v, 32); return v; }
DEV float xmax(float v) { v = fmaxf(v, __shfl_xor(v, 16)); v = fmaxf(v, __shfl_xor(v, 32)); return v; }
DEV float dpp_xor1(float v) { return __builtin_bit_cast(float, __builtin_amdgcn_mov_dpp(__builtin_bit_cast(int, v), 0xB1, 0xF, 0xF, true)); }
DEV float dpp_xor2(float v) { return __builtin_bit_cast(float, __builtin_amdgcn_mov_dpp(__builtin_bit_cast(int, v), 0x4E, 0xF, 0xF, true)); }
DEV float dpp_ror4(float v) { return __builtin_bit_cast(float, __builtin_amdgcn_mov_dpp(__builtin_bit_cast(int, v), 0x124, 0xF, 0xF, true)); }
DEV float dpp_ror8(float v) { return __builtin_bit_cast(float, __builtin_amdgcn_mov_dpp(__builtin_bit_cast(int, v), 0x128, 0xF, 0xF, true)); }
DEV unsigned uni(unsigned v) { return __builtin_amdgcn_readfirstlane(v); }
#define WT 31
template <bool W> DEV void st16(char* p, u32x4 v) {
  if constexpr (W) asm volatile("global_store_dwordx4 %0, %1, off sc0 sc1\n\ts_nop 1" ::"v"(p), "v"(v) : "memory");
  else *(u32x4*)p = v;
}
template <bool W> DEV void st8(char* p, u32x2 v) {
  if constexpr (W) asm volatile("global_store_dwordx2 %0, %1, off sc0 sc1" ::"v"(p), "v"(v) : "memory");
  else *(u32x2*)p = v;
}

template <int A, int B> struct Min { static constexpr int v = A < B ? A : B; };

template <int KT, int NK, int NS, int P, class Issue, class Compute>
DEV void pipe_steps(Issue& issue, Compute& compute) {
  if constexpr (KT < NK) {
    wait_bar<(Min<KT + NS - 1, NK>::v - KT - 1) * P>();
    if constexpr (KT + NS - 1 < NK) issue((KT + NS - 1) % NS);
    compute(KT % NS, KT);
    pipe_steps<KT + 1, NK, NS, P>(issue, compute);
  }
}

constexpr int K1_STAGE = 16384 + 12288, K1_NS = 5;
#define SB __builtin_amdgcn_sched_barrier(0)
DEV float xsum2(float v) {
  auto a = __builtin_amdgcn_permlane16_swap(__float_as_uint(v), __float_as_uint(v), false, false);
  v = __uint_as_float(a[0]) + __uint_as_float(a[1]);
  auto b = __builtin_amdgcn_permlane32_swap(__float_as_uint(v), __float_as_uint(v), false, false);
  return __uint_as_float(b[0]) + __uint_as_float(b[1]);
}

DEV void qkv_phase(char* smem, unsigned lds0, const char* __restrict__ x, const char* __restrict__ w, char* __restrict__ qkv,
                   int cb, int tb) {
  const int tid = threadIdx.x, lane = tid & 63, wv = uni(tid >> 6);
  const int g = lane >> 4, i = lane & 15, q4 = (lane >> 2) & 3, p4 = lane & 3;
  const int tg = wv & 3, nh = wv >> 2;
  const int bi = blockIdx.x >> 3;

  const char* gx = x + (size_t)(tb * 128 + wv * 16) * 1024;
  const unsigned vt = (lane >> 3) * 1024 + (((lane & 7) ^ (lane >> 3)) << 4);
  const char* gw = w + cb * 192;
  unsigned vw0, vw1;
  {
    int qq = wv * 64 + lane;
    int k = qq / 12, c = (qq % 12) ^ (((k >> 3) & 1) << 1);
    vw0 = k * 3072 + c * 16;
    qq = 512 + wv * 32 + (lane & 31);
    k = qq / 12; c = (qq % 12) ^ (((k >> 3) & 1) << 1);
    vw1 = k * 3072 + c * 16;
  }
  const int k0 = bi & 7;
  auto dma = [&](int s, int j) {
    const int kk = (k0 + s) & 7;
    const unsigned base = lds0 + (s % K1_NS) * K1_STAGE;
    if (j == 0) dma16s(vt, gx + kk * 128, base + wv * 2048);
    else if (j == 1) dma16s(vt, gx + kk * 128 + 8192, base + wv * 2048 + 1024);
    else if (j == 2) dma16s(vw0, gw + kk * (64 * 3072), base + 16384 + wv * 1024);
    else dma16sm(vw1, gw + kk * (64 * 3072), base + 16384 + 8192 + wv * 512, 0xFFFFFFFFull);
  };
#pragma unroll
  for (int s = 0; s < K1_NS - 1; ++s)
#pragma unroll
    for (int j = 0; j < 4; ++j) dma(s, j);

  f32x4 acc[2][3], cs = f32x4{0, 0, 0, 0}, s1v = f32x4{0, 0, 0, 0};
#pragma unroll
  for (int a = 0; a < 3; ++a) { acc[0][a] = f32x4{0, 0, 0, 0}; acc[1][a] = f32x4{0, 0, 0, 0}; }
  float s2a = 0.f, s2b = 0.f;
  const bf16x8 ones = {0x3F80, 0x3F80, 0x3F80, 0x3F80, 0x3F80, 0x3F80, 0x3F80, 0x3F80};
  int toff[2][2];
#pragma unroll
  for (int tt = 0; tt < 2; ++tt) {
    toff[tt][0] = (tg * 32 + (tt ^ nh) * 16 + i) * 128 + ((g ^ (i & 7)) << 4);
    toff[tt][1] = (tg * 32 + (tt ^ nh) * 16 + i) * 128 + (((4 + g) ^ (i & 7)) << 4);
  }
  int woff[3], un[3];
#pragma unroll
  for (int nt = 0; nt < 3; ++nt) {
    int a = nt + tg; a = a >= 3 ? a - 3 : a; a = a >= 3 ? a - 3 : a;
    un[nt] = nh * 3 + a;
    woff[nt] = 16384 + (8 * g + q4) * 192 + ((un[nt] ^ (g & 1)) << 5) + p4 * 8;
  }
  auto ldB = [&](int tt, int kt, int ks) -> bf16x8 { return lds128(smem + (kt % K1_NS) * K1_STAGE + toff[tt][ks]); };
  auto ldA = [&](int nt, int kt, int ks) -> bf16x8 {
    const char* p = smem + (kt % K1_NS) * K1_STAGE + woff[nt] + ks * 6144;
    return cat(ldstr(p), ldstr(p + 768));
  };
  bf16x8 A0[3], A1[3], B0[2], B1[2];
  auto phase = [&](bf16x8 (&Ac)[3], bf16x8 (&Bc)[2], bf16x8 (&An)[3], bf16x8 (&Bn)[2], int kn, int ksn, int dk, int dj) {
    const u32x4 u = __builtin_bit_cast(u32x4, Bc[0]);
#pragma unroll
    for (int m = 0; m < 8; ++m) {
      if (kn < 8) {
        if (m == 0) { Bn[0] = ldB(0, kn, ksn); An[0] = ldA(0, kn, ksn); }
        else if (m == 1) Bn[1] = ldB(1, kn, ksn);
        else if (m < 4) An[m - 1] = ldA(m - 1, kn, ksn);
      }
      if (m < 6) acc[m & 1][m >> 1] = mfma16(Ac[m >> 1], Bc[m & 1], acc[m & 1][m >> 1]);
      else if (m == 6) cs = mfma16(Ac[0], ones, cs);
      else s1v = mfma16(ones, Bc[0], s1v);
      {
        float f = (m & 1) ? bfhi(u[m >> 1]) : bflo(u[m >> 1]);
        asm volatile("" : "+v"(f));
        if (m & 1) s2b = __builtin_fmaf(f, f, s2b); else s2a = __builtin_fmaf(f, f, s2a);
      }
      if (dk < 8 && (m == 2 || m == 5)) dma(dk, dj + (m == 5));
      SB;
    }
  };
  wait_bar<12>();
  B0[0] = ldB(0, 0, 0); A0[0] = ldA(0, 0, 0); B0[1] = ldB(1, 0, 0); A0[1] = ldA(1, 0, 0); A0[2] = ldA(2, 0, 0);
#pragma unroll
  for (int kt = 0; kt < 8; ++kt) {
    phase(A0, B0, A1, B1, kt, 1, kt + 4, 0);
    if (kt < 4) wait_bar<10>();
    else if (kt == 4) wait_bar<8>();
    else if (kt == 5) wait_bar<4>();
    else if (kt == 6) wait_bar<0>();
    phase(A1, B1, A0, B0, kt + 1, 0, kt + 4, 2);
  }

  float* st = (float*)(smem + 49152);
  float* csp = (float*)(smem + 49152 + 1024);
  const float s2 = xsum2(s2a + s2b);
  if (g == 0) {
    float mean = s1v[0] * (1.f / 512.f);
    float var = fmaxf(s2 * (1.f / 512.f) - mean * mean, 0.f);
    float rstd = rsqrtf(var + LN_EPS);
    *(f32x2*)(st + (tg * 32 + nh * 16 + i) * 2) = f32x2{-mean * rstd, rstd};
  }
  if (i == 0 && tg < 3) *(f32x4*)(csp + un[0] * 16 + 4 * g) = cs;
  bar();
  f32x2 ms[2];
#pragma unroll
  for (int tt = 0; tt < 2; ++tt) ms[tt] = *(const f32x2*)(st + (tg * 32 + (tt ^ nh) * 16 + i) * 2);
  const int otile = (g & 1) ^ nh, ohalf = g >> 1;
  const unsigned vo = ohalf * 512 + (((otile * 16 + i) ^ (8 * ohalf)) << 4);
#pragma unroll
  for (int nt = 0; nt < 3; ++nt) {
    f32x4 c = *(const f32x4*)(csp + un[nt] * 16 + 4 * g);
    unsigned o[2][2];
#pragma unroll
    for (int tt = 0; tt < 2; ++tt) {
      f32x4 y = acc[tt][nt] * ms[tt][1] + ms[tt][0] * c;
      o[tt][0] = pack2(y[0], y[1]); o[tt][1] = pack2(y[2], y[3]);
    }
    auto sx = __builtin_amdgcn_permlane16_swap(o[0][0], o[1][0], false, false);
    auto sy = __builtin_amdgcn_permlane16_swap(o[0][1], o[1][1], false, false);
    char* dst = qkv + (size_t)(cb * 6 + un[nt]) * (2048 * 32) + (size_t)(tb * 4 + tg) * 1024;
    st16<true>(dst + vo, u32x4{sx[0], sy[0], sx[1], sy[1]});
  }
}

constexpr int S0 = 0, S1 = 49152, S2 = 98304, CSP = 147456, MG_LDS = 163840;
typedef unsigned long long u64;
extern "C" __device__ u64 llvm_dispatch_id() __asm("llvm.amdgcn.dispatch.id");
DEV void flag_set(u64* f, u64 tag) {
  asm volatile("global_store_dwordx2 %0, %1, off sc0 sc1" ::"v"(f), "v"(tag) : "memory");
}
DEV void flag_wait(const u64* p, u64 tag) {
  for (int it = 0; it < (1 << 20); ++it) {
    u64 c;
    asm volatile("global_load_dwordx2 %0, %1, off sc0 sc1\n\ts_waitcnt vmcnt(0)" : "=v"(c) : "v"(p) : "memory");
    if (__builtin_amdgcn_ballot_w64(c != tag) == 0) break;
  }
}
template <int V> struct IC { static constexpr int value = V; };
DEV int opaque(int v) { asm volatile("" : "+v"(v)); return v; }
DEV int opaque_s(int v) { asm volatile("" : "+s"(v)); return v; }

struct CW {
  const char *g1, *g3; unsigned vw; unsigned long long wmask;
};
DEV CW c_setup(const char* w1, const char* w3, int b, int wv, int lane) {
  const int xcd = b & 7, bi = b >> 3;
  const int cb = (xcd & 3) * 4 + (bi & 3);
  CW c;
  c.g1 = w1 + (size_t)(wv * 8) * (F_ * 2) + cb * 224;
  c.g3 = w3 + (size_t)(wv * 8) * (F_ * 2) + cb * 224;
  const int unit = ((lane >> 1) & 7) ^ ((lane >> 4) | ((wv & 1) << 2));
  c.vw = (lane >> 4) * (F_ * 2) + unit * 32 + (lane & 1) * 16;
  c.wmask = __builtin_amdgcn_ballot_w64(unit < 7);
  return c;
}
DEV void c_dmaw(const CW& c, unsigned ldsw, int kt, int j) {
  const unsigned base = ldsw + (kt % 3) * 49152;
  if (j < 2) dma16sm(c.vw, c.g1 + (size_t)(kt * 64 + j * 4) * (F_ * 2), base + 16384 + j * 1024, c.wmask);
  else dma16sm(c.vw, c.g3 + (size_t)(kt * 64 + (j - 2) * 4) * (F_ * 2), base + 32768 + (j - 2) * 1024, c.wmask);
}
DEV constexpr int d_unit(int u) {
  return u < 3 ? S2 + u * 2048 : u < 6 ? S0 + (u - 3) * 2048 : u < 9 ? S1 + (u - 6) * 2048 : u == 9 ? CSP
       : u < 13 ? S2 + (u - 10) * 2048 : S0;
}
DEV constexpr int d_ustr(int u) { return u == 9 ? 2048 : 6144; }
DEV void d_dma(const char* gw2, unsigned vw, unsigned lds0, int wv, int u, int q) {
  dma16s(vw, gw2 + (size_t)(u * 16 + q * 8) * 1024, lds0 + d_unit(u) + wv * d_ustr(u) + q * 1024);
}

template <int NT>
DEV void c_body(char* smem, unsigned lds0, const char* gt, const CW& cw, unsigned vt, int wv, int lane,
                const char* __restrict__ stats, const char* __restrict__ csb, char* __restrict__ hh, int tok0, int cb, const char* gw2, unsigned vwd) {
  const int tg = wv & 3;
  const unsigned ldsw = lds0 + wv * 2048;
  const int g = lane >> 4, i = lane & 15, q4 = (lane >> 2) & 3, p4 = lane & 3;
  f32x4 acc[2][2 * NT];
#pragma unroll
  for (int a = 0; a < 2 * NT; ++a) { acc[0][a] = f32x4{0, 0, 0, 0}; acc[1][a] = f32x4{0, 0, 0, 0}; }
  const int toff0 = (tg * 32 + i) * 128 + ((g ^ (i & 7)) << 4);
  const int toff1 = (tg * 32 + i) * 128 + (((4 + g) ^ (i & 7)) << 4);
  const int odd = cb & 1;
  const int fsw = q4 | ((g & 1) << 2);
  int T[NT], wa[NT];
#pragma unroll
  for (int a = 0; a < NT; ++a) {
    T[a] = NT == 4 ? odd * 3 + a : (a < 2 ? (odd ? 1 : 4) + a : (odd ? 0 : 6));
    wa[a] = 16384 + (8 * g + q4) * 256 + ((T[a] ^ fsw) << 5) + p4 * 8;
  }
  auto dmat = [&](int kt, int j) { dma16s(vt, gt + kt * 128 + j * 8192, ldsw + (kt % 3) * 49152 + j * 1024); };
  auto dma = [&](int kt, int j) {
    if (j < 2) dmat(kt, j); else c_dmaw(cw, ldsw, kt, j - 2);
  };
  auto ldB = [&](bf16x8* B, int kt, int ks) {
    const char* sb = smem + (kt % 3) * 49152 + (ks ? toff1 : toff0);
    B[0] = lds128(sb); B[1] = lds128(sb + 2048);
  };
  auto ldA = [&](int a, int kt, int ks) -> bf16x8 {
    const char* wp = smem + (kt % 3) * 49152 + wa[a < NT ? a : a - NT] + (a < NT ? 0 : 16384) + ks * 8192;
    return cat(ldstr(wp), ldstr(wp + 1024));
  };
  auto mma = [&](int a, bf16x8 A, const bf16x8* B) {
    acc[0][a] = mfma16(A, B[0], acc[0][a]);
    acc[1][a] = mfma16(A, B[1], acc[1][a]);
  };
  bf16x8 A0[2 * NT], A1[2 * NT], B0[2], B1[2];
  bf16x8 sa[2], cp[4];
#pragma unroll
  for (int kt = 0; kt < 3; ++kt) { dmat(kt, 0); dmat(kt, 1); }
  gld16(sa[0], (unsigned)(i * 64 + g * 16), stats + (size_t)(tok0 + tg * 32) * 64);
  gld16(sa[1], (unsigned)(i * 64 + g * 16), stats + (size_t)(tok0 + tg * 32 + 16) * 64);
  const int cq = min(tg * 16 + (lane >> 2), 55);
  if (NT == 3) {
#pragma unroll
    for (int m = 0; m < 4; ++m) gld16(cp[m], (unsigned)(((lane & 3) * 4 + m) * 896 + cq * 16), csb + (size_t)cb * 14336);
  }
  constexpr int X = NT == 3 ? 6 : 2;
  wait_bar<4 + X>();
  float rs[2], mr[2];
  float* csp = (float*)(smem + CSP);
  ldB(B0, 0, 0);
#pragma unroll
  for (int a = 0; a < 2 * NT; ++a) A0[a] = ldA(a, 0, 0);
#pragma unroll
  for (int kt = 0; kt < 8; ++kt) {
    ldB(B1, kt, 1);
#pragma unroll
    for (int a = 0; a < 2 * NT; ++a) {
      if (a < NT) { A1[2 * a] = ldA(2 * a, kt, 1); A1[2 * a + 1] = ldA(2 * a + 1, kt, 1); }
      mma(a, A0[a], B0);
      __builtin_amdgcn_sched_barrier(0);
    }
    if (kt == 0) wait_bar<2 + X>();
    else if (kt != 1) wait_bar<6>();
    else if (NT == 3) asm volatile("s_waitcnt vmcnt(6) lgkmcnt(0)\n\ts_barrier" : "+v"(sa[0]), "+v"(sa[1]), "+v"(cp[0]), "+v"(cp[1]), "+v"(cp[2]), "+v"(cp[3]) : : "memory");
    else asm volatile("s_waitcnt vmcnt(6) lgkmcnt(0)\n\ts_barrier" : "+v"(sa[0]), "+v"(sa[1]) : : "memory");
    if (kt < 7) ldB(B0, kt + 1, 0);
    if (kt == 1) {
#pragma unroll
      for (int tt = 0; tt < 2; ++tt) {
        const f32x4 sv = __builtin_bit_cast(f32x4, sa[tt]);
        const float s1 = xsum2(sv[0] + sv[2]), s2 = xsum2(sv[1] + sv[3]);
        const float mean = s1 * (1.f / 512.f);
        const float var = fmaxf(s2 * (1.f / 512.f) - mean * mean, 0.f);
        rs[tt] = rsqrtf(var + LN_EPS); mr[tt] = mean * rs[tt];
      }
      if (NT == 3) {
        f32x4 c = (__builtin_bit_cast(f32x4, cp[0]) + __builtin_bit_cast(f32x4, cp[1])) +
                  (__builtin_bit_cast(f32x4, cp[2]) + __builtin_bit_cast(f32x4, cp[3]));
#pragma unroll
        for (int e = 0; e < 4; ++e) { c[e] += dpp_xor1(c[e]); c[e] += dpp_xor2(c[e]); }
        if ((lane & 3) == 0) *(f32x4*)(csp + cq * 4) = c;
      }
    }
#pragma unroll
    for (int a = 0; a < 2 * NT; ++a) {
      if (kt < 7 && a < NT) { A0[2 * a] = ldA(2 * a, kt + 1, 0); A0[2 * a + 1] = ldA(2 * a + 1, kt + 1, 0); }
      mma(a, A1[a], B1);
      if (a >= 2 * NT - 6) {
        const int j = a - (2 * NT - 6);
        if (kt + 3 < 8) dma(kt + 3, j);
        else d_dma(gw2, vwd, lds0, wv, (kt - 5) * 3 + (j >> 1), j & 1);
      }
      __builtin_amdgcn_sched_barrier(0);
    }
  }
  unsigned pk[2][NT][2];
#pragma unroll
  for (int tt = 0; tt < 2; ++tt) {
    char* dst = hh + ((size_t)((tok0 >> 4) + tg * 2 + tt) * 56) * 1024 + lane * 16;
#pragma unroll
    for (int a = 0; a < NT; ++a) {
      f32x4 cg = *(const f32x4*)(csp + T[a] * 16 + 4 * g);
      f32x4 cu = *(const f32x4*)(csp + (7 + T[a]) * 16 + 4 * g);
      float r[4];
#pragma unroll
      for (int e = 0; e < 4; ++e) {
        float ga = acc[tt][a][e] * rs[tt] - mr[tt] * cg[e];
        float up = acc[tt][NT + a][e] * rs[tt] - mr[tt] * cu[e];
        float sg = __builtin_amdgcn_rcpf(1.f + __builtin_amdgcn_exp2f(-1.4426950408889634f * ga));
        r[e] = ga * sg * up;
      }
      pk[tt][a][0] = pack2(r[0], r[1]); pk[tt][a][1] = pack2(r[2], r[3]);
      const int G = cb * 7 + T[a];
      if (a & 1) {
        st16<true>(dst + (size_t)(G >> 1) * 1024, u32x4{pk[tt][a - 1][0], pk[tt][a - 1][1], pk[tt][a][0], pk[tt][a][1]});
      } else if (a == NT - 1) {
        st8<true>(dst + (size_t)(G >> 1) * 1024 + (G & 1) * 8, u32x2{pk[tt][a][0], pk[tt][a][1]});
      }
    }
  }
}

__global__ __launch_bounds__(512) void k_mega(const char* __restrict__ x, const char* __restrict__ wqkv, const char* __restrict__ wo,
                                              const char* __restrict__ w1, const char* __restrict__ w3,
                                              const char* __restrict__ w2, char* __restrict__ ws, char* __restrict__ out) {
  extern __shared__ __attribute__((aligned(16))) char smem[];
  char* qkv = ws;
  char* o = ws + (6u << 20);
  char* h = ws + (8u << 20);
  char* stats = ws + (10u << 20);
  u64* flags = (u64*)(ws + (10u << 20) + (512u << 10));
  char* csb = ws + (10u << 20) + (768u << 10);
  char* hh = ws + (11u << 20);
  const u64 tag = ((u64)(size_t)__builtin_amdgcn_dispatch_ptr() << 20) + llvm_dispatch_id();
  const int b = blockIdx.x;
  const unsigned lds0 = (unsigned)(size_t)(LAS char*)smem;
  const int cbG = (b >> 3) & 7, tbG = (b & 7) * 4 + (b >> 6);
  const int cbC = (b & 3) * 4 + ((b >> 3) & 3), tbC = ((b >> 2) & 1) * 8 + (b >> 5);

  unsigned cpf = 0;
  {
    u64 pc;
    asm volatile("s_getpc_b64 %0" : "=s"(pc));
    const unsigned off = ((b >> 3) * 20 + threadIdx.x) * 64;
    if (threadIdx.x < 20 && off < 38 * 1024) asm volatile("global_load_dword %0, %1, %2" : "=v"(cpf) : "v"(off), "s"(pc) : "memory");
  }
  {
    const int cb = (b & 1) * 8 + ((b >> 3) & 7), tb = ((b & 7) >> 1) * 4 + (b >> 6);
    qkv_phase(smem, lds0, x, wqkv, qkv, cb, tb);
    asm volatile("s_waitcnt vmcnt(0) lgkmcnt(0)\n\ts_barrier" ::: "memory");
    asm volatile("" :: "v"(cpf));
    const int tid = threadIdx.x;
    if (tid < 9) {
      const int P = cb * 3 + tid / 3, d = tid % 3, which = P >> 4, hp = P & 15, qb = tb + d;
      if (which ? qb < 16 : d == 0)
        flag_set(flags + 24576 + ((hp >> 1) | ((hp & 1) << 3) | (qb << 4)) * 32 + (which ? (which - 1) * 3 + d : 6), tag);
    }
  }
  {
    const int tid = opaque(threadIdx.x), lane = tid & 63, wv = uni(tid >> 6);
    const int c32 = lane & 31, hf = lane >> 5, q4 = (lane >> 2) & 3, p4 = lane & 3;
    const int hp = (b & 7) * 2 + ((b >> 3) & 1), qb = b >> 4;
    const int q0 = qb * 128;
    const size_t HS = 2048 * 32;
    bf16x8 Q, cw0, cw1;
    if (wv == 0) {
      const int d = lane % 3;
      flag_wait(flags + 24576 + b * 32 + (lane >= 6 ? 6 : qb - d < 0 ? 6 : lane), tag);
    }
    asm volatile("s_barrier" ::: "memory");
    {
      const int r = wv >> 1;
      const char* src = qkv + (size_t)((1 + (r >> 1)) * 32 + hp * 2 + (r & 1)) * HS;
#pragma unroll
      for (int j = 0; j < 6; ++j) {
        const int kb = qb * 4 - 8 + (wv & 1) * 6 + j;
        dma16s(kb < 0 ? hf * 640 : lane * 16, src + (size_t)max(kb, 0) * 1024, lds0 + r * 12288 + ((wv & 1) * 6 + j) * 1024);
      }
    }
    const int u = wv >> 2, qg = wv & 3;
    const int qs = q0 + qg * 32;
    const unsigned vq = hf * 512 + ((c32 ^ (8 * hf)) << 4);
    gld16(Q, vq, qkv + (size_t)(hp * 2 + u) * HS + (size_t)qs * 32);
    const float c = 0.25f * 1.4426950408889634f;
    const float NEG = -1e30f;
    f32x16 cin0, cin8;
#pragma unroll
    for (int r = 0; r < 16; ++r) {
      const int kl = (r & 3) + 8 * (r >> 2) + 4 * hf;
      cin0[r] = kl > c32 ? 0.f : NEG; cin8[r] = kl <= c32 ? 0.f : NEG;
    }
    asm volatile("s_waitcnt vmcnt(0)\n\ts_barrier" : "+v"(Q), "+v"(cin0), "+v"(cin8) : : "memory");
    const unsigned vwo = (lane >> 3) * 1024 + ((((lane & 7) >> 1) ^ ((lane >> 4) & 3)) << 5) + (lane & 1) * 16;
    const char* gwo = wo + (size_t)(wv * 64) * 1024 + cbG * 128;
    const CW cw1s = c_setup(w1, w3, b, wv, lane);
    auto pf = [&](int k) {
      if (k < 2) {
        const int ch = max(wv - 1, 0) * 4 + (lane & 3);
        const char* pc = (ch >= 14 ? w3 + (ch - 14) * 16 : w1 + ch * 16) + (size_t)(tbC * 32 + (lane >> 2)) * (F_ * 2) + cbC * 224;
        if (k == 0) asm volatile("global_load_dwordx4 %0, %1, off" : "=v"(cw0) : "v"(pc) : "memory");
        else asm volatile("global_load_dwordx4 %0, %1, off" : "=v"(cw1) : "v"(pc + 16 * (F_ * 2)) : "memory");
      } else if (k < 10) dma16s(vwo, gwo + (size_t)((k - 2) * 8) * 1024, lds0 + S2 + wv * 8192 + (k - 2) * 1024);
      else if (k < 14) c_dmaw(cw1s, lds0 + wv * 2048, 1, k - 10);
      else if (k == 14) {
        const int c0 = b * 8 + wv;
        dma16s(lane * 16, w2 + (size_t)(c0 < 1792 ? c0 : c0 - 1792) * 1024, lds0 + S1 + wv * 1024);
      }
    };
    {
      const char* Kl = smem + u * 12288 + qg * 1024 + vq;
      const char* Vl[2];
#pragma unroll
      for (int e = 0; e < 2; ++e)
        Vl[e] = smem + 24576 + u * 12288 + qg * 1024 + (p4 >> 1) * 512 + (((e * 8 + 4 * hf + q4) ^ (8 * (p4 >> 1))) << 4) + (p4 & 1) * 8;
      f32x16 s[9];
#pragma unroll
      for (int t = 0; t < 9; ++t) {
        f32x16 z;
#pragma unroll
        for (int r = 0; r < 16; ++r) z[r] = 0.f;
        s[t] = mfma32(lds128(Kl + t * 1024), Q, t == 0 ? cin0 : t == 8 ? cin8 : z);
      }
      float m = s[8][0];
#pragma unroll
      for (int t = 0; t < 9; ++t)
#pragma unroll
        for (int r = 0; r < 16; ++r) m = fmaxf(m, s[t][r]);
      {
        auto sw = __builtin_amdgcn_permlane32_swap(__float_as_uint(m), __float_as_uint(m), false, false);
        m = fmaxf(__uint_as_float(sw[0]), __uint_as_float(sw[1]));
      }
      const f32x2 c2 = {c, c}, m2 = {-m * c, -m * c};
      const int nneg = qs >= 256 ? 0 : (256 - qs) >> 5;
      f32x16 oacc;
#pragma unroll
      for (int r = 0; r < 16; ++r) oacc[r] = 0.f;
      float ls0 = 0.f, ls1 = 0.f;
#pragma unroll
      for (int t = 0; t < 9; ++t) {
        const float mt = t < nneg ? NEG : m2[0];
        const f32x2 mt2 = {mt, mt};
#pragma unroll
        for (int sh = 0; sh < 2; ++sh) {
          u32x4 pb;
#pragma unroll
          for (int e = 0; e < 4; ++e) {
            f32x2 a = __builtin_elementwise_fma(f32x2{s[t][8 * sh + 2 * e], s[t][8 * sh + 2 * e + 1]}, c2, mt2);
            pb[e] = pack2(__builtin_amdgcn_exp2f(a[0]), __builtin_amdgcn_exp2f(a[1]));
          }
          ls0 = dot2(pb[0], 0x3F803F80u, ls0); ls1 = dot2(pb[1], 0x3F803F80u, ls1);
          ls0 = dot2(pb[2], 0x3F803F80u, ls0); ls1 = dot2(pb[3], 0x3F803F80u, ls1);
          const int vp = t * 1024 + sh * 256;
          oacc = mfma32(cat(ldstr(Vl[0] + vp), ldstr(Vl[1] + vp)), __builtin_bit_cast(bf16x8, pb), oacc);
          if (2 * t + sh < 15) pf(2 * t + sh);
        }
      }
      float ls = ls0 + ls1;
      {
        auto sw = __builtin_amdgcn_permlane32_swap(__float_as_uint(ls), __float_as_uint(ls), false, false);
        ls = __uint_as_float(sw[0]) + __uint_as_float(sw[1]);
      }
      const float inv = __builtin_amdgcn_rcpf(ls);
      const u32x4 ov = {pack2(oacc[0] * inv, oacc[1] * inv), pack2(oacc[2] * inv, oacc[3] * inv),
                        pack2(oacc[4] * inv, oacc[5] * inv), pack2(oacc[6] * inv, oacc[7] * inv)};
      st16<true>(o + ((size_t)((qs >> 4) + (c32 >> 4)) * 16 + hp) * 1024 + ((2 * u + hf) * 16 + (c32 & 15)) * 16, ov);
    }
    asm volatile("s_waitcnt vmcnt(0) lgkmcnt(0)\n\ts_barrier" : "+v"(cw0), "+v"(cw1) : : "memory");
    if (tid < 16) flag_set(flags + (((qb >> 1) | (tid << 3) | ((qb & 1) << 7)) * 32 + hp), tag);
    {
      const CW cw = c_setup(w1, w3, b, wv, lane);
#pragma unroll
      for (int j = 0; j < 4; ++j) c_dmaw(cw, lds0 + wv * 2048, 0, j);
    }
    if (wv == 0) { flag_wait(flags + b * 32 + (lane & 15), tag); }
    else {
      const u32x4 ua = __builtin_bit_cast(u32x4, cw0), ub = __builtin_bit_cast(u32x4, cw1);
      f32x4 lo, hi;
#pragma unroll
      for (int e = 0; e < 2; ++e) {
        lo[2 * e] = bflo(ua[e]) + bflo(ub[e]); lo[2 * e + 1] = bfhi(ua[e]) + bfhi(ub[e]);
        hi[2 * e] = bflo(ua[2 + e]) + bflo(ub[2 + e]); hi[2 * e + 1] = bfhi(ua[2 + e]) + bfhi(ub[2 + e]);
      }
#pragma unroll
      for (int e = 0; e < 4; ++e) {
        lo[e] += dpp_ror4(lo[e]); lo[e] += dpp_ror8(lo[e]); lo[e] = xsum2(lo[e]);
        hi[e] += dpp_ror4(hi[e]); hi[e] += dpp_ror8(hi[e]); hi[e] = xsum2(hi[e]);
      }
      if (lane < 4) {
        char* d = csb + (size_t)(cbC * 16 + tbC) * 896 + ((wv - 1) * 4 + lane) * 32;
        st16<true>(d, __builtin_bit_cast(u32x4, lo));
        st16<true>(d + 16, __builtin_bit_cast(u32x4, hi));
      }
      asm volatile("s_waitcnt vmcnt(0)" ::: "memory");
    }
    asm volatile("s_barrier" ::: "memory");
    if (tid >= 64 && tid < 80) flag_set(flags + 8192 + ((cbC >> 2) | ((tid & 1) << 2) | ((cbC & 3) << 3) | (((tid & 15) >> 1) << 5)) * 32 + 16 + tbC, tag);
  }
  {
    const int tid = opaque(threadIdx.x), lane = tid & 63, wv = uni(tid >> 6);
    const int g = lane >> 4, i = lane & 15, q4 = (lane >> 2) & 3, p4 = lane & 3;
    const int kp = wv & 3, th = wv >> 2;
    bf16x8 B[4][2], rs;
    const char* ga = o + ((size_t)(tbG * 4 + th * 2) * 16 + kp * 4) * 1024;
#pragma unroll
    for (int s = 0; s < 4; ++s) {
      gld16(B[s][0], lane * 16, ga + s * 1024);
      gld16(B[s][1], lane * 16, ga + (16 + s) * 1024);
    }
    const size_t ooff = (size_t)(tbG * 64 + wv * 8) * 1024 + cbG * 128;
    const unsigned vo = (lane >> 3) * 1024 + (lane & 7) * 16;
    gld16(rs, vo, x + ooff);
    const int sw = (2 * (g & 1) + (q4 >> 1)) & 3;
    const char* wr = smem + S2 + kp * 16384 + (16 * (g >> 1) + 4 * (g & 1) + q4) * 128 + p4 * 8;
    bf16x8 A[4][4];
#pragma unroll
    for (int s = 0; s < 4; ++s)
#pragma unroll
      for (int nt = 0; nt < 4; ++nt) {
        const char* p = wr + s * 4096 + ((nt ^ sw) << 5);
        A[s][nt] = cat(ldstr(p), ldstr(p + 1024));
      }
    f32x4 acc[2][4];
#pragma unroll
    for (int t = 0; t < 2; ++t)
#pragma unroll
      for (int n = 0; n < 4; ++n) acc[t][n] = f32x4{0, 0, 0, 0};
    asm volatile("s_waitcnt vmcnt(7)" : "+v"(B[0][0]), "+v"(B[0][1]) : : "memory");
#pragma unroll
    for (int nt = 0; nt < 4; ++nt) { acc[0][nt] = mfma16(A[0][nt], B[0][0], acc[0][nt]); acc[1][nt] = mfma16(A[0][nt], B[0][1], acc[1][nt]); }
    asm volatile("s_waitcnt vmcnt(5)" : "+v"(B[1][0]), "+v"(B[1][1]) : : "memory");
#pragma unroll
    for (int nt = 0; nt < 4; ++nt) { acc[0][nt] = mfma16(A[1][nt], B[1][0], acc[0][nt]); acc[1][nt] = mfma16(A[1][nt], B[1][1], acc[1][nt]); }
    asm volatile("s_waitcnt vmcnt(3)" : "+v"(B[2][0]), "+v"(B[2][1]) : : "memory");
#pragma unroll
    for (int nt = 0; nt < 4; ++nt) { acc[0][nt] = mfma16(A[2][nt], B[2][0], acc[0][nt]); acc[1][nt] = mfma16(A[2][nt], B[2][1], acc[1][nt]); }
    asm volatile("s_waitcnt vmcnt(1)" : "+v"(B[3][0]), "+v"(B[3][1]) : : "memory");
#pragma unroll
    for (int nt = 0; nt < 4; ++nt) { acc[0][nt] = mfma16(A[3][nt], B[3][0], acc[0][nt]); acc[1][nt] = mfma16(A[3][nt], B[3][1], acc[1][nt]); }
    bar();
    {
      const int mo = S2 + kp * 16384 + g * 256 + ((i ^ ((g >> 1) * 2)) << 4);
#pragma unroll
      for (int tt = 0; tt < 2; ++tt)
#pragma unroll
        for (int nt = 0; nt < 4; ++nt)
          *(LAS f32x4*)(LAS char*)(smem + (mo ^ (nt * 64)) + ((th * 2 + tt) * 4 + nt) * 1024) = acc[tt][nt];
    }
    asm volatile("s_waitcnt vmcnt(0) lgkmcnt(0)\n\ts_barrier" : "+v"(rs) : : "memory");
    {
      const int c = lane & 7;
      const int tok = (lane >> 3) + 8 * (wv & 1);
      const char* rp = smem + S2 + ((wv >> 1) * 4 + (c >> 1)) * 1024 + (c & 1) * 512 + ((tok ^ (2 * c)) << 4);
      f32x4 v0 = f32x4{0, 0, 0, 0}, v1 = f32x4{0, 0, 0, 0};
#pragma unroll
      for (int p = 0; p < 4; ++p) {
        v0 += *(const LAS f32x4*)(LAS const char*)(rp + p * 16384);
        v1 += *(const LAS f32x4*)(LAS const char*)(rp + p * 16384 + 256);
      }
      u32x4 r = __builtin_bit_cast(u32x4, rs);
      u32x4 ov = {pack2(v0[0] + bflo(r[0]), v0[1] + bfhi(r[0])), pack2(v0[2] + bflo(r[1]), v0[3] + bfhi(r[1])),
                  pack2(v1[0] + bflo(r[2]), v1[1] + bfhi(r[2])), pack2(v1[2] + bflo(r[3]), v1[3] + bfhi(r[3]))};
      st16<true>(h + ooff + vo, ov);
      float s1 = 0.f, s2 = 0.f;
#pragma unroll
      for (int e = 0; e < 4; ++e) { s1 = dot2(ov[e], 0x3F803F80u, s1); s2 = dot2(ov[e], ov[e], s2); }
#pragma unroll
      for (int m = 1; m < 8; m <<= 1) { s1 += __shfl_xor(s1, m); s2 += __shfl_xor(s2, m); }
      if (c == 0) st8<true>(stats + ((size_t)(tbG * 64 + wv * 8 + (lane >> 3)) * 8 + cbG) * 8,
                            u32x2{__float_as_uint(s1), __float_as_uint(s2)});
    }
    asm volatile("s_waitcnt vmcnt(0) lgkmcnt(0)\n\ts_barrier" ::: "memory");
    if (tid < 16) flag_set(flags + 8192 + ((tid & 3) | ((tbG >> 4) << 2) | ((tid >> 2) << 3) | (((tbG >> 1) & 7) << 5)) * 32 + (tbG & 1) * 8 + cbG, tag);
    {
      const CW cw = c_setup(w1, w3, b, wv, lane);
#pragma unroll
      for (int j = 0; j < 4; ++j) c_dmaw(cw, lds0 + wv * 2048, 2, j);
    }
    if (wv == 0) flag_wait(flags + 8192 + b * 32 + (lane & 31), tag);
    asm volatile("s_barrier" ::: "memory");
  }
  const unsigned vwd_ = 0;
  {
    const int tid = opaque(threadIdx.x), lane = tid & 63, wv = uni(tid >> 6);
    const char* gt = h + (size_t)(tbC * 128 + wv * 16) * 1024;
    const unsigned vt = (lane >> 3) * 1024 + (((lane & 7) ^ (lane >> 3)) << 4);
    const CW cw = c_setup(w1, w3, b, wv, lane);
    const unsigned vwd = (lane >> 3) * 1024 + ((((lane & 7) >> 1) ^ ((lane >> 4) & 3)) << 5) + (lane & 1) * 16;
    const char* gw2 = w2 + (size_t)(wv * 224) * 1024 + cbG * 128;
    if ((wv >> 2) == 0) c_body<4>(smem, lds0, gt, cw, vt, wv, lane, stats, csb, hh, tbC * 128, cbC, gw2, vwd);
    else c_body<3>(smem, lds0, gt, cw, vt, wv, lane, stats, csb, hh, tbC * 128, cbC, gw2, vwd);
  }
  {
    const int tid = opaque(threadIdx.x), lane = tid & 63, wv = uni(tid >> 6);
    const int g = lane >> 4, i = lane & 15, q4 = (lane >> 2) & 3, p4 = lane & 3;
    asm volatile("s_waitcnt vmcnt(0) lgkmcnt(0)\n\ts_barrier" ::: "memory");
    if (tid < 16) flag_set(flags + 16384 + ((tbC >> 1) | (tid << 3) | ((tbC & 1) << 7)) * 32 + cbC, tag);
    const unsigned vwd = (lane >> 3) * 1024 + ((((lane & 7) >> 1) ^ ((lane >> 4) & 3)) << 5) + (lane & 1) * 16;
    const char* gw2 = w2 + (size_t)(wv * 224) * 1024 + cbG * 128;
    d_dma(gw2, vwd, lds0, wv, 9, 0); d_dma(gw2, vwd, lds0, wv, 9, 1);
    const int sw = (2 * g + (q4 >> 1)) & 3;
    const int ro = (4 * g + q4) * 128 + p4 * 8;
    int rn[4];
#pragma unroll
    for (int nt = 0; nt < 4; ++nt) rn[nt] = ro + ((nt ^ sw) << 5);
    auto lda = [&](bf16x8* Af, int j) {
      const char* p0 = smem + d_unit(2 * j) + wv * d_ustr(2 * j);
      const char* p1 = smem + d_unit(2 * j + 1) + wv * d_ustr(2 * j + 1);
#pragma unroll
      for (int nt = 0; nt < 4; ++nt) Af[nt] = cat(ldstr(p0 + rn[nt]), ldstr(p1 + rn[nt]));
    };
    bf16x8 A[2][4], B[7][4], rs;
    lda(A[0], 0); lda(A[1], 1);
    asm volatile("s_waitcnt lgkmcnt(0)" : "+v"(A[0][0]), "+v"(A[0][1]), "+v"(A[0][2]), "+v"(A[0][3]), "+v"(A[1][0]), "+v"(A[1][1]), "+v"(A[1][2]), "+v"(A[1][3]) : : "memory");
#pragma unroll
    for (int u = 10; u < 14; ++u) { d_dma(gw2, vwd, lds0, wv, u, 0); d_dma(gw2, vwd, lds0, wv, u, 1); }
    if (wv == 0) flag_wait(flags + 16384 + b * 32 + (lane & 15), tag);
    asm volatile("s_barrier" ::: "memory");
    const char* ga = hh + ((size_t)(tbG * 4) * 56 + wv * 7) * 1024;
#pragma unroll
    for (int j = 0; j < 7; ++j)
#pragma unroll
      for (int tt = 0; tt < 4; ++tt) gld16(B[j][tt], lane * 16, ga + (size_t)(tt * 56 + j) * 1024);
    const size_t ooff = (size_t)(tbG * 64 + wv * 8) * 1024 + cbG * 128;
    const unsigned vo = (lane >> 3) * 1024 + (lane & 7) * 16;
    gld16(rs, vo, h + ooff);
    f32x4 acc[4][4];
#pragma unroll
    for (int t = 0; t < 4; ++t)
#pragma unroll
      for (int n = 0; n < 4; ++n) acc[t][n] = f32x4{0, 0, 0, 0};
    auto step = [&](auto J) {
      constexpr int j = decltype(J)::value;
      asm volatile("s_waitcnt vmcnt(%4)" : "+v"(B[j][0]), "+v"(B[j][1]), "+v"(B[j][2]), "+v"(B[j][3]) : "n"(25 - 4 * j) : "memory");
#pragma unroll
      for (int nt = 0; nt < 4; ++nt)
#pragma unroll
        for (int tt = 0; tt < 4; ++tt) acc[tt][nt] = mfma16(A[j & 1][nt], B[j][tt], acc[tt][nt]);
      __builtin_amdgcn_sched_barrier(0);
      if constexpr (j + 2 < 7) lda(A[j & 1], j + 2);
    };
    step(IC<0>{}); step(IC<1>{}); step(IC<2>{}); step(IC<3>{}); step(IC<4>{}); step(IC<5>{}); step(IC<6>{});
    {
      const int mo = wv * 6144 + g * 256 + ((i ^ ((g >> 1) * 2)) << 4);
#pragma unroll
      for (int tt = 0; tt < 4; ++tt)
#pragma unroll
        for (int nt = 0; nt < 4; ++nt) {
          const int t = tt * 4 + nt;
          const int base = t < 6 ? S2 + t * 1024 : t < 12 ? S0 + (t - 6) * 1024 : S1 + (t - 12) * 1024;
          *(LAS f32x4*)(LAS char*)(smem + (mo ^ (nt * 64)) + base) = acc[tt][nt];
        }
    }
    asm volatile("s_waitcnt vmcnt(0) lgkmcnt(0)\n\ts_barrier" : "+v"(rs) : : "memory");
    {
      const int c = lane & 7;
      const int tok = (lane >> 3) + 8 * (wv & 1);
      const int t = (wv >> 1) * 4 + (c >> 1);
      const int base = t < 6 ? S2 + t * 1024 : t < 12 ? S0 + (t - 6) * 1024 : S1 + (t - 12) * 1024;
      const char* rp = smem + base + (c & 1) * 512 + ((tok ^ (2 * c)) << 4);
      f32x4 v0 = f32x4{0, 0, 0, 0}, v1 = f32x4{0, 0, 0, 0};
#pragma unroll
      for (int p = 0; p < 8; ++p) {
        v0 += *(const LAS f32x4*)(LAS const char*)(rp + p * 6144);
        v1 += *(const LAS f32x4*)(LAS const char*)(rp + p * 6144 + 256);
      }
      u32x4 r = __builtin_bit_cast(u32x4, rs);
      u32x4 ov = {pack2(v0[0] + bflo(r[0]), v0[1] + bfhi(r[0])), pack2(v0[2] + bflo(r[1]), v0[3] + bfhi(r[1])),
                  pack2(v1[0] + bflo(r[2]), v1[1] + bfhi(r[2])), pack2(v1[2] + bflo(r[3]), v1[3] + bfhi(r[3]))};
      st16<true>(out + ooff + vo, ov);
    }
  }
}

extern "C" void kernel_launch(void* const* d_in, const int* in_sizes, int n_in, void* d_out, int out_size,
                              void* d_ws, size_t ws_size, hipStream_t stream) {
  const char* x = (const char*)d_in[0];
  const char* wqkv = (const char*)d_in[1];
  const char* wo = (const char*)d_in[2];
  const char* w1 = (const char*)d_in[4];
  const char* w3 = (const char*)d_in[5];
  const char* w2 = (const char*)d_in[6];
  char* ws = (char*)d_ws;
  (void)hipFuncSetAttribute((const void*)k_mega, hipFuncAttributeMaxDynamicSharedMemorySize, MG_LDS);
  k_mega<<<256, 512, MG_LDS, stream>>>(x, wqkv, wo, w1, w3, w2, ws, (char*)d_out);
}
```

```cpp
#include <hip/hip_runtime.h>
#include <hip/hip_bf16.h>
#include <stdint.h>

typedef short bf16x8 __attribute__((ext_vector_type(8)));
typedef short s16x4 __attribute__((ext_vector_type(4)));
typedef float f32x4 __attribute__((ext_vector_type(4)));
typedef float f32x2 __attribute__((ext_vector_type(2)));
typedef unsigned u32x4 __attribute__((ext_vector_type(4)));
typedef unsigned u32x2 __attribute__((ext_vector_type(2)));
typedef __bf16 bf16x2 __attribute__((ext_vector_type(2)));
#define LAS __attribute__((address_space(3)))
#define DEV __device__ __forceinline__

constexpr int S_ = 2048, D_ = 512, H_ = 32, F_ = 1792;
constexpr float LN_EPS = 1e-5f;

typedef float f32x16 __attribute__((ext_vector_type(16)));
DEV f32x16 mfma32(bf16x8 a, bf16x8 b, f32x16 c) { return __builtin_amdgcn_mfma_f32_32x32x16_bf16(a, b, c, 0, 0, 0); }
DEV f32x4 mfma16(bf16x8 a, bf16x8 b, f32x4 c) { return __builtin_amdgcn_mfma_f32_16x16x32_bf16(a, b, c, 0, 0, 0); }
DEV unsigned pack2(float a, float b) {
  f32x2 v = {a, b};
  return __builtin_bit_cast(unsigned, __builtin_convertvector(v, bf16x2));
}
DEV float bflo(unsigned u) { return __builtin_bit_cast(float, u << 16); }
DEV float bfhi(unsigned u) { return __builtin_bit_cast(float, u & 0xffff0000u); }
DEV float dot2(unsigned a, unsigned b, float c) {
  return __builtin_amdgcn_fdot2_f32_bf16(__builtin_bit_cast(bf16x2, a), __builtin_bit_cast(bf16x2, b), c, false);
}
DEV void dma16(const char* gptr, unsigned ldsbase) {
  unsigned sv;
  asm volatile("s_mov_b32 %0, m0\n\ts_mov_b32 m0, %2\n\ts_nop 0\n\tglobal_load_lds_dwordx4 %1, off\n\ts_mov_b32 m0, %0"
               : "=&s"(sv) : "v"(gptr), "s"(ldsbase) : "memory");
}
DEV void dma16s(unsigned voff, const char* sbase, unsigned ldsbase) {
  unsigned sv;
  asm volatile("s_mov_b32 %0, m0\n\ts_mov_b32 m0, %3\n\ts_nop 0\n\tglobal_load_lds_dwordx4 %1, %2\n\ts_mov_b32 m0, %0"
               : "=&s"(sv) : "v"(voff), "s"(sbase), "s"(ldsbase) : "memory");
}
DEV void dma16sm(unsigned voff, const char* sbase, unsigned ldsbase, unsigned long long mask) {
  unsigned sv;
  asm volatile("s_mov_b32 %0, m0\n\ts_mov_b32 m0, %3\n\ts_mov_b64 exec, %4\n\tglobal_load_lds_dwordx4 %1, %2\n\ts_mov_b64 exec, -1\n\ts_mov_b32 m0, %0"
               : "=&s"(sv) : "v"(voff), "s"(sbase), "s"(ldsbase), "s"(mask) : "memory");
}
DEV void gld16(bf16x8& d, unsigned voff, const char* sbase) {
  asm volatile("global_load_dwordx4 %0, %1, %2" : "=v"(d) : "v"(voff), "s"(sbase) : "memory");
}
template <int N> DEV void wait_bar() { asm volatile("s_waitcnt vmcnt(%0) lgkmcnt(0)\n\ts_barrier" ::"n"(N) : "memory"); }
DEV void bar() { asm volatile("s_waitcnt lgkmcnt(0)\n\ts_barrier" ::: "memory"); }
DEV bf16x8 lds128(const char* p) { return *(const LAS bf16x8*)(LAS const char*)p; }
DEV s16x4 ldstr(const char* p) { return __builtin_amdgcn_ds_read_tr16_b64_v4i16((LAS s16x4*)p); }
DEV bf16x8 cat(s16x4 a, s16x4 b) { return __builtin_shufflevector(a, b, 0, 1, 2, 3, 4, 5, 6, 7); }
DEV float xsum(float v) { v += __shfl_xor(v, 16); v += __shfl_xor(v, 32); return v; }
DEV float xmax(float v) { v = fmaxf(v, __shfl_xor(v, 16)); v = fmaxf(v, __shfl_xor(v, 32)); return v; }
DEV float dpp_xor1(float v) { return __builtin_bit_cast(float, __builtin_amdgcn_mov_dpp(__builtin_bit_cast(int, v), 0xB1, 0xF, 0xF, true)); }
DEV float dpp_xor2(float v) { return __builtin_bit_cast(float, __builtin_amdgcn_mov_dpp(__builtin_bit_cast(int, v), 0x4E, 0xF, 0xF, true)); }
DEV float dpp_ror4(float v) { return __builtin_bit_cast(float, __builtin_amdgcn_mov_dpp(__builtin_bit_cast(int, v), 0x124, 0xF, 0xF, true)); }
DEV float dpp_ror8(float v) { return __builtin_bit_cast(float, __builtin_amdgcn_mov_dpp(__builtin_bit_cast(int, v), 0x128, 0xF, 0xF, true)); }
DEV unsigned uni(unsigned v) { return __builtin_amdgcn_readfirstlane(v); }
#define WT 31
template <bool W> DEV void st16(char* p, u32x4 v) {
  if constexpr (W) asm volatile("global_store_dwordx4 %0, %1, off sc0 sc1\n\ts_nop 1" ::"v"(p), "v"(v) : "memory");
  else *(u32x4*)p = v;
}
template <bool W> DEV void st8(char* p, u32x2 v) {
  if constexpr (W) asm volatile("global_store_dwordx2 %0, %1, off sc0 sc1" ::"v"(p), "v"(v) : "memory");
  else *(u32x2*)p = v;
}

template <int A, int B> struct Min { static constexpr int v = A < B ? A : B; };

template <int KT, int NK, int NS, int P, class Issue, class Compute>
DEV void pipe_steps(Issue& issue, Compute& compute) {
  if constexpr (KT < NK) {
    wait_bar<(Min<KT + NS - 1, NK>::v - KT - 1) * P>();
    if constexpr (KT + NS - 1 < NK) issue((KT + NS - 1) % NS);
    compute(KT % NS, KT);
    pipe_steps<KT + 1, NK, NS, P>(issue, compute);
  }
}

constexpr int K1_STAGE = 16384 + 12288, K1_NS = 5;
#define SB __builtin_amdgcn_sched_barrier(0)
DEV float xsum2(float v) {
  auto a = __builtin_amdgcn_permlane16_swap(__float_as_uint(v), __float_as_uint(v), false, false);
  v = __uint_as_float(a[0]) + __uint_as_float(a[1]);
  auto b = __builtin_amdgcn_permlane32_swap(__float_as_uint(v), __float_as_uint(v), false, false);
  return __uint_as_float(b[0]) + __uint_as_float(b[1]);
}

DEV void qkv_phase(char* smem, unsigned lds0, const char* __restrict__ x, const char* __restrict__ w, char* __restrict__ qkv,
                   int cb, int tb) {
  const int tid = threadIdx.x, lane = tid & 63, wv = uni(tid >> 6);
  const int g = lane >> 4, i = lane & 15, q4 = (lane >> 2) & 3, p4 = lane & 3;
  const int tg = wv & 3, nh = wv >> 2;
  const int bi = blockIdx.x >> 3;

  const char* gx = x + (size_t)(tb * 128 + wv * 16) * 1024;
  const unsigned vt = (lane >> 3) * 1024 + (((lane & 7) ^ (lane >> 3)) << 4);
  const char* gw = w + cb * 192;
  unsigned vw0, vw1;
  {
    int qq = wv * 64 + lane;
    int k = qq / 12, c = (qq % 12) ^ (((k >> 3) & 1) << 1);
    vw0 = k * 3072 + c * 16;
    qq = 512 + wv * 32 + (lane & 31);
    k = qq / 12; c = (qq % 12) ^ (((k >> 3) & 1) << 1);
    vw1 = k * 3072 + c * 16;
  }
  const int k0 = bi & 7;
  auto dma = [&](int s, int j) {
    const int kk = (k0 + s) & 7;
    const unsigned base = lds0 + (s % K1_NS) * K1_STAGE;
    if (j == 0) dma16s(vt, gx + kk * 128, base + wv * 2048);
    else if (j == 1) dma16s(vt, gx + kk * 128 + 8192, base + wv * 2048 + 1024);
    else if (j == 2) dma16s(vw0, gw + kk * (64 * 3072), base + 16384 + wv * 1024);
    else dma16sm(vw1, gw + kk * (64 * 3072), base + 16384 + 8192 + wv * 512, 0xFFFFFFFFull);
  };
#pragma unroll
  for (int s = 0; s < K1_NS - 1; ++s)
#pragma unroll
    for (int j = 0; j < 4; ++j) dma(s, j);

  f32x4 acc[2][3], cs = f32x4{0, 0, 0, 0}, s1v = f32x4{0, 0, 0, 0};
#pragma unroll
  for (int a = 0; a < 3; ++a) { acc[0][a] = f32x4{0, 0, 0, 0}; acc[1][a] = f32x4{0, 0, 0, 0}; }
  float s2a = 0.f, s2b = 0.f;
  const bf16x8 ones = {0x3F80, 0x3F80, 0x3F80, 0x3F80, 0x3F80, 0x3F80, 0x3F80, 0x3F80};
  int toff[2][2];
#pragma unroll
  for (int tt = 0; tt < 2; ++tt) {
    toff[tt][0] = (tg * 32 + (tt ^ nh) * 16 + i) * 128 + ((g ^ (i & 7)) << 4);
    toff[tt][1] = (tg * 32 + (tt ^ nh) * 16 + i) * 128 + (((4 + g) ^ (i & 7)) << 4);
  }
  int woff[3], un[3];
#pragma unroll
  for (int nt = 0; nt < 3; ++nt) {
    int a = nt + tg; a = a >= 3 ? a - 3 : a; a = a >= 3 ? a - 3 : a;
    un[nt] = nh * 3 + a;
    woff[nt] = 16384 + (8 * g + q4) * 192 + ((un[nt] ^ (g & 1)) << 5) + p4 * 8;
  }
  auto ldB = [&](int tt, int kt, int ks) -> bf16x8 { return lds128(smem + (kt % K1_NS) * K1_STAGE + toff[tt][ks]); };
  auto ldA = [&](int nt, int kt, int ks) -> bf16x8 {
    const char* p = smem + (kt % K1_NS) * K1_STAGE + woff[nt] + ks * 6144;
    return cat(ldstr(p), ldstr(p + 768));
  };
  bf16x8 A0[3], A1[3], B0[2], B1[2];
  auto phase = [&](bf16x8 (&Ac)[3], bf16x8 (&Bc)[2], bf16x8 (&An)[3], bf16x8 (&Bn)[2], int kn, int ksn, int dk, int dj) {
    const u32x4 u = __builtin_bit_cast(u32x4, Bc[0]);
#pragma unroll
    for (int m = 0; m < 8; ++m) {
      if (kn < 8) {
        if (m == 0) { Bn[0] = ldB(0, kn, ksn); An[0] = ldA(0, kn, ksn); }
        else if (m == 1) Bn[1] = ldB(1, kn, ksn);
        else if (m < 4) An[m - 1] = ldA(m - 1, kn, ksn);
      }
      if (m < 6) acc[m & 1][m >> 1] = mfma16(Ac[m >> 1], Bc[m & 1], acc[m & 1][m >> 1]);
      else if (m == 6) cs = mfma16(Ac[0], ones, cs);
      else s1v = mfma16(ones, Bc[0], s1v);
      {
        float f = (m & 1) ? bfhi(u[m >> 1]) : bflo(u[m >> 1]);
        asm volatile("" : "+v"(f));
        if (m & 1) s2b = __builtin_fmaf(f, f, s2b); else s2a = __builtin_fmaf(f, f, s2a);
      }
      if (dk < 8 && (m == 2 || m == 5)) dma(dk, dj + (m == 5));
      SB;
    }
  };
  wait_bar<12>();
  B0[0] = ldB(0, 0, 0); A0[0] = ldA(0, 0, 0); B0[1] = ldB(1, 0, 0); A0[1] = ldA(1, 0, 0); A0[2] = ldA(2, 0, 0);
#pragma unroll
  for (int kt = 0; kt < 8; ++kt) {
    phase(A0, B0, A1, B1, kt, 1, kt + 4, 0);
    if (kt < 4) wait_bar<10>();
    else if (kt == 4) wait_bar<8>();
    else if (kt == 5) wait_bar<4>();
    else if (kt == 6) wait_bar<0>();
    phase(A1, B1, A0, B0, kt + 1, 0, kt + 4, 2);
  }

  float* st = (float*)(smem + 49152);
  float* csp = (float*)(smem + 49152 + 1024);
  const float s2 = xsum2(s2a + s2b);
  if (g == 0) {
    float mean = s1v[0] * (1.f / 512.f);
    float var = fmaxf(s2 * (1.f / 512.f) - mean * mean, 0.f);
    float rstd = rsqrtf(var + LN_EPS);
    *(f32x2*)(st + (tg * 32 + nh * 16 + i) * 2) = f32x2{-mean * rstd, rstd};
  }
  if (i == 0 && tg < 3) *(f32x4*)(csp + un[0] * 16 + 4 * g) = cs;
  bar();
  f32x2 ms[2];
#pragma unroll
  for (int tt = 0; tt < 2; ++tt) ms[tt] = *(const f32x2*)(st + (tg * 32 + (tt ^ nh) * 16 + i) * 2);
  const int otile = (g & 1) ^ nh, ohalf = g >> 1;
  const unsigned vo = ohalf * 512 + (((otile * 16 + i) ^ (8 * ohalf)) << 4);
#pragma unroll
  for (int nt = 0; nt < 3; ++nt) {
    f32x4 c = *(const f32x4*)(csp + un[nt] * 16 + 4 * g);
    unsigned o[2][2];
#pragma unroll
    for (int tt = 0; tt < 2; ++tt) {
      f32x4 y = acc[tt][nt] * ms[tt][1] + ms[tt][0] * c;
      o[tt][0] = pack2(y[0], y[1]); o[tt][1] = pack2(y[2], y[3]);
    }
    auto sx = __builtin_amdgcn_permlane16_swap(o[0][0], o[1][0], false, false);
    auto sy = __builtin_amdgcn_permlane16_swap(o[0][1], o[1][1], false, false);
    char* dst = qkv + (size_t)(cb * 6 + un[nt]) * (2048 * 32) + (size_t)(tb * 4 + tg) * 1024;
    st16<true>(dst + vo, u32x4{sx[0], sy[0], sx[1], sy[1]});
  }
}

constexpr int S0 = 0, S1 = 49152, S2 = 98304, CSP = 147456, MG_LDS = 163840;
typedef unsigned long long u64;
extern "C" __device__ u64 llvm_dispatch_id() __asm("llvm.amdgcn.dispatch.id");
DEV void flag_set(u64* f, u64 tag) {
  asm volatile("global_store_dwordx2 %0, %1, off sc0 sc1" ::"v"(f), "v"(tag) : "memory");
}
DEV void flag_wait(const u64* p, u64 tag) {
  for (int it = 0; it < (1 << 20); ++it) {
    u64 c;
    asm volatile("global_load_dwordx2 %0, %1, off sc0 sc1\n\ts_waitcnt vmcnt(0)" : "=v"(c) : "v"(p) : "memory");
    if (__builtin_amdgcn_ballot_w64(c != tag) == 0) break;
  }
}
template <int V> struct IC { static constexpr int value = V; };
DEV int opaque(int v) { asm volatile("" : "+v"(v)); return v; }
DEV int opaque_s(int v) { asm volatile("" : "+s"(v)); return v; }

struct CW {
  const char *g1, *g3; unsigned vw; unsigned long long wmask;
};
DEV CW c_setup(const char* w1, const char* w3, int b, int wv, int lane) {
  const int xcd = b & 7, bi = b >> 3;
  const int cb = (xcd & 3) * 4 + (bi & 3);
  CW c;
  c.g1 = w1 + (size_t)(wv * 8) * (F_ * 2) + cb * 224;
  c.g3 = w3 + (size_t)(wv * 8) * (F_ * 2) + cb * 224;
  const int unit = ((lane >> 1) & 7) ^ ((lane >> 4) | ((wv & 1) << 2));
  c.vw = (lane >> 4) * (F_ * 2) + unit * 32 + (lane & 1) * 16;
  c.wmask = __builtin_amdgcn_ballot_w64(unit < 7);
  return c;
}
DEV void c_dmaw(const CW& c, unsigned ldsw, int kt, int j) {
  const unsigned base = ldsw + (kt % 3) * 49152;
  if (j < 2) dma16sm(c.vw, c.g1 + (size_t)(kt * 64 + j * 4) * (F_ * 2), base + 16384 + j * 1024, c.wmask);
  else dma16sm(c.vw, c.g3 + (size_t)(kt * 64 + (j - 2) * 4) * (F_ * 2), base + 32768 + (j - 2) * 1024, c.wmask);
}
DEV constexpr int d_unit(int u) {
  return u < 3 ? S2 + u * 2048 : u < 6 ? S0 + (u - 3) * 2048 : u < 9 ? S1 + (u - 6) * 2048 : u == 9 ? CSP
       : u < 13 ? S2 + (u - 10) * 2048 : S0;
}
DEV constexpr int d_ustr(int u) { return u == 9 ? 2048 : 6144; }
DEV void d_dma(const char* gw2, unsigned vw, unsigned lds0, int wv, int u, int q) {
  dma16s(vw, gw2 + (size_t)(u * 16 + q * 8) * 1024, lds0 + d_unit(u) + wv * d_ustr(u) + q * 1024);
}

template <int NT>
DEV void c_body(char* smem, unsigned lds0, const char* gt, const CW& cw, unsigned vt, int wv, int lane,
                const char* __restrict__ stats, const char* __restrict__ csb, char* __restrict__ hh, int tok0, int cb, const char* gw2, unsigned vwd) {
  const int tg = wv & 3;
  const unsigned ldsw = lds0 + wv * 2048;
  const int g = lane >> 4, i = lane & 15, q4 = (lane >> 2) & 3, p4 = lane & 3;
  f32x4 acc[2][2 * NT];
#pragma unroll
  for (int a = 0; a < 2 * NT; ++a) { acc[0][a] = f32x4{0, 0, 0, 0}; acc[1][a] = f32x4{0, 0, 0, 0}; }
  const int toff0 = (tg * 32 + i) * 128 + ((g ^ (i & 7)) << 4);
  const int toff1 = (tg * 32 + i) * 128 + (((4 + g) ^ (i & 7)) << 4);
  const int odd = cb & 1;
  const int fsw = q4 | ((g & 1) << 2);
  int T[NT], wa[NT];
#pragma unroll
  for (int a = 0; a < NT; ++a) {
    T[a] = NT == 4 ? odd * 3 + a : (a < 2 ? (odd ? 1 : 4) + a : (odd ? 0 : 6));
    wa[a] = 16384 + (8 * g + q4) * 256 + ((T[a] ^ fsw) << 5) + p4 * 8;
  }
  auto dmat = [&](int kt, int j) { dma16s(vt, gt + kt * 128 + j * 8192, ldsw + (kt % 3) * 49152 + j * 1024); };
  auto dma = [&](int kt, int j) {
    if (j < 2) dmat(kt, j); else c_dmaw(cw, ldsw, kt, j - 2);
  };
  auto ldB = [&](bf16x8* B, int kt, int ks) {
    const char* sb = smem + (kt % 3) * 49152 + (ks ? toff1 : toff0);
    B[0] = lds128(sb); B[1] = lds128(sb + 2048);
  };
  auto ldA = [&](int a, int kt, int ks) -> bf16x8 {
    const char* wp = smem + (kt % 3) * 49152 + wa[a < NT ? a : a - NT] + (a < NT ? 0 : 16384) + ks * 8192;
    return cat(ldstr(wp), ldstr(wp + 1024));
  };
  auto mma = [&](int a, bf16x8 A, const bf16x8* B) {
    acc[0][a] = mfma16(A, B[0], acc[0][a]);
    acc[1][a] = mfma16(A, B[1], acc[1][a]);
  };
  bf16x8 A0[2 * NT], A1[2 * NT], B0[2], B1[2];
  bf16x8 sa[2], cp[4];
#pragma unroll
  for (int kt = 0; kt < 3; ++kt) { dmat(kt, 0); dmat(kt, 1); }
  gld16(sa[0], (unsigned)(i * 64 + g * 16), stats + (size_t)(tok0 + tg * 32) * 64);
  gld16(sa[1], (unsigned)(i * 64 + g * 16), stats + (size_t)(tok0 + tg * 32 + 16) * 64);
  const int cq = min(tg * 16 + (lane >> 2), 55);
  if (NT == 3) {
#pragma unroll
    for (int m = 0; m < 4; ++m) gld16(cp[m], (unsigned)(((lane & 3) * 4 + m) * 896 + cq * 16), csb + (size_t)cb * 14336);
  }
  constexpr int X = NT == 3 ? 6 : 2;
  wait_bar<4 + X>();
  float rs[2], mr[2];
  float* csp = (float*)(smem + CSP);
  ldB(B0, 0, 0);
#pragma unroll
  for (int a = 0; a < 2 * NT; ++a) A0[a] = ldA(a, 0, 0);
#pragma unroll
  for (int kt = 0; kt < 8; ++kt) {
    ldB(B1, kt, 1);
#pragma unroll
    for (int a = 0; a < 2 * NT; ++a) {
      if (a < NT) { A1[2 * a] = ldA(2 * a, kt, 1); A1[2 * a + 1] = ldA(2 * a + 1, kt, 1); }
      mma(a, A0[a], B0);
      __builtin_amdgcn_sched_barrier(0);
    }
    if (kt == 0) wait_bar<2 + X>();
    else if (kt != 1) wait_bar<6>();
    else if (NT == 3) asm volatile("s_waitcnt vmcnt(6) lgkmcnt(0)\n\ts_barrier" : "+v"(sa[0]), "+v"(sa[1]), "+v"(cp[0]), "+v"(cp[1]), "+v"(cp[2]), "+v"(cp[3]) : : "memory");
    else asm volatile("s_waitcnt vmcnt(6) lgkmcnt(0)\n\ts_barrier" : "+v"(sa[0]), "+v"(sa[1]) : : "memory");
    if (kt < 7) ldB(B0, kt + 1, 0);
    if (kt == 1) {
#pragma unroll
      for (int tt = 0; tt < 2; ++tt) {
        const f32x4 sv = __builtin_bit_cast(f32x4, sa[tt]);
        const float s1 = xsum2(sv[0] + sv[2]), s2 = xsum2(sv[1] + sv[3]);
        const float mean = s1 * (1.f / 512.f);
        const float var = fmaxf(s2 * (1.f / 512.f) - mean * mean, 0.f);
        rs[tt] = rsqrtf(var + LN_EPS); mr[tt] = mean * rs[tt];
      }
      if (NT == 3) {
        f32x4 c = (__builtin_bit_cast(f32x4, cp[0]) + __builtin_bit_cast(f32x4, cp[1])) +
                  (__builtin_bit_cast(f32x4, cp[2]) + __builtin_bit_cast(f32x4, cp[3]));
#pragma unroll
        for (int e = 0; e < 4; ++e) { c[e] += dpp_xor1(c[e]); c[e] += dpp_xor2(c[e]); }
        if ((lane & 3) == 0) *(f32x4*)(csp + cq * 4) = c;
      }
    }
#pragma unroll
    for (int a = 0; a < 2 * NT; ++a) {
      if (kt < 7 && a < NT) { A0[2 * a] = ldA(2 * a, kt + 1, 0); A0[2 * a + 1] = ldA(2 * a + 1, kt + 1, 0); }
      mma(a, A1[a], B1);
      if (a >= 2 * NT - 6) {
        const int j = a - (2 * NT - 6);
        if (kt + 3 < 8) dma(kt + 3, j);
        else d_dma(gw2, vwd, lds0, wv, (kt - 5) * 3 + (j >> 1), j & 1);
      }
      __builtin_amdgcn_sched_barrier(0);
    }
  }
  unsigned pk[2][NT][2];
#pragma unroll
  for (int tt = 0; tt < 2; ++tt) {
    char* dst = hh + ((size_t)((tok0 >> 4) + tg * 2 + tt) * 56) * 1024 + lane * 16;
#pragma unroll
    for (int a = 0; a < NT; ++a) {
      f32x4 cg = *(const f32x4*)(csp + T[a] * 16 + 4 * g);
      f32x4 cu = *(const f32x4*)(csp + (7 + T[a]) * 16 + 4 * g);
      float r[4];
#pragma unroll
      for (int e = 0; e < 4; ++e) {
        float ga = acc[tt][a][e] * rs[tt] - mr[tt] * cg[e];
        float up = acc[tt][NT + a][e] * rs[tt] - mr[tt] * cu[e];
        float sg = __builtin_amdgcn_rcpf(1.f + __builtin_amdgcn_exp2f(-1.4426950408889634f * ga));
        r[e] = ga * sg * up;
      }
      pk[tt][a][0] = pack2(r[0], r[1]); pk[tt][a][1] = pack2(r[2], r[3]);
      const int G = cb * 7 + T[a];
      if (a & 1) {
        st16<true>(dst + (size_t)(G >> 1) * 1024, u32x4{pk[tt][a - 1][0], pk[tt][a - 1][1], pk[tt][a][0], pk[tt][a][1]});
      } else if (a == NT - 1) {
        st8<true>(dst + (size_t)(G >> 1) * 1024 + (G & 1) * 8, u32x2{pk[tt][a][0], pk[tt][a][1]});
      }
    }
  }
}

__global__ __launch_bounds__(512) void k_mega(const char* __restrict__ x, const char* __restrict__ wqkv, const char* __restrict__ wo,
                                              const char* __restrict__ w1, const char* __restrict__ w3,
                                              const char* __restrict__ w2, char* __restrict__ ws, char* __restrict__ out) {
  extern __shared__ __attribute__((aligned(16))) char smem[];
  char* qkv = ws;
  char* o = ws + (6u << 20);
  char* h = ws + (8u << 20);
  char* stats = ws + (10u << 20);
  u64* flags = (u64*)(ws + (10u << 20) + (512u << 10));
  char* csb = ws + (10u << 20) + (768u << 10);
  char* hh = ws + (11u << 20);
  const u64 tag = ((u64)(size_t)__builtin_amdgcn_dispatch_ptr() << 20) + llvm_dispatch_id();
  const int b = blockIdx.x;
  const unsigned lds0 = (unsigned)(size_t)(LAS char*)smem;
  const int cbG = (b >> 3) & 7, tbG = (b & 7) * 4 + (b >> 6);
  const int cbC = (b & 3) * 4 + ((b >> 3) & 3), tbC = ((b >> 2) & 1) * 8 + (b >> 5);

  unsigned cpf = 0;
  {
    u64 pc;
    unsigned len;
    asm volatile("s_getpc_b64 %0\n.Lmega_pc:\n\ts_mov_b32 %1, .Lmega_end-.Lmega_pc" : "=s"(pc), "=s"(len));
    const unsigned off = ((b >> 3) * 20 + threadIdx.x) * 64;
    if (threadIdx.x < 20 && off < len) asm volatile("global_load_dword %0, %1, %2" : "=v"(cpf) : "v"(off), "s"(pc) : "memory");
  }
  {
    const int cb = (b & 1) * 8 + ((b >> 3) & 7), tb = ((b & 7) >> 1) * 4 + (b >> 6);
    qkv_phase(smem, lds0, x, wqkv, qkv, cb, tb);
    asm volatile("s_waitcnt vmcnt(0) lgkmcnt(0)\n\ts_barrier" ::: "memory");
    asm volatile("" :: "v"(cpf));
    const int tid = threadIdx.x;
    if (tid < 9) {
      const int P = cb * 3 + tid / 3, d = tid % 3, which = P >> 4, hp = P & 15, qb = tb + d;
      if (which ? qb < 16 : d == 0)
        flag_set(flags + 24576 + ((hp >> 1) | ((hp & 1) << 3) | (qb << 4)) * 32 + (which ? (which - 1) * 3 + d : 6), tag);
    }
  }
  {
    const int tid = opaque(threadIdx.x), lane = tid & 63, wv = uni(tid >> 6);
    const int c32 = lane & 31, hf = lane >> 5, q4 = (lane >> 2) & 3, p4 = lane & 3;
    const int hp = (b & 7) * 2 + ((b >> 3) & 1), qb = b >> 4;
    const int q0 = qb * 128;
    const size_t HS = 2048 * 32;
    bf16x8 Q, cw0, cw1;
    if (wv == 0) {
      const int d = lane % 3;
      flag_wait(flags + 24576 + b * 32 + (lane >= 6 ? 6 : qb - d < 0 ? 6 : lane), tag);
    }
    asm volatile("s_barrier" ::: "memory");
    {
      const int r = wv >> 1;
      const char* src = qkv + (size_t)((1 + (r >> 1)) * 32 + hp * 2 + (r & 1)) * HS;
#pragma unroll
      for (int j = 0; j < 6; ++j) {
        const int kb = qb * 4 - 8 + (wv & 1) * 6 + j;
        dma16s(kb < 0 ? hf * 640 : lane * 16, src + (size_t)max(kb, 0) * 1024, lds0 + r * 12288 + ((wv & 1) * 6 + j) * 1024);
      }
    }
    const int u = wv >> 2, qg = wv & 3;
    const int qs = q0 + qg * 32;
    const unsigned vq = hf * 512 + ((c32 ^ (8 * hf)) << 4);
    gld16(Q, vq, qkv + (size_t)(hp * 2 + u) * HS + (size_t)qs * 32);
    const float c = 0.25f * 1.4426950408889634f;
    const float NEG = -1e30f;
    f32x16 cin0, cin8;
#pragma unroll
    for (int r = 0; r < 16; ++r) {
      const int kl = (r & 3) + 8 * (r >> 2) + 4 * hf;
      cin0[r] = kl > c32 ? 0.f : NEG; cin8[r] = kl <= c32 ? 0.f : NEG;
    }
    asm volatile("s_waitcnt vmcnt(0)\n\ts_barrier" : "+v"(Q), "+v"(cin0), "+v"(cin8) : : "memory");
    const unsigned vwo = (lane >> 3) * 1024 + ((((lane & 7) >> 1) ^ ((lane >> 4) & 3)) << 5) + (lane & 1) * 16;
    const char* gwo = wo + (size_t)(wv * 64) * 1024 + cbG * 128;
    const CW cw1s = c_setup(w1, w3, b, wv, lane);
    auto pf = [&](int k) {
      if (k < 2) {
        const int ch = max(wv - 1, 0) * 4 + (lane & 3);
        const char* pc = (ch >= 14 ? w3 + (ch - 14) * 16 : w1 + ch * 16) + (size_t)(tbC * 32 + (lane >> 2)) * (F_ * 2) + cbC * 224;
        if (k == 0) asm volatile("global_load_dwordx4 %0, %1, off" : "=v"(cw0) : "v"(pc) : "memory");
        else asm volatile("global_load_dwordx4 %0, %1, off" : "=v"(cw1) : "v"(pc + 16 * (F_ * 2)) : "memory");
      } else if (k < 10) dma16s(vwo, gwo + (size_t)((k - 2) * 8) * 1024, lds0 + S2 + wv * 8192 + (k - 2) * 1024);
      else if (k < 14) c_dmaw(cw1s, lds0 + wv * 2048, 1, k - 10);
      else if (k == 14) {
        const int c0 = b * 8 + wv;
        dma16s(lane * 16, w2 + (size_t)(c0 < 1792 ? c0 : c0 - 1792) * 1024, lds0 + S1 + wv * 1024);
      }
    };
    {
      const char* Kl = smem + u * 12288 + qg * 1024 + vq;
      const char* Vl[2];
#pragma unroll
      for (int e = 0; e < 2; ++e)
        Vl[e] = smem + 24576 + u * 12288 + qg * 1024 + (p4 >> 1) * 512 + (((e * 8 + 4 * hf + q4) ^ (8 * (p4 >> 1))) << 4) + (p4 & 1) * 8;
      f32x16 s[9];
#pragma unroll
      for (int t = 0; t < 9; ++t) {
        f32x16 z;
#pragma unroll
        for (int r = 0; r < 16; ++r) z[r] = 0.f;
        s[t] = mfma32(lds128(Kl + t * 1024), Q, t == 0 ? cin0 : t == 8 ? cin8 : z);
      }
      float m = s[8][0];
#pragma unroll
      for (int t = 0; t < 9; ++t)
#pragma unroll
        for (int r = 0; r < 16; ++r) m = fmaxf(m, s[t][r]);
      {
        auto sw = __builtin_amdgcn_permlane32_swap(__float_as_uint(m), __float_as_uint(m), false, false);
        m = fmaxf(__uint_as_float(sw[0]), __uint_as_float(sw[1]));
      }
      const f32x2 c2 = {c, c}, m2 = {-m * c, -m * c};
      const int nneg = qs >= 256 ? 0 : (256 - qs) >> 5;
      f32x16 oacc;
#pragma unroll
      for (int r = 0; r < 16; ++r) oacc[r] = 0.f;
      float ls0 = 0.f, ls1 = 0.f;
#pragma unroll
      for (int t = 0; t < 9; ++t) {
        const float mt = t < nneg ? NEG : m2[0];
        const f32x2 mt2 = {mt, mt};
#pragma unroll
        for (int sh = 0; sh < 2; ++sh) {
          u32x4 pb;
#pragma unroll
          for (int e = 0; e < 4; ++e) {
            f32x2 a = __builtin_elementwise_fma(f32x2{s[t][8 * sh + 2 * e], s[t][8 * sh + 2 * e + 1]}, c2, mt2);
            pb[e] = pack2(__builtin_amdgcn_exp2f(a[0]), __builtin_amdgcn_exp2f(a[1]));
          }
          ls0 = dot2(pb[0], 0x3F803F80u, ls0); ls1 = dot2(pb[1], 0x3F803F80u, ls1);
          ls0 = dot2(pb[2], 0x3F803F80u, ls0); ls1 = dot2(pb[3], 0x3F803F80u, ls1);
          const int vp = t * 1024 + sh * 256;
          oacc = mfma32(cat(ldstr(Vl[0] + vp), ldstr(Vl[1] + vp)), __builtin_bit_cast(bf16x8, pb), oacc);
          if (2 * t + sh < 15) pf(2 * t + sh);
        }
      }
      float ls = ls0 + ls1;
      {
        auto sw = __builtin_amdgcn_permlane32_swap(__float_as_uint(ls), __float_as_uint(ls), false, false);
        ls = __uint_as_float(sw[0]) + __uint_as_float(sw[1]);
      }
      const float inv = __builtin_amdgcn_rcpf(ls);
      const u32x4 ov = {pack2(oacc[0] * inv, oacc[1] * inv), pack2(oacc[2] * inv, oacc[3] * inv),
                        pack2(oacc[4] * inv, oacc[5] * inv), pack2(oacc[6] * inv, oacc[7] * inv)};
      st16<true>(o + ((size_t)((qs >> 4) + (c32 >> 4)) * 16 + hp) * 1024 + ((2 * u + hf) * 16 + (c32 & 15)) * 16, ov);
    }
    asm volatile("s_waitcnt vmcnt(0) lgkmcnt(0)\n\ts_barrier" : "+v"(cw0), "+v"(cw1) : : "memory");
    if (tid < 16) flag_set(flags + (((qb >> 1) | (tid << 3) | ((qb & 1) << 7)) * 32 + hp), tag);
    {
      const CW cw = c_setup(w1, w3, b, wv, lane);
#pragma unroll
      for (int j = 0; j < 4; ++j) c_dmaw(cw, lds0 + wv * 2048, 0, j);
    }
    if (wv == 0) { flag_wait(flags + b * 32 + (lane & 15), tag); }
    else {
      const u32x4 ua = __builtin_bit_cast(u32x4, cw0), ub = __builtin_bit_cast(u32x4, cw1);
      f32x4 lo, hi;
#pragma unroll
      for (int e = 0; e < 2; ++e) {
        lo[2 * e] = bflo(ua[e]) + bflo(ub[e]); lo[2 * e + 1] = bfhi(ua[e]) + bfhi(ub[e]);
        hi[2 * e] = bflo(ua[2 + e]) + bflo(ub[2 + e]); hi[2 * e + 1] = bfhi(ua[2 + e]) + bfhi(ub[2 + e]);
      }
#pragma unroll
      for (int e = 0; e < 4; ++e) {
        lo[e] += dpp_ror4(lo[e]); lo[e] += dpp_ror8(lo[e]); lo[e] = xsum2(lo[e]);
        hi[e] += dpp_ror4(hi[e]); hi[e] += dpp_ror8(hi[e]); hi[e] = xsum2(hi[e]);
      }
      if (lane < 4) {
        char* d = csb + (size_t)(cbC * 16 + tbC) * 896 + ((wv - 1) * 4 + lane) * 32;
        st16<true>(d, __builtin_bit_cast(u32x4, lo));
        st16<true>(d + 16, __builtin_bit_cast(u32x4, hi));
      }
      asm volatile("s_waitcnt vmcnt(0)" ::: "memory");
    }
    asm volatile("s_barrier" ::: "memory");
    if (tid >= 64 && tid < 80) flag_set(flags + 8192 + ((cbC >> 2) | ((tid & 1) << 2) | ((cbC & 3) << 3) | (((tid & 15) >> 1) << 5)) * 32 + 16 + tbC, tag);
  }
  {
    const int tid = opaque(threadIdx.x), lane = tid & 63, wv = uni(tid >> 6);
    const int g = lane >> 4, i = lane & 15, q4 = (lane >> 2) & 3, p4 = lane & 3;
    const int kp = wv & 3, th = wv >> 2;
    bf16x8 B[4][2], rs;
    const char* ga = o + ((size_t)(tbG * 4 + th * 2) * 16 + kp * 4) * 1024;
#pragma unroll
    for (int s = 0; s < 4; ++s) {
      gld16(B[s][0], lane * 16, ga + s * 1024);
      gld16(B[s][1], lane * 16, ga + (16 + s) * 1024);
    }
    const size_t ooff = (size_t)(tbG * 64 + wv * 8) * 1024 + cbG * 128;
    const unsigned vo = (lane >> 3) * 1024 + (lane & 7) * 16;
    gld16(rs, vo, x + ooff);
    const int sw = (2 * (g & 1) + (q4 >> 1)) & 3;
    const char* wr = smem + S2 + kp * 16384 + (16 * (g >> 1) + 4 * (g & 1) + q4) * 128 + p4 * 8;
    bf16x8 A[4][4];
#pragma unroll
    for (int s = 0; s < 4; ++s)
#pragma unroll
      for (int nt = 0; nt < 4; ++nt) {
        const char* p = wr + s * 4096 + ((nt ^ sw) << 5);
        A[s][nt] = cat(ldstr(p), ldstr(p + 1024));
      }
    f32x4 acc[2][4];
#pragma unroll
    for (int t = 0; t < 2; ++t)
#pragma unroll
      for (int n = 0; n < 4; ++n) acc[t][n] = f32x4{0, 0, 0, 0};
    asm volatile("s_waitcnt vmcnt(7)" : "+v"(B[0][0]), "+v"(B[0][1]) : : "memory");
#pragma unroll
    for (int nt = 0; nt < 4; ++nt) { acc[0][nt] = mfma16(A[0][nt], B[0][0], acc[0][nt]); acc[1][nt] = mfma16(A[0][nt], B[0][1], acc[1][nt]); }
    asm volatile("s_waitcnt vmcnt(5)" : "+v"(B[1][0]), "+v"(B[1][1]) : : "memory");
#pragma unroll
    for (int nt = 0; nt < 4; ++nt) { acc[0][nt] = mfma16(A[1][nt], B[1][0], acc[0][nt]); acc[1][nt] = mfma16(A[1][nt], B[1][1], acc[1][nt]); }
    asm volatile("s_waitcnt vmcnt(3)" : "+v"(B[2][0]), "+v"(B[2][1]) : : "memory");
#pragma unroll
    for (int nt = 0; nt < 4; ++nt) { acc[0][nt] = mfma16(A[2][nt], B[2][0], acc[0][nt]); acc[1][nt] = mfma16(A[2][nt], B[2][1], acc[1][nt]); }
    asm volatile("s_waitcnt vmcnt(1)" : "+v"(B[3][0]), "+v"(B[3][1]) : : "memory");
#pragma unroll
    for (int nt = 0; nt < 4; ++nt) { acc[0][nt] = mfma16(A[3][nt], B[3][0], acc[0][nt]); acc[1][nt] = mfma16(A[3][nt], B[3][1], acc[1][nt]); }
    bar();
    {
      const int mo = S2 + kp * 16384 + g * 256 + ((i ^ ((g >> 1) * 2)) << 4);
#pragma unroll
      for (int tt = 0; tt < 2; ++tt)
#pragma unroll
        for (int nt = 0; nt < 4; ++nt)
          *(LAS f32x4*)(LAS char*)(smem + (mo ^ (nt * 64)) + ((th * 2 + tt) * 4 + nt) * 1024) = acc[tt][nt];
    }
    asm volatile("s_waitcnt vmcnt(0) lgkmcnt(0)\n\ts_barrier" : "+v"(rs) : : "memory");
    {
      const int c = lane & 7;
      const int tok = (lane >> 3) + 8 * (wv & 1);
      const char* rp = smem + S2 + ((wv >> 1) * 4 + (c >> 1)) * 1024 + (c & 1) * 512 + ((tok ^ (2 * c)) << 4);
      f32x4 v0 = f32x4{0, 0, 0, 0}, v1 = f32x4{0, 0, 0, 0};
#pragma unroll
      for (int p = 0; p < 4; ++p) {
        v0 += *(const LAS f32x4*)(LAS const char*)(rp + p * 16384);
        v1 += *(const LAS f32x4*)(LAS const char*)(rp + p * 16384 + 256);
      }
      u32x4 r = __builtin_bit_cast(u32x4, rs);
      u32x4 ov = {pack2(v0[0] + bflo(r[0]), v0[1] + bfhi(r[0])), pack2(v0[2] + bflo(r[1]), v0[3] + bfhi(r[1])),
                  pack2(v1[0] + bflo(r[2]), v1[1] + bfhi(r[2])), pack2(v1[2] + bflo(r[3]), v1[3] + bfhi(r[3]))};
      st16<true>(h + ooff + vo, ov);
      float s1 = 0.f, s2 = 0.f;
#pragma unroll
      for (int e = 0; e < 4; ++e) { s1 = dot2(ov[e], 0x3F803F80u, s1); s2 = dot2(ov[e], ov[e], s2); }
#pragma unroll
      for (int m = 1; m < 8; m <<= 1) { s1 += __shfl_xor(s1, m); s2 += __shfl_xor(s2, m); }
      if (c == 0) st8<true>(stats + ((size_t)(tbG * 64 + wv * 8 + (lane >> 3)) * 8 + cbG) * 8,
                            u32x2{__float_as_uint(s1), __float_as_uint(s2)});
    }
    asm volatile("s_waitcnt vmcnt(0) lgkmcnt(0)\n\ts_barrier" ::: "memory");
    if (tid < 16) flag_set(flags + 8192 + ((tid & 3) | ((tbG >> 4) << 2) | ((tid >> 2) << 3) | (((tbG >> 1) & 7) << 5)) * 32 + (tbG & 1) * 8 + cbG, tag);
    {
      const CW cw = c_setup(w1, w3, b, wv, lane);
#pragma unroll
      for (int j = 0; j < 4; ++j) c_dmaw(cw, lds0 + wv * 2048, 2, j);
    }
    if (wv == 0) flag_wait(flags + 8192 + b * 32 + (lane & 31), tag);
    asm volatile("s_barrier" ::: "memory");
  }
  const unsigned vwd_ = 0;
  {
    const int tid = opaque(threadIdx.x), lane = tid & 63, wv = uni(tid >> 6);
    const char* gt = h + (size_t)(tbC * 128 + wv * 16) * 1024;
    const unsigned vt = (lane >> 3) * 1024 + (((lane & 7) ^ (lane >> 3)) << 4);
    const CW cw = c_setup(w1, w3, b, wv, lane);
    const unsigned vwd = (lane >> 3) * 1024 + ((((lane & 7) >> 1) ^ ((lane >> 4) & 3)) << 5) + (lane & 1) * 16;
    const char* gw2 = w2 + (size_t)(wv * 224) * 1024 + cbG * 128;
    if ((wv >> 2) == 0) c_body<4>(smem, lds0, gt, cw, vt, wv, lane, stats, csb, hh, tbC * 128, cbC, gw2, vwd);
    else c_body<3>(smem, lds0, gt, cw, vt, wv, lane, stats, csb, hh, tbC * 128, cbC, gw2, vwd);
  }
  {
    const int tid = opaque(threadIdx.x), lane = tid & 63, wv = uni(tid >> 6);
    const int g = lane >> 4, i = lane & 15, q4 = (lane >> 2) & 3, p4 = lane & 3;
    asm volatile("s_waitcnt vmcnt(0) lgkmcnt(0)\n\ts_barrier" ::: "memory");
    if (tid < 16) flag_set(flags + 16384 + ((tbC >> 1) | (tid << 3) | ((tbC & 1) << 7)) * 32 + cbC, tag);
    const unsigned vwd = (lane >> 3) * 1024 + ((((lane & 7) >> 1) ^ ((lane >> 4) & 3)) << 5) + (lane & 1) * 16;
    const char* gw2 = w2 + (size_t)(wv * 224) * 1024 + cbG * 128;
    d_dma(gw2, vwd, lds0, wv, 9, 0); d_dma(gw2, vwd, lds0, wv, 9, 1);
    const int sw = (2 * g + (q4 >> 1)) & 3;
    const int ro = (4 * g + q4) * 128 + p4 * 8;
    int rn[4];
#pragma unroll
    for (int nt = 0; nt < 4; ++nt) rn[nt] = ro + ((nt ^ sw) << 5);
    auto lda = [&](bf16x8* Af, int j) {
      const char* p0 = smem + d_unit(2 * j) + wv * d_ustr(2 * j);
      const char* p1 = smem + d_unit(2 * j + 1) + wv * d_ustr(2 * j + 1);
#pragma unroll
      for (int nt = 0; nt < 4; ++nt) Af[nt] = cat(ldstr(p0 + rn[nt]), ldstr(p1 + rn[nt]));
    };
    bf16x8 A[2][4], B[7][4], rs;
    lda(A[0], 0); lda(A[1], 1);
    asm volatile("s_waitcnt lgkmcnt(0)" : "+v"(A[0][0]), "+v"(A[0][1]), "+v"(A[0][2]), "+v"(A[0][3]), "+v"(A[1][0]), "+v"(A[1][1]), "+v"(A[1][2]), "+v"(A[1][3]) : : "memory");
#pragma unroll
    for (int u = 10; u < 14; ++u) { d_dma(gw2, vwd, lds0, wv, u, 0); d_dma(gw2, vwd, lds0, wv, u, 1); }
    if (wv == 0) flag_wait(flags + 16384 + b * 32 + (lane & 15), tag);
    asm volatile("s_barrier" ::: "memory");
    const char* ga = hh + ((size_t)(tbG * 4) * 56 + wv * 7) * 1024;
#pragma unroll
    for (int j = 0; j < 7; ++j)
#pragma unroll
      for (int tt = 0; tt < 4; ++tt) gld16(B[j][tt], lane * 16, ga + (size_t)(tt * 56 + j) * 1024);
    const size_t ooff = (size_t)(tbG * 64 + wv * 8) * 1024 + cbG * 128;
    const unsigned vo = (lane >> 3) * 1024 + (lane & 7) * 16;
    gld16(rs, vo, h + ooff);
    f32x4 acc[4][4];
#pragma unroll
    for (int t = 0; t < 4; ++t)
#pragma unroll
      for (int n = 0; n < 4; ++n) acc[t][n] = f32x4{0, 0, 0, 0};
    auto step = [&](auto J) {
      constexpr int j = decltype(J)::value;
      asm volatile("s_waitcnt vmcnt(%4)" : "+v"(B[j][0]), "+v"(B[j][1]), "+v"(B[j][2]), "+v"(B[j][3]) : "n"(25 - 4 * j) : "memory");
#pragma unroll
      for (int nt = 0; nt < 4; ++nt)
#pragma unroll
        for (int tt = 0; tt < 4; ++tt) acc[tt][nt] = mfma16(A[j & 1][nt], B[j][tt], acc[tt][nt]);
      __builtin_amdgcn_sched_barrier(0);
      if constexpr (j + 2 < 7) lda(A[j & 1], j + 2);
    };
    step(IC<0>{}); step(IC<1>{}); step(IC<2>{}); step(IC<3>{}); step(IC<4>{}); step(IC<5>{}); step(IC<6>{});
    {
      const int mo = wv * 6144 + g * 256 + ((i ^ ((g >> 1) * 2)) << 4);
#pragma unroll
      for (int tt = 0; tt < 4; ++tt)
#pragma unroll
        for (int nt = 0; nt < 4; ++nt) {
          const int t = tt * 4 + nt;
          const int base = t < 6 ? S2 + t * 1024 : t < 12 ? S0 + (t - 6) * 1024 : S1 + (t - 12) * 1024;
          *(LAS f32x4*)(LAS char*)(smem + (mo ^ (nt * 64)) + base) = acc[tt][nt];
        }
    }
    asm volatile("s_waitcnt vmcnt(0) lgkmcnt(0)\n\ts_barrier" : "+v"(rs) : : "memory");
    {
      const int c = lane & 7;
      const int tok = (lane >> 3) + 8 * (wv & 1);
      const int t = (wv >> 1) * 4 + (c >> 1);
      const int base = t < 6 ? S2 + t * 1024 : t < 12 ? S0 + (t - 6) * 1024 : S1 + (t - 12) * 1024;
      const char* rp = smem + base + (c & 1) * 512 + ((tok ^ (2 * c)) << 4);
      f32x4 v0 = f32x4{0, 0, 0, 0}, v1 = f32x4{0, 0, 0, 0};
#pragma unroll
      for (int p = 0; p < 8; ++p) {
        v0 += *(const LAS f32x4*)(LAS const char*)(rp + p * 6144);
        v1 += *(const LAS f32x4*)(LAS const char*)(rp + p * 6144 + 256);
      }
      u32x4 r = __builtin_bit_cast(u32x4, rs);
      u32x4 ov = {pack2(v0[0] + bflo(r[0]), v0[1] + bfhi(r[0])), pack2(v0[2] + bflo(r[1]), v0[3] + bfhi(r[1])),
                  pack2(v1[0] + bflo(r[2]), v1[1] + bfhi(r[2])), pack2(v1[2] + bflo(r[3]), v1[3] + bfhi(r[3]))};
      st16<true>(out + ooff + vo, ov);
    }
  }
  asm volatile(".Lmega_end:");
}

extern "C" void kernel_launch(void* const* d_in, const int* in_sizes, int n_in, void* d_out, int out_size,
                              void* d_ws, size_t ws_size, hipStream_t stream) {
  const char* x = (const char*)d_in[0];
  const char* wqkv = (const char*)d_in[1];
  const char* wo = (const char*)d_in[2];
  const char* w1 = (const char*)d_in[4];
  const char* w3 = (const char*)d_in[5];
  const char* w2 = (const char*)d_in[6];
  char* ws = (char*)d_ws;
  (void)hipFuncSetAttribute((const void*)k_mega, hipFuncAttributeMaxDynamicSharedMemorySize, MG_LDS);
  k_mega<<<256, 512, MG_LDS, stream>>>(x, wqkv, wo, w1, w3, w2, ws, (char*)d_out);
}
```

```cpp
#include <hip/hip_runtime.h>
#include <hip/hip_bf16.h>
#include <stdint.h>

typedef short bf16x8 __attribute__((ext_vector_type(8)));
typedef short s16x4 __attribute__((ext_vector_type(4)));
typedef float f32x4 __attribute__((ext_vector_type(4)));
typedef float f32x2 __attribute__((ext_vector_type(2)));
typedef unsigned u32x4 __attribute__((ext_vector_type(4)));
typedef unsigned u32x2 __attribute__((ext_vector_type(2)));
typedef __bf16 bf16x2 __attribute__((ext_vector_type(2)));
#define LAS __attribute__((address_space(3)))
#define DEV __device__ __forceinline__

constexpr int S_ = 2048, D_ = 512, H_ = 32, F_ = 1792;
constexpr float LN_EPS = 1e-5f;

typedef float f32x16 __attribute__((ext_vector_type(16)));
DEV f32x16 mfma32(bf16x8 a, bf16x8 b, f32x16 c) { return __builtin_amdgcn_mfma_f32_32x32x16_bf16(a, b, c, 0, 0, 0); }
DEV f32x4 mfma16(bf16x8 a, bf16x8 b, f32x4 c) { return __builtin_amdgcn_mfma_f32_16x16x32_bf16(a, b, c, 0, 0, 0); }
DEV unsigned pack2(float a, float b) {
  f32x2 v = {a, b};
  return __builtin_bit_cast(unsigned, __builtin_convertvector(v, bf16x2));
}
DEV float bflo(unsigned u) { return __builtin_bit_cast(float, u << 16); }
DEV float bfhi(unsigned u) { return __builtin_bit_cast(float, u & 0xffff0000u); }
DEV float dot2(unsigned a, unsigned b, float c) {
  return __builtin_amdgcn_fdot2_f32_bf16(__builtin_bit_cast(bf16x2, a), __builtin_bit_cast(bf16x2, b), c, false);
}
DEV void dma16(const char* gptr, unsigned ldsbase) {
  unsigned sv;
  asm volatile("s_mov_b32 %0, m0\n\ts_mov_b32 m0, %2\n\ts_nop 0\n\tglobal_load_lds_dwordx4 %1, off\n\ts_mov_b32 m0, %0"
               : "=&s"(sv) : "v"(gptr), "s"(ldsbase) : "memory");
}
DEV void dma16s(unsigned voff, const char* sbase, unsigned ldsbase) {
  unsigned sv;
  asm volatile("s_mov_b32 %0, m0\n\ts_mov_b32 m0, %3\n\ts_nop 0\n\tglobal_load_lds_dwordx4 %1, %2\n\ts_mov_b32 m0, %0"
               : "=&s"(sv) : "v"(voff), "s"(sbase), "s"(ldsbase) : "memory");
}
DEV void dma16sm(unsigned voff, const char* sbase, unsigned ldsbase, unsigned long long mask) {
  unsigned sv;
  asm volatile("s_mov_b32 %0, m0\n\ts_mov_b32 m0, %3\n\ts_mov_b64 exec, %4\n\tglobal_load_lds_dwordx4 %1, %2\n\ts_mov_b64 exec, -1\n\ts_mov_b32 m0, %0"
               : "=&s"(sv) : "v"(voff), "s"(sbase), "s"(ldsbase), "s"(mask) : "memory");
}
DEV void gld16(bf16x8& d, unsigned voff, const char* sbase) {
  asm volatile("global_load_dwordx4 %0, %1, %2" : "=v"(d) : "v"(voff), "s"(sbase) : "memory");
}
template <int N> DEV void wait_bar() { asm volatile("s_waitcnt vmcnt(%0) lgkmcnt(0)\n\ts_barrier" ::"n"(N) : "memory"); }
DEV void bar() { asm volatile("s_waitcnt lgkmcnt(0)\n\ts_barrier" ::: "memory"); }
DEV bf16x8 lds128(const char* p) { return *(const LAS bf16x8*)(LAS const char*)p; }
DEV s16x4 ldstr(const char* p) { return __builtin_amdgcn_ds_read_tr16_b64_v4i16((LAS s16x4*)p); }
DEV bf16x8 cat(s16x4 a, s16x4 b) { return __builtin_shufflevector(a, b, 0, 1, 2, 3, 4, 5, 6, 7); }
DEV float xsum(float v) { v += __shfl_xor(v, 16); v += __shfl_xor(v, 32); return v; }
DEV float xmax(float v) { v = fmaxf(v, __shfl_xor(v, 16)); v = fmaxf(v, __shfl_xor(v, 32)); return v; }
DEV float dpp_xor1(float v) { return __builtin_bit_cast(float, __builtin_amdgcn_mov_dpp(__builtin_bit_cast(int, v), 0xB1, 0xF, 0xF, true)); }
DEV float dpp_xor2(float v) { return __builtin_bit_cast(float, __builtin_amdgcn_mov_dpp(__builtin_bit_cast(int, v), 0x4E, 0xF, 0xF, true)); }
DEV float dpp_ror4(float v) { return __builtin_bit_cast(float, __builtin_amdgcn_mov_dpp(__builtin_bit_cast(int, v), 0x124, 0xF, 0xF, true)); }
DEV float dpp_ror8(float v) { return __builtin_bit_cast(float, __builtin_amdgcn_mov_dpp(__builtin_bit_cast(int, v), 0x128, 0xF, 0xF, true)); }
DEV unsigned uni(unsigned v) { return __builtin_amdgcn_readfirstlane(v); }
#define WT 31
template <bool W> DEV void st16(char* p, u32x4 v) {
  if constexpr (W) asm volatile("global_store_dwordx4 %0, %1, off sc0 sc1\n\ts_nop 1" ::"v"(p), "v"(v) : "memory");
  else *(u32x4*)p = v;
}
template <bool W> DEV void st8(char* p, u32x2 v) {
  if constexpr (W) asm volatile("global_store_dwordx2 %0, %1, off sc0 sc1" ::"v"(p), "v"(v) : "memory");
  else *(u32x2*)p = v;
}

template <int A, int B> struct Min { static constexpr int v = A < B ? A : B; };

template <int KT, int NK, int NS, int P, class Issue, class Compute>
DEV void pipe_steps(Issue& issue, Compute& compute) {
  if constexpr (KT < NK) {
    wait_bar<(Min<KT + NS - 1, NK>::v - KT - 1) * P>();
    if constexpr (KT + NS - 1 < NK) issue((KT + NS - 1) % NS);
    compute(KT % NS, KT);
    pipe_steps<KT + 1, NK, NS, P>(issue, compute);
  }
}

constexpr int K1_STAGE = 16384 + 12288, K1_NS = 5;
#define SB __builtin_amdgcn_sched_barrier(0)
DEV float xsum2(float v) {
  auto a = __builtin_amdgcn_permlane16_swap(__float_as_uint(v), __float_as_uint(v), false, false);
  v = __uint_as_float(a[0]) + __uint_as_float(a[1]);
  auto b = __builtin_amdgcn_permlane32_swap(__float_as_uint(v), __float_as_uint(v), false, false);
  return __uint_as_float(b[0]) + __uint_as_float(b[1]);
}

DEV void qkv_phase(char* smem, unsigned lds0, const char* __restrict__ x, const char* __restrict__ w, char* __restrict__ qkv,
                   int cb, int tb) {
  const int tid = threadIdx.x, lane = tid & 63, wv = uni(tid >> 6);
  const int g = lane >> 4, i = lane & 15, q4 = (lane >> 2) & 3, p4 = lane & 3;
  const int tg = wv & 3, nh = wv >> 2;
  const int bi = blockIdx.x >> 3;

  const char* gx = x + (size_t)(tb * 128 + wv * 16) * 1024;
  const unsigned vt = (lane >> 3) * 1024 + (((lane & 7) ^ (lane >> 3)) << 4);
  const char* gw = w + cb * 192;
  unsigned vw0, vw1;
  {
    int qq = wv * 64 + lane;
    int k = qq / 12, c = (qq % 12) ^ (((k >> 3) & 1) << 1);
    vw0 = k * 3072 + c * 16;
    qq = 512 + wv * 32 + (lane & 31);
    k = qq / 12; c = (qq % 12) ^ (((k >> 3) & 1) << 1);
    vw1 = k * 3072 + c * 16;
  }
  const int k0 = bi & 7;
  auto dma = [&](int s, int j) {
    const int kk = (k0 + s) & 7;
    const unsigned base = lds0 + (s % K1_NS) * K1_STAGE;
    if (j == 0) dma16s(vt, gx + kk * 128, base + wv * 2048);
    else if (j == 1) dma16s(vt, gx + kk * 128 + 8192, base + wv * 2048 + 1024);
    else if (j == 2) dma16s(vw0, gw + kk * (64 * 3072), base + 16384 + wv * 1024);
    else dma16sm(vw1, gw + kk * (64 * 3072), base + 16384 + 8192 + wv * 512, 0xFFFFFFFFull);
  };
#pragma unroll
  for (int s = 0; s < K1_NS - 1; ++s)
#pragma unroll
    for (int j = 0; j < 4; ++j) dma(s, j);

  f32x4 acc[2][3], cs = f32x4{0, 0, 0, 0}, s1v = f32x4{0, 0, 0, 0};
#pragma unroll
  for (int a = 0; a < 3; ++a) { acc[0][a] = f32x4{0, 0, 0, 0}; acc[1][a] = f32x4{0, 0, 0, 0}; }
  float s2a = 0.f, s2b = 0.f;
  const bf16x8 ones = {0x3F80, 0x3F80, 0x3F80, 0x3F80, 0x3F80, 0x3F80, 0x3F80, 0x3F80};
  int toff[2][2];
#pragma unroll
  for (int tt = 0; tt < 2; ++tt) {
    toff[tt][0] = (tg * 32 + (tt ^ nh) * 16 + i) * 128 + ((g ^ (i & 7)) << 4);
    toff[tt][1] = (tg * 32 + (tt ^ nh) * 16 + i) * 128 + (((4 + g) ^ (i & 7)) << 4);
  }
  int woff[3], un[3];
#pragma unroll
  for (int nt = 0; nt < 3; ++nt) {
    int a = nt + tg; a = a >= 3 ? a - 3 : a; a = a >= 3 ? a - 3 : a;
    un[nt] = nh * 3 + a;
    woff[nt] = 16384 + (8 * g + q4) * 192 + ((un[nt] ^ (g & 1)) << 5) + p4 * 8;
  }
  auto ldB = [&](int tt, int kt, int ks) -> bf16x8 { return lds128(smem + (kt % K1_NS) * K1_STAGE + toff[tt][ks]); };
  auto ldA = [&](int nt, int kt, int ks) -> bf16x8 {
    const char* p = smem + (kt % K1_NS) * K1_STAGE + woff[nt] + ks * 6144;
    return cat(ldstr(p), ldstr(p + 768));
  };
  bf16x8 A0[3], A1[3], B0[2], B1[2];
  auto phase = [&](bf16x8 (&Ac)[3], bf16x8 (&Bc)[2], bf16x8 (&An)[3], bf16x8 (&Bn)[2], int kn, int ksn, int dk, int dj) {
    const u32x4 u = __builtin_bit_cast(u32x4, Bc[0]);
#pragma unroll
    for (int m = 0; m < 8; ++m) {
      if (kn < 8) {
        if (m == 0) { Bn[0] = ldB(0, kn, ksn); An[0] = ldA(0, kn, ksn); }
        else if (m == 1) Bn[1] = ldB(1, kn, ksn);
        else if (m < 4) An[m - 1] = ldA(m - 1, kn, ksn);
      }
      if (m < 6) acc[m & 1][m >> 1] = mfma16(Ac[m >> 1], Bc[m & 1], acc[m & 1][m >> 1]);
      else if (m == 6) cs = mfma16(Ac[0], ones, cs);
      else s1v = mfma16(ones, Bc[0], s1v);
      {
        float f = (m & 1) ? bfhi(u[m >> 1]) : bflo(u[m >> 1]);
        asm volatile("" : "+v"(f));
        if (m & 1) s2b = __builtin_fmaf(f, f, s2b); else s2a = __builtin_fmaf(f, f, s2a);
      }
      if (dk < 8 && (m == 2 || m == 5)) dma(dk, dj + (m == 5));
      SB;
    }
  };
  wait_bar<12>();
  B0[0] = ldB(0, 0, 0); A0[0] = ldA(0, 0, 0); B0[1] = ldB(1, 0, 0); A0[1] = ldA(1, 0, 0); A0[2] = ldA(2, 0, 0);
#pragma unroll
  for (int kt = 0; kt < 8; ++kt) {
    phase(A0, B0, A1, B1, kt, 1, kt + 4, 0);
    if (kt < 4) wait_bar<10>();
    else if (kt == 4) wait_bar<8>();
    else if (kt == 5) wait_bar<4>();
    else if (kt == 6) wait_bar<0>();
    phase(A1, B1, A0, B0, kt + 1, 0, kt + 4, 2);
  }

  float* st = (float*)(smem + 49152);
  float* csp = (float*)(smem + 49152 + 1024);
  const float s2 = xsum2(s2a + s2b);
  if (g == 0) {
    float mean = s1v[0] * (1.f / 512.f);
    float var = fmaxf(s2 * (1.f / 512.f) - mean * mean, 0.f);
    float rstd = rsqrtf(var + LN_EPS);
    *(f32x2*)(st + (tg * 32 + nh * 16 + i) * 2) = f32x2{-mean * rstd, rstd};
  }
  if (i == 0 && tg < 3) *(f32x4*)(csp + un[0] * 16 + 4 * g) = cs;
  bar();
  f32x2 ms[2];
#pragma unroll
  for (int tt = 0; tt < 2; ++tt) ms[tt] = *(const f32x2*)(st + (tg * 32 + (tt ^ nh) * 16 + i) * 2);
  const int otile = (g & 1) ^ nh, ohalf = g >> 1;
  const unsigned vo = ohalf * 512 + (((otile * 16 + i) ^ (8 * ohalf)) << 4);
#pragma unroll
  for (int nt = 0; nt < 3; ++nt) {
    f32x4 c = *(const f32x4*)(csp + un[nt] * 16 + 4 * g);
    const float qs = cb * 6 + un[nt] < 32 ? 0.25f * 1.4426950408889634f : 1.f;
    unsigned o[2][2];
#pragma unroll
    for (int tt = 0; tt < 2; ++tt) {
      f32x4 y = acc[tt][nt] * (ms[tt][1] * qs) + (ms[tt][0] * qs) * c;
      o[tt][0] = pack2(y[0], y[1]); o[tt][1] = pack2(y[2], y[3]);
    }
    auto sx = __builtin_amdgcn_permlane16_swap(o[0][0], o[1][0], false, false);
    auto sy = __builtin_amdgcn_permlane16_swap(o[0][1], o[1][1], false, false);
    char* dst = qkv + (size_t)(cb * 6 + un[nt]) * (2048 * 32) + (size_t)(tb * 4 + tg) * 1024;
    st16<true>(dst + vo, u32x4{sx[0], sy[0], sx[1], sy[1]});
  }
}

constexpr int S0 = 0, S1 = 49152, S2 = 98304, CSP = 147456, MG_LDS = 163840;
typedef unsigned long long u64;
extern "C" __device__ u64 llvm_dispatch_id() __asm("llvm.amdgcn.dispatch.id");
DEV void flag_set(u64* f, u64 tag) {
  asm volatile("global_store_dwordx2 %0, %1, off sc0 sc1" ::"v"(f), "v"(tag) : "memory");
}
DEV void flag_wait(const u64* p, u64 tag) {
  for (int it = 0; it < (1 << 20); ++it) {
    u64 c;
    asm volatile("global_load_dwordx2 %0, %1, off sc0 sc1\n\ts_waitcnt vmcnt(0)" : "=v"(c) : "v"(p) : "memory");
    if (__builtin_amdgcn_ballot_w64(c != tag) == 0) break;
  }
}
template <int V> struct IC { static constexpr int value = V; };
DEV int opaque(int v) { asm volatile("" : "+v"(v)); return v; }
DEV int opaque_s(int v) { asm volatile("" : "+s"(v)); return v; }

struct CW {
  const char *g1, *g3; unsigned vw; unsigned long long wmask;
};
DEV CW c_setup(const char* w1, const char* w3, int b, int wv, int lane) {
  const int xcd = b & 7, bi = b >> 3;
  const int cb = (xcd & 3) * 4 + (bi & 3);
  CW c;
  c.g1 = w1 + (size_t)(wv * 8) * (F_ * 2) + cb * 224;
  c.g3 = w3 + (size_t)(wv * 8) * (F_ * 2) + cb * 224;
  const int unit = ((lane >> 1) & 7) ^ ((lane >> 4) | ((wv & 1) << 2));
  c.vw = (lane >> 4) * (F_ * 2) + unit * 32 + (lane & 1) * 16;
  c.wmask = __builtin_amdgcn_ballot_w64(unit < 7);
  return c;
}
DEV void c_dmaw(const CW& c, unsigned ldsw, int kt, int j) {
  const unsigned base = ldsw + (kt % 3) * 49152;
  if (j < 2) dma16sm(c.vw, c.g1 + (size_t)(kt * 64 + j * 4) * (F_ * 2), base + 16384 + j * 1024, c.wmask);
  else dma16sm(c.vw, c.g3 + (size_t)(kt * 64 + (j - 2) * 4) * (F_ * 2), base + 32768 + (j - 2) * 1024, c.wmask);
}
DEV constexpr int d_unit(int u) {
  return u < 3 ? S2 + u * 2048 : u < 6 ? S0 + (u - 3) * 2048 : u < 9 ? S1 + (u - 6) * 2048 : u == 9 ? CSP
       : u < 13 ? S2 + (u - 10) * 2048 : S0;
}
DEV constexpr int d_ustr(int u) { return u == 9 ? 2048 : 6144; }
DEV void d_dma(const char* gw2, unsigned vw, unsigned lds0, int wv, int u, int q) {
  dma16s(vw, gw2 + (size_t)(u * 16 + q * 8) * 1024, lds0 + d_unit(u) + wv * d_ustr(u) + q * 1024);
}

template <int NT>
DEV void c_body(char* smem, unsigned lds0, const char* gt, const CW& cw, unsigned vt, int wv, int lane,
                const char* __restrict__ stats, const char* __restrict__ csb, char* __restrict__ hh, int tok0, int cb, const char* gw2, unsigned vwd) {
  const int tg = wv & 3;
  const unsigned ldsw = lds0 + wv * 2048;
  const int g = lane >> 4, i = lane & 15, q4 = (lane >> 2) & 3, p4 = lane & 3;
  f32x4 acc[2][2 * NT];
#pragma unroll
  for (int a = 0; a < 2 * NT; ++a) { acc[0][a] = f32x4{0, 0, 0, 0}; acc[1][a] = f32x4{0, 0, 0, 0}; }
  const int toff0 = (tg * 32 + i) * 128 + ((g ^ (i & 7)) << 4);
  const int toff1 = (tg * 32 + i) * 128 + (((4 + g) ^ (i & 7)) << 4);
  const int odd = cb & 1;
  const int fsw = q4 | ((g & 1) << 2);
  int T[NT], wa[NT];
#pragma unroll
  for (int a = 0; a < NT; ++a) {
    T[a] = NT == 4 ? odd * 3 + a : (a < 2 ? (odd ? 1 : 4) + a : (odd ? 0 : 6));
    wa[a] = 16384 + (8 * g + q4) * 256 + ((T[a] ^ fsw) << 5) + p4 * 8;
  }
  auto dmat = [&](int kt, int j) { dma16s(vt, gt + kt * 128 + j * 8192, ldsw + (kt % 3) * 49152 + j * 1024); };
  auto dma = [&](int kt, int j) {
    if (j < 2) dmat(kt, j); else c_dmaw(cw, ldsw, kt, j - 2);
  };
  auto ldB = [&](bf16x8* B, int kt, int ks) {
    const char* sb = smem + (kt % 3) * 49152 + (ks ? toff1 : toff0);
    B[0] = lds128(sb); B[1] = lds128(sb + 2048);
  };
  auto ldA = [&](int a, int kt, int ks) -> bf16x8 {
    const char* wp = smem + (kt % 3) * 49152 + wa[a < NT ? a : a - NT] + (a < NT ? 0 : 16384) + ks * 8192;
    return cat(ldstr(wp), ldstr(wp + 1024));
  };
  auto mma = [&](int a, bf16x8 A, const bf16x8* B) {
    acc[0][a] = mfma16(A, B[0], acc[0][a]);
    acc[1][a] = mfma16(A, B[1], acc[1][a]);
  };
  bf16x8 A0[2 * NT], A1[2 * NT], B0[2], B1[2];
  bf16x8 sa[2], cp[4];
#pragma unroll
  for (int kt = 0; kt < 3; ++kt) { dmat(kt, 0); dmat(kt, 1); }
  gld16(sa[0], (unsigned)(i * 64 + g * 16), stats + (size_t)(tok0 + tg * 32) * 64);
  gld16(sa[1], (unsigned)(i * 64 + g * 16), stats + (size_t)(tok0 + tg * 32 + 16) * 64);
  const int cq = min(tg * 16 + (lane >> 2), 55);
  if (NT == 3) {
#pragma unroll
    for (int m = 0; m < 4; ++m) gld16(cp[m], (unsigned)(((lane & 3) * 4 + m) * 896 + cq * 16), csb + (size_t)cb * 14336);
  }
  constexpr int X = NT == 3 ? 6 : 2;
  wait_bar<4 + X>();
  float rs[2], mr[2];
  float* csp = (float*)(smem + CSP);
  ldB(B0, 0, 0);
#pragma unroll
  for (int a = 0; a < 2 * NT; ++a) A0[a] = ldA(a, 0, 0);
#pragma unroll
  for (int kt = 0; kt < 8; ++kt) {
    ldB(B1, kt, 1);
#pragma unroll
    for (int a = 0; a < 2 * NT; ++a) {
      if (a < NT) { A1[2 * a] = ldA(2 * a, kt, 1); A1[2 * a + 1] = ldA(2 * a + 1, kt, 1); }
      mma(a, A0[a], B0);
      __builtin_amdgcn_sched_barrier(0);
    }
    if (kt == 0) wait_bar<2 + X>();
    else if (kt != 1) wait_bar<6>();
    else if (NT == 3) asm volatile("s_waitcnt vmcnt(6) lgkmcnt(0)\n\ts_barrier" : "+v"(sa[0]), "+v"(sa[1]), "+v"(cp[0]), "+v"(cp[1]), "+v"(cp[2]), "+v"(cp[3]) : : "memory");
    else asm volatile("s_waitcnt vmcnt(6) lgkmcnt(0)\n\ts_barrier" : "+v"(sa[0]), "+v"(sa[1]) : : "memory");
    if (kt < 7) ldB(B0, kt + 1, 0);
    if (kt == 1) {
#pragma unroll
      for (int tt = 0; tt < 2; ++tt) {
        const f32x4 sv = __builtin_bit_cast(f32x4, sa[tt]);
        const float s1 = xsum2(sv[0] + sv[2]), s2 = xsum2(sv[1] + sv[3]);
        const float mean = s1 * (1.f / 512.f);
        const float var = fmaxf(s2 * (1.f / 512.f) - mean * mean, 0.f);
        rs[tt] = rsqrtf(var + LN_EPS); mr[tt] = mean * rs[tt];
      }
      if (NT == 3) {
        f32x4 c = (__builtin_bit_cast(f32x4, cp[0]) + __builtin_bit_cast(f32x4, cp[1])) +
                  (__builtin_bit_cast(f32x4, cp[2]) + __builtin_bit_cast(f32x4, cp[3]));
#pragma unroll
        for (int e = 0; e < 4; ++e) { c[e] += dpp_xor1(c[e]); c[e] += dpp_xor2(c[e]); }
        if ((lane & 3) == 0) *(f32x4*)(csp + cq * 4) = c;
      }
    }
#pragma unroll
    for (int a = 0; a < 2 * NT; ++a) {
      if (kt < 7 && a < NT) { A0[2 * a] = ldA(2 * a, kt + 1, 0); A0[2 * a + 1] = ldA(2 * a + 1, kt + 1, 0); }
      mma(a, A1[a], B1);
      if (a >= 2 * NT - 6) {
        const int j = a - (2 * NT - 6);
        if (kt + 3 < 8) dma(kt + 3, j);
        else d_dma(gw2, vwd, lds0, wv, (kt - 5) * 3 + (j >> 1), j & 1);
      }
      __builtin_amdgcn_sched_barrier(0);
    }
  }
  unsigned pk[2][NT][2];
#pragma unroll
  for (int tt = 0; tt < 2; ++tt) {
    char* dst = hh + ((size_t)((tok0 >> 4) + tg * 2 + tt) * 56) * 1024 + lane * 16;
#pragma unroll
    for (int a = 0; a < NT; ++a) {
      f32x4 cg = *(const f32x4*)(csp + T[a] * 16 + 4 * g);
      f32x4 cu = *(const f32x4*)(csp + (7 + T[a]) * 16 + 4 * g);
      float r[4];
#pragma unroll
      for (int e = 0; e < 4; ++e) {
        float ga = acc[tt][a][e] * rs[tt] - mr[tt] * cg[e];
        float up = acc[tt][NT + a][e] * rs[tt] - mr[tt] * cu[e];
        float sg = __builtin_amdgcn_rcpf(1.f + __builtin_amdgcn_exp2f(-1.4426950408889634f * ga));
        r[e] = ga * sg * up;
      }
      pk[tt][a][0] = pack2(r[0], r[1]); pk[tt][a][1] = pack2(r[2], r[3]);
      const int G = cb * 7 + T[a];
      if (a & 1) {
        st16<true>(dst + (size_t)(G >> 1) * 1024, u32x4{pk[tt][a - 1][0], pk[tt][a - 1][1], pk[tt][a][0], pk[tt][a][1]});
      } else if (a == NT - 1) {
        st8<true>(dst + (size_t)(G >> 1) * 1024 + (G & 1) * 8, u32x2{pk[tt][a][0], pk[tt][a][1]});
      }
    }
  }
}

__global__ __launch_bounds__(512) void k_mega(const char* __restrict__ x, const char* __restrict__ wqkv, const char* __restrict__ wo,
                                              const char* __restrict__ w1, const char* __restrict__ w3,
                                              const char* __restrict__ w2, char* __restrict__ ws, char* __restrict__ out) {
  extern __shared__ __attribute__((aligned(16))) char smem[];
  char* qkv = ws;
  char* o = ws + (6u << 20);
  char* h = ws + (8u << 20);
  char* stats = ws + (10u << 20);
  u64* flags = (u64*)(ws + (10u << 20) + (512u << 10));
  char* csb = ws + (10u << 20) + (768u << 10);
  char* hh = ws + (11u << 20);
  const u64 tag = ((u64)(size_t)__builtin_amdgcn_dispatch_ptr() << 20) + llvm_dispatch_id();
  const int b = blockIdx.x;
  const unsigned lds0 = (unsigned)(size_t)(LAS char*)smem;
  const int cbG = (b >> 3) & 7, tbG = (b & 7) * 4 + (b >> 6);
  const int cbC = (b & 3) * 4 + ((b >> 3) & 3), tbC = ((b >> 2) & 1) * 8 + (b >> 5);

  unsigned cpf = 0;
  {
    u64 pc;
    unsigned len;
    asm volatile("s_getpc_b64 %0\n.Lmega_pc:\n\ts_mov_b32 %1, .Lmega_end-.Lmega_pc" : "=s"(pc), "=s"(len));
    const unsigned off = ((b >> 3) * 20 + threadIdx.x) * 64;
    if (threadIdx.x < 20 && off < len) asm volatile("global_load_dword %0, %1, %2" : "=v"(cpf) : "v"(off), "s"(pc) : "memory");
  }
  {
    const int cb = (b & 1) * 8 + ((b >> 3) & 7), tb = ((b & 7) >> 1) * 4 + (b >> 6);
    qkv_phase(smem, lds0, x, wqkv, qkv, cb, tb);
    asm volatile("s_waitcnt vmcnt(0) lgkmcnt(0)\n\ts_barrier" ::: "memory");
    asm volatile("" :: "v"(cpf));
    const int tid = threadIdx.x;
    if (tid < 9) {
      const int P = cb * 3 + tid / 3, d = tid % 3, which = P >> 4, hp = P & 15, qb = tb + d;
      if (which ? qb < 16 : d == 0)
        flag_set(flags + 24576 + ((hp >> 1) | ((hp & 1) << 3) | (qb << 4)) * 32 + (which ? (which - 1) * 3 + d : 6), tag);
    }
  }
  {
    const int tid = opaque(threadIdx.x), lane = tid & 63, wv = uni(tid >> 6);
    const int c32 = lane & 31, hf = lane >> 5, q4 = (lane >> 2) & 3, p4 = lane & 3;
    const int hp = (b & 7) * 2 + ((b >> 3) & 1), qb = b >> 4;
    const int q0 = qb * 128;
    const size_t HS = 2048 * 32;
    bf16x8 Q, cw0, cw1;
    {
      const unsigned one = qb * 4 - 8 + wv >= 0 ? 0x3F803F80u : 0u;
      *(LAS u32x4*)(LAS char*)(smem + S1 + tid * 16) = u32x4{one, one, one, one};
      if (tid < 256) *(LAS u32x4*)(LAS char*)(smem + S1 + 8192 + tid * 16) = u32x4{0x3F803F80u, 0x3F803F80u, 0x3F803F80u, 0x3F803F80u};
    }
    if (wv == 0) {
      const int d = lane % 3;
      flag_wait(flags + 24576 + b * 32 + (lane >= 6 ? 6 : qb - d < 0 ? 6 : lane), tag);
    }
    asm volatile("s_barrier" ::: "memory");
    {
      const int r = wv >> 1;
      const char* src = qkv + (size_t)((1 + (r >> 1)) * 32 + hp * 2 + (r & 1)) * HS;
#pragma unroll
      for (int j = 0; j < 6; ++j) {
        const int kb = qb * 4 - 8 + (wv & 1) * 6 + j;
        if (kb < 0 && r >= 2) *(LAS u32x4*)(LAS char*)(smem + r * 12288 + ((wv & 1) * 6 + j) * 1024 + lane * 16) = u32x4{0, 0, 0, 0};
        else dma16s(kb < 0 ? hf * 640 : lane * 16, src + (size_t)max(kb, 0) * 1024, lds0 + r * 12288 + ((wv & 1) * 6 + j) * 1024);
      }
    }
    const int u = wv >> 2, qg = wv & 3;
    const int qs = q0 + qg * 32;
    const unsigned vq = hf * 512 + ((c32 ^ (8 * hf)) << 4);
    gld16(Q, vq, qkv + (size_t)(hp * 2 + u) * HS + (size_t)qs * 32);
    const float NEG = -1e30f;
    f32x16 cin0, cin8;
    u32x4 mk[2];
#pragma unroll
    for (int r = 0; r < 16; ++r) {
      const int kl = (r & 3) + 8 * (r >> 2) + 4 * hf;
      cin0[r] = kl > c32 ? 0.f : NEG; cin8[r] = kl <= c32 ? 0.f : NEG;
      if (r & 1) mk[r >> 3][(r >> 1) & 3] = (kl > c32 ? 0xFFFF0000u : 0u) | (kl - 1 > c32 ? 0xFFFFu : 0u);
    }
    asm volatile("s_waitcnt vmcnt(0) lgkmcnt(0)\n\ts_barrier" : "+v"(Q), "+v"(cin0), "+v"(cin8), "+v"(mk[0]), "+v"(mk[1]) : : "memory");
    const unsigned vwo = (lane >> 3) * 1024 + ((((lane & 7) >> 1) ^ ((lane >> 4) & 3)) << 5) + (lane & 1) * 16;
    const char* gwo = wo + (size_t)(wv * 64) * 1024 + cbG * 128;
    const CW cw1s = c_setup(w1, w3, b, wv, lane);
    auto pf = [&](int k) {
      if (k < 2) {
        const int ch = max(wv - 1, 0) * 4 + (lane & 3);
        const char* pc = (ch >= 14 ? w3 + (ch - 14) * 16 : w1 + ch * 16) + (size_t)(tbC * 32 + (lane >> 2)) * (F_ * 2) + cbC * 224;
        if (k == 0) asm volatile("global_load_dwordx4 %0, %1, off" : "=v"(cw0) : "v"(pc) : "memory");
        else asm volatile("global_load_dwordx4 %0, %1, off" : "=v"(cw1) : "v"(pc + 16 * (F_ * 2)) : "memory");
      } else if (k < 10) dma16s(vwo, gwo + (size_t)((k - 2) * 8) * 1024, lds0 + S2 + wv * 8192 + (k - 2) * 1024);
      else if (k < 14) c_dmaw(cw1s, lds0 + wv * 2048, 1, k - 10);
      else if (k == 14) {
        const int c0 = b * 8 + wv;
        dma16s(lane * 16, w2 + (size_t)(c0 < 1792 ? c0 : c0 - 1792) * 1024, lds0 + S1 + 12288 + (wv & 3) * 1024);
      }
    };
    {
      const char* Kl = smem + u * 12288 + qg * 1024 + vq;
      const char* Vl[2];
#pragma unroll
      for (int e = 0; e < 2; ++e)
        Vl[e] = c32 < 16 ? smem + 24576 + u * 12288 + qg * 1024 + (p4 >> 1) * 512 + (((e * 8 + 4 * hf + q4) ^ (8 * (p4 >> 1))) << 4) + (p4 & 1) * 8
                         : smem + S1 + qg * 1024 + (hf ? 0 : 64);
      f32x16 z;
#pragma unroll
      for (int r = 0; r < 16; ++r) z[r] = 0.f;
      bf16x8 Kf[9];
#pragma unroll
      for (int t = 0; t < 9; ++t) Kf[t] = lds128(Kl + t * 1024);
      f32x16 sm = mfma32(Kf[0], Q, cin0);
      {
        const f32x16 s8 = mfma32(Kf[8], Q, cin8);
#pragma unroll
        for (int r = 0; r < 16; ++r) sm[r] = fmaxf(sm[r], s8[r]);
      }
      float m = sm[0];
#pragma unroll
      for (int r = 1; r < 16; ++r) m = fmaxf(m, sm[r]);
#pragma unroll
      for (int t = 1; t < 8; ++t) {
        const f32x16 s = mfma32(Kf[t], Q, z);
#pragma unroll
        for (int r = 0; r < 16; ++r) m = fmaxf(m, s[r]);
      }
      {
        auto sw = __builtin_amdgcn_permlane32_swap(__float_as_uint(m), __float_as_uint(m), false, false);
        m = fmaxf(__uint_as_float(sw[0]), __uint_as_float(sw[1]));
      }
      f32x16 cm, oacc;
#pragma unroll
      for (int r = 0; r < 16; ++r) { cm[r] = -m; oacc[r] = 0.f; }
      f32x16 sn = mfma32(Kf[1], Q, cm);
      {
        u32x4 pb[2];
#pragma unroll
        for (int j = 0; j < 8; ++j)
          pb[j >> 2][j & 3] = pack2(__builtin_amdgcn_exp2f(sm[2 * j] - m), __builtin_amdgcn_exp2f(sm[2 * j + 1] - m));
#pragma unroll
        for (int sh = 0; sh < 2; ++sh) {
          const u32x4 p0 = pb[sh] & mk[sh], p8 = pb[sh] ^ p0;
          oacc = mfma32(cat(ldstr(Vl[0] + sh * 256), ldstr(Vl[1] + sh * 256)), __builtin_bit_cast(bf16x8, p0), oacc);
          pf(2 * sh);
          oacc = mfma32(cat(ldstr(Vl[0] + 8192 + sh * 256), ldstr(Vl[1] + 8192 + sh * 256)), __builtin_bit_cast(bf16x8, p8), oacc);
          pf(2 * sh + 1);
        }
      }
      SB;
#pragma unroll
      for (int t = 1; t < 8; ++t) {
        const f32x16 s = sn;
        if (t < 7) sn = mfma32(Kf[t + 1], Q, cm);
#pragma unroll
        for (int sh = 0; sh < 2; ++sh) {
          u32x4 pb;
#pragma unroll
          for (int e = 0; e < 4; ++e)
            pb[e] = pack2(__builtin_amdgcn_exp2f(s[8 * sh + 2 * e]), __builtin_amdgcn_exp2f(s[8 * sh + 2 * e + 1]));
          const int vp = t * 1024 + sh * 256;
          oacc = mfma32(cat(ldstr(Vl[0] + vp), ldstr(Vl[1] + vp)), __builtin_bit_cast(bf16x8, pb), oacc);
          if (2 * t + sh + 2 < 15) pf(2 * t + sh + 2);
        }
        SB;
      }
      const float inv = __builtin_amdgcn_rcpf(oacc[8]);
      const u32x4 ov = {pack2(oacc[0] * inv, oacc[1] * inv), pack2(oacc[2] * inv, oacc[3] * inv),
                        pack2(oacc[4] * inv, oacc[5] * inv), pack2(oacc[6] * inv, oacc[7] * inv)};
      st16<true>(o + ((size_t)((qs >> 4) + (c32 >> 4)) * 16 + hp) * 1024 + ((2 * u + hf) * 16 + (c32 & 15)) * 16, ov);
    }
    asm volatile("s_waitcnt vmcnt(0) lgkmcnt(0)\n\ts_barrier" : "+v"(cw0), "+v"(cw1) : : "memory");
    if (tid < 16) flag_set(flags + (((qb >> 1) | (tid << 3) | ((qb & 1) << 7)) * 32 + hp), tag);
    {
      const CW cw = c_setup(w1, w3, b, wv, lane);
#pragma unroll
      for (int j = 0; j < 4; ++j) c_dmaw(cw, lds0 + wv * 2048, 0, j);
    }
    if (wv == 0) { flag_wait(flags + b * 32 + (lane & 15), tag); }
    else {
      const u32x4 ua = __builtin_bit_cast(u32x4, cw0), ub = __builtin_bit_cast(u32x4, cw1);
      f32x4 lo, hi;
#pragma unroll
      for (int e = 0; e < 2; ++e) {
        lo[2 * e] = bflo(ua[e]) + bflo(ub[e]); lo[2 * e + 1] = bfhi(ua[e]) + bfhi(ub[e]);
        hi[2 * e] = bflo(ua[2 + e]) + bflo(ub[2 + e]); hi[2 * e + 1] = bfhi(ua[2 + e]) + bfhi(ub[2 + e]);
      }
#pragma unroll
      for (int e = 0; e < 4; ++e) {
        lo[e] += dpp_ror4(lo[e]); lo[e] += dpp_ror8(lo[e]); lo[e] = xsum2(lo[e]);
        hi[e] += dpp_ror4(hi[e]); hi[e] += dpp_ror8(hi[e]); hi[e] = xsum2(hi[e]);
      }
      if (lane < 4) {
        char* d = csb + (size_t)(cbC * 16 + tbC) * 896 + ((wv - 1) * 4 + lane) * 32;
        st16<true>(d, __builtin_bit_cast(u32x4, lo));
        st16<true>(d + 16, __builtin_bit_cast(u32x4, hi));
      }
      asm volatile("s_waitcnt vmcnt(0)" ::: "memory");
    }
    asm volatile("s_barrier" ::: "memory");
    if (tid >= 64 && tid < 80) flag_set(flags + 8192 + ((cbC >> 2) | ((tid & 1) << 2) | ((cbC & 3) << 3) | (((tid & 15) >> 1) << 5)) * 32 + 16 + tbC, tag);
  }
  {
    const int tid = opaque(threadIdx.x), lane = tid & 63, wv = uni(tid >> 6);
    const int g = lane >> 4, i = lane & 15, q4 = (lane >> 2) & 3, p4 = lane & 3;
    const int kp = wv & 3, th = wv >> 2;
    bf16x8 B[4][2], rs;
    const char* ga = o + ((size_t)(tbG * 4 + th * 2) * 16 + kp * 4) * 1024;
#pragma unroll
    for (int s = 0; s < 4; ++s) {
      gld16(B[s][0], lane * 16, ga + s * 1024);
      gld16(B[s][1], lane * 16, ga + (16 + s) * 1024);
    }
    const size_t ooff = (size_t)(tbG * 64 + wv * 8) * 1024 + cbG * 128;
    const unsigned vo = (lane >> 3) * 1024 + (lane & 7) * 16;
    gld16(rs, vo, x + ooff);
    const int sw = (2 * (g & 1) + (q4 >> 1)) & 3;
    const char* wr = smem + S2 + kp * 16384 + (16 * (g >> 1) + 4 * (g & 1) + q4) * 128 + p4 * 8;
    bf16x8 A[4][4];
#pragma unroll
    for (int s = 0; s < 4; ++s)
#pragma unroll
      for (int nt = 0; nt < 4; ++nt) {
        const char* p = wr + s * 4096 + ((nt ^ sw) << 5);
        A[s][nt] = cat(ldstr(p), ldstr(p + 1024));
      }
    f32x4 acc[2][4];
#pragma unroll
    for (int t = 0; t < 2; ++t)
#pragma unroll
      for (int n = 0; n < 4; ++n) acc[t][n] = f32x4{0, 0, 0, 0};
    asm volatile("s_waitcnt vmcnt(7)" : "+v"(B[0][0]), "+v"(B[0][1]) : : "memory");
#pragma unroll
    for (int nt = 0; nt < 4; ++nt) { acc[0][nt] = mfma16(A[0][nt], B[0][0], acc[0][nt]); acc[1][nt] = mfma16(A[0][nt], B[0][1], acc[1][nt]); }
    asm volatile("s_waitcnt vmcnt(5)" : "+v"(B[1][0]), "+v"(B[1][1]) : : "memory");
#pragma unroll
    for (int nt = 0; nt < 4; ++nt) { acc[0][nt] = mfma16(A[1][nt], B[1][0], acc[0][nt]); acc[1][nt] = mfma16(A[1][nt], B[1][1], acc[1][nt]); }
    asm volatile("s_waitcnt vmcnt(3)" : "+v"(B[2][0]), "+v"(B[2][1]) : : "memory");
#pragma unroll
    for (int nt = 0; nt < 4; ++nt) { acc[0][nt] = mfma16(A[2][nt], B[2][0], acc[0][nt]); acc[1][nt] = mfma16(A[2][nt], B[2][1], acc[1][nt]); }
    asm volatile("s_waitcnt vmcnt(1)" : "+v"(B[3][0]), "+v"(B[3][1]) : : "memory");
#pragma unroll
    for (int nt = 0; nt < 4; ++nt) { acc[0][nt] = mfma16(A[3][nt], B[3][0], acc[0][nt]); acc[1][nt] = mfma16(A[3][nt], B[3][1], acc[1][nt]); }
    bar();
    {
      const int mo = S2 + kp * 16384 + g * 256 + ((i ^ ((g >> 1) * 2)) << 4);
#pragma unroll
      for (int tt = 0; tt < 2; ++tt)
#pragma unroll
        for (int nt = 0; nt < 4; ++nt)
          *(LAS f32x4*)(LAS char*)(smem + (mo ^ (nt * 64)) + ((th * 2 + tt) * 4 + nt) * 1024) = acc[tt][nt];
    }
    asm volatile("s_waitcnt vmcnt(0) lgkmcnt(0)\n\ts_barrier" : "+v"(rs) : : "memory");
    {
      const int c = lane & 7;
      const int tok = (lane >> 3) + 8 * (wv & 1);
      const char* rp = smem + S2 + ((wv >> 1) * 4 + (c >> 1)) * 1024 + (c & 1) * 512 + ((tok ^ (2 * c)) << 4);
      f32x4 v0 = f32x4{0, 0, 0, 0}, v1 = f32x4{0, 0, 0, 0};
#pragma unroll
      for (int p = 0; p < 4; ++p) {
        v0 += *(const LAS f32x4*)(LAS const char*)(rp + p * 16384);
        v1 += *(const LAS f32x4*)(LAS const char*)(rp + p * 16384 + 256);
      }
      u32x4 r = __builtin_bit_cast(u32x4, rs);
      u32x4 ov = {pack2(v0[0] + bflo(r[0]), v0[1] + bfhi(r[0])), pack2(v0[2] + bflo(r[1]), v0[3] + bfhi(r[1])),
                  pack2(v1[0] + bflo(r[2]), v1[1] + bfhi(r[2])), pack2(v1[2] + bflo(r[3]), v1[3] + bfhi(r[3]))};
      st16<true>(h + ooff + vo, ov);
      float s1 = 0.f, s2 = 0.f;
#pragma unroll
      for (int e = 0; e < 4; ++e) { s1 = dot2(ov[e], 0x3F803F80u, s1); s2 = dot2(ov[e], ov[e], s2); }
#pragma unroll
      for (int m = 1; m < 8; m <<= 1) { s1 += __shfl_xor(s1, m); s2 += __shfl_xor(s2, m); }
      if (c == 0) st8<true>(stats + ((size_t)(tbG * 64 + wv * 8 + (lane >> 3)) * 8 + cbG) * 8,
                            u32x2{__float_as_uint(s1), __float_as_uint(s2)});
    }
    asm volatile("s_waitcnt vmcnt(0) lgkmcnt(0)\n\ts_barrier" ::: "memory");
    if (tid < 16) flag_set(flags + 8192 + ((tid & 3) | ((tbG >> 4) << 2) | ((tid >> 2) << 3) | (((tbG >> 1) & 7) << 5)) * 32 + (tbG & 1) * 8 + cbG, tag);
    {
      const CW cw = c_setup(w1, w3, b, wv, lane);
#pragma unroll
      for (int j = 0; j < 4; ++j) c_dmaw(cw, lds0 + wv * 2048, 2, j);
    }
    if (wv == 0) flag_wait(flags + 8192 + b * 32 + (lane & 31), tag);
    asm volatile("s_barrier" ::: "memory");
  }
  const unsigned vwd_ = 0;
  {
    const int tid = opaque(threadIdx.x), lane = tid & 63, wv = uni(tid >> 6);
    const char* gt = h + (size_t)(tbC * 128 + wv * 16) * 1024;
    const unsigned vt = (lane >> 3) * 1024 + (((lane & 7) ^ (lane >> 3)) << 4);
    const CW cw = c_setup(w1, w3, b, wv, lane);
    const unsigned vwd = (lane >> 3) * 1024 + ((((lane & 7) >> 1) ^ ((lane >> 4) & 3)) << 5) + (lane & 1) * 16;
    const char* gw2 = w2 + (size_t)(wv * 224) * 1024 + cbG * 128;
    if ((wv >> 2) == 0) c_body<4>(smem, lds0, gt, cw, vt, wv, lane, stats, csb, hh, tbC * 128, cbC, gw2, vwd);
    else c_body<3>(smem, lds0, gt, cw, vt, wv, lane, stats, csb, hh, tbC * 128, cbC, gw2, vwd);
  }
  {
    const int tid = opaque(threadIdx.x), lane = tid & 63, wv = uni(tid >> 6);
    const int g = lane >> 4, i = lane & 15, q4 = (lane >> 2) & 3, p4 = lane & 3;
    asm volatile("s_waitcnt vmcnt(0) lgkmcnt(0)\n\ts_barrier" ::: "memory");
    if (tid < 16) flag_set(flags + 16384 + ((tbC >> 1) | (tid << 3) | ((tbC & 1) << 7)) * 32 + cbC, tag);
    const unsigned vwd = (lane >> 3) * 1024 + ((((lane & 7) >> 1) ^ ((lane >> 4) & 3)) << 5) + (lane & 1) * 16;
    const char* gw2 = w2 + (size_t)(wv * 224) * 1024 + cbG * 128;
    d_dma(gw2, vwd, lds0, wv, 9, 0); d_dma(gw2, vwd, lds0, wv, 9, 1);
    const int sw = (2 * g + (q4 >> 1)) & 3;
    const int ro = (4 * g + q4) * 128 + p4 * 8;
    int rn[4];
#pragma unroll
    for (int nt = 0; nt < 4; ++nt) rn[nt] = ro + ((nt ^ sw) << 5);
    auto lda = [&](bf16x8* Af, int j) {
      const char* p0 = smem + d_unit(2 * j) + wv * d_ustr(2 * j);
      const char* p1 = smem + d_unit(2 * j + 1) + wv * d_ustr(2 * j + 1);
#pragma unroll
      for (int nt = 0; nt < 4; ++nt) Af[nt] = cat(ldstr(p0 + rn[nt]), ldstr(p1 + rn[nt]));
    };
    bf16x8 A[2][4], B[7][4], rs;
    lda(A[0], 0); lda(A[1], 1);
    asm volatile("s_waitcnt lgkmcnt(0)" : "+v"(A[0][0]), "+v"(A[0][1]), "+v"(A[0][2]), "+v"(A[0][3]), "+v"(A[1][0]), "+v"(A[1][1]), "+v"(A[1][2]), "+v"(A[1][3]) : : "memory");
#pragma unroll
    for (int u = 10; u < 14; ++u) { d_dma(gw2, vwd, lds0, wv, u, 0); d_dma(gw2, vwd, lds0, wv, u, 1); }
    if (wv == 0) flag_wait(flags + 16384 + b * 32 + (lane & 15), tag);
    asm volatile("s_barrier" ::: "memory");
    const char* ga = hh + ((size_t)(tbG * 4) * 56 + wv * 7) * 1024;
#pragma unroll
    for (int j = 0; j < 7; ++j)
#pragma unroll
      for (int tt = 0; tt < 4; ++tt) gld16(B[j][tt], lane * 16, ga + (size_t)(tt * 56 + j) * 1024);
    const size_t ooff = (size_t)(tbG * 64 + wv * 8) * 1024 + cbG * 128;
    const unsigned vo = (lane >> 3) * 1024 + (lane & 7) * 16;
    gld16(rs, vo, h + ooff);
    f32x4 acc[4][4];
#pragma unroll
    for (int t = 0; t < 4; ++t)
#pragma unroll
      for (int n = 0; n < 4; ++n) acc[t][n] = f32x4{0, 0, 0, 0};
    auto step = [&](auto J) {
      constexpr int j = decltype(J)::value;
      asm volatile("s_waitcnt vmcnt(%4)" : "+v"(B[j][0]), "+v"(B[j][1]), "+v"(B[j][2]), "+v"(B[j][3]) : "n"(25 - 4 * j) : "memory");
#pragma unroll
      for (int nt = 0; nt < 4; ++nt)
#pragma unroll
        for (int tt = 0; tt < 4; ++tt) acc[tt][nt] = mfma16(A[j & 1][nt], B[j][tt], acc[tt][nt]);
      __builtin_amdgcn_sched_barrier(0);
      if constexpr (j + 2 < 7) lda(A[j & 1], j + 2);
    };
    step(IC<0>{}); step(IC<1>{}); step(IC<2>{}); step(IC<3>{}); step(IC<4>{}); step(IC<5>{}); step(IC<6>{});
    {
      const int mo = wv * 6144 + g * 256 + ((i ^ ((g >> 1) * 2)) << 4);
#pragma unroll
      for (int tt = 0; tt < 4; ++tt)
#pragma unroll
        for (int nt = 0; nt < 4; ++nt) {
          const int t = tt * 4 + nt;
          const int base = t < 6 ? S2 + t * 1024 : t < 12 ? S0 + (t - 6) * 1024 : S1 + (t - 12) * 1024;
          *(LAS f32x4*)(LAS char*)(smem + (mo ^ (nt * 64)) + base) = acc[tt][nt];
        }
    }
    asm volatile("s_waitcnt vmcnt(0) lgkmcnt(0)\n\ts_barrier" : "+v"(rs) : : "memory");
    {
      const int c = lane & 7;
      const int tok = (lane >> 3) + 8 * (wv & 1);
      const int t = (wv >> 1) * 4 + (c >> 1);
      const int base = t < 6 ? S2 + t * 1024 : t < 12 ? S0 + (t - 6) * 1024 : S1 + (t - 12) * 1024;
      const char* rp = smem + base + (c & 1) * 512 + ((tok ^ (2 * c)) << 4);
      f32x4 v0 = f32x4{0, 0, 0, 0}, v1 = f32x4{0, 0, 0, 0};
#pragma unroll
      for (int p = 0; p < 8; ++p) {
        v0 += *(const LAS f32x4*)(LAS const char*)(rp + p * 6144);
        v1 += *(const LAS f32x4*)(LAS const char*)(rp + p * 6144 + 256);
      }
      u32x4 r = __builtin_bit_cast(u32x4, rs);
      u32x4 ov = {pack2(v0[0] + bflo(r[0]), v0[1] + bfhi(r[0])), pack2(v0[2] + bflo(r[1]), v0[3] + bfhi(r[1])),
                  pack2(v1[0] + bflo(r[2]), v1[1] + bfhi(r[2])), pack2(v1[2] + bflo(r[3]), v1[3] + bfhi(r[3]))};
      st16<true>(out + ooff + vo, ov);
    }
  }
  asm volatile(".Lmega_end:");
}

extern "C" void kernel_launch(void* const* d_in, const int* in_sizes, int n_in, void* d_out, int out_size,
                              void* d_ws, size_t ws_size, hipStream_t stream) {
  const char* x = (const char*)d_in[0];
  const char* wqkv = (const char*)d_in[1];
  const char* wo = (const char*)d_in[2];
  const char* w1 = (const char*)d_in[4];
  const char* w3 = (const char*)d_in[5];
  const char* w2 = (const char*)d_in[6];
  char* ws = (char*)d_ws;
  (void)hipFuncSetAttribute((const void*)k_mega, hipFuncAttributeMaxDynamicSharedMemorySize, MG_LDS);
  k_mega<<<256, 512, MG_LDS, stream>>>(x, wqkv, wo, w1, w3, w2, ws, (char*)d_out);
}
```

```cpp
#include <hip/hip_runtime.h>
#include <hip/hip_bf16.h>
#include <stdint.h>

typedef short bf16x8 __attribute__((ext_vector_type(8)));
typedef short s16x4 __attribute__((ext_vector_type(4)));
typedef float f32x4 __attribute__((ext_vector_type(4)));
typedef float f32x2 __attribute__((ext_vector_type(2)));
typedef unsigned u32x4 __attribute__((ext_vector_type(4)));
typedef unsigned u32x2 __attribute__((ext_vector_type(2)));
typedef __bf16 bf16x2 __attribute__((ext_vector_type(2)));
#define LAS __attribute__((address_space(3)))
#define DEV __device__ __forceinline__

constexpr int S_ = 2048, D_ = 512, H_ = 32, F_ = 1792;
constexpr float LN_EPS = 1e-5f;

typedef float f32x16 __attribute__((ext_vector_type(16)));
DEV f32x16 mfma32(bf16x8 a, bf16x8 b, f32x16 c) { return __builtin_amdgcn_mfma_f32_32x32x16_bf16(a, b, c, 0, 0, 0); }
DEV f32x4 mfma16(bf16x8 a, bf16x8 b, f32x4 c) { return __builtin_amdgcn_mfma_f32_16x16x32_bf16(a, b, c, 0, 0, 0); }
DEV unsigned pack2(float a, float b) {
  f32x2 v = {a, b};
  return __builtin_bit_cast(unsigned, __builtin_convertvector(v, bf16x2));
}
DEV float bflo(unsigned u) { return __builtin_bit_cast(float, u << 16); }
DEV float bfhi(unsigned u) { return __builtin_bit_cast(float, u & 0xffff0000u); }
DEV float dot2(unsigned a, unsigned b, float c) {
  return __builtin_amdgcn_fdot2_f32_bf16(__builtin_bit_cast(bf16x2, a), __builtin_bit_cast(bf16x2, b), c, false);
}
DEV void dma16(const char* gptr, unsigned ldsbase) {
  unsigned sv;
  asm volatile("s_mov_b32 %0, m0\n\ts_mov_b32 m0, %2\n\ts_nop 0\n\tglobal_load_lds_dwordx4 %1, off\n\ts_mov_b32 m0, %0"
               : "=&s"(sv) : "v"(gptr), "s"(ldsbase) : "memory");
}
DEV void dma16s(unsigned voff, const char* sbase, unsigned ldsbase) {
  unsigned sv;
  asm volatile("s_mov_b32 %0, m0\n\ts_mov_b32 m0, %3\n\ts_nop 0\n\tglobal_load_lds_dwordx4 %1, %2\n\ts_mov_b32 m0, %0"
               : "=&s"(sv) : "v"(voff), "s"(sbase), "s"(ldsbase) : "memory");
}
DEV void dma16sm(unsigned voff, const char* sbase, unsigned ldsbase, unsigned long long mask) {
  unsigned sv;
  asm volatile("s_mov_b32 %0, m0\n\ts_mov_b32 m0, %3\n\ts_mov_b64 exec, %4\n\tglobal_load_lds_dwordx4 %1, %2\n\ts_mov_b64 exec, -1\n\ts_mov_b32 m0, %0"
               : "=&s"(sv) : "v"(voff), "s"(sbase), "s"(ldsbase), "s"(mask) : "memory");
}
DEV void gld16(bf16x8& d, unsigned voff, const char* sbase) {
  asm volatile("global_load_dwordx4 %0, %1, %2" : "=v"(d) : "v"(voff), "s"(sbase) : "memory");
}
DEV void gld8(u32x2& d, unsigned voff, const char* sbase) {
  asm volatile("global_load_dwordx2 %0, %1, %2" : "=v"(d) : "v"(voff), "s"(sbase) : "memory");
}
template <int N> DEV void wait_bar() { asm volatile("s_waitcnt vmcnt(%0) lgkmcnt(0)\n\ts_barrier" ::"n"(N) : "memory"); }
DEV void bar() { asm volatile("s_waitcnt lgkmcnt(0)\n\ts_barrier" ::: "memory"); }
DEV bf16x8 lds128(const char* p) { return *(const LAS bf16x8*)(LAS const char*)p; }
DEV s16x4 ldstr(const char* p) { return __builtin_amdgcn_ds_read_tr16_b64_v4i16((LAS s16x4*)p); }
DEV bf16x8 cat(s16x4 a, s16x4 b) { return __builtin_shufflevector(a, b, 0, 1, 2, 3, 4, 5, 6, 7); }
DEV float xsum(float v) { v += __shfl_xor(v, 16); v += __shfl_xor(v, 32); return v; }
DEV float xmax(float v) { v = fmaxf(v, __shfl_xor(v, 16)); v = fmaxf(v, __shfl_xor(v, 32)); return v; }
DEV float dpp_xor1(float v) { return __builtin_bit_cast(float, __builtin_amdgcn_mov_dpp(__builtin_bit_cast(int, v), 0xB1, 0xF, 0xF, true)); }
DEV float dpp_xor2(float v) { return __builtin_bit_cast(float, __builtin_amdgcn_mov_dpp(__builtin_bit_cast(int, v), 0x4E, 0xF, 0xF, true)); }
DEV float dpp_ror4(float v) { return __builtin_bit_cast(float, __builtin_amdgcn_mov_dpp(__builtin_bit_cast(int, v), 0x124, 0xF, 0xF, true)); }
DEV float dpp_ror8(float v) { return __builtin_bit_cast(float, __builtin_amdgcn_mov_dpp(__builtin_bit_cast(int, v), 0x128, 0xF, 0xF, true)); }
DEV unsigned uni(unsigned v) { return __builtin_amdgcn_readfirstlane(v); }
#define WT 31
template <bool W> DEV void st16(char* p, u32x4 v) {
  if constexpr (W) asm volatile("global_store_dwordx4 %0, %1, off sc0 sc1\n\ts_nop 1" ::"v"(p), "v"(v) : "memory");
  else *(u32x4*)p = v;
}
template <bool W> DEV void st8(char* p, u32x2 v) {
  if constexpr (W) asm volatile("global_store_dwordx2 %0, %1, off sc0 sc1" ::"v"(p), "v"(v) : "memory");
  else *(u32x2*)p = v;
}

template <int A, int B> struct Min { static constexpr int v = A < B ? A : B; };

template <int KT, int NK, int NS, int P, class Issue, class Compute>
DEV void pipe_steps(Issue& issue, Compute& compute) {
  if constexpr (KT < NK) {
    wait_bar<(Min<KT + NS - 1, NK>::v - KT - 1) * P>();
    if constexpr (KT + NS - 1 < NK) issue((KT + NS - 1) % NS);
    compute(KT % NS, KT);
    pipe_steps<KT + 1, NK, NS, P>(issue, compute);
  }
}

constexpr int K1_STAGE = 16384 + 12288, K1_NS = 5;
#define SB __builtin_amdgcn_sched_barrier(0)
DEV float xsum2(float v) {
  auto a = __builtin_amdgcn_permlane16_swap(__float_as_uint(v), __float_as_uint(v), false, false);
  v = __uint_as_float(a[0]) + __uint_as_float(a[1]);
  auto b = __builtin_amdgcn_permlane32_swap(__float_as_uint(v), __float_as_uint(v), false, false);
  return __uint_as_float(b[0]) + __uint_as_float(b[1]);
}

DEV void qkv_phase(char* smem, unsigned lds0, const char* __restrict__ x, const char* __restrict__ w, char* __restrict__ qkv,
                   int cb, int tb) {
  const int tid = threadIdx.x, lane = tid & 63, wv = uni(tid >> 6);
  const int g = lane >> 4, i = lane & 15, q4 = (lane >> 2) & 3, p4 = lane & 3;
  const int tg = wv & 3, nh = wv >> 2;
  const int bi = blockIdx.x >> 3;

  const char* gx = x + (size_t)(tb * 128 + wv * 16) * 1024;
  const unsigned vt = (lane >> 3) * 1024 + (((lane & 7) ^ (lane >> 3)) << 4);
  const char* gw = w + cb * 192;
  unsigned vw0, vw1;
  {
    int qq = wv * 64 + lane;
    int k = qq / 12, c = (qq % 12) ^ (((k >> 3) & 1) << 1);
    vw0 = k * 3072 + c * 16;
    qq = 512 + wv * 32 + (lane & 31);
    k = qq / 12; c = (qq % 12) ^ (((k >> 3) & 1) << 1);
    vw1 = k * 3072 + c * 16;
  }
  const int k0 = bi & 7;
  auto dma = [&](int s, int j) {
    const int kk = (k0 + s) & 7;
    const unsigned base = lds0 + (s % K1_NS) * K1_STAGE;
    if (j == 0) dma16s(vt, gx + kk * 128, base + wv * 2048);
    else if (j == 1) dma16s(vt, gx + kk * 128 + 8192, base + wv * 2048 + 1024);
    else if (j == 2) dma16s(vw0, gw + kk * (64 * 3072), base + 16384 + wv * 1024);
    else dma16sm(vw1, gw + kk * (64 * 3072), base + 16384 + 8192 + wv * 512, 0xFFFFFFFFull);
  };
#pragma unroll
  for (int s = 0; s < K1_NS - 1; ++s)
#pragma unroll
    for (int j = 0; j < 4; ++j) dma(s, j);

  f32x4 acc[2][3], cs = f32x4{0, 0, 0, 0}, s1v = f32x4{0, 0, 0, 0};
#pragma unroll
  for (int a = 0; a < 3; ++a) { acc[0][a] = f32x4{0, 0, 0, 0}; acc[1][a] = f32x4{0, 0, 0, 0}; }
  float s2a = 0.f, s2b = 0.f;
  const bf16x8 ones = {0x3F80, 0x3F80, 0x3F80, 0x3F80, 0x3F80, 0x3F80, 0x3F80, 0x3F80};
  int toff[2][2];
#pragma unroll
  for (int tt = 0; tt < 2; ++tt) {
    toff[tt][0] = (tg * 32 + (tt ^ nh) * 16 + i) * 128 + ((g ^ (i & 7)) << 4);
    toff[tt][1] = (tg * 32 + (tt ^ nh) * 16 + i) * 128 + (((4 + g) ^ (i & 7)) << 4);
  }
  int woff[3], un[3];
#pragma unroll
  for (int nt = 0; nt < 3; ++nt) {
    int a = nt + tg; a = a >= 3 ? a - 3 : a; a = a >= 3 ? a - 3 : a;
    un[nt] = nh * 3 + a;
    woff[nt] = 16384 + (8 * g + q4) * 192 + ((un[nt] ^ (g & 1)) << 5) + p4 * 8;
  }
  auto ldB = [&](int tt, int kt, int ks) -> bf16x8 { return lds128(smem + (kt % K1_NS) * K1_STAGE + toff[tt][ks]); };
  auto ldA = [&](int nt, int kt, int ks) -> bf16x8 {
    const char* p = smem + (kt % K1_NS) * K1_STAGE + woff[nt] + ks * 6144;
    return cat(ldstr(p), ldstr(p + 768));
  };
  bf16x8 A0[3], A1[3], B0[2], B1[2];
  auto phase = [&](bf16x8 (&Ac)[3], bf16x8 (&Bc)[2], bf16x8 (&An)[3], bf16x8 (&Bn)[2], int kn, int ksn, int dk, int dj) {
    const u32x4 u = __builtin_bit_cast(u32x4, Bc[0]);
#pragma unroll
    for (int m = 0; m < 8; ++m) {
      if (kn < 8) {
        if (m == 0) { Bn[0] = ldB(0, kn, ksn); An[0] = ldA(0, kn, ksn); }
        else if (m == 1) Bn[1] = ldB(1, kn, ksn);
        else if (m < 4) An[m - 1] = ldA(m - 1, kn, ksn);
      }
      if (m < 6) acc[m & 1][m >> 1] = mfma16(Ac[m >> 1], Bc[m & 1], acc[m & 1][m >> 1]);
      else if (m == 6) cs = mfma16(Ac[0], ones, cs);
      else s1v = mfma16(ones, Bc[0], s1v);
      {
        float f = (m & 1) ? bfhi(u[m >> 1]) : bflo(u[m >> 1]);
        asm volatile("" : "+v"(f));
        if (m & 1) s2b = __builtin_fmaf(f, f, s2b); else s2a = __builtin_fmaf(f, f, s2a);
      }
      if (dk < 8 && (m == 2 || m == 5)) dma(dk, dj + (m == 5));
      SB;
    }
  };
  wait_bar<12>();
  B0[0] = ldB(0, 0, 0); A0[0] = ldA(0, 0, 0); B0[1] = ldB(1, 0, 0); A0[1] = ldA(1, 0, 0); A0[2] = ldA(2, 0, 0);
#pragma unroll
  for (int kt = 0; kt < 8; ++kt) {
    phase(A0, B0, A1, B1, kt, 1, kt + 4, 0);
    if (kt < 4) wait_bar<10>();
    else if (kt == 4) wait_bar<8>();
    else if (kt == 5) wait_bar<4>();
    else if (kt == 6) wait_bar<0>();
    phase(A1, B1, A0, B0, kt + 1, 0, kt + 4, 2);
  }

  float* st = (float*)(smem + 49152);
  float* csp = (float*)(smem + 49152 + 1024);
  const float s2 = xsum2(s2a + s2b);
  if (g == 0) {
    float mean = s1v[0] * (1.f / 512.f);
    float var = fmaxf(s2 * (1.f / 512.f) - mean * mean, 0.f);
    float rstd = rsqrtf(var + LN_EPS);
    *(f32x2*)(st + (tg * 32 + nh * 16 + i) * 2) = f32x2{-mean * rstd, rstd};
  }
  if (i == 0 && tg < 3) *(f32x4*)(csp + un[0] * 16 + 4 * g) = cs;
  bar();
  f32x2 ms[2];
#pragma unroll
  for (int tt = 0; tt < 2; ++tt) ms[tt] = *(const f32x2*)(st + (tg * 32 + (tt ^ nh) * 16 + i) * 2);
  const int otile = (g & 1) ^ nh, ohalf = g >> 1;
  const unsigned vo = ohalf * 512 + (((otile * 16 + i) ^ (8 * ohalf)) << 4);
#pragma unroll
  for (int nt = 0; nt < 3; ++nt) {
    f32x4 c = *(const f32x4*)(csp + un[nt] * 16 + 4 * g);
    const float qs = cb * 6 + un[nt] < 32 ? 0.25f * 1.4426950408889634f : 1.f;
    unsigned o[2][2];
#pragma unroll
    for (int tt = 0; tt < 2; ++tt) {
      f32x4 y = acc[tt][nt] * (ms[tt][1] * qs) + (ms[tt][0] * qs) * c;
      o[tt][0] = pack2(y[0], y[1]); o[tt][1] = pack2(y[2], y[3]);
    }
    auto sx = __builtin_amdgcn_permlane16_swap(o[0][0], o[1][0], false, false);
    auto sy = __builtin_amdgcn_permlane16_swap(o[0][1], o[1][1], false, false);
    char* dst = qkv + (size_t)(cb * 6 + un[nt]) * (2048 * 32) + (size_t)(tb * 4 + tg) * 1024;
    st16<true>(dst + vo, u32x4{sx[0], sy[0], sx[1], sy[1]});
  }
}

constexpr int S0 = 0, S1 = 49152, S2 = 98304, CSP = 147456, MG_LDS = 163840;
typedef unsigned long long u64;
extern "C" __device__ u64 llvm_dispatch_id() __asm("llvm.amdgcn.dispatch.id");
DEV void flag_set(u64* f, u64 tag) {
  asm volatile("global_store_dwordx2 %0, %1, off sc0 sc1" ::"v"(f), "v"(tag) : "memory");
}
DEV void flag_wait(const u64* p, u64 tag) {
  for (int it = 0; it < (1 << 20); ++it) {
    u64 c;
    asm volatile("global_load_dwordx2 %0, %1, off sc0 sc1\n\ts_waitcnt vmcnt(0)" : "=v"(c) : "v"(p) : "memory");
    if (__builtin_amdgcn_ballot_w64(c != tag) == 0) break;
  }
}
template <int V> struct IC { static constexpr int value = V; };
DEV int opaque(int v) { asm volatile("" : "+v"(v)); return v; }
DEV int opaque_s(int v) { asm volatile("" : "+s"(v)); return v; }

struct CW {
  const char *g1, *g3; unsigned vw; unsigned long long wmask;
};
DEV CW c_setup(const char* w1, const char* w3, int b, int wv, int lane) {
  const int xcd = b & 7, bi = b >> 3;
  const int cb = (xcd & 3) * 4 + (bi & 3);
  CW c;
  c.g1 = w1 + (size_t)(wv * 8) * (F_ * 2) + cb * 224;
  c.g3 = w3 + (size_t)(wv * 8) * (F_ * 2) + cb * 224;
  const int unit = ((lane >> 1) & 7) ^ ((lane >> 4) | ((wv & 1) << 2));
  c.vw = (lane >> 4) * (F_ * 2) + unit * 32 + (lane & 1) * 16;
  c.wmask = __builtin_amdgcn_ballot_w64(unit < 7);
  return c;
}
DEV void c_dmaw(const CW& c, unsigned ldsw, int kt, int j) {
  const unsigned base = ldsw + (kt % 3) * 49152;
  if (j < 2) dma16sm(c.vw, c.g1 + (size_t)(kt * 64 + j * 4) * (F_ * 2), base + 16384 + j * 1024, c.wmask);
  else dma16sm(c.vw, c.g3 + (size_t)(kt * 64 + (j - 2) * 4) * (F_ * 2), base + 32768 + (j - 2) * 1024, c.wmask);
}
DEV constexpr int d_unit(int u) {
  return u < 3 ? S2 + u * 2048 : u < 6 ? S0 + (u - 3) * 2048 : u < 9 ? S1 + (u - 6) * 2048 : u == 9 ? CSP
       : u < 13 ? S2 + (u - 10) * 2048 : S0;
}
DEV constexpr int d_ustr(int u) { return u == 9 ? 2048 : 6144; }
DEV void d_dma(const char* gw2, unsigned vw, unsigned lds0, int wv, int u, int q) {
  dma16s(vw, gw2 + (size_t)(u * 16 + q * 8) * 1024, lds0 + d_unit(u) + wv * d_ustr(u) + q * 1024);
}

template <int NT>
DEV void c_body(char* smem, unsigned lds0, const char* gt, const CW& cw, unsigned vt, int wv, int lane,
                const char* __restrict__ stats, const char* __restrict__ csb, char* __restrict__ hh, int tok0, int cb, const char* gw2, unsigned vwd) {
  const int tg = wv & 3;
  const unsigned ldsw = lds0 + wv * 2048;
  const int g = lane >> 4, i = lane & 15, q4 = (lane >> 2) & 3, p4 = lane & 3;
  f32x4 acc[2][2 * NT];
#pragma unroll
  for (int a = 0; a < 2 * NT; ++a) { acc[0][a] = f32x4{0, 0, 0, 0}; acc[1][a] = f32x4{0, 0, 0, 0}; }
  const int toff0 = (tg * 32 + i) * 128 + ((g ^ (i & 7)) << 4);
  const int toff1 = (tg * 32 + i) * 128 + (((4 + g) ^ (i & 7)) << 4);
  const int odd = cb & 1;
  const int fsw = q4 | ((g & 1) << 2);
  int T[NT], wa[NT];
#pragma unroll
  for (int a = 0; a < NT; ++a) {
    T[a] = NT == 4 ? odd * 3 + a : (a < 2 ? (odd ? 1 : 4) + a : (odd ? 0 : 6));
    wa[a] = 16384 + (8 * g + q4) * 256 + ((T[a] ^ fsw) << 5) + p4 * 8;
  }
  auto dmat = [&](int kt, int j) { dma16s(vt, gt + kt * 128 + j * 8192, ldsw + (kt % 3) * 49152 + j * 1024); };
  auto dma = [&](int kt, int j) {
    if (j < 2) dmat(kt, j); else c_dmaw(cw, ldsw, kt, j - 2);
  };
  auto ldB = [&](bf16x8* B, int kt, int ks) {
    const char* sb = smem + (kt % 3) * 49152 + (ks ? toff1 : toff0);
    B[0] = lds128(sb); B[1] = lds128(sb + 2048);
  };
  auto ldA = [&](int a, int kt, int ks) -> bf16x8 {
    const char* wp = smem + (kt % 3) * 49152 + wa[a < NT ? a : a - NT] + (a < NT ? 0 : 16384) + ks * 8192;
    return cat(ldstr(wp), ldstr(wp + 1024));
  };
  auto mma = [&](int a, bf16x8 A, const bf16x8* B) {
    acc[0][a] = mfma16(A, B[0], acc[0][a]);
    acc[1][a] = mfma16(A, B[1], acc[1][a]);
  };
  bf16x8 A0[2 * NT], A1[2 * NT], B0[2], B1[2];
  bf16x8 cp[4];
  u32x2 sa[4];
#pragma unroll
  for (int kt = 0; kt < 3; ++kt) { dmat(kt, 0); dmat(kt, 1); }
#pragma unroll
  for (int m = 0; m < 4; ++m)
    gld8(sa[m], (unsigned)((i >> 3) * 512 + g * 128 + (i & 7) * 8), stats + (size_t)(tok0 + tg * 32 + (m >> 1) * 16) * 64 + (m & 1) * 64);
  const int cq = min(tg * 16 + (lane >> 2), 55);
  if (NT == 3) {
#pragma unroll
    for (int m = 0; m < 4; ++m) gld16(cp[m], (unsigned)(((lane & 3) * 4 + m) * 896 + cq * 16), csb + (size_t)cb * 14336);
  }
  constexpr int X = NT == 3 ? 8 : 4;
  wait_bar<4 + X>();
  float rs[2], mr[2];
  float* csp = (float*)(smem + CSP);
  ldB(B0, 0, 0);
#pragma unroll
  for (int a = 0; a < 2 * NT; ++a) A0[a] = ldA(a, 0, 0);
#pragma unroll
  for (int kt = 0; kt < 8; ++kt) {
    ldB(B1, kt, 1);
#pragma unroll
    for (int a = 0; a < 2 * NT; ++a) {
      if (a < NT) { A1[2 * a] = ldA(2 * a, kt, 1); A1[2 * a + 1] = ldA(2 * a + 1, kt, 1); }
      mma(a, A0[a], B0);
      __builtin_amdgcn_sched_barrier(0);
    }
    if (kt == 0) wait_bar<2 + X>();
    else if (kt != 1) wait_bar<6>();
    else if (NT == 3) asm volatile("s_waitcnt vmcnt(6) lgkmcnt(0)\n\ts_barrier" : "+v"(sa[0]), "+v"(sa[1]), "+v"(sa[2]), "+v"(sa[3]), "+v"(cp[0]), "+v"(cp[1]), "+v"(cp[2]), "+v"(cp[3]) : : "memory");
    else asm volatile("s_waitcnt vmcnt(6) lgkmcnt(0)\n\ts_barrier" : "+v"(sa[0]), "+v"(sa[1]), "+v"(sa[2]), "+v"(sa[3]) : : "memory");
    if (kt < 7) ldB(B0, kt + 1, 0);
    if (kt == 1) {
#pragma unroll
      for (int tt = 0; tt < 2; ++tt) {
        const f32x2 sv0 = __builtin_bit_cast(f32x2, sa[2 * tt]), sv1 = __builtin_bit_cast(f32x2, sa[2 * tt + 1]);
        const float s1 = xsum2(sv0[0] + sv1[0]), s2 = xsum2(sv0[1] + sv1[1]);
        const float mean = s1 * (1.f / 512.f);
        const float var = fmaxf(s2 * (1.f / 512.f) - mean * mean, 0.f);
        rs[tt] = rsqrtf(var + LN_EPS); mr[tt] = mean * rs[tt];
      }
      if (NT == 3) {
        f32x4 c = (__builtin_bit_cast(f32x4, cp[0]) + __builtin_bit_cast(f32x4, cp[1])) +
                  (__builtin_bit_cast(f32x4, cp[2]) + __builtin_bit_cast(f32x4, cp[3]));
#pragma unroll
        for (int e = 0; e < 4; ++e) { c[e] += dpp_xor1(c[e]); c[e] += dpp_xor2(c[e]); }
        if ((lane & 3) == 0) *(f32x4*)(csp + cq * 4) = c;
      }
    }
#pragma unroll
    for (int a = 0; a < 2 * NT; ++a) {
      if (kt < 7 && a < NT) { A0[2 * a] = ldA(2 * a, kt + 1, 0); A0[2 * a + 1] = ldA(2 * a + 1, kt + 1, 0); }
      mma(a, A1[a], B1);
      if (a >= 2 * NT - 6) {
        const int j = a - (2 * NT - 6);
        if (kt + 3 < 8) dma(kt + 3, j);
        else d_dma(gw2, vwd, lds0, wv, (kt - 5) * 3 + (j >> 1), j & 1);
      }
      __builtin_amdgcn_sched_barrier(0);
    }
  }
  unsigned pk[2][NT][2];
#pragma unroll
  for (int tt = 0; tt < 2; ++tt) {
    char* dst = hh + ((size_t)((tok0 >> 4) + tg * 2 + tt) * 56) * 1024 + lane * 16;
#pragma unroll
    for (int a = 0; a < NT; ++a) {
      f32x4 cg = *(const f32x4*)(csp + T[a] * 16 + 4 * g);
      f32x4 cu = *(const f32x4*)(csp + (7 + T[a]) * 16 + 4 * g);
      float r[4];
#pragma unroll
      for (int e = 0; e < 4; ++e) {
        float ga = acc[tt][a][e] * rs[tt] - mr[tt] * cg[e];
        float up = acc[tt][NT + a][e] * rs[tt] - mr[tt] * cu[e];
        float sg = __builtin_amdgcn_rcpf(1.f + __builtin_amdgcn_exp2f(-1.4426950408889634f * ga));
        r[e] = ga * sg * up;
      }
      pk[tt][a][0] = pack2(r[0], r[1]); pk[tt][a][1] = pack2(r[2], r[3]);
      const int G = cb * 7 + T[a];
      if (a & 1) {
        st16<true>(dst + (size_t)(G >> 1) * 1024, u32x4{pk[tt][a - 1][0], pk[tt][a - 1][1], pk[tt][a][0], pk[tt][a][1]});
      } else if (a == NT - 1) {
        st8<true>(dst + (size_t)(G >> 1) * 1024 + (G & 1) * 512 - lane * 8, u32x2{pk[tt][a][0], pk[tt][a][1]});
      }
    }
  }
}

__global__ __launch_bounds__(512) void k_mega(const char* __restrict__ x, const char* __restrict__ wqkv, const char* __restrict__ wo,
                                              const char* __restrict__ w1, const char* __restrict__ w3,
                                              const char* __restrict__ w2, char* __restrict__ ws, char* __restrict__ out) {
  extern __shared__ __attribute__((aligned(16))) char smem[];
  char* qkv = ws;
  char* o = ws + (6u << 20);
  char* h = ws + (8u << 20);
  char* stats = ws + (10u << 20);
  u64* flags = (u64*)(ws + (10u << 20) + (512u << 10));
  char* csb = ws + (10u << 20) + (768u << 10);
  char* hh = ws + (11u << 20);
  const u64 tag = ((u64)(size_t)__builtin_amdgcn_dispatch_ptr() << 20) + llvm_dispatch_id();
  const int b = blockIdx.x;
  const unsigned lds0 = (unsigned)(size_t)(LAS char*)smem;
  const int cbG = (b >> 3) & 7, tbG = (b & 7) * 4 + (b >> 6);
  const int cbC = (b & 3) * 4 + ((b >> 3) & 3), tbC = ((b >> 2) & 1) * 8 + (b >> 5);

  unsigned cpf = 0;
  {
    u64 pc;
    unsigned len;
    asm volatile("s_getpc_b64 %0\n.Lmega_pc:\n\ts_mov_b32 %1, .Lmega_end-.Lmega_pc" : "=s"(pc), "=s"(len));
    const unsigned off = ((b >> 3) * 20 + threadIdx.x) * 64;
    if (threadIdx.x < 20 && off < len) asm volatile("global_load_dword %0, %1, %2" : "=v"(cpf) : "v"(off), "s"(pc) : "memory");
  }
  {
    const int cb = (b & 1) * 8 + ((b >> 3) & 7), tb = ((b & 7) >> 1) * 4 + (b >> 6);
    qkv_phase(smem, lds0, x, wqkv, qkv, cb, tb);
    asm volatile("s_waitcnt vmcnt(0) lgkmcnt(0)\n\ts_barrier" ::: "memory");
    asm volatile("" :: "v"(cpf));
    const int tid = threadIdx.x;
    if (tid < 9) {
      const int P = cb * 3 + tid / 3, d = tid % 3, which = P >> 4, hp = P & 15, qb = tb + d;
      if (which ? qb < 16 : d == 0)
        flag_set(flags + 24576 + ((hp >> 1) | ((hp & 1) << 3) | (qb << 4)) * 32 + (which ? (which - 1) * 3 + d : 6), tag);
    }
  }
  {
    const int tid = opaque(threadIdx.x), lane = tid & 63, wv = uni(tid >> 6);
    const int c32 = lane & 31, hf = lane >> 5, q4 = (lane >> 2) & 3, p4 = lane & 3;
    const int hp = (b & 7) * 2 + ((b >> 3) & 1), qb = b >> 4;
    const int q0 = qb * 128;
    const size_t HS = 2048 * 32;
    bf16x8 Q, cw0, cw1;
    {
      const unsigned one = qb * 4 - 8 + wv >= 0 ? 0x3F803F80u : 0u;
      *(LAS u32x4*)(LAS char*)(smem + S1 + tid * 16) = u32x4{one, one, one, one};
      if (tid < 256) *(LAS u32x4*)(LAS char*)(smem + S1 + 8192 + tid * 16) = u32x4{0x3F803F80u, 0x3F803F80u, 0x3F803F80u, 0x3F803F80u};
    }
    if (wv == 0) {
      const int d = lane % 3;
      flag_wait(flags + 24576 + b * 32 + (lane >= 6 ? 6 : qb - d < 0 ? 6 : lane), tag);
    }
    asm volatile("s_barrier" ::: "memory");
    {
      const int r = wv >> 1;
      const char* src = qkv + (size_t)((1 + (r >> 1)) * 32 + hp * 2 + (r & 1)) * HS;
#pragma unroll
      for (int j = 0; j < 6; ++j) {
        const int kb = qb * 4 - 8 + (wv & 1) * 6 + j;
        if (kb < 0 && r >= 2) *(LAS u32x4*)(LAS char*)(smem + r * 12288 + ((wv & 1) * 6 + j) * 1024 + lane * 16) = u32x4{0, 0, 0, 0};
        else dma16s(kb < 0 ? hf * 640 : lane * 16, src + (size_t)max(kb, 0) * 1024, lds0 + r * 12288 + ((wv & 1) * 6 + j) * 1024);
      }
    }
    const int u = wv >> 2, qg = wv & 3;
    const int qs = q0 + qg * 32;
    const unsigned vq = hf * 512 + ((c32 ^ (8 * hf)) << 4);
    gld16(Q, vq, qkv + (size_t)(hp * 2 + u) * HS + (size_t)qs * 32);
    const float NEG = -1e30f;
    f32x16 cin0, cin8;
    u32x4 mk[2];
#pragma unroll
    for (int r = 0; r < 16; ++r) {
      const int kl = (r & 3) + 8 * (r >> 2) + 4 * hf;
      cin0[r] = kl > c32 ? 0.f : NEG; cin8[r] = kl <= c32 ? 0.f : NEG;
      if (r & 1) mk[r >> 3][(r >> 1) & 3] = (kl > c32 ? 0xFFFF0000u : 0u) | (kl - 1 > c32 ? 0xFFFFu : 0u);
    }
    asm volatile("s_waitcnt vmcnt(0) lgkmcnt(0)\n\ts_barrier" : "+v"(Q), "+v"(cin0), "+v"(cin8), "+v"(mk[0]), "+v"(mk[1]) : : "memory");
    const unsigned vwo = (lane >> 3) * 1024 + ((((lane & 7) >> 1) ^ ((lane >> 4) & 3)) << 5) + (lane & 1) * 16;
    const char* gwo = wo + (size_t)(wv * 64) * 1024 + cbG * 128;
    const CW cw1s = c_setup(w1, w3, b, wv, lane);
    auto pf = [&](int k) {
      if (k < 2) {
        const int ch = max(wv - 1, 0) * 4 + (lane & 3);
        const char* pc = (ch >= 14 ? w3 + (ch - 14) * 16 : w1 + ch * 16) + (size_t)(tbC * 32 + (lane >> 2)) * (F_ * 2) + cbC * 224;
        if (k == 0) asm volatile("global_load_dwordx4 %0, %1, off" : "=v"(cw0) : "v"(pc) : "memory");
        else asm volatile("global_load_dwordx4 %0, %1, off" : "=v"(cw1) : "v"(pc + 16 * (F_ * 2)) : "memory");
      } else if (k < 10) dma16s(vwo, gwo + (size_t)((k - 2) * 8) * 1024, lds0 + S2 + wv * 8192 + (k - 2) * 1024);
      else if (k < 14) c_dmaw(cw1s, lds0 + wv * 2048, 1, k - 10);
      else if (k == 14) {
        const int c0 = b * 8 + wv;
        dma16s(lane * 16, w2 + (size_t)(c0 < 1792 ? c0 : c0 - 1792) * 1024, lds0 + S1 + 12288 + (wv & 3) * 1024);
      }
    };
    {
      const char* Kl = smem + u * 12288 + qg * 1024 + vq;
      const char* Vl[2];
#pragma unroll
      for (int e = 0; e < 2; ++e)
        Vl[e] = c32 < 16 ? smem + 24576 + u * 12288 + qg * 1024 + (p4 >> 1) * 512 + (((e * 8 + 4 * hf + q4) ^ (8 * (p4 >> 1))) << 4) + (p4 & 1) * 8
                         : smem + S1 + qg * 1024 + (hf ? 0 : 64);
      f32x16 z;
#pragma unroll
      for (int r = 0; r < 16; ++r) z[r] = 0.f;
      bf16x8 Kf[9];
#pragma unroll
      for (int t = 0; t < 9; ++t) Kf[t] = lds128(Kl + t * 1024);
      f32x16 sm = mfma32(Kf[0], Q, cin0);
      {
        const f32x16 s8 = mfma32(Kf[8], Q, cin8);
#pragma unroll
        for (int r = 0; r < 16; ++r) sm[r] = fmaxf(sm[r], s8[r]);
      }
      float m = sm[0];
#pragma unroll
      for (int r = 1; r < 16; ++r) m = fmaxf(m, sm[r]);
#pragma unroll
      for (int t = 1; t < 8; ++t) {
        const f32x16 s = mfma32(Kf[t], Q, z);
#pragma unroll
        for (int r = 0; r < 16; ++r) m = fmaxf(m, s[r]);
      }
      {
        auto sw = __builtin_amdgcn_permlane32_swap(__float_as_uint(m), __float_as_uint(m), false, false);
        m = fmaxf(__uint_as_float(sw[0]), __uint_as_float(sw[1]));
      }
      f32x16 cm, oacc;
#pragma unroll
      for (int r = 0; r < 16; ++r) { cm[r] = -m; oacc[r] = 0.f; }
      f32x16 sn = mfma32(Kf[1], Q, cm);
      {
        u32x4 pb[2];
#pragma unroll
        for (int j = 0; j < 8; ++j)
          pb[j >> 2][j & 3] = pack2(__builtin_amdgcn_exp2f(sm[2 * j] - m), __builtin_amdgcn_exp2f(sm[2 * j + 1] - m));
#pragma unroll
        for (int sh = 0; sh < 2; ++sh) {
          const u32x4 p0 = pb[sh] & mk[sh], p8 = pb[sh] ^ p0;
          oacc = mfma32(cat(ldstr(Vl[0] + sh * 256), ldstr(Vl[1] + sh * 256)), __builtin_bit_cast(bf16x8, p0), oacc);
          pf(2 * sh);
          oacc = mfma32(cat(ldstr(Vl[0] + 8192 + sh * 256), ldstr(Vl[1] + 8192 + sh * 256)), __builtin_bit_cast(bf16x8, p8), oacc);
          pf(2 * sh + 1);
        }
      }
      SB;
#pragma unroll
      for (int t = 1; t < 8; ++t) {
        const f32x16 s = sn;
        if (t < 7) sn = mfma32(Kf[t + 1], Q, cm);
#pragma unroll
        for (int sh = 0; sh < 2; ++sh) {
          u32x4 pb;
#pragma unroll
          for (int e = 0; e < 4; ++e)
            pb[e] = pack2(__builtin_amdgcn_exp2f(s[8 * sh + 2 * e]), __builtin_amdgcn_exp2f(s[8 * sh + 2 * e + 1]));
          const int vp = t * 1024 + sh * 256;
          oacc = mfma32(cat(ldstr(Vl[0] + vp), ldstr(Vl[1] + vp)), __builtin_bit_cast(bf16x8, pb), oacc);
          if (2 * t + sh + 2 < 15) pf(2 * t + sh + 2);
        }
        SB;
      }
      const float inv = __builtin_amdgcn_rcpf(oacc[8]);
      const u32x4 ov = {pack2(oacc[0] * inv, oacc[1] * inv), pack2(oacc[2] * inv, oacc[3] * inv),
                        pack2(oacc[4] * inv, oacc[5] * inv), pack2(oacc[6] * inv, oacc[7] * inv)};
      st16<true>(o + ((size_t)((qs >> 4) + (c32 >> 4)) * 16 + hp) * 1024 + ((2 * u + hf) * 16 + (c32 & 15)) * 16, ov);
    }
    asm volatile("s_waitcnt vmcnt(0) lgkmcnt(0)\n\ts_barrier" : "+v"(cw0), "+v"(cw1) : : "memory");
    if (tid < 16) flag_set(flags + (((qb >> 1) | (tid << 3) | ((qb & 1) << 7)) * 32 + hp), tag);
    {
      const CW cw = c_setup(w1, w3, b, wv, lane);
#pragma unroll
      for (int j = 0; j < 4; ++j) c_dmaw(cw, lds0 + wv * 2048, 0, j);
    }
    if (wv == 0) { flag_wait(flags + b * 32 + (lane & 15), tag); }
    else {
      const u32x4 ua = __builtin_bit_cast(u32x4, cw0), ub = __builtin_bit_cast(u32x4, cw1);
      f32x4 lo, hi;
#pragma unroll
      for (int e = 0; e < 2; ++e) {
        lo[2 * e] = bflo(ua[e]) + bflo(ub[e]); lo[2 * e + 1] = bfhi(ua[e]) + bfhi(ub[e]);
        hi[2 * e] = bflo(ua[2 + e]) + bflo(ub[2 + e]); hi[2 * e + 1] = bfhi(ua[2 + e]) + bfhi(ub[2 + e]);
      }
#pragma unroll
      for (int e = 0; e < 4; ++e) {
        lo[e] += dpp_ror4(lo[e]); lo[e] += dpp_ror8(lo[e]); lo[e] = xsum2(lo[e]);
        hi[e] += dpp_ror4(hi[e]); hi[e] += dpp_ror8(hi[e]); hi[e] = xsum2(hi[e]);
      }
      if (lane < 8) {
        char* d = csb + (size_t)(cbC * 16 + tbC) * 896 + ((wv - 1) * 4 + (lane & 3)) * 32 + (lane >> 2) * 16;
        st16<true>(d, __builtin_bit_cast(u32x4, lane < 4 ? lo : hi));
      }
      asm volatile("s_waitcnt vmcnt(0)" ::: "memory");
    }
    asm volatile("s_barrier" ::: "memory");
    if (tid >= 64 && tid < 80) flag_set(flags + 8192 + ((cbC >> 2) | ((tid & 1) << 2) | ((cbC & 3) << 3) | (((tid & 15) >> 1) << 5)) * 32 + 16 + tbC, tag);
  }
  {
    const int tid = opaque(threadIdx.x), lane = tid & 63, wv = uni(tid >> 6);
    const int g = lane >> 4, i = lane & 15, q4 = (lane >> 2) & 3, p4 = lane & 3;
    const int kp = wv & 3, th = wv >> 2;
    bf16x8 B[4][2], rs;
    const char* ga = o + ((size_t)(tbG * 4 + th * 2) * 16 + kp * 4) * 1024;
#pragma unroll
    for (int s = 0; s < 4; ++s) {
      gld16(B[s][0], lane * 16, ga + s * 1024);
      gld16(B[s][1], lane * 16, ga + (16 + s) * 1024);
    }
    const size_t ooff = (size_t)(tbG * 64 + wv * 8) * 1024 + cbG * 128;
    const unsigned vo = (lane >> 3) * 1024 + (lane & 7) * 16;
    gld16(rs, vo, x + ooff);
    const int sw = (2 * (g & 1) + (q4 >> 1)) & 3;
    const char* wr = smem + S2 + kp * 16384 + (16 * (g >> 1) + 4 * (g & 1) + q4) * 128 + p4 * 8;
    bf16x8 A[4][4];
#pragma unroll
    for (int s = 0; s < 4; ++s)
#pragma unroll
      for (int nt = 0; nt < 4; ++nt) {
        const char* p = wr + s * 4096 + ((nt ^ sw) << 5);
        A[s][nt] = cat(ldstr(p), ldstr(p + 1024));
      }
    f32x4 acc[2][4];
#pragma unroll
    for (int t = 0; t < 2; ++t)
#pragma unroll
      for (int n = 0; n < 4; ++n) acc[t][n] = f32x4{0, 0, 0, 0};
    asm volatile("s_waitcnt vmcnt(7)" : "+v"(B[0][0]), "+v"(B[0][1]) : : "memory");
#pragma unroll
    for (int nt = 0; nt < 4; ++nt) { acc[0][nt] = mfma16(A[0][nt], B[0][0], acc[0][nt]); acc[1][nt] = mfma16(A[0][nt], B[0][1], acc[1][nt]); }
    asm volatile("s_waitcnt vmcnt(5)" : "+v"(B[1][0]), "+v"(B[1][1]) : : "memory");
#pragma unroll
    for (int nt = 0; nt < 4; ++nt) { acc[0][nt] = mfma16(A[1][nt], B[1][0], acc[0][nt]); acc[1][nt] = mfma16(A[1][nt], B[1][1], acc[1][nt]); }
    asm volatile("s_waitcnt vmcnt(3)" : "+v"(B[2][0]), "+v"(B[2][1]) : : "memory");
#pragma unroll
    for (int nt = 0; nt < 4; ++nt) { acc[0][nt] = mfma16(A[2][nt], B[2][0], acc[0][nt]); acc[1][nt] = mfma16(A[2][nt], B[2][1], acc[1][nt]); }
    asm volatile("s_waitcnt vmcnt(1)" : "+v"(B[3][0]), "+v"(B[3][1]) : : "memory");
#pragma unroll
    for (int nt = 0; nt < 4; ++nt) { acc[0][nt] = mfma16(A[3][nt], B[3][0], acc[0][nt]); acc[1][nt] = mfma16(A[3][nt], B[3][1], acc[1][nt]); }
    bar();
    {
      const int mo = S2 + kp * 16384 + g * 256 + ((i ^ ((g >> 1) * 2)) << 4);
#pragma unroll
      for (int tt = 0; tt < 2; ++tt)
#pragma unroll
        for (int nt = 0; nt < 4; ++nt)
          *(LAS f32x4*)(LAS char*)(smem + (mo ^ (nt * 64)) + ((th * 2 + tt) * 4 + nt) * 1024) = acc[tt][nt];
    }
    asm volatile("s_waitcnt vmcnt(0) lgkmcnt(0)\n\ts_barrier" : "+v"(rs) : : "memory");
    {
      const int c = lane & 7;
      const int tok = (lane >> 3) + 8 * (wv & 1);
      const char* rp = smem + S2 + ((wv >> 1) * 4 + (c >> 1)) * 1024 + (c & 1) * 512 + ((tok ^ (2 * c)) << 4);
      f32x4 v0 = f32x4{0, 0, 0, 0}, v1 = f32x4{0, 0, 0, 0};
#pragma unroll
      for (int p = 0; p < 4; ++p) {
        v0 += *(const LAS f32x4*)(LAS const char*)(rp + p * 16384);
        v1 += *(const LAS f32x4*)(LAS const char*)(rp + p * 16384 + 256);
      }
      u32x4 r = __builtin_bit_cast(u32x4, rs);
      u32x4 ov = {pack2(v0[0] + bflo(r[0]), v0[1] + bfhi(r[0])), pack2(v0[2] + bflo(r[1]), v0[3] + bfhi(r[1])),
                  pack2(v1[0] + bflo(r[2]), v1[1] + bfhi(r[2])), pack2(v1[2] + bflo(r[3]), v1[3] + bfhi(r[3]))};
      st16<true>(h + ooff + vo, ov);
      float s1 = 0.f, s2 = 0.f;
#pragma unroll
      for (int e = 0; e < 4; ++e) { s1 = dot2(ov[e], 0x3F803F80u, s1); s2 = dot2(ov[e], ov[e], s2); }
#pragma unroll
      for (int m = 1; m < 8; m <<= 1) { s1 += __shfl_xor(s1, m); s2 += __shfl_xor(s2, m); }
      if (c == 0) st8<true>(stats + ((size_t)(tbG * 8 + wv) * 8 + cbG) * 64 + (lane >> 3) * 8,
                            u32x2{__float_as_uint(s1), __float_as_uint(s2)});
    }
    asm volatile("s_waitcnt vmcnt(0) lgkmcnt(0)\n\ts_barrier" ::: "memory");
    if (tid < 16) flag_set(flags + 8192 + ((tid & 3) | ((tbG >> 4) << 2) | ((tid >> 2) << 3) | (((tbG >> 1) & 7) << 5)) * 32 + (tbG & 1) * 8 + cbG, tag);
    {
      const CW cw = c_setup(w1, w3, b, wv, lane);
#pragma unroll
      for (int j = 0; j < 4; ++j) c_dmaw(cw, lds0 + wv * 2048, 2, j);
    }
    if (wv == 0) flag_wait(flags + 8192 + b * 32 + (lane & 31), tag);
    asm volatile("s_barrier" ::: "memory");
  }
  const unsigned vwd_ = 0;
  {
    const int tid = opaque(threadIdx.x), lane = tid & 63, wv = uni(tid >> 6);
    const char* gt = h + (size_t)(tbC * 128 + wv * 16) * 1024;
    const unsigned vt = (lane >> 3) * 1024 + (((lane & 7) ^ (lane >> 3)) << 4);
    const CW cw = c_setup(w1, w3, b, wv, lane);
    const unsigned vwd = (lane >> 3) * 1024 + ((((lane & 7) >> 1) ^ ((lane >> 4) & 3)) << 5) + (lane & 1) * 16;
    const char* gw2 = w2 + (size_t)(wv * 224) * 1024 + cbG * 128;
    if ((wv >> 2) == 0) c_body<4>(smem, lds0, gt, cw, vt, wv, lane, stats, csb, hh, tbC * 128, cbC, gw2, vwd);
    else c_body<3>(smem, lds0, gt, cw, vt, wv, lane, stats, csb, hh, tbC * 128, cbC, gw2, vwd);
  }
  {
    const int tid = opaque(threadIdx.x), lane = tid & 63, wv = uni(tid >> 6);
    const int g = lane >> 4, i = lane & 15, q4 = (lane >> 2) & 3, p4 = lane & 3;
    asm volatile("s_waitcnt vmcnt(0) lgkmcnt(0)\n\ts_barrier" ::: "memory");
    if (tid < 16) flag_set(flags + 16384 + ((tbC >> 1) | (tid << 3) | ((tbC & 1) << 7)) * 32 + cbC, tag);
    const unsigned vwd = (lane >> 3) * 1024 + ((((lane & 7) >> 1) ^ ((lane >> 4) & 3)) << 5) + (lane & 1) * 16;
    const char* gw2 = w2 + (size_t)(wv * 224) * 1024 + cbG * 128;
    d_dma(gw2, vwd, lds0, wv, 9, 0); d_dma(gw2, vwd, lds0, wv, 9, 1);
    const int sw = (2 * g + (q4 >> 1)) & 3;
    const int ro = (4 * g + q4) * 128 + p4 * 8;
    int rn[4];
#pragma unroll
    for (int nt = 0; nt < 4; ++nt) rn[nt] = ro + ((nt ^ sw) << 5);
    auto lda = [&](bf16x8* Af, int j) {
      const char* p0 = smem + d_unit(2 * j) + wv * d_ustr(2 * j);
      const char* p1 = smem + d_unit(2 * j + 1) + wv * d_ustr(2 * j + 1);
#pragma unroll
      for (int nt = 0; nt < 4; ++nt) Af[nt] = cat(ldstr(p0 + rn[nt]), ldstr(p1 + rn[nt]));
    };
    bf16x8 A[2][4], B[7][4], rs;
    lda(A[0], 0); lda(A[1], 1);
    asm volatile("s_waitcnt lgkmcnt(0)" : "+v"(A[0][0]), "+v"(A[0][1]), "+v"(A[0][2]), "+v"(A[0][3]), "+v"(A[1][0]), "+v"(A[1][1]), "+v"(A[1][2]), "+v"(A[1][3]) : : "memory");
#pragma unroll
    for (int u = 10; u < 14; ++u) { d_dma(gw2, vwd, lds0, wv, u, 0); d_dma(gw2, vwd, lds0, wv, u, 1); }
    if (wv == 0) flag_wait(flags + 16384 + b * 32 + (lane & 15), tag);
    asm volatile("s_barrier" ::: "memory");
    const char* ga = hh + ((size_t)(tbG * 4) * 56 + wv * 7) * 1024;
    u32x2 Bl[4], Bh[4];
#pragma unroll
    for (int j = 0; j < 7; ++j)
#pragma unroll
      for (int tt = 0; tt < 4; ++tt) {
        if (j == 3) {
          gld8(Bl[tt], lane * 8, ga + (size_t)(tt * 56 + j) * 1024);
          gld8(Bh[tt], lane * 8, ga + (size_t)(tt * 56 + j) * 1024 + 512);
        } else gld16(B[j][tt], lane * 16, ga + (size_t)(tt * 56 + j) * 1024);
      }
    const size_t ooff = (size_t)(tbG * 64 + wv * 8) * 1024 + cbG * 128;
    const unsigned vo = (lane >> 3) * 1024 + (lane & 7) * 16;
    gld16(rs, vo, h + ooff);
    f32x4 acc[4][4];
#pragma unroll
    for (int t = 0; t < 4; ++t)
#pragma unroll
      for (int n = 0; n < 4; ++n) acc[t][n] = f32x4{0, 0, 0, 0};
    auto step = [&](auto J) {
      constexpr int j = decltype(J)::value;
      if constexpr (j == 3) {
        asm volatile("s_waitcnt vmcnt(13)" : "+v"(Bl[0]), "+v"(Bl[1]), "+v"(Bl[2]), "+v"(Bl[3]), "+v"(Bh[0]), "+v"(Bh[1]), "+v"(Bh[2]), "+v"(Bh[3]) : : "memory");
#pragma unroll
        for (int tt = 0; tt < 4; ++tt) B[3][tt] = __builtin_bit_cast(bf16x8, u32x4{Bl[tt][0], Bl[tt][1], Bh[tt][0], Bh[tt][1]});
      } else
        asm volatile("s_waitcnt vmcnt(%4)" : "+v"(B[j][0]), "+v"(B[j][1]), "+v"(B[j][2]), "+v"(B[j][3]) : "n"((j < 3 ? 29 : 25) - 4 * j) : "memory");
#pragma unroll
      for (int nt = 0; nt < 4; ++nt)
#pragma unroll
        for (int tt = 0; tt < 4; ++tt) acc[tt][nt] = mfma16(A[j & 1][nt], B[j][tt], acc[tt][nt]);
      __builtin_amdgcn_sched_barrier(0);
      if constexpr (j + 2 < 7) lda(A[j & 1], j + 2);
    };
    step(IC<0>{}); step(IC<1>{}); step(IC<2>{}); step(IC<3>{}); step(IC<4>{}); step(IC<5>{}); step(IC<6>{});
    {
      const int mo = wv * 6144 + g * 256 + ((i ^ ((g >> 1) * 2)) << 4);
#pragma unroll
      for (int tt = 0; tt < 4; ++tt)
#pragma unroll
        for (int nt = 0; nt < 4; ++nt) {
          const int t = tt * 4 + nt;
          const int base = t < 6 ? S2 + t * 1024 : t < 12 ? S0 + (t - 6) * 1024 : S1 + (t - 12) * 1024;
          *(LAS f32x4*)(LAS char*)(smem + (mo ^ (nt * 64)) + base) = acc[tt][nt];
        }
    }
    asm volatile("s_waitcnt vmcnt(0) lgkmcnt(0)\n\ts_barrier" : "+v"(rs) : : "memory");
    {
      const int c = lane & 7;
      const int tok = (lane >> 3) + 8 * (wv & 1);
      const int t = (wv >> 1) * 4 + (c >> 1);
      const int base = t < 6 ? S2 + t * 1024 : t < 12 ? S0 + (t - 6) * 1024 : S1 + (t - 12) * 1024;
      const char* rp = smem + base + (c & 1) * 512 + ((tok ^ (2 * c)) << 4);
      f32x4 v0 = f32x4{0, 0, 0, 0}, v1 = f32x4{0, 0, 0, 0};
#pragma unroll
      for (int p = 0; p < 8; ++p) {
        v0 += *(const LAS f32x4*)(LAS const char*)(rp + p * 6144);
        v1 += *(const LAS f32x4*)(LAS const char*)(rp + p * 6144 + 256);
      }
      u32x4 r = __builtin_bit_cast(u32x4, rs);
      u32x4 ov = {pack2(v0[0] + bflo(r[0]), v0[1] + bfhi(r[0])), pack2(v0[2] + bflo(r[1]), v0[3] + bfhi(r[1])),
                  pack2(v1[0] + bflo(r[2]), v1[1] + bfhi(r[2])), pack2(v1[2] + bflo(r[3]), v1[3] + bfhi(r[3]))};
      st16<true>(out + ooff + vo, ov);
    }
  }
  asm volatile(".Lmega_end:");
}

extern "C" void kernel_launch(void* const* d_in, const int* in_sizes, int n_in, void* d_out, int out_size,
                              void* d_ws, size_t ws_size, hipStream_t stream) {
  const char* x = (const char*)d_in[0];
  const char* wqkv = (const char*)d_in[1];
  const char* wo = (const char*)d_in[2];
  const char* w1 = (const char*)d_in[4];
  const char* w3 = (const char*)d_in[5];
  const char* w2 = (const char*)d_in[6];
  char* ws = (char*)d_ws;
  (void)hipFuncSetAttribute((const void*)k_mega, hipFuncAttributeMaxDynamicSharedMemorySize, MG_LDS);
  k_mega<<<256, 512, MG_LDS, stream>>>(x, wqkv, wo, w1, w3, w2, ws, (char*)d_out);
}
```

```cpp
#include <hip/hip_runtime.h>
#include <hip/hip_bf16.h>
#include <stdint.h>

typedef short bf16x8 __attribute__((ext_vector_type(8)));
typedef short s16x4 __attribute__((ext_vector_type(4)));
typedef float f32x4 __attribute__((ext_vector_type(4)));
typedef float f32x2 __attribute__((ext_vector_type(2)));
typedef unsigned u32x4 __attribute__((ext_vector_type(4)));
typedef unsigned u32x2 __attribute__((ext_vector_type(2)));
typedef __bf16 bf16x2 __attribute__((ext_vector_type(2)));
#define LAS __attribute__((address_space(3)))
#define DEV __device__ __forceinline__

constexpr int S_ = 2048, D_ = 512, H_ = 32, F_ = 1792;
constexpr float LN_EPS = 1e-5f;

typedef float f32x16 __attribute__((ext_vector_type(16)));
DEV f32x16 mfma32(bf16x8 a, bf16x8 b, f32x16 c) { return __builtin_amdgcn_mfma_f32_32x32x16_bf16(a, b, c, 0, 0, 0); }
DEV f32x4 mfma16(bf16x8 a, bf16x8 b, f32x4 c) { return __builtin_amdgcn_mfma_f32_16x16x32_bf16(a, b, c, 0, 0, 0); }
DEV unsigned pack2(float a, float b) {
  f32x2 v = {a, b};
  return __builtin_bit_cast(unsigned, __builtin_convertvector(v, bf16x2));
}
DEV float bflo(unsigned u) { return __builtin_bit_cast(float, u << 16); }
DEV float bfhi(unsigned u) { return __builtin_bit_cast(float, u & 0xffff0000u); }
DEV float dot2(unsigned a, unsigned b, float c) {
  return __builtin_amdgcn_fdot2_f32_bf16(__builtin_bit_cast(bf16x2, a), __builtin_bit_cast(bf16x2, b), c, false);
}
DEV void dma16(const char* gptr, unsigned ldsbase) {
  unsigned sv;
  asm volatile("s_mov_b32 %0, m0\n\ts_mov_b32 m0, %2\n\ts_nop 0\n\tglobal_load_lds_dwordx4 %1, off\n\ts_mov_b32 m0, %0"
               : "=&s"(sv) : "v"(gptr), "s"(ldsbase) : "memory");
}
DEV void dma16s(unsigned voff, const char* sbase, unsigned ldsbase) {
  unsigned sv;
  asm volatile("s_mov_b32 %0, m0\n\ts_mov_b32 m0, %3\n\ts_nop 0\n\tglobal_load_lds_dwordx4 %1, %2\n\ts_mov_b32 m0, %0"
               : "=&s"(sv) : "v"(voff), "s"(sbase), "s"(ldsbase) : "memory");
}
DEV void dma16sm(unsigned voff, const char* sbase, unsigned ldsbase, unsigned long long mask) {
  unsigned sv;
  asm volatile("s_mov_b32 %0, m0\n\ts_mov_b32 m0, %3\n\ts_mov_b64 exec, %4\n\tglobal_load_lds_dwordx4 %1, %2\n\ts_mov_b64 exec, -1\n\ts_mov_b32 m0, %0"
               : "=&s"(sv) : "v"(voff), "s"(sbase), "s"(ldsbase), "s"(mask) : "memory");
}
DEV void gld16(bf16x8& d, unsigned voff, const char* sbase) {
  asm volatile("global_load_dwordx4 %0, %1, %2" : "=v"(d) : "v"(voff), "s"(sbase) : "memory");
}
DEV void gld8(u32x2& d, unsigned voff, const char* sbase) {
  asm volatile("global_load_dwordx2 %0, %1, %2" : "=v"(d) : "v"(voff), "s"(sbase) : "memory");
}
template <int N> DEV void wait_bar() { asm volatile("s_waitcnt vmcnt(%0) lgkmcnt(0)\n\ts_barrier" ::"n"(N) : "memory"); }
DEV void bar() { asm volatile("s_waitcnt lgkmcnt(0)\n\ts_barrier" ::: "memory"); }
DEV bf16x8 lds128(const char* p) { return *(const LAS bf16x8*)(LAS const char*)p; }
DEV s16x4 ldstr(const char* p) { return __builtin_amdgcn_ds_read_tr16_b64_v4i16((LAS s16x4*)p); }
DEV bf16x8 cat(s16x4 a, s16x4 b) { return __builtin_shufflevector(a, b, 0, 1, 2, 3, 4, 5, 6, 7); }
DEV float xsum(float v) { v += __shfl_xor(v, 16); v += __shfl_xor(v, 32); return v; }
DEV float xmax(float v) { v = fmaxf(v, __shfl_xor(v, 16)); v = fmaxf(v, __shfl_xor(v, 32)); return v; }
DEV float dpp_xor1(float v) { return __builtin_bit_cast(float, __builtin_amdgcn_mov_dpp(__builtin_bit_cast(int, v), 0xB1, 0xF, 0xF, true)); }
DEV float dpp_xor2(float v) { return __builtin_bit_cast(float, __builtin_amdgcn_mov_dpp(__builtin_bit_cast(int, v), 0x4E, 0xF, 0xF, true)); }
DEV float dpp_ror4(float v) { return __builtin_bit_cast(float, __builtin_amdgcn_mov_dpp(__builtin_bit_cast(int, v), 0x124, 0xF, 0xF, true)); }
DEV float dpp_ror8(float v) { return __builtin_bit_cast(float, __builtin_amdgcn_mov_dpp(__builtin_bit_cast(int, v), 0x128, 0xF, 0xF, true)); }
DEV unsigned uni(unsigned v) { return __builtin_amdgcn_readfirstlane(v); }
#define WT 31
template <bool W> DEV void st16(char* p, u32x4 v) {
  if constexpr (W) asm volatile("global_store_dwordx4 %0, %1, off sc0 sc1\n\ts_nop 1" ::"v"(p), "v"(v) : "memory");
  else *(u32x4*)p = v;
}
template <bool W> DEV void st8(char* p, u32x2 v) {
  if constexpr (W) asm volatile("global_store_dwordx2 %0, %1, off sc0 sc1" ::"v"(p), "v"(v) : "memory");
  else *(u32x2*)p = v;
}

template <int A, int B> struct Min { static constexpr int v = A < B ? A : B; };

template <int KT, int NK, int NS, int P, class Issue, class Compute>
DEV void pipe_steps(Issue& issue, Compute& compute) {
  if constexpr (KT < NK) {
    wait_bar<(Min<KT + NS - 1, NK>::v - KT - 1) * P>();
    if constexpr (KT + NS - 1 < NK) issue((KT + NS - 1) % NS);
    compute(KT % NS, KT);
    pipe_steps<KT + 1, NK, NS, P>(issue, compute);
  }
}

constexpr int K1_STAGE = 16384 + 12288, K1_NS = 5;
#define SB __builtin_amdgcn_sched_barrier(0)
DEV float xsum2(float v) {
  auto a = __builtin_amdgcn_permlane16_swap(__float_as_uint(v), __float_as_uint(v), false, false);
  v = __uint_as_float(a[0]) + __uint_as_float(a[1]);
  auto b = __builtin_amdgcn_permlane32_swap(__float_as_uint(v), __float_as_uint(v), false, false);
  return __uint_as_float(b[0]) + __uint_as_float(b[1]);
}

DEV void qkv_phase(char* smem, unsigned lds0, const char* __restrict__ x, const char* __restrict__ w, char* __restrict__ qkv,
                   int cb, int tb) {
  const int tid = threadIdx.x, lane = tid & 63, wv = uni(tid >> 6);
  const int g = lane >> 4, i = lane & 15, q4 = (lane >> 2) & 3, p4 = lane & 3;
  const int tg = wv & 3, nh = wv >> 2;
  const int bi = blockIdx.x >> 3;

  const char* gx = x + (size_t)(tb * 128 + wv * 16) * 1024;
  const unsigned vt = (lane >> 3) * 1024 + (((lane & 7) ^ (lane >> 3)) << 4);
  const char* gw = w + cb * 192;
  unsigned vw0, vw1;
  {
    int qq = wv * 64 + lane;
    int k = qq / 12, c = (qq % 12) ^ (((k >> 3) & 1) << 1);
    vw0 = k * 3072 + c * 16;
    qq = 512 + wv * 32 + (lane & 31);
    k = qq / 12; c = (qq % 12) ^ (((k >> 3) & 1) << 1);
    vw1 = k * 3072 + c * 16;
  }
  const int k0 = bi & 7;
  auto dma = [&](int s, int j) {
    const int kk = (k0 + s) & 7;
    const unsigned base = lds0 + (s % K1_NS) * K1_STAGE;
    if (j == 0) dma16s(vt, gx + kk * 128, base + wv * 2048);
    else if (j == 1) dma16s(vt, gx + kk * 128 + 8192, base + wv * 2048 + 1024);
    else if (j == 2) dma16s(vw0, gw + kk * (64 * 3072), base + 16384 + wv * 1024);
    else dma16sm(vw1, gw + kk * (64 * 3072), base + 16384 + 8192 + wv * 512, 0xFFFFFFFFull);
  };
#pragma unroll
  for (int s = 0; s < 2; ++s)
#pragma unroll
    for (int j = 0; j < 4; ++j) dma(s, j);

  f32x4 acc[2][3], cs = f32x4{0, 0, 0, 0}, s1v = f32x4{0, 0, 0, 0}, gr = f32x4{0, 0, 0, 0};
#pragma unroll
  for (int a = 0; a < 3; ++a) { acc[0][a] = f32x4{0, 0, 0, 0}; acc[1][a] = f32x4{0, 0, 0, 0}; }
  const bf16x8 ones = {0x3F80, 0x3F80, 0x3F80, 0x3F80, 0x3F80, 0x3F80, 0x3F80, 0x3F80};
  int toff[2][2];
#pragma unroll
  for (int tt = 0; tt < 2; ++tt) {
    toff[tt][0] = (tg * 32 + (tt ^ nh) * 16 + i) * 128 + ((g ^ (i & 7)) << 4);
    toff[tt][1] = (tg * 32 + (tt ^ nh) * 16 + i) * 128 + (((4 + g) ^ (i & 7)) << 4);
  }
  int woff[3], un[3];
#pragma unroll
  for (int nt = 0; nt < 3; ++nt) {
    int a = nt + tg; a = a >= 3 ? a - 3 : a; a = a >= 3 ? a - 3 : a;
    un[nt] = nh * 3 + a;
    woff[nt] = 16384 + (8 * g + q4) * 192 + ((un[nt] ^ (g & 1)) << 5) + p4 * 8;
  }
  auto ldB = [&](int tt, int kt, int ks) -> bf16x8 { return lds128(smem + (kt % K1_NS) * K1_STAGE + toff[tt][ks]); };
  auto ldA = [&](int nt, int kt, int ks) -> bf16x8 {
    const char* p = smem + (kt % K1_NS) * K1_STAGE + woff[nt] + ks * 6144;
    return cat(ldstr(p), ldstr(p + 768));
  };
  bf16x8 A0[3], A1[3], B0[2], B1[2];
  auto phase = [&](bf16x8 (&Ac)[3], bf16x8 (&Bc)[2], bf16x8 (&An)[3], bf16x8 (&Bn)[2], int kn, int ksn, int dk, int dj, int nd) {
#pragma unroll
    for (int m = 0; m < 9; ++m) {
      if (kn < 8) {
        if (m == 0) { Bn[0] = ldB(0, kn, ksn); An[0] = ldA(0, kn, ksn); }
        else if (m == 1) Bn[1] = ldB(1, kn, ksn);
        else if (m < 4) An[m - 1] = ldA(m - 1, kn, ksn);
      }
      if (m < 6) acc[m & 1][m >> 1] = mfma16(Ac[m >> 1], Bc[m & 1], acc[m & 1][m >> 1]);
      else if (m == 6) cs = mfma16(Ac[0], ones, cs);
      else if (m == 7) s1v = mfma16(ones, Bc[0], s1v);
      else gr = mfma16(Bc[0], Bc[0], gr);
      if (dk < 8) {
        if (nd == 2) { if (m == 2 || m == 5) dma(dk, dj + (m == 5)); }
        else { if (m == 1 || m == 2) dma(dk, m - 1); else if (m == 4 || m == 5) dma(dk, m - 2); }
      }
      SB;
    }
  };
  wait_bar<4>();
  B0[0] = ldB(0, 0, 0); A0[0] = ldA(0, 0, 0); B0[1] = ldB(1, 0, 0); A0[1] = ldA(1, 0, 0); A0[2] = ldA(2, 0, 0);
#pragma unroll
  for (int kt = 0; kt < 8; ++kt) {
    if (kt < 2) phase(A0, B0, A1, B1, kt, 1, 2 * kt + 2, 0, 4); else phase(A0, B0, A1, B1, kt, 1, kt + 4, 0, 2);
    if (kt == 0) wait_bar<4>();
    else if (kt == 1) wait_bar<8>();
    else if (kt < 4) wait_bar<10>();
    else if (kt == 4) wait_bar<8>();
    else if (kt == 5) wait_bar<4>();
    else if (kt == 6) wait_bar<0>();
    if (kt < 2) phase(A1, B1, A0, B0, kt + 1, 0, 2 * kt + 3, 0, 4); else phase(A1, B1, A0, B0, kt + 1, 0, kt + 4, 2, 2);
  }

  float* st = (float*)(smem + 49152);
  float* csp = (float*)(smem + 49152 + 1024);
  if (g == (i >> 2)) {
    const float s2 = (i & 2) ? ((i & 1) ? gr[3] : gr[2]) : ((i & 1) ? gr[1] : gr[0]);
    float mean = s1v[0] * (1.f / 512.f);
    float var = fmaxf(s2 * (1.f / 512.f) - mean * mean, 0.f);
    float rstd = rsqrtf(var + LN_EPS);
    *(f32x2*)(st + (tg * 32 + nh * 16 + i) * 2) = f32x2{-mean * rstd, rstd};
  }
  if (i == 0 && tg < 3) *(f32x4*)(csp + un[0] * 16 + 4 * g) = cs;
  bar();
  f32x2 ms[2];
#pragma unroll
  for (int tt = 0; tt < 2; ++tt) ms[tt] = *(const f32x2*)(st + (tg * 32 + (tt ^ nh) * 16 + i) * 2);
  const int otile = (g & 1) ^ nh, ohalf = g >> 1;
  const unsigned vo = ohalf * 512 + (((otile * 16 + i) ^ (8 * ohalf)) << 4);
#pragma unroll
  for (int nt = 0; nt < 3; ++nt) {
    f32x4 c = *(const f32x4*)(csp + un[nt] * 16 + 4 * g);
    const float qs = cb * 6 + un[nt] < 32 ? 0.25f * 1.4426950408889634f : 1.f;
    unsigned o[2][2];
#pragma unroll
    for (int tt = 0; tt < 2; ++tt) {
      f32x4 y = acc[tt][nt] * (ms[tt][1] * qs) + (ms[tt][0] * qs) * c;
      o[tt][0] = pack2(y[0], y[1]); o[tt][1] = pack2(y[2], y[3]);
    }
    auto sx = __builtin_amdgcn_permlane16_swap(o[0][0], o[1][0], false, false);
    auto sy = __builtin_amdgcn_permlane16_swap(o[0][1], o[1][1], false, false);
    char* dst = qkv + (size_t)(cb * 6 + un[nt]) * (2048 * 32) + (size_t)(tb * 4 + tg) * 1024;
    st16<true>(dst + vo, u32x4{sx[0], sy[0], sx[1], sy[1]});
  }
}

constexpr int S0 = 0, S1 = 49152, S2 = 98304, CSP = 147456, MG_LDS = 163840;
typedef unsigned long long u64;
extern "C" __device__ u64 llvm_dispatch_id() __asm("llvm.amdgcn.dispatch.id");
DEV void flag_set(u64* f, u64 tag) {
  asm volatile("global_store_dwordx2 %0, %1, off sc0 sc1" ::"v"(f), "v"(tag) : "memory");
}
DEV void flag_wait(const u64* p, u64 tag) {
  for (int it = 0; it < (1 << 20); ++it) {
    u64 c;
    asm volatile("global_load_dwordx2 %0, %1, off sc0 sc1\n\ts_waitcnt vmcnt(0)" : "=v"(c) : "v"(p) : "memory");
    if (__builtin_amdgcn_ballot_w64(c != tag) == 0) break;
  }
}
template <int V> struct IC { static constexpr int value = V; };
DEV int opaque(int v) { asm volatile("" : "+v"(v)); return v; }
DEV int opaque_s(int v) { asm volatile("" : "+s"(v)); return v; }

struct CW {
  const char *g1, *g3; unsigned vw; unsigned long long wmask;
};
DEV CW c_setup(const char* w1, const char* w3, int b, int wv, int lane) {
  const int xcd = b & 7, bi = b >> 3;
  const int cb = (xcd & 3) * 4 + (bi & 3);
  CW c;
  c.g1 = w1 + (size_t)(wv * 8) * (F_ * 2) + cb * 224;
  c.g3 = w3 + (size_t)(wv * 8) * (F_ * 2) + cb * 224;
  const int unit = ((lane >> 1) & 7) ^ ((lane >> 4) | ((wv & 1) << 2));
  c.vw = (lane >> 4) * (F_ * 2) + unit * 32 + (lane & 1) * 16;
  c.wmask = __builtin_amdgcn_ballot_w64(unit < 7);
  return c;
}
DEV void c_dmaw(const CW& c, unsigned ldsw, int kt, int j) {
  const unsigned base = ldsw + (kt % 3) * 49152;
  if (j < 2) dma16sm(c.vw, c.g1 + (size_t)(kt * 64 + j * 4) * (F_ * 2), base + 16384 + j * 1024, c.wmask);
  else dma16sm(c.vw, c.g3 + (size_t)(kt * 64 + (j - 2) * 4) * (F_ * 2), base + 32768 + (j - 2) * 1024, c.wmask);
}
DEV constexpr int d_unit(int u) {
  return u < 3 ? S2 + u * 2048 : u < 6 ? S0 + (u - 3) * 2048 : u < 9 ? S1 + (u - 6) * 2048 : u == 9 ? CSP
       : u < 13 ? S2 + (u - 10) * 2048 : S0;
}
DEV constexpr int d_ustr(int u) { return u == 9 ? 2048 : 6144; }
DEV void d_dma(const char* gw2, unsigned vw, unsigned lds0, int wv, int u, int q) {
  dma16s(vw, gw2 + (size_t)(u * 16 + q * 8) * 1024, lds0 + d_unit(u) + wv * d_ustr(u) + q * 1024);
}

template <int NT>
DEV void c_body(char* smem, unsigned lds0, const char* gt, const CW& cw, unsigned vt, int wv, int lane,
                const char* __restrict__ stats, const char* __restrict__ csb, char* __restrict__ hh, int tok0, int cb, const char* gw2, unsigned vwd) {
  const int tg = wv & 3;
  const unsigned ldsw = lds0 + wv * 2048;
  const int g = lane >> 4, i = lane & 15, q4 = (lane >> 2) & 3, p4 = lane & 3;
  f32x4 acc[2][2 * NT];
#pragma unroll
  for (int a = 0; a < 2 * NT; ++a) { acc[0][a] = f32x4{0, 0, 0, 0}; acc[1][a] = f32x4{0, 0, 0, 0}; }
  const int toff0 = (tg * 32 + i) * 128 + ((g ^ (i & 7)) << 4);
  const int toff1 = (tg * 32 + i) * 128 + (((4 + g) ^ (i & 7)) << 4);
  const int odd = cb & 1;
  const int fsw = q4 | ((g & 1) << 2);
  int T[NT], wa[NT];
#pragma unroll
  for (int a = 0; a < NT; ++a) {
    T[a] = NT == 4 ? odd * 3 + a : (a < 2 ? (odd ? 1 : 4) + a : (odd ? 0 : 6));
    wa[a] = 16384 + (8 * g + q4) * 256 + ((T[a] ^ fsw) << 5) + p4 * 8;
  }
  auto dmat = [&](int kt, int j) { dma16s(vt, gt + kt * 128 + j * 8192, ldsw + (kt % 3) * 49152 + j * 1024); };
  auto dma = [&](int kt, int j) {
    if (j < 2) dmat(kt, j); else c_dmaw(cw, ldsw, kt, j - 2);
  };
  auto ldB = [&](bf16x8* B, int kt, int ks) {
    const char* sb = smem + (kt % 3) * 49152 + (ks ? toff1 : toff0);
    B[0] = lds128(sb); B[1] = lds128(sb + 2048);
  };
  auto ldA = [&](int a, int kt, int ks) -> bf16x8 {
    const char* wp = smem + (kt % 3) * 49152 + wa[a < NT ? a : a - NT] + (a < NT ? 0 : 16384) + ks * 8192;
    return cat(ldstr(wp), ldstr(wp + 1024));
  };
  auto mma = [&](int a, bf16x8 A, const bf16x8* B) {
    acc[0][a] = mfma16(A, B[0], acc[0][a]);
    acc[1][a] = mfma16(A, B[1], acc[1][a]);
  };
  bf16x8 A0[2 * NT], A1[2 * NT], B0[2], B1[2];
  bf16x8 cp[4];
  u32x2 sa[4];
#pragma unroll
  for (int kt = 0; kt < 3; ++kt) { dmat(kt, 0); dmat(kt, 1); }
#pragma unroll
  for (int m = 0; m < 4; ++m)
    gld8(sa[m], (unsigned)((i >> 3) * 512 + g * 128 + (i & 7) * 8), stats + (size_t)(tok0 + tg * 32 + (m >> 1) * 16) * 64 + (m & 1) * 64);
  const int cq = min(tg * 16 + (lane >> 2), 55);
  if (NT == 3) {
#pragma unroll
    for (int m = 0; m < 4; ++m) gld16(cp[m], (unsigned)(((lane & 3) * 4 + m) * 896 + cq * 16), csb + (size_t)cb * 14336);
  }
  constexpr int X = NT == 3 ? 8 : 4;
  wait_bar<4 + X>();
  float rs[2], mr[2];
  float* csp = (float*)(smem + CSP);
  ldB(B0, 0, 0);
#pragma unroll
  for (int a = 0; a < 2 * NT; ++a) A0[a] = ldA(a, 0, 0);
#pragma unroll
  for (int kt = 0; kt < 8; ++kt) {
    ldB(B1, kt, 1);
#pragma unroll
    for (int a = 0; a < 2 * NT; ++a) {
      if (a < NT) { A1[2 * a] = ldA(2 * a, kt, 1); A1[2 * a + 1] = ldA(2 * a + 1, kt, 1); }
      mma(a, A0[a], B0);
      __builtin_amdgcn_sched_barrier(0);
    }
    if (kt == 0) wait_bar<2 + X>();
    else if (kt != 1) wait_bar<6>();
    else if (NT == 3) asm volatile("s_waitcnt vmcnt(6) lgkmcnt(0)\n\ts_barrier" : "+v"(sa[0]), "+v"(sa[1]), "+v"(sa[2]), "+v"(sa[3]), "+v"(cp[0]), "+v"(cp[1]), "+v"(cp[2]), "+v"(cp[3]) : : "memory");
    else asm volatile("s_waitcnt vmcnt(6) lgkmcnt(0)\n\ts_barrier" : "+v"(sa[0]), "+v"(sa[1]), "+v"(sa[2]), "+v"(sa[3]) : : "memory");
    if (kt < 7) ldB(B0, kt + 1, 0);
    if (kt == 1) {
#pragma unroll
      for (int tt = 0; tt < 2; ++tt) {
        const f32x2 sv0 = __builtin_bit_cast(f32x2, sa[2 * tt]), sv1 = __builtin_bit_cast(f32x2, sa[2 * tt + 1]);
        const float s1 = xsum2(sv0[0] + sv1[0]), s2 = xsum2(sv0[1] + sv1[1]);
        const float mean = s1 * (1.f / 512.f);
        const float var = fmaxf(s2 * (1.f / 512.f) - mean * mean, 0.f);
        rs[tt] = rsqrtf(var + LN_EPS); mr[tt] = mean * rs[tt];
      }
      if (NT == 3) {
        f32x4 c = (__builtin_bit_cast(f32x4, cp[0]) + __builtin_bit_cast(f32x4, cp[1])) +
                  (__builtin_bit_cast(f32x4, cp[2]) + __builtin_bit_cast(f32x4, cp[3]));
#pragma unroll
        for (int e = 0; e < 4; ++e) { c[e] += dpp_xor1(c[e]); c[e] += dpp_xor2(c[e]); }
        if ((lane & 3) == 0) *(f32x4*)(csp + cq * 4) = c;
      }
    }
#pragma unroll
    for (int a = 0; a < 2 * NT; ++a) {
      if (kt < 7 && a < NT) { A0[2 * a] = ldA(2 * a, kt + 1, 0); A0[2 * a + 1] = ldA(2 * a + 1, kt + 1, 0); }
      mma(a, A1[a], B1);
      if (a >= 2 * NT - 6) {
        const int j = a - (2 * NT - 6);
        if (kt + 3 < 8) dma(kt + 3, j);
        else d_dma(gw2, vwd, lds0, wv, (kt - 5) * 3 + (j >> 1), j & 1);
      }
      __builtin_amdgcn_sched_barrier(0);
    }
  }
  unsigned pk[2][NT][2];
#pragma unroll
  for (int tt = 0; tt < 2; ++tt) {
    char* dst = hh + ((size_t)((tok0 >> 4) + tg * 2 + tt) * 56) * 1024 + lane * 16;
#pragma unroll
    for (int a = 0; a < NT; ++a) {
      f32x4 cg = *(const f32x4*)(csp + T[a] * 16 + 4 * g);
      f32x4 cu = *(const f32x4*)(csp + (7 + T[a]) * 16 + 4 * g);
      float r[4];
#pragma unroll
      for (int e = 0; e < 4; ++e) {
        float ga = acc[tt][a][e] * rs[tt] - mr[tt] * cg[e];
        float up = acc[tt][NT + a][e] * rs[tt] - mr[tt] * cu[e];
        float sg = __builtin_amdgcn_rcpf(1.f + __builtin_amdgcn_exp2f(-1.4426950408889634f * ga));
        r[e] = ga * sg * up;
      }
      pk[tt][a][0] = pack2(r[0], r[1]); pk[tt][a][1] = pack2(r[2], r[3]);
      const int G = cb * 7 + T[a];
      if (a & 1) {
        st16<true>(dst + (size_t)(G >> 1) * 1024, u32x4{pk[tt][a - 1][0], pk[tt][a - 1][1], pk[tt][a][0], pk[tt][a][1]});
      } else if (a == NT - 1) {
        st8<true>(dst + (size_t)(G >> 1) * 1024 + (G & 1) * 512 - lane * 8, u32x2{pk[tt][a][0], pk[tt][a][1]});
      }
    }
  }
}

__global__ __launch_bounds__(512) void k_mega(const char* __restrict__ x, const char* __restrict__ wqkv, const char* __restrict__ wo,
                                              const char* __restrict__ w1, const char* __restrict__ w3,
                                              const char* __restrict__ w2, char* __restrict__ ws, char* __restrict__ out) {
  extern __shared__ __attribute__((aligned(16))) char smem[];
  char* qkv = ws;
  char* o = ws + (6u << 20);
  char* h = ws + (8u << 20);
  char* stats = ws + (10u << 20);
  u64* flags = (u64*)(ws + (10u << 20) + (512u << 10));
  char* csb = ws + (10u << 20) + (768u << 10);
  char* hh = ws + (11u << 20);
  const u64 tag = ((u64)(size_t)__builtin_amdgcn_dispatch_ptr() << 20) + llvm_dispatch_id();
  const int b = blockIdx.x;
  const unsigned lds0 = (unsigned)(size_t)(LAS char*)smem;
  const int cbG = (b >> 3) & 7, tbG = (b & 7) * 4 + (b >> 6);
  const int cbC = (b & 3) * 4 + ((b >> 3) & 3), tbC = ((b >> 2) & 1) * 8 + (b >> 5);

  unsigned cpf = 0;
  {
    u64 pc;
    unsigned len;
    asm volatile("s_getpc_b64 %0\n.Lmega_pc:\n\ts_mov_b32 %1, .Lmega_end-.Lmega_pc" : "=s"(pc), "=s"(len));
    const unsigned off = ((b >> 3) * 26 + threadIdx.x) * 64;
    if (threadIdx.x < 26 && off < len) asm volatile("global_load_dword %0, %1, %2" : "=v"(cpf) : "v"(off), "s"(pc) : "memory");
  }
  {
    const int cb = (b & 1) * 8 + ((b >> 3) & 7), tb = ((b & 7) >> 1) * 4 + (b >> 6);
    qkv_phase(smem, lds0, x, wqkv, qkv, cb, tb);
    asm volatile("s_waitcnt vmcnt(0) lgkmcnt(0)\n\ts_barrier" ::: "memory");
    asm volatile("" :: "v"(cpf));
    const int tid = threadIdx.x;
    if (tid < 9) {
      const int P = cb * 3 + tid / 3, d = tid % 3, which = P >> 4, hp = P & 15, qb = tb + d;
      if (which ? qb < 16 : d == 0)
        flag_set(flags + 24576 + ((hp >> 1) | ((hp & 1) << 3) | (qb << 4)) * 32 + (which ? (which - 1) * 3 + d : 6), tag);
    }
  }
  {
    const int tid = opaque(threadIdx.x), lane = tid & 63, wv = uni(tid >> 6);
    const int c32 = lane & 31, hf = lane >> 5, q4 = (lane >> 2) & 3, p4 = lane & 3;
    const int hp = (b & 7) * 2 + ((b >> 3) & 1), qb = b >> 4;
    const int q0 = qb * 128;
    const size_t HS = 2048 * 32;
    bf16x8 Q, cw0, cw1;
    {
      const unsigned one = qb * 4 - 8 + wv >= 0 ? 0x3F803F80u : 0u;
      *(LAS u32x4*)(LAS char*)(smem + S1 + tid * 16) = u32x4{one, one, one, one};
      if (tid < 256) *(LAS u32x4*)(LAS char*)(smem + S1 + 8192 + tid * 16) = u32x4{0x3F803F80u, 0x3F803F80u, 0x3F803F80u, 0x3F803F80u};
    }
    if (wv == 0) {
      const int d = lane % 3;
      flag_wait(flags + 24576 + b * 32 + (lane >= 6 ? 6 : qb - d < 0 ? 6 : lane), tag);
    }
    asm volatile("s_barrier" ::: "memory");
    {
      const int r = wv >> 1;
      const char* src = qkv + (size_t)((1 + (r >> 1)) * 32 + hp * 2 + (r & 1)) * HS;
#pragma unroll
      for (int j = 0; j < 6; ++j) {
        const int kb = qb * 4 - 8 + (wv & 1) * 6 + j;
        if (kb < 0 && r >= 2) *(LAS u32x4*)(LAS char*)(smem + r * 12288 + ((wv & 1) * 6 + j) * 1024 + lane * 16) = u32x4{0, 0, 0, 0};
        else dma16s(kb < 0 ? hf * 640 : lane * 16, src + (size_t)max(kb, 0) * 1024, lds0 + r * 12288 + ((wv & 1) * 6 + j) * 1024);
      }
    }
    const int u = wv >> 2, qg = wv & 3;
    const int qs = q0 + qg * 32;
    const unsigned vq = hf * 512 + ((c32 ^ (8 * hf)) << 4);
    gld16(Q, vq, qkv + (size_t)(hp * 2 + u) * HS + (size_t)qs * 32);
    const float NEG = -1e30f;
    f32x16 cin0, cin8;
    u32x4 mk[2];
#pragma unroll
    for (int r = 0; r < 16; ++r) {
      const int kl = (r & 3) + 8 * (r >> 2) + 4 * hf;
      cin0[r] = kl > c32 ? 0.f : NEG; cin8[r] = kl <= c32 ? 0.f : NEG;
      if (r & 1) mk[r >> 3][(r >> 1) & 3] = (kl > c32 ? 0xFFFF0000u : 0u) | (kl - 1 > c32 ? 0xFFFFu : 0u);
    }
    asm volatile("s_waitcnt vmcnt(0) lgkmcnt(0)\n\ts_barrier" : "+v"(Q), "+v"(cin0), "+v"(cin8), "+v"(mk[0]), "+v"(mk[1]) : : "memory");
    const unsigned vwo = (lane >> 3) * 1024 + ((((lane & 7) >> 1) ^ ((lane >> 4) & 3)) << 5) + (lane & 1) * 16;
    const char* gwo = wo + (size_t)(wv * 64) * 1024 + cbG * 128;
    const CW cw1s = c_setup(w1, w3, b, wv, lane);
    auto pf = [&](int k) {
      if (k < 2) {
        const int ch = max(wv - 1, 0) * 4 + (lane & 3);
        const char* pc = (ch >= 14 ? w3 + (ch - 14) * 16 : w1 + ch * 16) + (size_t)(tbC * 32 + (lane >> 2)) * (F_ * 2) + cbC * 224;
        if (k == 0) asm volatile("global_load_dwordx4 %0, %1, off" : "=v"(cw0) : "v"(pc) : "memory");
        else asm volatile("global_load_dwordx4 %0, %1, off" : "=v"(cw1) : "v"(pc + 16 * (F_ * 2)) : "memory");
      } else if (k < 10) dma16s(vwo, gwo + (size_t)((k - 2) * 8) * 1024, lds0 + S2 + wv * 8192 + (k - 2) * 1024);
      else if (k < 14) c_dmaw(cw1s, lds0 + wv * 2048, 1, k - 10);
      else if (k == 14) {
        const int c0 = b * 8 + wv;
        dma16s(lane * 16, w2 + (size_t)(c0 < 1792 ? c0 : c0 - 1792) * 1024, lds0 + S1 + 12288 + (wv & 3) * 1024);
      }
    };
    {
      const char* Kl = smem + u * 12288 + qg * 1024 + vq;
      const char* Vl[2];
#pragma unroll
      for (int e = 0; e < 2; ++e)
        Vl[e] = c32 < 16 ? smem + 24576 + u * 12288 + qg * 1024 + (p4 >> 1) * 512 + (((e * 8 + 4 * hf + q4) ^ (8 * (p4 >> 1))) << 4) + (p4 & 1) * 8
                         : smem + S1 + qg * 1024 + (hf ? 0 : 64);
      f32x16 z;
#pragma unroll
      for (int r = 0; r < 16; ++r) z[r] = 0.f;
      bf16x8 Kf[9];
#pragma unroll
      for (int t = 0; t < 9; ++t) Kf[t] = lds128(Kl + t * 1024);
      f32x16 sm = mfma32(Kf[0], Q, cin0);
      {
        const f32x16 s8 = mfma32(Kf[8], Q, cin8);
#pragma unroll
        for (int r = 0; r < 16; ++r) sm[r] = fmaxf(sm[r], s8[r]);
      }
      float m = sm[0];
#pragma unroll
      for (int r = 1; r < 16; ++r) m = fmaxf(m, sm[r]);
#pragma unroll
      for (int t = 1; t < 8; ++t) {
        const f32x16 s = mfma32(Kf[t], Q, z);
#pragma unroll
        for (int r = 0; r < 16; ++r) m = fmaxf(m, s[r]);
      }
      {
        auto sw = __builtin_amdgcn_permlane32_swap(__float_as_uint(m), __float_as_uint(m), false, false);
        m = fmaxf(__uint_as_float(sw[0]), __uint_as_float(sw[1]));
      }
      f32x16 cm, oacc;
#pragma unroll
      for (int r = 0; r < 16; ++r) { cm[r] = -m; oacc[r] = 0.f; }
      f32x16 sn = mfma32(Kf[1], Q, cm);
      {
        u32x4 pb[2];
#pragma unroll
        for (int j = 0; j < 8; ++j)
          pb[j >> 2][j & 3] = pack2(__builtin_amdgcn_exp2f(sm[2 * j] - m), __builtin_amdgcn_exp2f(sm[2 * j + 1] - m));
#pragma unroll
        for (int sh = 0; sh < 2; ++sh) {
          const u32x4 p0 = pb[sh] & mk[sh], p8 = pb[sh] ^ p0;
          oacc = mfma32(cat(ldstr(Vl[0] + sh * 256), ldstr(Vl[1] + sh * 256)), __builtin_bit_cast(bf16x8, p0), oacc);
          pf(2 * sh);
          oacc = mfma32(cat(ldstr(Vl[0] + 8192 + sh * 256), ldstr(Vl[1] + 8192 + sh * 256)), __builtin_bit_cast(bf16x8, p8), oacc);
          pf(2 * sh + 1);
        }
      }
      SB;
#pragma unroll
      for (int t = 1; t < 8; ++t) {
        const f32x16 s = sn;
        if (t < 7) sn = mfma32(Kf[t + 1], Q, cm);
#pragma unroll
        for (int sh = 0; sh < 2; ++sh) {
          u32x4 pb;
#pragma unroll
          for (int e = 0; e < 4; ++e)
            pb[e] = pack2(__builtin_amdgcn_exp2f(s[8 * sh + 2 * e]), __builtin_amdgcn_exp2f(s[8 * sh + 2 * e + 1]));
          const int vp = t * 1024 + sh * 256;
          oacc = mfma32(cat(ldstr(Vl[0] + vp), ldstr(Vl[1] + vp)), __builtin_bit_cast(bf16x8, pb), oacc);
          if (2 * t + sh + 2 < 15) pf(2 * t + sh + 2);
        }
        SB;
      }
      const float inv = __builtin_amdgcn_rcpf(oacc[8]);
      const u32x4 ov = {pack2(oacc[0] * inv, oacc[1] * inv), pack2(oacc[2] * inv, oacc[3] * inv),
                        pack2(oacc[4] * inv, oacc[5] * inv), pack2(oacc[6] * inv, oacc[7] * inv)};
      st16<true>(o + ((size_t)((qs >> 4) + (c32 >> 4)) * 16 + hp) * 1024 + ((2 * u + hf) * 16 + (c32 & 15)) * 16, ov);
    }
    asm volatile("s_waitcnt vmcnt(0) lgkmcnt(0)\n\ts_barrier" : "+v"(cw0), "+v"(cw1) : : "memory");
    if (tid < 16) flag_set(flags + (((qb >> 1) | (tid << 3) | ((qb & 1) << 7)) * 32 + hp), tag);
    {
      const CW cw = c_setup(w1, w3, b, wv, lane);
#pragma unroll
      for (int j = 0; j < 4; ++j) c_dmaw(cw, lds0 + wv * 2048, 0, j);
    }
    if (wv == 0) { flag_wait(flags + b * 32 + (lane & 15), tag); }
    else {
      const u32x4 ua = __builtin_bit_cast(u32x4, cw0), ub = __builtin_bit_cast(u32x4, cw1);
      f32x4 lo, hi;
#pragma unroll
      for (int e = 0; e < 2; ++e) {
        lo[2 * e] = bflo(ua[e]) + bflo(ub[e]); lo[2 * e + 1] = bfhi(ua[e]) + bfhi(ub[e]);
        hi[2 * e] = bflo(ua[2 + e]) + bflo(ub[2 + e]); hi[2 * e + 1] = bfhi(ua[2 + e]) + bfhi(ub[2 + e]);
      }
#pragma unroll
      for (int e = 0; e < 4; ++e) {
        lo[e] += dpp_ror4(lo[e]); lo[e] += dpp_ror8(lo[e]); lo[e] = xsum2(lo[e]);
        hi[e] += dpp_ror4(hi[e]); hi[e] += dpp_ror8(hi[e]); hi[e] = xsum2(hi[e]);
      }
      if (lane < 8) {
        char* d = csb + (size_t)(cbC * 16 + tbC) * 896 + ((wv - 1) * 4 + (lane & 3)) * 32 + (lane >> 2) * 16;
        st16<true>(d, __builtin_bit_cast(u32x4, lane < 4 ? lo : hi));
      }
      asm volatile("s_waitcnt vmcnt(0)" ::: "memory");
    }
    asm volatile("s_barrier" ::: "memory");
    if (tid >= 64 && tid < 80) flag_set(flags + 8192 + ((cbC >> 2) | ((tid & 1) << 2) | ((cbC & 3) << 3) | (((tid & 15) >> 1) << 5)) * 32 + 16 + tbC, tag);
  }
  {
    const int tid = opaque(threadIdx.x), lane = tid & 63, wv = uni(tid >> 6);
    const int g = lane >> 4, i = lane & 15, q4 = (lane >> 2) & 3, p4 = lane & 3;
    const int kp = wv & 3, th = wv >> 2;
    bf16x8 B[4][2], rs;
    const char* ga = o + ((size_t)(tbG * 4 + th * 2) * 16 + kp * 4) * 1024;
#pragma unroll
    for (int s = 0; s < 4; ++s) {
      gld16(B[s][0], lane * 16, ga + s * 1024);
      gld16(B[s][1], lane * 16, ga + (16 + s) * 1024);
    }
    const size_t ooff = (size_t)(tbG * 64 + wv * 8) * 1024 + cbG * 128;
    const unsigned vo = (lane >> 3) * 1024 + (lane & 7) * 16;
    gld16(rs, vo, x + ooff);
    const int sw = (2 * (g & 1) + (q4 >> 1)) & 3;
    const char* wr = smem + S2 + kp * 16384 + (16 * (g >> 1) + 4 * (g & 1) + q4) * 128 + p4 * 8;
    bf16x8 A[4][4];
#pragma unroll
    for (int s = 0; s < 4; ++s)
#pragma unroll
      for (int nt = 0; nt < 4; ++nt) {
        const char* p = wr + s * 4096 + ((nt ^ sw) << 5);
        A[s][nt] = cat(ldstr(p), ldstr(p + 1024));
      }
    f32x4 acc[2][4];
#pragma unroll
    for (int t = 0; t < 2; ++t)
#pragma unroll
      for (int n = 0; n < 4; ++n) acc[t][n] = f32x4{0, 0, 0, 0};
    asm volatile("s_waitcnt vmcnt(7)" : "+v"(B[0][0]), "+v"(B[0][1]) : : "memory");
#pragma unroll
    for (int nt = 0; nt < 4; ++nt) { acc[0][nt] = mfma16(A[0][nt], B[0][0], acc[0][nt]); acc[1][nt] = mfma16(A[0][nt], B[0][1], acc[1][nt]); }
    asm volatile("s_waitcnt vmcnt(5)" : "+v"(B[1][0]), "+v"(B[1][1]) : : "memory");
#pragma unroll
    for (int nt = 0; nt < 4; ++nt) { acc[0][nt] = mfma16(A[1][nt], B[1][0], acc[0][nt]); acc[1][nt] = mfma16(A[1][nt], B[1][1], acc[1][nt]); }
    asm volatile("s_waitcnt vmcnt(3)" : "+v"(B[2][0]), "+v"(B[2][1]) : : "memory");
#pragma unroll
    for (int nt = 0; nt < 4; ++nt) { acc[0][nt] = mfma16(A[2][nt], B[2][0], acc[0][nt]); acc[1][nt] = mfma16(A[2][nt], B[2][1], acc[1][nt]); }
    asm volatile("s_waitcnt vmcnt(1)" : "+v"(B[3][0]), "+v"(B[3][1]) : : "memory");
#pragma unroll
    for (int nt = 0; nt < 4; ++nt) { acc[0][nt] = mfma16(A[3][nt], B[3][0], acc[0][nt]); acc[1][nt] = mfma16(A[3][nt], B[3][1], acc[1][nt]); }
    bar();
    {
      const int mo = S2 + kp * 16384 + g * 256 + ((i ^ ((g >> 1) * 2)) << 4);
#pragma unroll
      for (int tt = 0; tt < 2; ++tt)
#pragma unroll
        for (int nt = 0; nt < 4; ++nt)
          *(LAS f32x4*)(LAS char*)(smem + (mo ^ (nt * 64)) + ((th * 2 + tt) * 4 + nt) * 1024) = acc[tt][nt];
    }
    asm volatile("s_waitcnt vmcnt(0) lgkmcnt(0)\n\ts_barrier" : "+v"(rs) : : "memory");
    {
      const int c = lane & 7;
      const int tok = (lane >> 3) + 8 * (wv & 1);
      const char* rp = smem + S2 + ((wv >> 1) * 4 + (c >> 1)) * 1024 + (c & 1) * 512 + ((tok ^ (2 * c)) << 4);
      f32x4 v0 = f32x4{0, 0, 0, 0}, v1 = f32x4{0, 0, 0, 0};
#pragma unroll
      for (int p = 0; p < 4; ++p) {
        v0 += *(const LAS f32x4*)(LAS const char*)(rp + p * 16384);
        v1 += *(const LAS f32x4*)(LAS const char*)(rp + p * 16384 + 256);
      }
      u32x4 r = __builtin_bit_cast(u32x4, rs);
      u32x4 ov = {pack2(v0[0] + bflo(r[0]), v0[1] + bfhi(r[0])), pack2(v0[2] + bflo(r[1]), v0[3] + bfhi(r[1])),
                  pack2(v1[0] + bflo(r[2]), v1[1] + bfhi(r[2])), pack2(v1[2] + bflo(r[3]), v1[3] + bfhi(r[3]))};
      st16<true>(h + ooff + vo, ov);
      float s1 = 0.f, s2 = 0.f;
#pragma unroll
      for (int e = 0; e < 4; ++e) { s1 = dot2(ov[e], 0x3F803F80u, s1); s2 = dot2(ov[e], ov[e], s2); }
#pragma unroll
      for (int m = 1; m < 8; m <<= 1) { s1 += __shfl_xor(s1, m); s2 += __shfl_xor(s2, m); }
      if (c == 0) st8<true>(stats + ((size_t)(tbG * 8 + wv) * 8 + cbG) * 64 + (lane >> 3) * 8,
                            u32x2{__float_as_uint(s1), __float_as_uint(s2)});
    }
    asm volatile("s_waitcnt vmcnt(0) lgkmcnt(0)\n\ts_barrier" ::: "memory");
    if (tid < 16) flag_set(flags + 8192 + ((tid & 3) | ((tbG >> 4) << 2) | ((tid >> 2) << 3) | (((tbG >> 1) & 7) << 5)) * 32 + (tbG & 1) * 8 + cbG, tag);
    {
      const CW cw = c_setup(w1, w3, b, wv, lane);
#pragma unroll
      for (int j = 0; j < 4; ++j) c_dmaw(cw, lds0 + wv * 2048, 2, j);
    }
    if (wv == 0) flag_wait(flags + 8192 + b * 32 + (lane & 31), tag);
    asm volatile("s_barrier" ::: "memory");
  }
  const unsigned vwd_ = 0;
  {
    const int tid = opaque(threadIdx.x), lane = tid & 63, wv = uni(tid >> 6);
    const char* gt = h + (size_t)(tbC * 128 + wv * 16) * 1024;
    const unsigned vt = (lane >> 3) * 1024 + (((lane & 7) ^ (lane >> 3)) << 4);
    const CW cw = c_setup(w1, w3, b, wv, lane);
    const unsigned vwd = (lane >> 3) * 1024 + ((((lane & 7) >> 1) ^ ((lane >> 4) & 3)) << 5) + (lane & 1) * 16;
    const char* gw2 = w2 + (size_t)(wv * 224) * 1024 + cbG * 128;
    if ((wv >> 2) == 0) c_body<4>(smem, lds0, gt, cw, vt, wv, lane, stats, csb, hh, tbC * 128, cbC, gw2, vwd);
    else c_body<3>(smem, lds0, gt, cw, vt, wv, lane, stats, csb, hh, tbC * 128, cbC, gw2, vwd);
  }
  {
    const int tid = opaque(threadIdx.x), lane = tid & 63, wv = uni(tid >> 6);
    const int g = lane >> 4, i = lane & 15, q4 = (lane >> 2) & 3, p4 = lane & 3;
    asm volatile("s_waitcnt vmcnt(0) lgkmcnt(0)\n\ts_barrier" ::: "memory");
    if (tid < 16) flag_set(flags + 16384 + ((tbC >> 1) | (tid << 3) | ((tbC & 1) << 7)) * 32 + cbC, tag);
    const unsigned vwd = (lane >> 3) * 1024 + ((((lane & 7) >> 1) ^ ((lane >> 4) & 3)) << 5) + (lane & 1) * 16;
    const char* gw2 = w2 + (size_t)(wv * 224) * 1024 + cbG * 128;
    d_dma(gw2, vwd, lds0, wv, 9, 0); d_dma(gw2, vwd, lds0, wv, 9, 1);
    const int sw = (2 * g + (q4 >> 1)) & 3;
    const int ro = (4 * g + q4) * 128 + p4 * 8;
    int rn[4];
#pragma unroll
    for (int nt = 0; nt < 4; ++nt) rn[nt] = ro + ((nt ^ sw) << 5);
    auto lda = [&](bf16x8* Af, int j) {
      const char* p0 = smem + d_unit(2 * j) + wv * d_ustr(2 * j);
      const char* p1 = smem + d_unit(2 * j + 1) + wv * d_ustr(2 * j + 1);
#pragma unroll
      for (int nt = 0; nt < 4; ++nt) Af[nt] = cat(ldstr(p0 + rn[nt]), ldstr(p1 + rn[nt]));
    };
    bf16x8 A[2][4], B[7][4], rs;
    lda(A[0], 0); lda(A[1], 1);
    asm volatile("s_waitcnt lgkmcnt(0)" : "+v"(A[0][0]), "+v"(A[0][1]), "+v"(A[0][2]), "+v"(A[0][3]), "+v"(A[1][0]), "+v"(A[1][1]), "+v"(A[1][2]), "+v"(A[1][3]) : : "memory");
#pragma unroll
    for (int u = 10; u < 14; ++u) { d_dma(gw2, vwd, lds0, wv, u, 0); d_dma(gw2, vwd, lds0, wv, u, 1); }
    if (wv == 0) flag_wait(flags + 16384 + b * 32 + (lane & 15), tag);
    asm volatile("s_barrier" ::: "memory");
    const char* ga = hh + ((size_t)(tbG * 4) * 56 + wv * 7) * 1024;
    u32x2 Bl[4], Bh[4];
#pragma unroll
    for (int j = 0; j < 7; ++j)
#pragma unroll
      for (int tt = 0; tt < 4; ++tt) {
        if (j == 3) {
          gld8(Bl[tt], lane * 8, ga + (size_t)(tt * 56 + j) * 1024);
          gld8(Bh[tt], lane * 8, ga + (size_t)(tt * 56 + j) * 1024 + 512);
        } else gld16(B[j][tt], lane * 16, ga + (size_t)(tt * 56 + j) * 1024);
      }
    const size_t ooff = (size_t)(tbG * 64 + wv * 8) * 1024 + cbG * 128;
    const unsigned vo = (lane >> 3) * 1024 + (lane & 7) * 16;
    gld16(rs, vo, h + ooff);
    f32x4 acc[4][4];
#pragma unroll
    for (int t = 0; t < 4; ++t)
#pragma unroll
      for (int n = 0; n < 4; ++n) acc[t][n] = f32x4{0, 0, 0, 0};
    auto step = [&](auto J) {
      constexpr int j = decltype(J)::value;
      if constexpr (j == 3) {
        asm volatile("s_waitcnt vmcnt(13)" : "+v"(Bl[0]), "+v"(Bl[1]), "+v"(Bl[2]), "+v"(Bl[3]), "+v"(Bh[0]), "+v"(Bh[1]), "+v"(Bh[2]), "+v"(Bh[3]) : : "memory");
#pragma unroll
        for (int tt = 0; tt < 4; ++tt) B[3][tt] = __builtin_bit_cast(bf16x8, u32x4{Bl[tt][0], Bl[tt][1], Bh[tt][0], Bh[tt][1]});
      } else
        asm volatile("s_waitcnt vmcnt(%4)" : "+v"(B[j][0]), "+v"(B[j][1]), "+v"(B[j][2]), "+v"(B[j][3]) : "n"((j < 3 ? 29 : 25) - 4 * j) : "memory");
#pragma unroll
      for (int nt = 0; nt < 4; ++nt)
#pragma unroll
        for (int tt = 0; tt < 4; ++tt) acc[tt][nt] = mfma16(A[j & 1][nt], B[j][tt], acc[tt][nt]);
      __builtin_amdgcn_sched_barrier(0);
      if constexpr (j + 2 < 7) lda(A[j & 1], j + 2);
    };
    step(IC<0>{}); step(IC<1>{}); step(IC<2>{}); step(IC<3>{}); step(IC<4>{}); step(IC<5>{}); step(IC<6>{});
    {
      const int mo = wv * 6144 + g * 256 + ((i ^ ((g >> 1) * 2)) << 4);
#pragma unroll
      for (int tt = 0; tt < 4; ++tt)
#pragma unroll
        for (int nt = 0; nt < 4; ++nt) {
          const int t = tt * 4 + nt;
          const int base = t < 6 ? S2 + t * 1024 : t < 12 ? S0 + (t - 6) * 1024 : S1 + (t - 12) * 1024;
          *(LAS f32x4*)(LAS char*)(smem + (mo ^ (nt * 64)) + base) = acc[tt][nt];
        }
    }
    asm volatile("s_waitcnt vmcnt(0) lgkmcnt(0)\n\ts_barrier" : "+v"(rs) : : "memory");
    {
      const int c = lane & 7;
      const int tok = (lane >> 3) + 8 * (wv & 1);
      const int t = (wv >> 1) * 4 + (c >> 1);
      const int base = t < 6 ? S2 + t * 1024 : t < 12 ? S0 + (t - 6) * 1024 : S1 + (t - 12) * 1024;
      const char* rp = smem + base + (c & 1) * 512 + ((tok ^ (2 * c)) << 4);
      f32x4 v0 = f32x4{0, 0, 0, 0}, v1 = f32x4{0, 0, 0, 0};
#pragma unroll
      for (int p = 0; p < 8; ++p) {
        v0 += *(const LAS f32x4*)(LAS const char*)(rp + p * 6144);
        v1 += *(const LAS f32x4*)(LAS const char*)(rp + p * 6144 + 256);
      }
      u32x4 r = __builtin_bit_cast(u32x4, rs);
      u32x4 ov = {pack2(v0[0] + bflo(r[0]), v0[1] + bfhi(r[0])), pack2(v0[2] + bflo(r[1]), v0[3] + bfhi(r[1])),
                  pack2(v1[0] + bflo(r[2]), v1[1] + bfhi(r[2])), pack2(v1[2] + bflo(r[3]), v1[3] + bfhi(r[3]))};
      st16<true>(out + ooff + vo, ov);
    }
  }
  asm volatile(".Lmega_end:");
}

extern "C" void kernel_launch(void* const* d_in, const int* in_sizes, int n_in, void* d_out, int out_size,
                              void* d_ws, size_t ws_size, hipStream_t stream) {
  const char* x = (const char*)d_in[0];
  const char* wqkv = (const char*)d_in[1];
  const char* wo = (const char*)d_in[2];
  const char* w1 = (const char*)d_in[4];
  const char* w3 = (const char*)d_in[5];
  const char* w2 = (const char*)d_in[6];
  char* ws = (char*)d_ws;
  (void)hipFuncSetAttribute((const void*)k_mega, hipFuncAttributeMaxDynamicSharedMemorySize, MG_LDS);
  k_mega<<<256, 512, MG_LDS, stream>>>(x, wqkv, wo, w1, w3, w2, ws, (char*)d_out);
}
```

```cpp
#include <hip/hip_runtime.h>
#include <hip/hip_bf16.h>
#include <stdint.h>

typedef short bf16x8 __attribute__((ext_vector_type(8)));
typedef short s16x4 __attribute__((ext_vector_type(4)));
typedef float f32x4 __attribute__((ext_vector_type(4)));
typedef float f32x2 __attribute__((ext_vector_type(2)));
typedef unsigned u32x4 __attribute__((ext_vector_type(4)));
typedef unsigned u32x2 __attribute__((ext_vector_type(2)));
typedef __bf16 bf16x2 __attribute__((ext_vector_type(2)));
#define LAS __attribute__((address_space(3)))
#define DEV __device__ __forceinline__

constexpr int S_ = 2048, D_ = 512, H_ = 32, F_ = 1792;
constexpr float LN_EPS = 1e-5f;

typedef float f32x16 __attribute__((ext_vector_type(16)));
DEV f32x16 mfma32(bf16x8 a, bf16x8 b, f32x16 c) { return __builtin_amdgcn_mfma_f32_32x32x16_bf16(a, b, c, 0, 0, 0); }
DEV f32x4 mfma16(bf16x8 a, bf16x8 b, f32x4 c) { return __builtin_amdgcn_mfma_f32_16x16x32_bf16(a, b, c, 0, 0, 0); }
DEV unsigned pack2(float a, float b) {
  f32x2 v = {a, b};
  return __builtin_bit_cast(unsigned, __builtin_convertvector(v, bf16x2));
}
DEV float bflo(unsigned u) { return __builtin_bit_cast(float, u << 16); }
DEV float bfhi(unsigned u) { return __builtin_bit_cast(float, u & 0xffff0000u); }
DEV float dot2(unsigned a, unsigned b, float c) {
  return __builtin_amdgcn_fdot2_f32_bf16(__builtin_bit_cast(bf16x2, a), __builtin_bit_cast(bf16x2, b), c, false);
}
DEV void dma16(const char* gptr, unsigned ldsbase) {
  unsigned sv;
  asm volatile("s_mov_b32 %0, m0\n\ts_mov_b32 m0, %2\n\ts_nop 0\n\tglobal_load_lds_dwordx4 %1, off\n\ts_mov_b32 m0, %0"
               : "=&s"(sv) : "v"(gptr), "s"(ldsbase) : "memory");
}
DEV void dma16s(unsigned voff, const char* sbase, unsigned ldsbase) {
  unsigned sv;
  asm volatile("s_mov_b32 %0, m0\n\ts_mov_b32 m0, %3\n\ts_nop 0\n\tglobal_load_lds_dwordx4 %1, %2\n\ts_mov_b32 m0, %0"
               : "=&s"(sv) : "v"(voff), "s"(sbase), "s"(ldsbase) : "memory");
}
DEV void dma16sm(unsigned voff, const char* sbase, unsigned ldsbase, unsigned long long mask) {
  unsigned sv;
  asm volatile("s_mov_b32 %0, m0\n\ts_mov_b32 m0, %3\n\ts_mov_b64 exec, %4\n\tglobal_load_lds_dwordx4 %1, %2\n\ts_mov_b64 exec, -1\n\ts_mov_b32 m0, %0"
               : "=&s"(sv) : "v"(voff), "s"(sbase), "s"(ldsbase), "s"(mask) : "memory");
}
DEV void gld16(bf16x8& d, unsigned voff, const char* sbase) {
  asm volatile("global_load_dwordx4 %0, %1, %2" : "=v"(d) : "v"(voff), "s"(sbase) : "memory");
}
DEV void gld8(u32x2& d, unsigned voff, const char* sbase) {
  asm volatile("global_load_dwordx2 %0, %1, %2" : "=v"(d) : "v"(voff), "s"(sbase) : "memory");
}
template <int N> DEV void wait_bar() { asm volatile("s_waitcnt vmcnt(%0) lgkmcnt(0)\n\ts_barrier" ::"n"(N) : "memory"); }
DEV void bar() { asm volatile("s_waitcnt lgkmcnt(0)\n\ts_barrier" ::: "memory"); }
DEV bf16x8 lds128(const char* p) { return *(const LAS bf16x8*)(LAS const char*)p; }
DEV s16x4 ldstr(const char* p) { return __builtin_amdgcn_ds_read_tr16_b64_v4i16((LAS s16x4*)p); }
DEV bf16x8 cat(s16x4 a, s16x4 b) { return __builtin_shufflevector(a, b, 0, 1, 2, 3, 4, 5, 6, 7); }
DEV float xsum(float v) { v += __shfl_xor(v, 16); v += __shfl_xor(v, 32); return v; }
DEV float xmax(float v) { v = fmaxf(v, __shfl_xor(v, 16)); v = fmaxf(v, __shfl_xor(v, 32)); return v; }
DEV float dpp_xor1(float v) { return __builtin_bit_cast(float, __builtin_amdgcn_mov_dpp(__builtin_bit_cast(int, v), 0xB1, 0xF, 0xF, true)); }
DEV float dpp_xor2(float v) { return __builtin_bit_cast(float, __builtin_amdgcn_mov_dpp(__builtin_bit_cast(int, v), 0x4E, 0xF, 0xF, true)); }
DEV float dpp_ror4(float v) { return __builtin_bit_cast(float, __builtin_amdgcn_mov_dpp(__builtin_bit_cast(int, v), 0x124, 0xF, 0xF, true)); }
DEV float dpp_ror8(float v) { return __builtin_bit_cast(float, __builtin_amdgcn_mov_dpp(__builtin_bit_cast(int, v), 0x128, 0xF, 0xF, true)); }
DEV unsigned uni(unsigned v) { return __builtin_amdgcn_readfirstlane(v); }
#define UP 3
#define WT 31
template <bool W> DEV void st16(char* p, u32x4 v) {
  if constexpr (W) asm volatile("global_store_dwordx4 %0, %1, off sc0 sc1\n\ts_nop 1" ::"v"(p), "v"(v) : "memory");
  else *(u32x4*)p = v;
}
template <bool W> DEV void st8(char* p, u32x2 v) {
  if constexpr (W) asm volatile("global_store_dwordx2 %0, %1, off sc0 sc1" ::"v"(p), "v"(v) : "memory");
  else *(u32x2*)p = v;
}

template <int A, int B> struct Min { static constexpr int v = A < B ? A : B; };

template <int KT, int NK, int NS, int P, class Issue, class Compute>
DEV void pipe_steps(Issue& issue, Compute& compute) {
  if constexpr (KT < NK) {
    wait_bar<(Min<KT + NS - 1, NK>::v - KT - 1) * P>();
    if constexpr (KT + NS - 1 < NK) issue((KT + NS - 1) % NS);
    compute(KT % NS, KT);
    pipe_steps<KT + 1, NK, NS, P>(issue, compute);
  }
}

constexpr int K1_STAGE = 16384 + 12288, K1_NS = 5;
#define SB __builtin_amdgcn_sched_barrier(0)
DEV float xsum2(float v) {
  auto a = __builtin_amdgcn_permlane16_swap(__float_as_uint(v), __float_as_uint(v), false, false);
  v = __uint_as_float(a[0]) + __uint_as_float(a[1]);
  auto b = __builtin_amdgcn_permlane32_swap(__float_as_uint(v), __float_as_uint(v), false, false);
  return __uint_as_float(b[0]) + __uint_as_float(b[1]);
}

DEV void qkv_phase(char* smem, unsigned lds0, const char* __restrict__ x, const char* __restrict__ w, char* __restrict__ qkv,
                   int cb, int tb) {
  const int tid = threadIdx.x, lane = tid & 63, wv = uni(tid >> 6);
  const int g = lane >> 4, i = lane & 15, q4 = (lane >> 2) & 3, p4 = lane & 3;
  const int tg = wv & 3, nh = wv >> 2;
  const int bi = blockIdx.x >> 3;

  const char* gx = x + (size_t)(tb * 128 + wv * 16) * 1024;
  const unsigned vt = (lane >> 3) * 1024 + (((lane & 7) ^ (lane >> 3)) << 4);
  const char* gw = w + cb * 192;
  unsigned vw0, vw1;
  {
    int qq = wv * 64 + lane;
    int k = qq / 12, c = (qq % 12) ^ (((k >> 3) & 1) << 1);
    vw0 = k * 3072 + c * 16;
    qq = 512 + wv * 32 + (lane & 31);
    k = qq / 12; c = (qq % 12) ^ (((k >> 3) & 1) << 1);
    vw1 = k * 3072 + c * 16;
  }
  const int k0 = bi & 7;
  auto dma = [&](int s, int j) {
    const int kk = (k0 + s) & 7;
    const unsigned base = lds0 + (s % K1_NS) * K1_STAGE;
    if (j == 0) dma16s(vt, gx + kk * 128, base + wv * 2048);
    else if (j == 1) dma16s(vt, gx + kk * 128 + 8192, base + wv * 2048 + 1024);
    else if (j == 2) dma16s(vw0, gw + kk * (64 * 3072), base + 16384 + wv * 1024);
    else dma16sm(vw1, gw + kk * (64 * 3072), base + 16384 + 8192 + wv * 512, 0xFFFFFFFFull);
  };
#pragma unroll
  for (int s = 0; s < UP; ++s)
#pragma unroll
    for (int j = 0; j < 4; ++j) dma(s, j);

  f32x4 acc[2][3], cs = f32x4{0, 0, 0, 0}, s1v = f32x4{0, 0, 0, 0}, gr = f32x4{0, 0, 0, 0};
#pragma unroll
  for (int a = 0; a < 3; ++a) { acc[0][a] = f32x4{0, 0, 0, 0}; acc[1][a] = f32x4{0, 0, 0, 0}; }
  const bf16x8 ones = {0x3F80, 0x3F80, 0x3F80, 0x3F80, 0x3F80, 0x3F80, 0x3F80, 0x3F80};
  int toff[2][2];
#pragma unroll
  for (int tt = 0; tt < 2; ++tt) {
    toff[tt][0] = (tg * 32 + (tt ^ nh) * 16 + i) * 128 + ((g ^ (i & 7)) << 4);
    toff[tt][1] = (tg * 32 + (tt ^ nh) * 16 + i) * 128 + (((4 + g) ^ (i & 7)) << 4);
  }
  int woff[3], un[3];
#pragma unroll
  for (int nt = 0; nt < 3; ++nt) {
    int a = nt + tg; a = a >= 3 ? a - 3 : a; a = a >= 3 ? a - 3 : a;
    un[nt] = nh * 3 + a;
    woff[nt] = 16384 + (8 * g + q4) * 192 + ((un[nt] ^ (g & 1)) << 5) + p4 * 8;
  }
  auto ldB = [&](int tt, int kt, int ks) -> bf16x8 { return lds128(smem + (kt % K1_NS) * K1_STAGE + toff[tt][ks]); };
  auto ldA = [&](int nt, int kt, int ks) -> bf16x8 {
    const char* p = smem + (kt % K1_NS) * K1_STAGE + woff[nt] + ks * 6144;
    return cat(ldstr(p), ldstr(p + 768));
  };
  bf16x8 A0[3], A1[3], B0[2], B1[2];
  auto phase = [&](bf16x8 (&Ac)[3], bf16x8 (&Bc)[2], bf16x8 (&An)[3], bf16x8 (&Bn)[2], int kn, int ksn, int dk, int dj, int nd) {
#pragma unroll
    for (int m = 0; m < 9; ++m) {
      if (kn < 8) {
        if (m == 0) { Bn[0] = ldB(0, kn, ksn); An[0] = ldA(0, kn, ksn); }
        else if (m == 1) Bn[1] = ldB(1, kn, ksn);
        else if (m < 4) An[m - 1] = ldA(m - 1, kn, ksn);
      }
      if (m < 6) acc[m & 1][m >> 1] = mfma16(Ac[m >> 1], Bc[m & 1], acc[m & 1][m >> 1]);
      else if (m == 6) cs = mfma16(Ac[0], ones, cs);
      else if (m == 7) s1v = mfma16(ones, Bc[0], s1v);
      else gr = mfma16(Bc[0], Bc[0], gr);
      if (dk < 8) {
        if (nd == 2) { if (m == 2 || m == 5) dma(dk, dj + (m == 5)); }
        else { if (m == 1 || m == 2) dma(dk, m - 1); else if (m == 4 || m == 5) dma(dk, m - 2); }
      }
      SB;
    }
  };
  wait_bar<4 * UP - 4>();
  B0[0] = ldB(0, 0, 0); A0[0] = ldA(0, 0, 0); B0[1] = ldB(1, 0, 0); A0[1] = ldA(1, 0, 0); A0[2] = ldA(2, 0, 0);
#pragma unroll
  for (int kt = 0; kt < 8; ++kt) {
    const int d1 = UP == 5 ? 8 : UP == 4 ? (kt == 0 ? 4 : 8) : (kt == 0 ? 3 : kt == 1 ? 5 : 8);
    const int d2 = UP == 3 ? (kt == 0 ? 4 : kt == 1 ? 6 : kt == 2 ? 7 : 8) : kt + 5;
    phase(A0, B0, A1, B1, kt, 1, d1, 0, 4);
    if (kt == 0) wait_bar<UP == 3 ? 8 : 12>();
    else if (kt < 4) wait_bar<12>();
    else if (kt == 4) wait_bar<8>();
    else if (kt == 5) wait_bar<4>();
    else if (kt == 6) wait_bar<0>();
    phase(A1, B1, A0, B0, kt + 1, 0, d2, 0, 4);
  }

  float* st = (float*)(smem + 49152);
  float* csp = (float*)(smem + 49152 + 1024);
  if (g == (i >> 2)) {
    const float s2 = (i & 2) ? ((i & 1) ? gr[3] : gr[2]) : ((i & 1) ? gr[1] : gr[0]);
    float mean = s1v[0] * (1.f / 512.f);
    float var = fmaxf(s2 * (1.f / 512.f) - mean * mean, 0.f);
    float rstd = rsqrtf(var + LN_EPS);
    *(f32x2*)(st + (tg * 32 + nh * 16 + i) * 2) = f32x2{-mean * rstd, rstd};
  }
  if (i == 0 && tg < 3) *(f32x4*)(csp + un[0] * 16 + 4 * g) = cs;
  bar();
  f32x2 ms[2];
#pragma unroll
  for (int tt = 0; tt < 2; ++tt) ms[tt] = *(const f32x2*)(st + (tg * 32 + (tt ^ nh) * 16 + i) * 2);
  const int otile = (g & 1) ^ nh, ohalf = g >> 1;
  const unsigned vo = ohalf * 512 + (((otile * 16 + i) ^ (8 * ohalf)) << 4);
#pragma unroll
  for (int nt = 0; nt < 3; ++nt) {
    f32x4 c = *(const f32x4*)(csp + un[nt] * 16 + 4 * g);
    const float qs = cb * 6 + un[nt] < 32 ? 0.25f * 1.4426950408889634f : 1.f;
    unsigned o[2][2];
#pragma unroll
    for (int tt = 0; tt < 2; ++tt) {
      f32x4 y = acc[tt][nt] * (ms[tt][1] * qs) + (ms[tt][0] * qs) * c;
      o[tt][0] = pack2(y[0], y[1]); o[tt][1] = pack2(y[2], y[3]);
    }
    auto sx = __builtin_amdgcn_permlane16_swap(o[0][0], o[1][0], false, false);
    auto sy = __builtin_amdgcn_permlane16_swap(o[0][1], o[1][1], false, false);
    char* dst = qkv + (size_t)(cb * 6 + un[nt]) * (2048 * 32) + (size_t)(tb * 4 + tg) * 1024;
    st16<true>(dst + vo, u32x4{sx[0], sy[0], sx[1], sy[1]});
  }
}

constexpr int S0 = 0, S1 = 49152, S2 = 98304, CSP = 147456, MG_LDS = 163840;
typedef unsigned long long u64;
extern "C" __device__ u64 llvm_dispatch_id() __asm("llvm.amdgcn.dispatch.id");
DEV void flag_set(u64* f, u64 tag) {
  asm volatile("global_store_dwordx2 %0, %1, off sc0 sc1" ::"v"(f), "v"(tag) : "memory");
}
DEV void flag_wait(const u64* p, u64 tag) {
  for (int it = 0; it < (1 << 20); ++it) {
    u64 c;
    asm volatile("global_load_dwordx2 %0, %1, off sc0 sc1\n\ts_waitcnt vmcnt(0)" : "=v"(c) : "v"(p) : "memory");
    if (__builtin_amdgcn_ballot_w64(c != tag) == 0) break;
  }
}
template <int V> struct IC { static constexpr int value = V; };
DEV int opaque(int v) { asm volatile("" : "+v"(v)); return v; }
DEV int opaque_s(int v) { asm volatile("" : "+s"(v)); return v; }

struct CW {
  const char *g1, *g3; unsigned vw; unsigned long long wmask;
};
DEV CW c_setup(const char* w1, const char* w3, int b, int wv, int lane) {
  const int xcd = b & 7, bi = b >> 3;
  const int cb = (xcd & 3) * 4 + (bi & 3);
  CW c;
  c.g1 = w1 + (size_t)(wv * 8) * (F_ * 2) + cb * 224;
  c.g3 = w3 + (size_t)(wv * 8) * (F_ * 2) + cb * 224;
  const int unit = ((lane >> 1) & 7) ^ ((lane >> 4) | ((wv & 1) << 2));
  c.vw = (lane >> 4) * (F_ * 2) + unit * 32 + (lane & 1) * 16;
  c.wmask = __builtin_amdgcn_ballot_w64(unit < 7);
  return c;
}
DEV void c_dmaw(const CW& c, unsigned ldsw, int kt, int j) {
  const unsigned base = ldsw + (kt % 3) * 49152;
  if (j < 2) dma16sm(c.vw, c.g1 + (size_t)(kt * 64 + j * 4) * (F_ * 2), base + 16384 + j * 1024, c.wmask);
  else dma16sm(c.vw, c.g3 + (size_t)(kt * 64 + (j - 2) * 4) * (F_ * 2), base + 32768 + (j - 2) * 1024, c.wmask);
}
DEV constexpr int d_unit(int u) {
  return u < 3 ? S2 + u * 2048 : u < 6 ? S0 + (u - 3) * 2048 : u < 9 ? S1 + (u - 6) * 2048 : u == 9 ? CSP
       : u < 13 ? S2 + (u - 10) * 2048 : S0;
}
DEV constexpr int d_ustr(int u) { return u == 9 ? 2048 : 6144; }
DEV void d_dma(const char* gw2, unsigned vw, unsigned lds0, int wv, int u, int q) {
  dma16s(vw, gw2 + (size_t)(u * 16 + q * 8) * 1024, lds0 + d_unit(u) + wv * d_ustr(u) + q * 1024);
}

template <int NT>
DEV void c_body(char* smem, unsigned lds0, const char* gt, const CW& cw, unsigned vt, int wv, int lane,
                const char* __restrict__ stats, const char* __restrict__ csb, char* __restrict__ hh, int tok0, int cb, const char* gw2, unsigned vwd) {
  const int tg = wv & 3;
  const unsigned ldsw = lds0 + wv * 2048;
  const int g = lane >> 4, i = lane & 15, q4 = (lane >> 2) & 3, p4 = lane & 3;
  f32x4 acc[2][2 * NT];
#pragma unroll
  for (int a = 0; a < 2 * NT; ++a) { acc[0][a] = f32x4{0, 0, 0, 0}; acc[1][a] = f32x4{0, 0, 0, 0}; }
  const int toff0 = (tg * 32 + i) * 128 + ((g ^ (i & 7)) << 4);
  const int toff1 = (tg * 32 + i) * 128 + (((4 + g) ^ (i & 7)) << 4);
  const int odd = cb & 1;
  const int fsw = q4 | ((g & 1) << 2);
  int T[NT], wa[NT];
#pragma unroll
  for (int a = 0; a < NT; ++a) {
    T[a] = NT == 4 ? odd * 3 + a : (a < 2 ? (odd ? 1 : 4) + a : (odd ? 0 : 6));
    wa[a] = 16384 + (8 * g + q4) * 256 + ((T[a] ^ fsw) << 5) + p4 * 8;
  }
  auto dmat = [&](int kt, int j) { dma16s(vt, gt + kt * 128 + j * 8192, ldsw + (kt % 3) * 49152 + j * 1024); };
  auto dma = [&](int kt, int j) {
    if (j < 2) dmat(kt, j); else c_dmaw(cw, ldsw, kt, j - 2);
  };
  auto ldB = [&](bf16x8* B, int kt, int ks) {
    const char* sb = smem + (kt % 3) * 49152 + (ks ? toff1 : toff0);
    B[0] = lds128(sb); B[1] = lds128(sb + 2048);
  };
  auto ldA = [&](int a, int kt, int ks) -> bf16x8 {
    const char* wp = smem + (kt % 3) * 49152 + wa[a < NT ? a : a - NT] + (a < NT ? 0 : 16384) + ks * 8192;
    return cat(ldstr(wp), ldstr(wp + 1024));
  };
  auto mma = [&](int a, bf16x8 A, const bf16x8* B) {
    acc[0][a] = mfma16(A, B[0], acc[0][a]);
    acc[1][a] = mfma16(A, B[1], acc[1][a]);
  };
  bf16x8 A0[2 * NT], A1[2 * NT], B0[2], B1[2];
  bf16x8 cp[4];
  u32x2 sa[4];
#pragma unroll
  for (int kt = 0; kt < 3; ++kt) { dmat(kt, 0); dmat(kt, 1); }
#pragma unroll
  for (int m = 0; m < 4; ++m)
    gld8(sa[m], (unsigned)((i >> 3) * 512 + g * 128 + (i & 7) * 8), stats + (size_t)(tok0 + tg * 32 + (m >> 1) * 16) * 64 + (m & 1) * 64);
  const int cq = min(tg * 16 + (lane >> 2), 55);
  if (NT == 3) {
#pragma unroll
    for (int m = 0; m < 4; ++m) gld16(cp[m], (unsigned)(((lane & 3) * 4 + m) * 896 + cq * 16), csb + (size_t)cb * 14336);
  }
  constexpr int X = NT == 3 ? 8 : 4;
  wait_bar<4 + X>();
  float rs[2], mr[2];
  float* csp = (float*)(smem + CSP);
  ldB(B0, 0, 0);
#pragma unroll
  for (int a = 0; a < 2 * NT; ++a) A0[a] = ldA(a, 0, 0);
#pragma unroll
  for (int kt = 0; kt < 8; ++kt) {
    ldB(B1, kt, 1);
#pragma unroll
    for (int a = 0; a < 2 * NT; ++a) {
      if (a < NT) { A1[2 * a] = ldA(2 * a, kt, 1); A1[2 * a + 1] = ldA(2 * a + 1, kt, 1); }
      mma(a, A0[a], B0);
      __builtin_amdgcn_sched_barrier(0);
    }
    if (kt == 0) wait_bar<2 + X>();
    else if (kt != 1) wait_bar<6>();
    else if (NT == 3) asm volatile("s_waitcnt vmcnt(6) lgkmcnt(0)\n\ts_barrier" : "+v"(sa[0]), "+v"(sa[1]), "+v"(sa[2]), "+v"(sa[3]), "+v"(cp[0]), "+v"(cp[1]), "+v"(cp[2]), "+v"(cp[3]) : : "memory");
    else asm volatile("s_waitcnt vmcnt(6) lgkmcnt(0)\n\ts_barrier" : "+v"(sa[0]), "+v"(sa[1]), "+v"(sa[2]), "+v"(sa[3]) : : "memory");
    if (kt < 7) ldB(B0, kt + 1, 0);
    if (kt == 1) {
#pragma unroll
      for (int tt = 0; tt < 2; ++tt) {
        const f32x2 sv0 = __builtin_bit_cast(f32x2, sa[2 * tt]), sv1 = __builtin_bit_cast(f32x2, sa[2 * tt + 1]);
        const float s1 = xsum2(sv0[0] + sv1[0]), s2 = xsum2(sv0[1] + sv1[1]);
        const float mean = s1 * (1.f / 512.f);
        const float var = fmaxf(s2 * (1.f / 512.f) - mean * mean, 0.f);
        rs[tt] = rsqrtf(var + LN_EPS); mr[tt] = mean * rs[tt];
      }
      if (NT == 3) {
        f32x4 c = (__builtin_bit_cast(f32x4, cp[0]) + __builtin_bit_cast(f32x4, cp[1])) +
                  (__builtin_bit_cast(f32x4, cp[2]) + __builtin_bit_cast(f32x4, cp[3]));
#pragma unroll
        for (int e = 0; e < 4; ++e) { c[e] += dpp_xor1(c[e]); c[e] += dpp_xor2(c[e]); }
        if ((lane & 3) == 0) *(f32x4*)(csp + cq * 4) = c;
      }
    }
#pragma unroll
    for (int a = 0; a < 2 * NT; ++a) {
      if (kt < 7 && a < NT) { A0[2 * a] = ldA(2 * a, kt + 1, 0); A0[2 * a + 1] = ldA(2 * a + 1, kt + 1, 0); }
      mma(a, A1[a], B1);
      if (a >= 2 * NT - 6) {
        const int j = a - (2 * NT - 6);
        if (kt + 3 < 8) dma(kt + 3, j);
        else d_dma(gw2, vwd, lds0, wv, (kt - 5) * 3 + (j >> 1), j & 1);
      }
      __builtin_amdgcn_sched_barrier(0);
    }
  }
  unsigned pk[2][NT][2];
#pragma unroll
  for (int tt = 0; tt < 2; ++tt) {
    char* dst = hh + ((size_t)((tok0 >> 4) + tg * 2 + tt) * 56) * 1024 + lane * 16;
#pragma unroll
    for (int a = 0; a < NT; ++a) {
      f32x4 cg = *(const f32x4*)(csp + T[a] * 16 + 4 * g);
      f32x4 cu = *(const f32x4*)(csp + (7 + T[a]) * 16 + 4 * g);
      float r[4];
#pragma unroll
      for (int e = 0; e < 4; ++e) {
        float ga = acc[tt][a][e] * rs[tt] - mr[tt] * cg[e];
        float up = acc[tt][NT + a][e] * rs[tt] - mr[tt] * cu[e];
        float sg = __builtin_amdgcn_rcpf(1.f + __builtin_amdgcn_exp2f(-1.4426950408889634f * ga));
        r[e] = ga * sg * up;
      }
      pk[tt][a][0] = pack2(r[0], r[1]); pk[tt][a][1] = pack2(r[2], r[3]);
      const int G = cb * 7 + T[a];
      if (a & 1) {
        st16<true>(dst + (size_t)(G >> 1) * 1024, u32x4{pk[tt][a - 1][0], pk[tt][a - 1][1], pk[tt][a][0], pk[tt][a][1]});
      } else if (a == NT - 1) {
        st8<true>(dst + (size_t)(G >> 1) * 1024 + (G & 1) * 512 - lane * 8, u32x2{pk[tt][a][0], pk[tt][a][1]});
      }
    }
  }
}

__global__ __launch_bounds__(512) void k_mega(const char* __restrict__ x, const char* __restrict__ wqkv, const char* __restrict__ wo,
                                              const char* __restrict__ w1, const char* __restrict__ w3,
                                              const char* __restrict__ w2, char* __restrict__ ws, char* __restrict__ out) {
  extern __shared__ __attribute__((aligned(16))) char smem[];
  char* qkv = ws;
  char* o = ws + (6u << 20);
  char* h = ws + (8u << 20);
  char* stats = ws + (10u << 20);
  u64* flags = (u64*)(ws + (10u << 20) + (512u << 10));
  char* csb = ws + (10u << 20) + (768u << 10);
  char* hh = ws + (11u << 20);
  const u64 tag = ((u64)(size_t)__builtin_amdgcn_dispatch_ptr() << 20) + llvm_dispatch_id();
  const int b = blockIdx.x;
  const unsigned lds0 = (unsigned)(size_t)(LAS char*)smem;
  const int cbG = (b >> 3) & 7, tbG = (b & 7) * 4 + (b >> 6);
  const int cbC = (b & 3) * 4 + ((b >> 3) & 3), tbC = ((b >> 2) & 1) * 8 + (b >> 5);

  unsigned cpf = 0;
  {
    u64 pc;
    unsigned len;
    asm volatile("s_getpc_b64 %0\n.Lmega_pc:\n\ts_mov_b32 %1, .Lmega_end-.Lmega_pc" : "=s"(pc), "=s"(len));
    const unsigned off = ((b >> 3) * 26 + threadIdx.x) * 64;
    if (threadIdx.x < 26 && off < len) asm volatile("global_load_dword %0, %1, %2" : "=v"(cpf) : "v"(off), "s"(pc) : "memory");
  }
  {
    const int cb = (b & 1) * 8 + ((b >> 3) & 7), tb = ((b & 7) >> 1) * 4 + (b >> 6);
    qkv_phase(smem, lds0, x, wqkv, qkv, cb, tb);
    asm volatile("s_waitcnt vmcnt(0) lgkmcnt(0)\n\ts_barrier" ::: "memory");
    asm volatile("" :: "v"(cpf));
    const int tid = threadIdx.x;
    if (tid < 9) {
      const int P = cb * 3 + tid / 3, d = tid % 3, which = P >> 4, hp = P & 15, qb = tb + d;
      if (which ? qb < 16 : d == 0)
        flag_set(flags + 24576 + ((hp >> 1) | ((hp & 1) << 3) | (qb << 4)) * 32 + (which ? (which - 1) * 3 + d : 6), tag);
    }
  }
  {
    const int tid = opaque(threadIdx.x), lane = tid & 63, wv = uni(tid >> 6);
    const int c32 = lane & 31, hf = lane >> 5, q4 = (lane >> 2) & 3, p4 = lane & 3;
    const int hp = (b & 7) * 2 + ((b >> 3) & 1), qb = b >> 4;
    const int q0 = qb * 128;
    const size_t HS = 2048 * 32;
    bf16x8 Q, cw0, cw1;
    {
      const unsigned one = qb * 4 - 8 + wv >= 0 ? 0x3F803F80u : 0u;
      *(LAS u32x4*)(LAS char*)(smem + S1 + tid * 16) = u32x4{one, one, one, one};
      if (tid < 256) *(LAS u32x4*)(LAS char*)(smem + S1 + 8192 + tid * 16) = u32x4{0x3F803F80u, 0x3F803F80u, 0x3F803F80u, 0x3F803F80u};
    }
    if (wv == 0) {
      const int d = lane % 3;
      flag_wait(flags + 24576 + b * 32 + (lane >= 6 ? 6 : qb - d < 0 ? 6 : lane), tag);
    }
    asm volatile("s_barrier" ::: "memory");
    {
      const int r = wv >> 1;
      const char* src = qkv + (size_t)((1 + (r >> 1)) * 32 + hp * 2 + (r & 1)) * HS;
#pragma unroll
      for (int j = 0; j < 6; ++j) {
        const int kb = qb * 4 - 8 + (wv & 1) * 6 + j;
        if (kb < 0 && r >= 2) *(LAS u32x4*)(LAS char*)(smem + r * 12288 + ((wv & 1) * 6 + j) * 1024 + lane * 16) = u32x4{0, 0, 0, 0};
        else dma16s(kb < 0 ? hf * 640 : lane * 16, src + (size_t)max(kb, 0) * 1024, lds0 + r * 12288 + ((wv & 1) * 6 + j) * 1024);
      }
    }
    const int u = wv >> 2, qg = wv & 3;
    const int qs = q0 + qg * 32;
    const unsigned vq = hf * 512 + ((c32 ^ (8 * hf)) << 4);
    gld16(Q, vq, qkv + (size_t)(hp * 2 + u) * HS + (size_t)qs * 32);
    const float NEG = -1e30f;
    f32x16 cin0, cin8;
    u32x4 mk[2];
#pragma unroll
    for (int r = 0; r < 16; ++r) {
      const int kl = (r & 3) + 8 * (r >> 2) + 4 * hf;
      cin0[r] = kl > c32 ? 0.f : NEG; cin8[r] = kl <= c32 ? 0.f : NEG;
      if (r & 1) mk[r >> 3][(r >> 1) & 3] = (kl > c32 ? 0xFFFF0000u : 0u) | (kl - 1 > c32 ? 0xFFFFu : 0u);
    }
    asm volatile("s_waitcnt vmcnt(0) lgkmcnt(0)\n\ts_barrier" : "+v"(Q), "+v"(cin0), "+v"(cin8), "+v"(mk[0]), "+v"(mk[1]) : : "memory");
    const unsigned vwo = (lane >> 3) * 1024 + ((((lane & 7) >> 1) ^ ((lane >> 4) & 3)) << 5) + (lane & 1) * 16;
    const char* gwo = wo + (size_t)(wv * 64) * 1024 + cbG * 128;
    const CW cw1s = c_setup(w1, w3, b, wv, lane);
    auto pf = [&](int k) {
      if (k < 2) {
        const int ch = max(wv - 1, 0) * 4 + (lane & 3);
        const char* pc = (ch >= 14 ? w3 + (ch - 14) * 16 : w1 + ch * 16) + (size_t)(tbC * 32 + (lane >> 2)) * (F_ * 2) + cbC * 224;
        if (k == 0) asm volatile("global_load_dwordx4 %0, %1, off" : "=v"(cw0) : "v"(pc) : "memory");
        else asm volatile("global_load_dwordx4 %0, %1, off" : "=v"(cw1) : "v"(pc + 16 * (F_ * 2)) : "memory");
      } else if (k < 10) dma16s(vwo, gwo + (size_t)((k - 2) * 8) * 1024, lds0 + S2 + wv * 8192 + (k - 2) * 1024);
      else if (k < 14) c_dmaw(cw1s, lds0 + wv * 2048, 1, k - 10);
      else if (k == 14) {
        const int c0 = b * 8 + wv;
        dma16s(lane * 16, w2 + (size_t)(c0 < 1792 ? c0 : c0 - 1792) * 1024, lds0 + S1 + 12288 + (wv & 3) * 1024);
      }
    };
    {
      const char* Kl = smem + u * 12288 + qg * 1024 + vq;
      const char* Vl[2];
#pragma unroll
      for (int e = 0; e < 2; ++e)
        Vl[e] = c32 < 16 ? smem + 24576 + u * 12288 + qg * 1024 + (p4 >> 1) * 512 + (((e * 8 + 4 * hf + q4) ^ (8 * (p4 >> 1))) << 4) + (p4 & 1) * 8
                         : smem + S1 + qg * 1024 + (hf ? 0 : 64);
      f32x16 z;
#pragma unroll
      for (int r = 0; r < 16; ++r) z[r] = 0.f;
      bf16x8 Kf[9];
#pragma unroll
      for (int t = 0; t < 9; ++t) Kf[t] = lds128(Kl + t * 1024);
      f32x16 sm = mfma32(Kf[0], Q, cin0);
      {
        const f32x16 s8 = mfma32(Kf[8], Q, cin8);
#pragma unroll
        for (int r = 0; r < 16; ++r) sm[r] = fmaxf(sm[r], s8[r]);
      }
      float m = sm[0];
#pragma unroll
      for (int r = 1; r < 16; ++r) m = fmaxf(m, sm[r]);
#pragma unroll
      for (int t = 1; t < 8; ++t) {
        const f32x16 s = mfma32(Kf[t], Q, z);
#pragma unroll
        for (int r = 0; r < 16; ++r) m = fmaxf(m, s[r]);
      }
      {
        auto sw = __builtin_amdgcn_permlane32_swap(__float_as_uint(m), __float_as_uint(m), false, false);
        m = fmaxf(__uint_as_float(sw[0]), __uint_as_float(sw[1]));
      }
      f32x16 cm, oacc;
#pragma unroll
      for (int r = 0; r < 16; ++r) { cm[r] = -m; oacc[r] = 0.f; }
      f32x16 sn = mfma32(Kf[1], Q, cm);
      {
        u32x4 pb[2];
#pragma unroll
        for (int j = 0; j < 8; ++j)
          pb[j >> 2][j & 3] = pack2(__builtin_amdgcn_exp2f(sm[2 * j] - m), __builtin_amdgcn_exp2f(sm[2 * j + 1] - m));
#pragma unroll
        for (int sh = 0; sh < 2; ++sh) {
          const u32x4 p0 = pb[sh] & mk[sh], p8 = pb[sh] ^ p0;
          oacc = mfma32(cat(ldstr(Vl[0] + sh * 256), ldstr(Vl[1] + sh * 256)), __builtin_bit_cast(bf16x8, p0), oacc);
          pf(2 * sh);
          oacc = mfma32(cat(ldstr(Vl[0] + 8192 + sh * 256), ldstr(Vl[1] + 8192 + sh * 256)), __builtin_bit_cast(bf16x8, p8), oacc);
          pf(2 * sh + 1);
        }
      }
      SB;
#pragma unroll
      for (int t = 1; t < 8; ++t) {
        const f32x16 s = sn;
        if (t < 7) sn = mfma32(Kf[t + 1], Q, cm);
#pragma unroll
        for (int sh = 0; sh < 2; ++sh) {
          u32x4 pb;
#pragma unroll
          for (int e = 0; e < 4; ++e)
            pb[e] = pack2(__builtin_amdgcn_exp2f(s[8 * sh + 2 * e]), __builtin_amdgcn_exp2f(s[8 * sh + 2 * e + 1]));
          const int vp = t * 1024 + sh * 256;
          oacc = mfma32(cat(ldstr(Vl[0] + vp), ldstr(Vl[1] + vp)), __builtin_bit_cast(bf16x8, pb), oacc);
          if (2 * t + sh + 2 < 15) pf(2 * t + sh + 2);
        }
        SB;
      }
      const float inv = __builtin_amdgcn_rcpf(oacc[8]);
      const u32x4 ov = {pack2(oacc[0] * inv, oacc[1] * inv), pack2(oacc[2] * inv, oacc[3] * inv),
                        pack2(oacc[4] * inv, oacc[5] * inv), pack2(oacc[6] * inv, oacc[7] * inv)};
      st16<true>(o + ((size_t)((qs >> 4) + (c32 >> 4)) * 16 + hp) * 1024 + ((2 * u + hf) * 16 + (c32 & 15)) * 16, ov);
    }
    asm volatile("s_waitcnt vmcnt(0) lgkmcnt(0)\n\ts_barrier" : "+v"(cw0), "+v"(cw1) : : "memory");
    if (tid < 16) flag_set(flags + (((qb >> 1) | (tid << 3) | ((qb & 1) << 7)) * 32 + hp), tag);
    {
      const CW cw = c_setup(w1, w3, b, wv, lane);
#pragma unroll
      for (int j = 0; j < 4; ++j) c_dmaw(cw, lds0 + wv * 2048, 0, j);
    }
    if (wv == 0) { flag_wait(flags + b * 32 + (lane & 15), tag); }
    else {
      const u32x4 ua = __builtin_bit_cast(u32x4, cw0), ub = __builtin_bit_cast(u32x4, cw1);
      f32x4 lo, hi;
#pragma unroll
      for (int e = 0; e < 2; ++e) {
        lo[2 * e] = bflo(ua[e]) + bflo(ub[e]); lo[2 * e + 1] = bfhi(ua[e]) + bfhi(ub[e]);
        hi[2 * e] = bflo(ua[2 + e]) + bflo(ub[2 + e]); hi[2 * e + 1] = bfhi(ua[2 + e]) + bfhi(ub[2 + e]);
      }
#pragma unroll
      for (int e = 0; e < 4; ++e) {
        lo[e] += dpp_ror4(lo[e]); lo[e] += dpp_ror8(lo[e]); lo[e] = xsum2(lo[e]);
        hi[e] += dpp_ror4(hi[e]); hi[e] += dpp_ror8(hi[e]); hi[e] = xsum2(hi[e]);
      }
      if (lane < 8) {
        char* d = csb + (size_t)(cbC * 16 + tbC) * 896 + ((wv - 1) * 4 + (lane & 3)) * 32 + (lane >> 2) * 16;
        st16<true>(d, __builtin_bit_cast(u32x4, lane < 4 ? lo : hi));
      }
      asm volatile("s_waitcnt vmcnt(0)" ::: "memory");
    }
    asm volatile("s_barrier" ::: "memory");
    if (tid >= 64 && tid < 80) flag_set(flags + 8192 + ((cbC >> 2) | ((tid & 1) << 2) | ((cbC & 3) << 3) | (((tid & 15) >> 1) << 5)) * 32 + 16 + tbC, tag);
  }
  {
    const int tid = opaque(threadIdx.x), lane = tid & 63, wv = uni(tid >> 6);
    const int g = lane >> 4, i = lane & 15, q4 = (lane >> 2) & 3, p4 = lane & 3;
    const int kp = wv & 3, th = wv >> 2;
    bf16x8 B[4][2], rs;
    const char* ga = o + ((size_t)(tbG * 4 + th * 2) * 16 + kp * 4) * 1024;
#pragma unroll
    for (int s = 0; s < 4; ++s) {
      gld16(B[s][0], lane * 16, ga + s * 1024);
      gld16(B[s][1], lane * 16, ga + (16 + s) * 1024);
    }
    const size_t ooff = (size_t)(tbG * 64 + wv * 8) * 1024 + cbG * 128;
    const unsigned vo = (lane >> 3) * 1024 + (lane & 7) * 16;
    gld16(rs, vo, x + ooff);
    const int sw = (2 * (g & 1) + (q4 >> 1)) & 3;
    const char* wr = smem + S2 + kp * 16384 + (16 * (g >> 1) + 4 * (g & 1) + q4) * 128 + p4 * 8;
    bf16x8 A[4][4];
#pragma unroll
    for (int s = 0; s < 4; ++s)
#pragma unroll
      for (int nt = 0; nt < 4; ++nt) {
        const char* p = wr + s * 4096 + ((nt ^ sw) << 5);
        A[s][nt] = cat(ldstr(p), ldstr(p + 1024));
      }
    f32x4 acc[2][4];
#pragma unroll
    for (int t = 0; t < 2; ++t)
#pragma unroll
      for (int n = 0; n < 4; ++n) acc[t][n] = f32x4{0, 0, 0, 0};
    asm volatile("s_waitcnt vmcnt(7)" : "+v"(B[0][0]), "+v"(B[0][1]) : : "memory");
#pragma unroll
    for (int nt = 0; nt < 4; ++nt) { acc[0][nt] = mfma16(A[0][nt], B[0][0], acc[0][nt]); acc[1][nt] = mfma16(A[0][nt], B[0][1], acc[1][nt]); }
    asm volatile("s_waitcnt vmcnt(5)" : "+v"(B[1][0]), "+v"(B[1][1]) : : "memory");
#pragma unroll
    for (int nt = 0; nt < 4; ++nt) { acc[0][nt] = mfma16(A[1][nt], B[1][0], acc[0][nt]); acc[1][nt] = mfma16(A[1][nt], B[1][1], acc[1][nt]); }
    asm volatile("s_waitcnt vmcnt(3)" : "+v"(B[2][0]), "+v"(B[2][1]) : : "memory");
#pragma unroll
    for (int nt = 0; nt < 4; ++nt) { acc[0][nt] = mfma16(A[2][nt], B[2][0], acc[0][nt]); acc[1][nt] = mfma16(A[2][nt], B[2][1], acc[1][nt]); }
    asm volatile("s_waitcnt vmcnt(1)" : "+v"(B[3][0]), "+v"(B[3][1]) : : "memory");
#pragma unroll
    for (int nt = 0; nt < 4; ++nt) { acc[0][nt] = mfma16(A[3][nt], B[3][0], acc[0][nt]); acc[1][nt] = mfma16(A[3][nt], B[3][1], acc[1][nt]); }
    bar();
    {
      const int mo = S2 + kp * 16384 + g * 256 + ((i ^ ((g >> 1) * 2)) << 4);
#pragma unroll
      for (int tt = 0; tt < 2; ++tt)
#pragma unroll
        for (int nt = 0; nt < 4; ++nt)
          *(LAS f32x4*)(LAS char*)(smem + (mo ^ (nt * 64)) + ((th * 2 + tt) * 4 + nt) * 1024) = acc[tt][nt];
    }
    asm volatile("s_waitcnt vmcnt(0) lgkmcnt(0)\n\ts_barrier" : "+v"(rs) : : "memory");
    {
      const int c = lane & 7;
      const int tok = (lane >> 3) + 8 * (wv & 1);
      const char* rp = smem + S2 + ((wv >> 1) * 4 + (c >> 1)) * 1024 + (c & 1) * 512 + ((tok ^ (2 * c)) << 4);
      f32x4 v0 = f32x4{0, 0, 0, 0}, v1 = f32x4{0, 0, 0, 0};
#pragma unroll
      for (int p = 0; p < 4; ++p) {
        v0 += *(const LAS f32x4*)(LAS const char*)(rp + p * 16384);
        v1 += *(const LAS f32x4*)(LAS const char*)(rp + p * 16384 + 256);
      }
      u32x4 r = __builtin_bit_cast(u32x4, rs);
      u32x4 ov = {pack2(v0[0] + bflo(r[0]), v0[1] + bfhi(r[0])), pack2(v0[2] + bflo(r[1]), v0[3] + bfhi(r[1])),
                  pack2(v1[0] + bflo(r[2]), v1[1] + bfhi(r[2])), pack2(v1[2] + bflo(r[3]), v1[3] + bfhi(r[3]))};
      st16<true>(h + ooff + vo, ov);
      float s1 = 0.f, s2 = 0.f;
#pragma unroll
      for (int e = 0; e < 4; ++e) { s1 = dot2(ov[e], 0x3F803F80u, s1); s2 = dot2(ov[e], ov[e], s2); }
#pragma unroll
      for (int m = 1; m < 8; m <<= 1) { s1 += __shfl_xor(s1, m); s2 += __shfl_xor(s2, m); }
      if (c == 0) st8<true>(stats + ((size_t)(tbG * 8 + wv) * 8 + cbG) * 64 + (lane >> 3) * 8,
                            u32x2{__float_as_uint(s1), __float_as_uint(s2)});
    }
    asm volatile("s_waitcnt vmcnt(0) lgkmcnt(0)\n\ts_barrier" ::: "memory");
    if (tid < 16) flag_set(flags + 8192 + ((tid & 3) | ((tbG >> 4) << 2) | ((tid >> 2) << 3) | (((tbG >> 1) & 7) << 5)) * 32 + (tbG & 1) * 8 + cbG, tag);
    {
      const CW cw = c_setup(w1, w3, b, wv, lane);
#pragma unroll
      for (int j = 0; j < 4; ++j) c_dmaw(cw, lds0 + wv * 2048, 2, j);
    }
    if (wv == 0) flag_wait(flags + 8192 + b * 32 + (lane & 31), tag);
    asm volatile("s_barrier" ::: "memory");
  }
  const unsigned vwd_ = 0;
  {
    const int tid = opaque(threadIdx.x), lane = tid & 63, wv = uni(tid >> 6);
    const char* gt = h + (size_t)(tbC * 128 + wv * 16) * 1024;
    const unsigned vt = (lane >> 3) * 1024 + (((lane & 7) ^ (lane >> 3)) << 4);
    const CW cw = c_setup(w1, w3, b, wv, lane);
    const unsigned vwd = (lane >> 3) * 1024 + ((((lane & 7) >> 1) ^ ((lane >> 4) & 3)) << 5) + (lane & 1) * 16;
    const char* gw2 = w2 + (size_t)(wv * 224) * 1024 + cbG * 128;
    if ((wv >> 2) == 0) c_body<4>(smem, lds0, gt, cw, vt, wv, lane, stats, csb, hh, tbC * 128, cbC, gw2, vwd);
    else c_body<3>(smem, lds0, gt, cw, vt, wv, lane, stats, csb, hh, tbC * 128, cbC, gw2, vwd);
  }
  {
    const int tid = opaque(threadIdx.x), lane = tid & 63, wv = uni(tid >> 6);
    const int g = lane >> 4, i = lane & 15, q4 = (lane >> 2) & 3, p4 = lane & 3;
    asm volatile("s_waitcnt vmcnt(0) lgkmcnt(0)\n\ts_barrier" ::: "memory");
    if (tid < 16) flag_set(flags + 16384 + ((tbC >> 1) | (tid << 3) | ((tbC & 1) << 7)) * 32 + cbC, tag);
    const unsigned vwd = (lane >> 3) * 1024 + ((((lane & 7) >> 1) ^ ((lane >> 4) & 3)) << 5) + (lane & 1) * 16;
    const char* gw2 = w2 + (size_t)(wv * 224) * 1024 + cbG * 128;
    d_dma(gw2, vwd, lds0, wv, 9, 0); d_dma(gw2, vwd, lds0, wv, 9, 1);
    const int sw = (2 * g + (q4 >> 1)) & 3;
    const int ro = (4 * g + q4) * 128 + p4 * 8;
    int rn[4];
#pragma unroll
    for (int nt = 0; nt < 4; ++nt) rn[nt] = ro + ((nt ^ sw) << 5);
    auto lda = [&](bf16x8* Af, int j) {
      const char* p0 = smem + d_unit(2 * j) + wv * d_ustr(2 * j);
      const char* p1 = smem + d_unit(2 * j + 1) + wv * d_ustr(2 * j + 1);
#pragma unroll
      for (int nt = 0; nt < 4; ++nt) Af[nt] = cat(ldstr(p0 + rn[nt]), ldstr(p1 + rn[nt]));
    };
    bf16x8 A[2][4], B[7][4], rs;
    lda(A[0], 0); lda(A[1], 1);
    asm volatile("s_waitcnt lgkmcnt(0)" : "+v"(A[0][0]), "+v"(A[0][1]), "+v"(A[0][2]), "+v"(A[0][3]), "+v"(A[1][0]), "+v"(A[1][1]), "+v"(A[1][2]), "+v"(A[1][3]) : : "memory");
#pragma unroll
    for (int u = 10; u < 14; ++u) { d_dma(gw2, vwd, lds0, wv, u, 0); d_dma(gw2, vwd, lds0, wv, u, 1); }
    if (wv == 0) flag_wait(flags + 16384 + b * 32 + (lane & 15), tag);
    asm volatile("s_barrier" ::: "memory");
    const char* ga = hh + ((size_t)(tbG * 4) * 56 + wv * 7) * 1024;
    u32x2 Bl[4], Bh[4];
#pragma unroll
    for (int j = 0; j < 7; ++j)
#pragma unroll
      for (int tt = 0; tt < 4; ++tt) {
        if (j == 3) {
          gld8(Bl[tt], lane * 8, ga + (size_t)(tt * 56 + j) * 1024);
          gld8(Bh[tt], lane * 8, ga + (size_t)(tt * 56 + j) * 1024 + 512);
        } else gld16(B[j][tt], lane * 16, ga + (size_t)(tt * 56 + j) * 1024);
      }
    const size_t ooff = (size_t)(tbG * 64 + wv * 8) * 1024 + cbG * 128;
    const unsigned vo = (lane >> 3) * 1024 + (lane & 7) * 16;
    gld16(rs, vo, h + ooff);
    f32x4 acc[4][4];
#pragma unroll
    for (int t = 0; t < 4; ++t)
#pragma unroll
      for (int n = 0; n < 4; ++n) acc[t][n] = f32x4{0, 0, 0, 0};
    auto step = [&](auto J) {
      constexpr int j = decltype(J)::value;
      if constexpr (j == 3) {
        asm volatile("s_waitcnt vmcnt(13)" : "+v"(Bl[0]), "+v"(Bl[1]), "+v"(Bl[2]), "+v"(Bl[3]), "+v"(Bh[0]), "+v"(Bh[1]), "+v"(Bh[2]), "+v"(Bh[3]) : : "memory");
#pragma unroll
        for (int tt = 0; tt < 4; ++tt) B[3][tt] = __builtin_bit_cast(bf16x8, u32x4{Bl[tt][0], Bl[tt][1], Bh[tt][0], Bh[tt][1]});
      } else
        asm volatile("s_waitcnt vmcnt(%4)" : "+v"(B[j][0]), "+v"(B[j][1]), "+v"(B[j][2]), "+v"(B[j][3]) : "n"((j < 3 ? 29 : 25) - 4 * j) : "memory");
#pragma unroll
      for (int nt = 0; nt < 4; ++nt)
#pragma unroll
        for (int tt = 0; tt < 4; ++tt) acc[tt][nt] = mfma16(A[j & 1][nt], B[j][tt], acc[tt][nt]);
      __builtin_amdgcn_sched_barrier(0);
      if constexpr (j + 2 < 7) lda(A[j & 1], j + 2);
    };
    step(IC<0>{}); step(IC<1>{}); step(IC<2>{}); step(IC<3>{}); step(IC<4>{}); step(IC<5>{}); step(IC<6>{});
    {
      const int mo = wv * 6144 + g * 256 + ((i ^ ((g >> 1) * 2)) << 4);
#pragma unroll
      for (int tt = 0; tt < 4; ++tt)
#pragma unroll
        for (int nt = 0; nt < 4; ++nt) {
          const int t = tt * 4 + nt;
          const int base = t < 6 ? S2 + t * 1024 : t < 12 ? S0 + (t - 6) * 1024 : S1 + (t - 12) * 1024;
          *(LAS f32x4*)(LAS char*)(smem + (mo ^ (nt * 64)) + base) = acc[tt][nt];
        }
    }
    asm volatile("s_waitcnt vmcnt(0) lgkmcnt(0)\n\ts_barrier" : "+v"(rs) : : "memory");
    {
      const int c = lane & 7;
      const int tok = (lane >> 3) + 8 * (wv & 1);
      const int t = (wv >> 1) * 4 + (c >> 1);
      const int base = t < 6 ? S2 + t * 1024 : t < 12 ? S0 + (t - 6) * 1024 : S1 + (t - 12) * 1024;
      const char* rp = smem + base + (c & 1) * 512 + ((tok ^ (2 * c)) << 4);
      f32x4 v0 = f32x4{0, 0, 0, 0}, v1 = f32x4{0, 0, 0, 0};
#pragma unroll
      for (int p = 0; p < 8; ++p) {
        v0 += *(const LAS f32x4*)(LAS const char*)(rp + p * 6144);
        v1 += *(const LAS f32x4*)(LAS const char*)(rp + p * 6144 + 256);
      }
      u32x4 r = __builtin_bit_cast(u32x4, rs);
      u32x4 ov = {pack2(v0[0] + bflo(r[0]), v0[1] + bfhi(r[0])), pack2(v0[2] + bflo(r[1]), v0[3] + bfhi(r[1])),
                  pack2(v1[0] + bflo(r[2]), v1[1] + bfhi(r[2])), pack2(v1[2] + bflo(r[3]), v1[3] + bfhi(r[3]))};
      st16<true>(out + ooff + vo, ov);
    }
  }
  asm volatile(".Lmega_end:");
}

extern "C" void kernel_launch(void* const* d_in, const int* in_sizes, int n_in, void* d_out, int out_size,
                              void* d_ws, size_t ws_size, hipStream_t stream) {
  const char* x = (const char*)d_in[0];
  const char* wqkv = (const char*)d_in[1];
  const char* wo = (const char*)d_in[2];
  const char* w1 = (const char*)d_in[4];
  const char* w3 = (const char*)d_in[5];
  const char* w2 = (const char*)d_in[6];
  char* ws = (char*)d_ws;
  (void)hipFuncSetAttribute((const void*)k_mega, hipFuncAttributeMaxDynamicSharedMemorySize, MG_LDS);
  k_mega<<<256, 512, MG_LDS, stream>>>(x, wqkv, wo, w1, w3, w2, ws, (char*)d_out);
}
```

```cpp
#include <hip/hip_runtime.h>
#include <hip/hip_bf16.h>
#include <stdint.h>

typedef short bf16x8 __attribute__((ext_vector_type(8)));
typedef short s16x4 __attribute__((ext_vector_type(4)));
typedef float f32x4 __attribute__((ext_vector_type(4)));
typedef float f32x2 __attribute__((ext_vector_type(2)));
typedef unsigned u32x4 __attribute__((ext_vector_type(4)));
typedef unsigned u32x2 __attribute__((ext_vector_type(2)));
typedef __bf16 bf16x2 __attribute__((ext_vector_type(2)));
#define LAS __attribute__((address_space(3)))
#define DEV __device__ __forceinline__

constexpr int S_ = 2048, D_ = 512, H_ = 32, F_ = 1792;
constexpr float LN_EPS = 1e-5f;

typedef float f32x16 __attribute__((ext_vector_type(16)));
DEV f32x16 mfma32(bf16x8 a, bf16x8 b, f32x16 c) { return __builtin_amdgcn_mfma_f32_32x32x16_bf16(a, b, c, 0, 0, 0); }
DEV f32x4 mfma16(bf16x8 a, bf16x8 b, f32x4 c) { return __builtin_amdgcn_mfma_f32_16x16x32_bf16(a, b, c, 0, 0, 0); }
DEV unsigned pack2(float a, float b) {
  f32x2 v = {a, b};
  return __builtin_bit_cast(unsigned, __builtin_convertvector(v, bf16x2));
}
DEV float bflo(unsigned u) { return __builtin_bit_cast(float, u << 16); }
DEV float bfhi(unsigned u) { return __builtin_bit_cast(float, u & 0xffff0000u); }
DEV float dot2(unsigned a, unsigned b, float c) {
  return __builtin_amdgcn_fdot2_f32_bf16(__builtin_bit_cast(bf16x2, a), __builtin_bit_cast(bf16x2, b), c, false);
}
DEV void dma16(const char* gptr, unsigned ldsbase) {
  unsigned sv;
  asm volatile("s_mov_b32 %0, m0\n\ts_mov_b32 m0, %2\n\ts_nop 0\n\tglobal_load_lds_dwordx4 %1, off\n\ts_mov_b32 m0, %0"
               : "=&s"(sv) : "v"(gptr), "s"(ldsbase) : "memory");
}
DEV void dma16s(unsigned voff, const char* sbase, unsigned ldsbase) {
  unsigned sv;
  asm volatile("s_mov_b32 %0, m0\n\ts_mov_b32 m0, %3\n\ts_nop 0\n\tglobal_load_lds_dwordx4 %1, %2\n\ts_mov_b32 m0, %0"
               : "=&s"(sv) : "v"(voff), "s"(sbase), "s"(ldsbase) : "memory");
}
DEV void dma16sm(unsigned voff, const char* sbase, unsigned ldsbase, unsigned long long mask) {
  unsigned sv;
  asm volatile("s_mov_b32 %0, m0\n\ts_mov_b32 m0, %3\n\ts_mov_b64 exec, %4\n\tglobal_load_lds_dwordx4 %1, %2\n\ts_mov_b64 exec, -1\n\ts_mov_b32 m0, %0"
               : "=&s"(sv) : "v"(voff), "s"(sbase), "s"(ldsbase), "s"(mask) : "memory");
}
DEV void gld16(bf16x8& d, unsigned voff, const char* sbase) {
  asm volatile("global_load_dwordx4 %0, %1, %2" : "=v"(d) : "v"(voff), "s"(sbase) : "memory");
}
DEV void gld8(u32x2& d, unsigned voff, const char* sbase) {
  asm volatile("global_load_dwordx2 %0, %1, %2" : "=v"(d) : "v"(voff), "s"(sbase) : "memory");
}
template <int N> DEV void wait_bar() { asm volatile("s_waitcnt vmcnt(%0) lgkmcnt(0)\n\ts_barrier" ::"n"(N) : "memory"); }
DEV void bar() { asm volatile("s_waitcnt lgkmcnt(0)\n\ts_barrier" ::: "memory"); }
DEV bf16x8 lds128(const char* p) { return *(const LAS bf16x8*)(LAS const char*)p; }
DEV s16x4 ldstr(const char* p) { return __builtin_amdgcn_ds_read_tr16_b64_v4i16((LAS s16x4*)p); }
DEV bf16x8 cat(s16x4 a, s16x4 b) { return __builtin_shufflevector(a, b, 0, 1, 2, 3, 4, 5, 6, 7); }
DEV float xsum(float v) { v += __shfl_xor(v, 16); v += __shfl_xor(v, 32); return v; }
DEV float xmax(float v) { v = fmaxf(v, __shfl_xor(v, 16)); v = fmaxf(v, __shfl_xor(v, 32)); return v; }
DEV float dpp_xor1(float v) { return __builtin_bit_cast(float, __builtin_amdgcn_mov_dpp(__builtin_bit_cast(int, v), 0xB1, 0xF, 0xF, true)); }
DEV float dpp_xor2(float v) { return __builtin_bit_cast(float, __builtin_amdgcn_mov_dpp(__builtin_bit_cast(int, v), 0x4E, 0xF, 0xF, true)); }
DEV float dpp_ror4(float v) { return __builtin_bit_cast(float, __builtin_amdgcn_mov_dpp(__builtin_bit_cast(int, v), 0x124, 0xF, 0xF, true)); }
DEV float dpp_ror8(float v) { return __builtin_bit_cast(float, __builtin_amdgcn_mov_dpp(__builtin_bit_cast(int, v), 0x128, 0xF, 0xF, true)); }
DEV unsigned uni(unsigned v) { return __builtin_amdgcn_readfirstlane(v); }
#define UP 3
#define NPF 14
#define WT 31
template <bool W> DEV void st16(char* p, u32x4 v) {
  if constexpr (W) asm volatile("global_store_dwordx4 %0, %1, off sc0 sc1\n\ts_nop 1" ::"v"(p), "v"(v) : "memory");
  else *(u32x4*)p = v;
}
template <bool W> DEV void st8(char* p, u32x2 v) {
  if constexpr (W) asm volatile("global_store_dwordx2 %0, %1, off sc0 sc1" ::"v"(p), "v"(v) : "memory");
  else *(u32x2*)p = v;
}

template <int A, int B> struct Min { static constexpr int v = A < B ? A : B; };

template <int KT, int NK, int NS, int P, class Issue, class Compute>
DEV void pipe_steps(Issue& issue, Compute& compute) {
  if constexpr (KT < NK) {
    wait_bar<(Min<KT + NS - 1, NK>::v - KT - 1) * P>();
    if constexpr (KT + NS - 1 < NK) issue((KT + NS - 1) % NS);
    compute(KT % NS, KT);
    pipe_steps<KT + 1, NK, NS, P>(issue, compute);
  }
}

constexpr int K1_STAGE = 16384 + 12288, K1_NS = 5;
#define SB __builtin_amdgcn_sched_barrier(0)
DEV float xsum2(float v) {
  auto a = __builtin_amdgcn_permlane16_swap(__float_as_uint(v), __float_as_uint(v), false, false);
  v = __uint_as_float(a[0]) + __uint_as_float(a[1]);
  auto b = __builtin_amdgcn_permlane32_swap(__float_as_uint(v), __float_as_uint(v), false, false);
  return __uint_as_float(b[0]) + __uint_as_float(b[1]);
}

DEV void qkv_phase(char* smem, unsigned lds0, const char* __restrict__ x, const char* __restrict__ w, char* __restrict__ qkv,
                   int cb, int tb, const char* __restrict__ wo, const char* __restrict__ w1, const char* __restrict__ w3,
                   const char* __restrict__ w2, unsigned& tch) {
  const int tid = threadIdx.x, lane = tid & 63, wv = uni(tid >> 6);
  const int g = lane >> 4, i = lane & 15, q4 = (lane >> 2) & 3, p4 = lane & 3;
  const int tg = wv & 3, nh = wv >> 2;
  const int bi = blockIdx.x >> 3;

  const char* gx = x + (size_t)(tb * 128 + wv * 16) * 1024;
  const unsigned vt = (lane >> 3) * 1024 + (((lane & 7) ^ (lane >> 3)) << 4);
  const char* gw = w + cb * 192;
  unsigned vw0, vw1;
  {
    int qq = wv * 64 + lane;
    int k = qq / 12, c = (qq % 12) ^ (((k >> 3) & 1) << 1);
    vw0 = k * 3072 + c * 16;
    qq = 512 + wv * 32 + (lane & 31);
    k = qq / 12; c = (qq % 12) ^ (((k >> 3) & 1) << 1);
    vw1 = k * 3072 + c * 16;
  }
  const int k0 = bi & 7;
  auto dma = [&](int s, int j) {
    const int kk = (k0 + s) & 7;
    const unsigned base = lds0 + (s % K1_NS) * K1_STAGE;
    if (j == 0) dma16s(vt, gx + kk * 128, base + wv * 2048);
    else if (j == 1) dma16s(vt, gx + kk * 128 + 8192, base + wv * 2048 + 1024);
    else if (j == 2) dma16s(vw0, gw + kk * (64 * 3072), base + 16384 + wv * 1024);
    else dma16sm(vw1, gw + kk * (64 * 3072), base + 16384 + 8192 + wv * 512, 0xFFFFFFFFull);
  };
#pragma unroll
  for (int s = 0; s < UP; ++s)
#pragma unroll
    for (int j = 0; j < 4; ++j) dma(s, j);

  f32x4 acc[2][3], cs = f32x4{0, 0, 0, 0}, s1v = f32x4{0, 0, 0, 0}, gr = f32x4{0, 0, 0, 0};
#pragma unroll
  for (int a = 0; a < 3; ++a) { acc[0][a] = f32x4{0, 0, 0, 0}; acc[1][a] = f32x4{0, 0, 0, 0}; }
  const bf16x8 ones = {0x3F80, 0x3F80, 0x3F80, 0x3F80, 0x3F80, 0x3F80, 0x3F80, 0x3F80};
  int toff[2][2];
#pragma unroll
  for (int tt = 0; tt < 2; ++tt) {
    toff[tt][0] = (tg * 32 + (tt ^ nh) * 16 + i) * 128 + ((g ^ (i & 7)) << 4);
    toff[tt][1] = (tg * 32 + (tt ^ nh) * 16 + i) * 128 + (((4 + g) ^ (i & 7)) << 4);
  }
  int woff[3], un[3];
#pragma unroll
  for (int nt = 0; nt < 3; ++nt) {
    int a = nt + tg; a = a >= 3 ? a - 3 : a; a = a >= 3 ? a - 3 : a;
    un[nt] = nh * 3 + a;
    woff[nt] = 16384 + (8 * g + q4) * 192 + ((un[nt] ^ (g & 1)) << 5) + p4 * 8;
  }
  auto ldB = [&](int tt, int kt, int ks) -> bf16x8 { return lds128(smem + (kt % K1_NS) * K1_STAGE + toff[tt][ks]); };
  auto ldA = [&](int nt, int kt, int ks) -> bf16x8 {
    const char* p = smem + (kt % K1_NS) * K1_STAGE + woff[nt] + ks * 6144;
    return cat(ldstr(p), ldstr(p + 768));
  };
  bf16x8 A0[3], A1[3], B0[2], B1[2];
  auto phase = [&](bf16x8 (&Ac)[3], bf16x8 (&Bc)[2], bf16x8 (&An)[3], bf16x8 (&Bn)[2], int kn, int ksn, int dk, int dj, int nd) {
#pragma unroll
    for (int m = 0; m < 9; ++m) {
      if (kn < 8) {
        if (m == 0) { Bn[0] = ldB(0, kn, ksn); An[0] = ldA(0, kn, ksn); }
        else if (m == 1) Bn[1] = ldB(1, kn, ksn);
        else if (m < 4) An[m - 1] = ldA(m - 1, kn, ksn);
      }
      if (m < 6) acc[m & 1][m >> 1] = mfma16(Ac[m >> 1], Bc[m & 1], acc[m & 1][m >> 1]);
      else if (m == 6) cs = mfma16(Ac[0], ones, cs);
      else if (m == 7) s1v = mfma16(ones, Bc[0], s1v);
      else gr = mfma16(Bc[0], Bc[0], gr);
      if (dk < 8) {
        if (nd == 2) { if (m == 2 || m == 5) dma(dk, dj + (m == 5)); }
        else { if (m == 1 || m == 2) dma(dk, m - 1); else if (m == 4 || m == 5) dma(dk, m - 2); }
      }
      SB;
    }
  };
  wait_bar<4 * UP - 4>();
  B0[0] = ldB(0, 0, 0); A0[0] = ldA(0, 0, 0); B0[1] = ldB(1, 0, 0); A0[1] = ldA(1, 0, 0); A0[2] = ldA(2, 0, 0);
#pragma unroll
  for (int kt = 0; kt < 8; ++kt) {
    const int d1 = UP == 5 ? 8 : UP == 4 ? (kt == 0 ? 4 : 8) : (kt == 0 ? 3 : kt == 1 ? 5 : 8);
    const int d2 = UP == 3 ? (kt == 0 ? 4 : kt == 1 ? 6 : kt == 2 ? 7 : 8) : kt + 5;
    phase(A0, B0, A1, B1, kt, 1, d1, 0, 4);
    if (kt == 0) wait_bar<UP == 3 ? 8 : 12>();
    else if (kt < 4) wait_bar<12>();
    else if (kt == 4) wait_bar<8 + 1>();
    else if (kt == 5) wait_bar<4 + 1>();
    else if (kt == 6) wait_bar<0 + 1>();
    if (kt == 3) {
      const unsigned bb = blockIdx.x, xc = bb & 7, t2 = tid < 112 ? tid : tid - 112;
      const char* p = tid < 224 ? (tid < 112 ? w1 : w3) + (size_t)((((xc >> 2) ^ 1) * 256 + (bb >> 3) * 8 + t2 / 14) * 3584 + (xc & 3) * 896 + (t2 % 14) * 64)
                    : tid < 256 ? wo + (bb * 32 + tid - 224) * 64 : w2 + (bb * 112 + min(tid, 367) - 256) * 64;
      asm volatile("global_load_dword %0, %1, off" : "=v"(tch) : "v"(p) : "memory");
    }
    phase(A1, B1, A0, B0, kt + 1, 0, d2, 0, 4);
  }

  float* st = (float*)(smem + 49152);
  float* csp = (float*)(smem + 49152 + 1024);
  if (g == (i >> 2)) {
    const float s2 = (i & 2) ? ((i & 1) ? gr[3] : gr[2]) : ((i & 1) ? gr[1] : gr[0]);
    float mean = s1v[0] * (1.f / 512.f);
    float var = fmaxf(s2 * (1.f / 512.f) - mean * mean, 0.f);
    float rstd = rsqrtf(var + LN_EPS);
    *(f32x2*)(st + (tg * 32 + nh * 16 + i) * 2) = f32x2{-mean * rstd, rstd};
  }
  if (i == 0 && tg < 3) *(f32x4*)(csp + un[0] * 16 + 4 * g) = cs;
  bar();
  f32x2 ms[2];
#pragma unroll
  for (int tt = 0; tt < 2; ++tt) ms[tt] = *(const f32x2*)(st + (tg * 32 + (tt ^ nh) * 16 + i) * 2);
  const int otile = (g & 1) ^ nh, ohalf = g >> 1;
  const unsigned vo = ohalf * 512 + (((otile * 16 + i) ^ (8 * ohalf)) << 4);
#pragma unroll
  for (int nt = 0; nt < 3; ++nt) {
    f32x4 c = *(const f32x4*)(csp + un[nt] * 16 + 4 * g);
    const float qs = cb * 6 + un[nt] < 32 ? 0.25f * 1.4426950408889634f : 1.f;
    unsigned o[2][2];
#pragma unroll
    for (int tt = 0; tt < 2; ++tt) {
      f32x4 y = acc[tt][nt] * (ms[tt][1] * qs) + (ms[tt][0] * qs) * c;
      o[tt][0] = pack2(y[0], y[1]); o[tt][1] = pack2(y[2], y[3]);
    }
    auto sx = __builtin_amdgcn_permlane16_swap(o[0][0], o[1][0], false, false);
    auto sy = __builtin_amdgcn_permlane16_swap(o[0][1], o[1][1], false, false);
    char* dst = qkv + (size_t)(cb * 6 + un[nt]) * (2048 * 32) + (size_t)(tb * 4 + tg) * 1024;
    st16<true>(dst + vo, u32x4{sx[0], sy[0], sx[1], sy[1]});
  }
}

constexpr int S0 = 0, S1 = 49152, S2 = 98304, CSP = 147456, MG_LDS = 163840;
typedef unsigned long long u64;
extern "C" __device__ u64 llvm_dispatch_id() __asm("llvm.amdgcn.dispatch.id");
DEV void flag_set(u64* f, u64 tag) {
  asm volatile("global_store_dwordx2 %0, %1, off sc0 sc1" ::"v"(f), "v"(tag) : "memory");
}
DEV void flag_wait(const u64* p, u64 tag) {
  for (int it = 0; it < (1 << 20); ++it) {
    u64 c;
    asm volatile("global_load_dwordx2 %0, %1, off sc0 sc1\n\ts_waitcnt vmcnt(0)" : "=v"(c) : "v"(p) : "memory");
    if (__builtin_amdgcn_ballot_w64(c != tag) == 0) break;
  }
}
template <int V> struct IC { static constexpr int value = V; };
DEV int opaque(int v) { asm volatile("" : "+v"(v)); return v; }
DEV int opaque_s(int v) { asm volatile("" : "+s"(v)); return v; }

struct CW {
  const char *g1, *g3; unsigned vw; unsigned long long wmask;
};
DEV CW c_setup(const char* w1, const char* w3, int b, int wv, int lane) {
  const int xcd = b & 7, bi = b >> 3;
  const int cb = (xcd & 3) * 4 + (bi & 3);
  CW c;
  c.g1 = w1 + (size_t)(wv * 8) * (F_ * 2) + cb * 224;
  c.g3 = w3 + (size_t)(wv * 8) * (F_ * 2) + cb * 224;
  const int unit = ((lane >> 1) & 7) ^ ((lane >> 4) | ((wv & 1) << 2));
  c.vw = (lane >> 4) * (F_ * 2) + unit * 32 + (lane & 1) * 16;
  c.wmask = __builtin_amdgcn_ballot_w64(unit < 7);
  return c;
}
DEV void c_dmaw(const CW& c, unsigned ldsw, int kt, int j) {
  const unsigned base = ldsw + (kt % 3) * 49152;
  if (j < 2) dma16sm(c.vw, c.g1 + (size_t)(kt * 64 + j * 4) * (F_ * 2), base + 16384 + j * 1024, c.wmask);
  else dma16sm(c.vw, c.g3 + (size_t)(kt * 64 + (j - 2) * 4) * (F_ * 2), base + 32768 + (j - 2) * 1024, c.wmask);
}
DEV constexpr int d_unit(int u) {
  return u < 3 ? S2 + u * 2048 : u < 6 ? S0 + (u - 3) * 2048 : u < 9 ? S1 + (u - 6) * 2048 : u == 9 ? CSP
       : u < 13 ? S2 + (u - 10) * 2048 : S0;
}
DEV constexpr int d_ustr(int u) { return u == 9 ? 2048 : 6144; }
DEV void d_dma(const char* gw2, unsigned vw, unsigned lds0, int wv, int u, int q) {
  dma16s(vw, gw2 + (size_t)(u * 16 + q * 8) * 1024, lds0 + d_unit(u) + wv * d_ustr(u) + q * 1024);
}

template <int NT>
DEV void c_body(char* smem, unsigned lds0, const char* gt, const CW& cw, unsigned vt, int wv, int lane,
                const char* __restrict__ stats, const char* __restrict__ csb, char* __restrict__ hh, int tok0, int cb, const char* gw2, unsigned vwd) {
  const int tg = wv & 3;
  const unsigned ldsw = lds0 + wv * 2048;
  const int g = lane >> 4, i = lane & 15, q4 = (lane >> 2) & 3, p4 = lane & 3;
  f32x4 acc[2][2 * NT];
#pragma unroll
  for (int a = 0; a < 2 * NT; ++a) { acc[0][a] = f32x4{0, 0, 0, 0}; acc[1][a] = f32x4{0, 0, 0, 0}; }
  const int toff0 = (tg * 32 + i) * 128 + ((g ^ (i & 7)) << 4);
  const int toff1 = (tg * 32 + i) * 128 + (((4 + g) ^ (i & 7)) << 4);
  const int odd = cb & 1;
  const int fsw = q4 | ((g & 1) << 2);
  int T[NT], wa[NT];
#pragma unroll
  for (int a = 0; a < NT; ++a) {
    T[a] = NT == 4 ? odd * 3 + a : (a < 2 ? (odd ? 1 : 4) + a : (odd ? 0 : 6));
    wa[a] = 16384 + (8 * g + q4) * 256 + ((T[a] ^ fsw) << 5) + p4 * 8;
  }
  auto dmat = [&](int kt, int j) { dma16s(vt, gt + kt * 128 + j * 8192, ldsw + (kt % 3) * 49152 + j * 1024); };
  auto dma = [&](int kt, int j) {
    if (j < 2) dmat(kt, j); else c_dmaw(cw, ldsw, kt, j - 2);
  };
  auto ldB = [&](bf16x8* B, int kt, int ks) {
    const char* sb = smem + (kt % 3) * 49152 + (ks ? toff1 : toff0);
    B[0] = lds128(sb); B[1] = lds128(sb + 2048);
  };
  auto ldA = [&](int a, int kt, int ks) -> bf16x8 {
    const char* wp = smem + (kt % 3) * 49152 + wa[a < NT ? a : a - NT] + (a < NT ? 0 : 16384) + ks * 8192;
    return cat(ldstr(wp), ldstr(wp + 1024));
  };
  auto mma = [&](int a, bf16x8 A, const bf16x8* B) {
    acc[0][a] = mfma16(A, B[0], acc[0][a]);
    acc[1][a] = mfma16(A, B[1], acc[1][a]);
  };
  bf16x8 A0[2 * NT], A1[2 * NT], B0[2], B1[2];
  bf16x8 cp[4];
  u32x2 sa[4];
#pragma unroll
  for (int kt = 0; kt < 3; ++kt) { dmat(kt, 0); dmat(kt, 1); }
#pragma unroll
  for (int m = 0; m < 4; ++m)
    gld8(sa[m], (unsigned)((i >> 3) * 512 + g * 128 + (i & 7) * 8), stats + (size_t)(tok0 + tg * 32 + (m >> 1) * 16) * 64 + (m & 1) * 64);
  const int cq = min(tg * 16 + (lane >> 2), 55);
  if (NT == 3) {
#pragma unroll
    for (int m = 0; m < 4; ++m) gld16(cp[m], (unsigned)(((lane & 3) * 4 + m) * 896 + cq * 16), csb + (size_t)cb * 14336);
  }
  constexpr int X = NT == 3 ? 8 : 4;
  wait_bar<4 + X>();
  float rs[2], mr[2];
  float* csp = (float*)(smem + CSP);
  ldB(B0, 0, 0);
#pragma unroll
  for (int a = 0; a < 2 * NT; ++a) A0[a] = ldA(a, 0, 0);
#pragma unroll
  for (int kt = 0; kt < 8; ++kt) {
    ldB(B1, kt, 1);
#pragma unroll
    for (int a = 0; a < 2 * NT; ++a) {
      if (a < NT) { A1[2 * a] = ldA(2 * a, kt, 1); A1[2 * a + 1] = ldA(2 * a + 1, kt, 1); }
      mma(a, A0[a], B0);
      __builtin_amdgcn_sched_barrier(0);
    }
    if (kt == 0) wait_bar<2 + X>();
    else if (kt != 1) wait_bar<6>();
    else if (NT == 3) asm volatile("s_waitcnt vmcnt(6) lgkmcnt(0)\n\ts_barrier" : "+v"(sa[0]), "+v"(sa[1]), "+v"(sa[2]), "+v"(sa[3]), "+v"(cp[0]), "+v"(cp[1]), "+v"(cp[2]), "+v"(cp[3]) : : "memory");
    else asm volatile("s_waitcnt vmcnt(6) lgkmcnt(0)\n\ts_barrier" : "+v"(sa[0]), "+v"(sa[1]), "+v"(sa[2]), "+v"(sa[3]) : : "memory");
    if (kt < 7) ldB(B0, kt + 1, 0);
    if (kt == 1) {
#pragma unroll
      for (int tt = 0; tt < 2; ++tt) {
        const f32x2 sv0 = __builtin_bit_cast(f32x2, sa[2 * tt]), sv1 = __builtin_bit_cast(f32x2, sa[2 * tt + 1]);
        const float s1 = xsum2(sv0[0] + sv1[0]), s2 = xsum2(sv0[1] + sv1[1]);
        const float mean = s1 * (1.f / 512.f);
        const float var = fmaxf(s2 * (1.f / 512.f) - mean * mean, 0.f);
        rs[tt] = rsqrtf(var + LN_EPS); mr[tt] = mean * rs[tt];
      }
      if (NT == 3) {
        f32x4 c = (__builtin_bit_cast(f32x4, cp[0]) + __builtin_bit_cast(f32x4, cp[1])) +
                  (__builtin_bit_cast(f32x4, cp[2]) + __builtin_bit_cast(f32x4, cp[3]));
#pragma unroll
        for (int e = 0; e < 4; ++e) { c[e] += dpp_xor1(c[e]); c[e] += dpp_xor2(c[e]); }
        if ((lane & 3) == 0) *(f32x4*)(csp + cq * 4) = c;
      }
    }
#pragma unroll
    for (int a = 0; a < 2 * NT; ++a) {
      if (kt < 7 && a < NT) { A0[2 * a] = ldA(2 * a, kt + 1, 0); A0[2 * a + 1] = ldA(2 * a + 1, kt + 1, 0); }
      mma(a, A1[a], B1);
      if (a >= 2 * NT - 6) {
        const int j = a - (2 * NT - 6);
        if (kt + 3 < 8) dma(kt + 3, j);
        else d_dma(gw2, vwd, lds0, wv, (kt - 5) * 3 + (j >> 1), j & 1);
      }
      __builtin_amdgcn_sched_barrier(0);
    }
  }
  unsigned pk[2][NT][2];
#pragma unroll
  for (int tt = 0; tt < 2; ++tt) {
    char* dst = hh + ((size_t)((tok0 >> 4) + tg * 2 + tt) * 56) * 1024 + lane * 16;
#pragma unroll
    for (int a = 0; a < NT; ++a) {
      f32x4 cg = *(const f32x4*)(csp + T[a] * 16 + 4 * g);
      f32x4 cu = *(const f32x4*)(csp + (7 + T[a]) * 16 + 4 * g);
      float r[4];
#pragma unroll
      for (int e = 0; e < 4; ++e) {
        float ga = acc[tt][a][e] * rs[tt] - mr[tt] * cg[e];
        float up = acc[tt][NT + a][e] * rs[tt] - mr[tt] * cu[e];
        float sg = __builtin_amdgcn_rcpf(1.f + __builtin_amdgcn_exp2f(-1.4426950408889634f * ga));
        r[e] = ga * sg * up;
      }
      pk[tt][a][0] = pack2(r[0], r[1]); pk[tt][a][1] = pack2(r[2], r[3]);
      const int G = cb * 7 + T[a];
      if (a & 1) {
        st16<true>(dst + (size_t)(G >> 1) * 1024, u32x4{pk[tt][a - 1][0], pk[tt][a - 1][1], pk[tt][a][0], pk[tt][a][1]});
      } else if (a == NT - 1) {
        st8<true>(dst + (size_t)(G >> 1) * 1024 + (G & 1) * 512 - lane * 8, u32x2{pk[tt][a][0], pk[tt][a][1]});
      }
    }
  }
}

__global__ __launch_bounds__(512) void k_mega(const char* __restrict__ x, const char* __restrict__ wqkv, const char* __restrict__ wo,
                                              const char* __restrict__ w1, const char* __restrict__ w3,
                                              const char* __restrict__ w2, char* __restrict__ ws, char* __restrict__ out) {
  extern __shared__ __attribute__((aligned(16))) char smem[];
  char* qkv = ws;
  char* o = ws + (6u << 20);
  char* h = ws + (8u << 20);
  char* stats = ws + (10u << 20);
  u64* flags = (u64*)(ws + (10u << 20) + (512u << 10));
  char* csb = ws + (10u << 20) + (768u << 10);
  char* hh = ws + (11u << 20);
  const u64 tag = ((u64)(size_t)__builtin_amdgcn_dispatch_ptr() << 20) + llvm_dispatch_id();
  const int b = blockIdx.x;
  const unsigned lds0 = (unsigned)(size_t)(LAS char*)smem;
  const int cbG = (b >> 3) & 7, tbG = (b & 7) * 4 + (b >> 6);
  const int cbC = (b & 3) * 4 + ((b >> 3) & 3), tbC = ((b >> 2) & 1) * 8 + (b >> 5);

  unsigned cpf = 0;
  {
    u64 pc;
    unsigned len;
    asm volatile("s_getpc_b64 %0\n.Lmega_pc:\n\ts_mov_b32 %1, .Lmega_end-.Lmega_pc" : "=s"(pc), "=s"(len));
    const unsigned off = ((b >> 3) * 26 + threadIdx.x) * 64;
    if (threadIdx.x < 26 && off < len) asm volatile("global_load_dword %0, %1, %2" : "=v"(cpf) : "v"(off), "s"(pc) : "memory");
  }
  {
    const int cb = (b & 1) * 8 + ((b >> 3) & 7), tb = ((b & 7) >> 1) * 4 + (b >> 6);
    unsigned tch;
    qkv_phase(smem, lds0, x, wqkv, qkv, cb, tb, wo, w1, w3, w2, tch);
    asm volatile("s_waitcnt vmcnt(0) lgkmcnt(0)\n\ts_barrier" ::: "memory");
    asm volatile("" :: "v"(cpf), "v"(tch));
    const int tid = threadIdx.x;
    if (tid < 9) {
      const int P = cb * 3 + tid / 3, d = tid % 3, which = P >> 4, hp = P & 15, qb = tb + d;
      if (which ? qb < 16 : d == 0)
        flag_set(flags + 24576 + ((hp >> 1) | ((hp & 1) << 3) | (qb << 4)) * 32 + (which ? (which - 1) * 3 + d : 6), tag);
    }
  }
  {
    const int tid = opaque(threadIdx.x), lane = tid & 63, wv = uni(tid >> 6);
    const int c32 = lane & 31, hf = lane >> 5, q4 = (lane >> 2) & 3, p4 = lane & 3;
    const int hp = (b & 7) * 2 + ((b >> 3) & 1), qb = b >> 4;
    const int q0 = qb * 128;
    const size_t HS = 2048 * 32;
    bf16x8 Q, cw0, cw1;
    {
      const unsigned one = qb * 4 - 8 + wv >= 0 ? 0x3F803F80u : 0u;
      *(LAS u32x4*)(LAS char*)(smem + S1 + tid * 16) = u32x4{one, one, one, one};
      if (tid < 256) *(LAS u32x4*)(LAS char*)(smem + S1 + 8192 + tid * 16) = u32x4{0x3F803F80u, 0x3F803F80u, 0x3F803F80u, 0x3F803F80u};
    }
    if (wv == 0) {
      const int d = lane % 3;
      flag_wait(flags + 24576 + b * 32 + (lane >= 6 ? 6 : qb - d < 0 ? 6 : lane), tag);
    }
    asm volatile("s_barrier" ::: "memory");
    {
      const int r = wv >> 1;
      const char* src = qkv + (size_t)((1 + (r >> 1)) * 32 + hp * 2 + (r & 1)) * HS;
#pragma unroll
      for (int j = 0; j < 6; ++j) {
        const int kb = qb * 4 - 8 + (wv & 1) * 6 + j;
        if (kb < 0 && r >= 2) *(LAS u32x4*)(LAS char*)(smem + r * 12288 + ((wv & 1) * 6 + j) * 1024 + lane * 16) = u32x4{0, 0, 0, 0};
        else dma16s(kb < 0 ? hf * 640 : lane * 16, src + (size_t)max(kb, 0) * 1024, lds0 + r * 12288 + ((wv & 1) * 6 + j) * 1024);
      }
    }
    const int u = wv >> 2, qg = wv & 3;
    const int qs = q0 + qg * 32;
    const unsigned vq = hf * 512 + ((c32 ^ (8 * hf)) << 4);
    gld16(Q, vq, qkv + (size_t)(hp * 2 + u) * HS + (size_t)qs * 32);
    const float NEG = -1e30f;
    f32x16 cin0, cin8;
    u32x4 mk[2];
#pragma unroll
    for (int r = 0; r < 16; ++r) {
      const int kl = (r & 3) + 8 * (r >> 2) + 4 * hf;
      cin0[r] = kl > c32 ? 0.f : NEG; cin8[r] = kl <= c32 ? 0.f : NEG;
      if (r & 1) mk[r >> 3][(r >> 1) & 3] = (kl > c32 ? 0xFFFF0000u : 0u) | (kl - 1 > c32 ? 0xFFFFu : 0u);
    }
    asm volatile("s_waitcnt vmcnt(0) lgkmcnt(0)\n\ts_barrier" : "+v"(Q), "+v"(cin0), "+v"(cin8), "+v"(mk[0]), "+v"(mk[1]) : : "memory");
    const unsigned vwo = (lane >> 3) * 1024 + ((((lane & 7) >> 1) ^ ((lane >> 4) & 3)) << 5) + (lane & 1) * 16;
    const char* gwo = wo + (size_t)(wv * 64) * 1024 + cbG * 128;
    const CW cw1s = c_setup(w1, w3, b, wv, lane);
    auto pf = [&](int k) {
      if (k < 2) {
        const int ch = max(wv - 1, 0) * 4 + (lane & 3);
        const char* pc = (ch >= 14 ? w3 + (ch - 14) * 16 : w1 + ch * 16) + (size_t)(tbC * 32 + (lane >> 2)) * (F_ * 2) + cbC * 224;
        if (k == 0) asm volatile("global_load_dwordx4 %0, %1, off" : "=v"(cw0) : "v"(pc) : "memory");
        else asm volatile("global_load_dwordx4 %0, %1, off" : "=v"(cw1) : "v"(pc + 16 * (F_ * 2)) : "memory");
      } else if (k < 10) dma16s(vwo, gwo + (size_t)((k - 2) * 8) * 1024, lds0 + S2 + wv * 8192 + (k - 2) * 1024);
      else if (k < 14) c_dmaw(cw1s, lds0 + wv * 2048, 1, k - 10);
      else if (k == 14) {
        const int c0 = b * 8 + wv;
        dma16s(lane * 16, w2 + (size_t)(c0 < 1792 ? c0 : c0 - 1792) * 1024, lds0 + S1 + 12288 + (wv & 3) * 1024);
      }
    };
    {
      const char* Kl = smem + u * 12288 + qg * 1024 + vq;
      const char* Vl[2];
#pragma unroll
      for (int e = 0; e < 2; ++e)
        Vl[e] = c32 < 16 ? smem + 24576 + u * 12288 + qg * 1024 + (p4 >> 1) * 512 + (((e * 8 + 4 * hf + q4) ^ (8 * (p4 >> 1))) << 4) + (p4 & 1) * 8
                         : smem + S1 + qg * 1024 + (hf ? 0 : 64);
      f32x16 z;
#pragma unroll
      for (int r = 0; r < 16; ++r) z[r] = 0.f;
      bf16x8 Kf[9];
#pragma unroll
      for (int t = 0; t < 9; ++t) Kf[t] = lds128(Kl + t * 1024);
      f32x16 sm = mfma32(Kf[0], Q, cin0);
      {
        const f32x16 s8 = mfma32(Kf[8], Q, cin8);
#pragma unroll
        for (int r = 0; r < 16; ++r) sm[r] = fmaxf(sm[r], s8[r]);
      }
      float m = sm[0];
#pragma unroll
      for (int r = 1; r < 16; ++r) m = fmaxf(m, sm[r]);
#pragma unroll
      for (int t = 1; t < 8; ++t) {
        const f32x16 s = mfma32(Kf[t], Q, z);
#pragma unroll
        for (int r = 0; r < 16; ++r) m = fmaxf(m, s[r]);
      }
      {
        auto sw = __builtin_amdgcn_permlane32_swap(__float_as_uint(m), __float_as_uint(m), false, false);
        m = fmaxf(__uint_as_float(sw[0]), __uint_as_float(sw[1]));
      }
      f32x16 cm, oacc;
#pragma unroll
      for (int r = 0; r < 16; ++r) { cm[r] = -m; oacc[r] = 0.f; }
      f32x16 sn = mfma32(Kf[1], Q, cm);
      {
        u32x4 pb[2];
#pragma unroll
        for (int j = 0; j < 8; ++j)
          pb[j >> 2][j & 3] = pack2(__builtin_amdgcn_exp2f(sm[2 * j] - m), __builtin_amdgcn_exp2f(sm[2 * j + 1] - m));
#pragma unroll
        for (int sh = 0; sh < 2; ++sh) {
          const u32x4 p0 = pb[sh] & mk[sh], p8 = pb[sh] ^ p0;
          oacc = mfma32(cat(ldstr(Vl[0] + sh * 256), ldstr(Vl[1] + sh * 256)), __builtin_bit_cast(bf16x8, p0), oacc);
          pf(2 * sh);
          oacc = mfma32(cat(ldstr(Vl[0] + 8192 + sh * 256), ldstr(Vl[1] + 8192 + sh * 256)), __builtin_bit_cast(bf16x8, p8), oacc);
          pf(2 * sh + 1);
        }
      }
      SB;
#pragma unroll
      for (int t = 1; t < 8; ++t) {
        const f32x16 s = sn;
        if (t < 7) sn = mfma32(Kf[t + 1], Q, cm);
#pragma unroll
        for (int sh = 0; sh < 2; ++sh) {
          u32x4 pb;
#pragma unroll
          for (int e = 0; e < 4; ++e)
            pb[e] = pack2(__builtin_amdgcn_exp2f(s[8 * sh + 2 * e]), __builtin_amdgcn_exp2f(s[8 * sh + 2 * e + 1]));
          const int vp = t * 1024 + sh * 256;
          oacc = mfma32(cat(ldstr(Vl[0] + vp), ldstr(Vl[1] + vp)), __builtin_bit_cast(bf16x8, pb), oacc);
          if (2 * t + sh + 2 < NPF) pf(2 * t + sh + 2);
        }
        SB;
      }
      const float inv = __builtin_amdgcn_rcpf(oacc[8]);
      const u32x4 ov = {pack2(oacc[0] * inv, oacc[1] * inv), pack2(oacc[2] * inv, oacc[3] * inv),
                        pack2(oacc[4] * inv, oacc[5] * inv), pack2(oacc[6] * inv, oacc[7] * inv)};
      st16<true>(o + ((size_t)((qs >> 4) + (c32 >> 4)) * 16 + hp) * 1024 + ((2 * u + hf) * 16 + (c32 & 15)) * 16, ov);
    }
    asm volatile("s_waitcnt vmcnt(0) lgkmcnt(0)\n\ts_barrier" : "+v"(cw0), "+v"(cw1) : : "memory");
    if (tid < 16) flag_set(flags + (((qb >> 1) | (tid << 3) | ((qb & 1) << 7)) * 32 + hp), tag);
    {
      const CW cw = c_setup(w1, w3, b, wv, lane);
#pragma unroll
      for (int j = 0; j < 4; ++j) c_dmaw(cw, lds0 + wv * 2048, 0, j);
    }
    if (wv == 0) { flag_wait(flags + b * 32 + (lane & 15), tag); }
    else {
      const u32x4 ua = __builtin_bit_cast(u32x4, cw0), ub = __builtin_bit_cast(u32x4, cw1);
      f32x4 lo, hi;
#pragma unroll
      for (int e = 0; e < 2; ++e) {
        lo[2 * e] = bflo(ua[e]) + bflo(ub[e]); lo[2 * e + 1] = bfhi(ua[e]) + bfhi(ub[e]);
        hi[2 * e] = bflo(ua[2 + e]) + bflo(ub[2 + e]); hi[2 * e + 1] = bfhi(ua[2 + e]) + bfhi(ub[2 + e]);
      }
#pragma unroll
      for (int e = 0; e < 4; ++e) {
        lo[e] += dpp_ror4(lo[e]); lo[e] += dpp_ror8(lo[e]); lo[e] = xsum2(lo[e]);
        hi[e] += dpp_ror4(hi[e]); hi[e] += dpp_ror8(hi[e]); hi[e] = xsum2(hi[e]);
      }
      if (lane < 8) {
        char* d = csb + (size_t)(cbC * 16 + tbC) * 896 + ((wv - 1) * 4 + (lane & 3)) * 32 + (lane >> 2) * 16;
        st16<true>(d, __builtin_bit_cast(u32x4, lane < 4 ? lo : hi));
      }
      asm volatile("s_waitcnt vmcnt(0)" ::: "memory");
    }
    asm volatile("s_barrier" ::: "memory");
    if (tid >= 64 && tid < 80) flag_set(flags + 8192 + ((cbC >> 2) | ((tid & 1) << 2) | ((cbC & 3) << 3) | (((tid & 15) >> 1) << 5)) * 32 + 16 + tbC, tag);
  }
  {
    const int tid = opaque(threadIdx.x), lane = tid & 63, wv = uni(tid >> 6);
    const int g = lane >> 4, i = lane & 15, q4 = (lane >> 2) & 3, p4 = lane & 3;
    const int kp = wv & 3, th = wv >> 2;
    bf16x8 B[4][2], rs;
    const char* ga = o + ((size_t)(tbG * 4 + th * 2) * 16 + kp * 4) * 1024;
#pragma unroll
    for (int s = 0; s < 4; ++s) {
      gld16(B[s][0], lane * 16, ga + s * 1024);
      gld16(B[s][1], lane * 16, ga + (16 + s) * 1024);
    }
    const size_t ooff = (size_t)(tbG * 64 + wv * 8) * 1024 + cbG * 128;
    const unsigned vo = (lane >> 3) * 1024 + (lane & 7) * 16;
    gld16(rs, vo, x + ooff);
    const int sw = (2 * (g & 1) + (q4 >> 1)) & 3;
    const char* wr = smem + S2 + kp * 16384 + (16 * (g >> 1) + 4 * (g & 1) + q4) * 128 + p4 * 8;
    bf16x8 A[4][4];
#pragma unroll
    for (int s = 0; s < 4; ++s)
#pragma unroll
      for (int nt = 0; nt < 4; ++nt) {
        const char* p = wr + s * 4096 + ((nt ^ sw) << 5);
        A[s][nt] = cat(ldstr(p), ldstr(p + 1024));
      }
    f32x4 acc[2][4];
#pragma unroll
    for (int t = 0; t < 2; ++t)
#pragma unroll
      for (int n = 0; n < 4; ++n) acc[t][n] = f32x4{0, 0, 0, 0};
    asm volatile("s_waitcnt vmcnt(7)" : "+v"(B[0][0]), "+v"(B[0][1]) : : "memory");
#pragma unroll
    for (int nt = 0; nt < 4; ++nt) { acc[0][nt] = mfma16(A[0][nt], B[0][0], acc[0][nt]); acc[1][nt] = mfma16(A[0][nt], B[0][1], acc[1][nt]); }
    asm volatile("s_waitcnt vmcnt(5)" : "+v"(B[1][0]), "+v"(B[1][1]) : : "memory");
#pragma unroll
    for (int nt = 0; nt < 4; ++nt) { acc[0][nt] = mfma16(A[1][nt], B[1][0], acc[0][nt]); acc[1][nt] = mfma16(A[1][nt], B[1][1], acc[1][nt]); }
    asm volatile("s_waitcnt vmcnt(3)" : "+v"(B[2][0]), "+v"(B[2][1]) : : "memory");
#pragma unroll
    for (int nt = 0; nt < 4; ++nt) { acc[0][nt] = mfma16(A[2][nt], B[2][0], acc[0][nt]); acc[1][nt] = mfma16(A[2][nt], B[2][1], acc[1][nt]); }
    asm volatile("s_waitcnt vmcnt(1)" : "+v"(B[3][0]), "+v"(B[3][1]) : : "memory");
#pragma unroll
    for (int nt = 0; nt < 4; ++nt) { acc[0][nt] = mfma16(A[3][nt], B[3][0], acc[0][nt]); acc[1][nt] = mfma16(A[3][nt], B[3][1], acc[1][nt]); }
    bar();
    {
      const int mo = S2 + kp * 16384 + g * 256 + ((i ^ ((g >> 1) * 2)) << 4);
#pragma unroll
      for (int tt = 0; tt < 2; ++tt)
#pragma unroll
        for (int nt = 0; nt < 4; ++nt)
          *(LAS f32x4*)(LAS char*)(smem + (mo ^ (nt * 64)) + ((th * 2 + tt) * 4 + nt) * 1024) = acc[tt][nt];
    }
    asm volatile("s_waitcnt vmcnt(0) lgkmcnt(0)\n\ts_barrier" : "+v"(rs) : : "memory");
    {
      const int c = lane & 7;
      const int tok = (lane >> 3) + 8 * (wv & 1);
      const char* rp = smem + S2 + ((wv >> 1) * 4 + (c >> 1)) * 1024 + (c & 1) * 512 + ((tok ^ (2 * c)) << 4);
      f32x4 v0 = f32x4{0, 0, 0, 0}, v1 = f32x4{0, 0, 0, 0};
#pragma unroll
      for (int p = 0; p < 4; ++p) {
        v0 += *(const LAS f32x4*)(LAS const char*)(rp + p * 16384);
        v1 += *(const LAS f32x4*)(LAS const char*)(rp + p * 16384 + 256);
      }
      u32x4 r = __builtin_bit_cast(u32x4, rs);
      u32x4 ov = {pack2(v0[0] + bflo(r[0]), v0[1] + bfhi(r[0])), pack2(v0[2] + bflo(r[1]), v0[3] + bfhi(r[1])),
                  pack2(v1[0] + bflo(r[2]), v1[1] + bfhi(r[2])), pack2(v1[2] + bflo(r[3]), v1[3] + bfhi(r[3]))};
      st16<true>(h + ooff + vo, ov);
      float s1 = 0.f, s2 = 0.f;
#pragma unroll
      for (int e = 0; e < 4; ++e) { s1 = dot2(ov[e], 0x3F803F80u, s1); s2 = dot2(ov[e], ov[e], s2); }
#pragma unroll
      for (int m = 1; m < 8; m <<= 1) { s1 += __shfl_xor(s1, m); s2 += __shfl_xor(s2, m); }
      if (c == 0) st8<true>(stats + ((size_t)(tbG * 8 + wv) * 8 + cbG) * 64 + (lane >> 3) * 8,
                            u32x2{__float_as_uint(s1), __float_as_uint(s2)});
    }
    asm volatile("s_waitcnt vmcnt(0) lgkmcnt(0)\n\ts_barrier" ::: "memory");
    if (tid < 16) flag_set(flags + 8192 + ((tid & 3) | ((tbG >> 4) << 2) | ((tid >> 2) << 3) | (((tbG >> 1) & 7) << 5)) * 32 + (tbG & 1) * 8 + cbG, tag);
    {
      const CW cw = c_setup(w1, w3, b, wv, lane);
#pragma unroll
      for (int j = 0; j < 4; ++j) c_dmaw(cw, lds0 + wv * 2048, 2, j);
    }
    if (wv == 0) flag_wait(flags + 8192 + b * 32 + (lane & 31), tag);
    asm volatile("s_barrier" ::: "memory");
  }
  const unsigned vwd_ = 0;
  {
    const int tid = opaque(threadIdx.x), lane = tid & 63, wv = uni(tid >> 6);
    const char* gt = h + (size_t)(tbC * 128 + wv * 16) * 1024;
    const unsigned vt = (lane >> 3) * 1024 + (((lane & 7) ^ (lane >> 3)) << 4);
    const CW cw = c_setup(w1, w3, b, wv, lane);
    const unsigned vwd = (lane >> 3) * 1024 + ((((lane & 7) >> 1) ^ ((lane >> 4) & 3)) << 5) + (lane & 1) * 16;
    const char* gw2 = w2 + (size_t)(wv * 224) * 1024 + cbG * 128;
    if ((wv >> 2) == 0) c_body<4>(smem, lds0, gt, cw, vt, wv, lane, stats, csb, hh, tbC * 128, cbC, gw2, vwd);
    else c_body<3>(smem, lds0, gt, cw, vt, wv, lane, stats, csb, hh, tbC * 128, cbC, gw2, vwd);
  }
  {
    const int tid = opaque(threadIdx.x), lane = tid & 63, wv = uni(tid >> 6);
    const int g = lane >> 4, i = lane & 15, q4 = (lane >> 2) & 3, p4 = lane & 3;
    asm volatile("s_waitcnt vmcnt(0) lgkmcnt(0)\n\ts_barrier" ::: "memory");
    if (tid < 16) flag_set(flags + 16384 + ((tbC >> 1) | (tid << 3) | ((tbC & 1) << 7)) * 32 + cbC, tag);
    const unsigned vwd = (lane >> 3) * 1024 + ((((lane & 7) >> 1) ^ ((lane >> 4) & 3)) << 5) + (lane & 1) * 16;
    const char* gw2 = w2 + (size_t)(wv * 224) * 1024 + cbG * 128;
    d_dma(gw2, vwd, lds0, wv, 9, 0); d_dma(gw2, vwd, lds0, wv, 9, 1);
    const int sw = (2 * g + (q4 >> 1)) & 3;
    const int ro = (4 * g + q4) * 128 + p4 * 8;
    int rn[4];
#pragma unroll
    for (int nt = 0; nt < 4; ++nt) rn[nt] = ro + ((nt ^ sw) << 5);
    auto lda = [&](bf16x8* Af, int j) {
      const char* p0 = smem + d_unit(2 * j) + wv * d_ustr(2 * j);
      const char* p1 = smem + d_unit(2 * j + 1) + wv * d_ustr(2 * j + 1);
#pragma unroll
      for (int nt = 0; nt < 4; ++nt) Af[nt] = cat(ldstr(p0 + rn[nt]), ldstr(p1 + rn[nt]));
    };
    bf16x8 A[2][4], B[7][4], rs;
    lda(A[0], 0); lda(A[1], 1);
    asm volatile("s_waitcnt lgkmcnt(0)" : "+v"(A[0][0]), "+v"(A[0][1]), "+v"(A[0][2]), "+v"(A[0][3]), "+v"(A[1][0]), "+v"(A[1][1]), "+v"(A[1][2]), "+v"(A[1][3]) : : "memory");
#pragma unroll
    for (int u = 10; u < 14; ++u) { d_dma(gw2, vwd, lds0, wv, u, 0); d_dma(gw2, vwd, lds0, wv, u, 1); }
    if (wv == 0) flag_wait(flags + 16384 + b * 32 + (lane & 15), tag);
    asm volatile("s_barrier" ::: "memory");
    const char* ga = hh + ((size_t)(tbG * 4) * 56 + wv * 7) * 1024;
    u32x2 Bl[4], Bh[4];
#pragma unroll
    for (int j = 0; j < 7; ++j)
#pragma unroll
      for (int tt = 0; tt < 4; ++tt) {
        if (j == 3) {
          gld8(Bl[tt], lane * 8, ga + (size_t)(tt * 56 + j) * 1024);
          gld8(Bh[tt], lane * 8, ga + (size_t)(tt * 56 + j) * 1024 + 512);
        } else gld16(B[j][tt], lane * 16, ga + (size_t)(tt * 56 + j) * 1024);
      }
    const size_t ooff = (size_t)(tbG * 64 + wv * 8) * 1024 + cbG * 128;
    const unsigned vo = (lane >> 3) * 1024 + (lane & 7) * 16;
    gld16(rs, vo, h + ooff);
    f32x4 acc[4][4];
#pragma unroll
    for (int t = 0; t < 4; ++t)
#pragma unroll
      for (int n = 0; n < 4; ++n) acc[t][n] = f32x4{0, 0, 0, 0};
    auto step = [&](auto J) {
      constexpr int j = decltype(J)::value;
      if constexpr (j == 3) {
        asm volatile("s_waitcnt vmcnt(13)" : "+v"(Bl[0]), "+v"(Bl[1]), "+v"(Bl[2]), "+v"(Bl[3]), "+v"(Bh[0]), "+v"(Bh[1]), "+v"(Bh[2]), "+v"(Bh[3]) : : "memory");
#pragma unroll
        for (int tt = 0; tt < 4; ++tt) B[3][tt] = __builtin_bit_cast(bf16x8, u32x4{Bl[tt][0], Bl[tt][1], Bh[tt][0], Bh[tt][1]});
      } else
        asm volatile("s_waitcnt vmcnt(%4)" : "+v"(B[j][0]), "+v"(B[j][1]), "+v"(B[j][2]), "+v"(B[j][3]) : "n"((j < 3 ? 29 : 25) - 4 * j) : "memory");
#pragma unroll
      for (int nt = 0; nt < 4; ++nt)
#pragma unroll
        for (int tt = 0; tt < 4; ++tt) acc[tt][nt] = mfma16(A[j & 1][nt], B[j][tt], acc[tt][nt]);
      __builtin_amdgcn_sched_barrier(0);
      if constexpr (j + 2 < 7) lda(A[j & 1], j + 2);
    };
    step(IC<0>{}); step(IC<1>{}); step(IC<2>{}); step(IC<3>{}); step(IC<4>{}); step(IC<5>{}); step(IC<6>{});
    {
      const int mo = wv * 6144 + g * 256 + ((i ^ ((g >> 1) * 2)) << 4);
#pragma unroll
      for (int tt = 0; tt < 4; ++tt)
#pragma unroll
        for (int nt = 0; nt < 4; ++nt) {
          const int t = tt * 4 + nt;
          const int base = t < 6 ? S2 + t * 1024 : t < 12 ? S0 + (t - 6) * 1024 : S1 + (t - 12) * 1024;
          *(LAS f32x4*)(LAS char*)(smem + (mo ^ (nt * 64)) + base) = acc[tt][nt];
        }
    }
    asm volatile("s_waitcnt vmcnt(0) lgkmcnt(0)\n\ts_barrier" : "+v"(rs) : : "memory");
    {
      const int c = lane & 7;
      const int tok = (lane >> 3) + 8 * (wv & 1);
      const int t = (wv >> 1) * 4 + (c >> 1);
      const int base = t < 6 ? S2 + t * 1024 : t < 12 ? S0 + (t - 6) * 1024 : S1 + (t - 12) * 1024;
      const char* rp = smem + base + (c & 1) * 512 + ((tok ^ (2 * c)) << 4);
      f32x4 v0 = f32x4{0, 0, 0, 0}, v1 = f32x4{0, 0, 0, 0};
#pragma unroll
      for (int p = 0; p < 8; ++p) {
        v0 += *(const LAS f32x4*)(LAS const char*)(rp + p * 6144);
        v1 += *(const LAS f32x4*)(LAS const char*)(rp + p * 6144 + 256);
      }
      u32x4 r = __builtin_bit_cast(u32x4, rs);
      u32x4 ov = {pack2(v0[0] + bflo(r[0]), v0[1] + bfhi(r[0])), pack2(v0[2] + bflo(r[1]), v0[3] + bfhi(r[1])),
                  pack2(v1[0] + bflo(r[2]), v1[1] + bfhi(r[2])), pack2(v1[2] + bflo(r[3]), v1[3] + bfhi(r[3]))};
      st16<true>(out + ooff + vo, ov);
    }
  }
  asm volatile(".Lmega_end:");
}

extern "C" void kernel_launch(void* const* d_in, const int* in_sizes, int n_in, void* d_out, int out_size,
                              void* d_ws, size_t ws_size, hipStream_t stream) {
  const char* x = (const char*)d_in[0];
  const char* wqkv = (const char*)d_in[1];
  const char* wo = (const char*)d_in[2];
  const char* w1 = (const char*)d_in[4];
  const char* w3 = (const char*)d_in[5];
  const char* w2 = (const char*)d_in[6];
  char* ws = (char*)d_ws;
  (void)hipFuncSetAttribute((const void*)k_mega, hipFuncAttributeMaxDynamicSharedMemorySize, MG_LDS);
  k_mega<<<256, 512, MG_LDS, stream>>>(x, wqkv, wo, w1, w3, w2, ws, (char*)d_out);
}
```

```cpp
#include <hip/hip_runtime.h>
#include <hip/hip_bf16.h>
#include <stdint.h>

typedef short bf16x8 __attribute__((ext_vector_type(8)));
typedef short s16x4 __attribute__((ext_vector_type(4)));
typedef float f32x4 __attribute__((ext_vector_type(4)));
typedef float f32x2 __attribute__((ext_vector_type(2)));
typedef unsigned u32x4 __attribute__((ext_vector_type(4)));
typedef unsigned u32x2 __attribute__((ext_vector_type(2)));
typedef __bf16 bf16x2 __attribute__((ext_vector_type(2)));
#define LAS __attribute__((address_space(3)))
#define DEV __device__ __forceinline__

constexpr int S_ = 2048, D_ = 512, H_ = 32, F_ = 1792;
constexpr float LN_EPS = 1e-5f;

typedef float f32x16 __attribute__((ext_vector_type(16)));
DEV f32x16 mfma32(bf16x8 a, bf16x8 b, f32x16 c) { return __builtin_amdgcn_mfma_f32_32x32x16_bf16(a, b, c, 0, 0, 0); }
DEV f32x4 mfma16(bf16x8 a, bf16x8 b, f32x4 c) { return __builtin_amdgcn_mfma_f32_16x16x32_bf16(a, b, c, 0, 0, 0); }
DEV unsigned pack2(float a, float b) {
  f32x2 v = {a, b};
  return __builtin_bit_cast(unsigned, __builtin_convertvector(v, bf16x2));
}
DEV float bflo(unsigned u) { return __builtin_bit_cast(float, u << 16); }
DEV float bfhi(unsigned u) { return __builtin_bit_cast(float, u & 0xffff0000u); }
DEV float dot2(unsigned a, unsigned b, float c) {
  return __builtin_amdgcn_fdot2_f32_bf16(__builtin_bit_cast(bf16x2, a), __builtin_bit_cast(bf16x2, b), c, false);
}
DEV void dma16(const char* gptr, unsigned ldsbase) {
  unsigned sv;
  asm volatile("s_mov_b32 %0, m0\n\ts_mov_b32 m0, %2\n\ts_nop 0\n\tglobal_load_lds_dwordx4 %1, off\n\ts_mov_b32 m0, %0"
               : "=&s"(sv) : "v"(gptr), "s"(ldsbase) : "memory");
}
DEV void dma16s(unsigned voff, const char* sbase, unsigned ldsbase) {
  unsigned sv;
  asm volatile("s_mov_b32 %0, m0\n\ts_mov_b32 m0, %3\n\ts_nop 0\n\tglobal_load_lds_dwordx4 %1, %2\n\ts_mov_b32 m0, %0"
               : "=&s"(sv) : "v"(voff), "s"(sbase), "s"(ldsbase) : "memory");
}
DEV void dma16sm(unsigned voff, const char* sbase, unsigned ldsbase, unsigned long long mask) {
  unsigned sv;
  asm volatile("s_mov_b32 %0, m0\n\ts_mov_b32 m0, %3\n\ts_mov_b64 exec, %4\n\tglobal_load_lds_dwordx4 %1, %2\n\ts_mov_b64 exec, -1\n\ts_mov_b32 m0, %0"
               : "=&s"(sv) : "v"(voff), "s"(sbase), "s"(ldsbase), "s"(mask) : "memory");
}
DEV void gld16(bf16x8& d, unsigned voff, const char* sbase) {
  asm volatile("global_load_dwordx4 %0, %1, %2" : "=v"(d) : "v"(voff), "s"(sbase) : "memory");
}
DEV void gld8(u32x2& d, unsigned voff, const char* sbase) {
  asm volatile("global_load_dwordx2 %0, %1, %2" : "=v"(d) : "v"(voff), "s"(sbase) : "memory");
}
template <int N> DEV void wait_bar() { asm volatile("s_waitcnt vmcnt(%0) lgkmcnt(0)\n\ts_barrier" ::"n"(N) : "memory"); }
DEV void bar() { asm volatile("s_waitcnt lgkmcnt(0)\n\ts_barrier" ::: "memory"); }
DEV bf16x8 lds128(const char* p) { return *(const LAS bf16x8*)(LAS const char*)p; }
DEV s16x4 ldstr(const char* p) { return __builtin_amdgcn_ds_read_tr16_b64_v4i16((LAS s16x4*)p); }
DEV bf16x8 cat(s16x4 a, s16x4 b) { return __builtin_shufflevector(a, b, 0, 1, 2, 3, 4, 5, 6, 7); }
DEV float xsum(float v) { v += __shfl_xor(v, 16); v += __shfl_xor(v, 32); return v; }
DEV float xmax(float v) { v = fmaxf(v, __shfl_xor(v, 16)); v = fmaxf(v, __shfl_xor(v, 32)); return v; }
DEV float dpp_xor1(float v) { return __builtin_bit_cast(float, __builtin_amdgcn_mov_dpp(__builtin_bit_cast(int, v), 0xB1, 0xF, 0xF, true)); }
DEV float dpp_xor2(float v) { return __builtin_bit_cast(float, __builtin_amdgcn_mov_dpp(__builtin_bit_cast(int, v), 0x4E, 0xF, 0xF, true)); }
DEV float dpp_ror4(float v) { return __builtin_bit_cast(float, __builtin_amdgcn_mov_dpp(__builtin_bit_cast(int, v), 0x124, 0xF, 0xF, true)); }
DEV float dpp_ror8(float v) { return __builtin_bit_cast(float, __builtin_amdgcn_mov_dpp(__builtin_bit_cast(int, v), 0x128, 0xF, 0xF, true)); }
DEV unsigned uni(unsigned v) { return __builtin_amdgcn_readfirstlane(v); }
#define UP 3
#define NPF 14
#define PFE 2
#define PM 4
#define WT 31
template <bool W> DEV void st16(char* p, u32x4 v) {
  if constexpr (W) asm volatile("global_store_dwordx4 %0, %1, off sc0 sc1\n\ts_nop 1" ::"v"(p), "v"(v) : "memory");
  else *(u32x4*)p = v;
}
template <bool W> DEV void st8(char* p, u32x2 v) {
  if constexpr (W) asm volatile("global_store_dwordx2 %0, %1, off sc0 sc1" ::"v"(p), "v"(v) : "memory");
  else *(u32x2*)p = v;
}

template <int A, int B> struct Min { static constexpr int v = A < B ? A : B; };

template <int KT, int NK, int NS, int P, class Issue, class Compute>
DEV void pipe_steps(Issue& issue, Compute& compute) {
  if constexpr (KT < NK) {
    wait_bar<(Min<KT + NS - 1, NK>::v - KT - 1) * P>();
    if constexpr (KT + NS - 1 < NK) issue((KT + NS - 1) % NS);
    compute(KT % NS, KT);
    pipe_steps<KT + 1, NK, NS, P>(issue, compute);
  }
}

constexpr int K1_STAGE = 16384 + 12288, K1_NS = 5;
#define SB __builtin_amdgcn_sched_barrier(0)
DEV float xsum2(float v) {
  auto a = __builtin_amdgcn_permlane16_swap(__float_as_uint(v), __float_as_uint(v), false, false);
  v = __uint_as_float(a[0]) + __uint_as_float(a[1]);
  auto b = __builtin_amdgcn_permlane32_swap(__float_as_uint(v), __float_as_uint(v), false, false);
  return __uint_as_float(b[0]) + __uint_as_float(b[1]);
}

DEV void qkv_phase(char* smem, unsigned lds0, const char* __restrict__ x, const char* __restrict__ w, char* __restrict__ qkv,
                   int cb, int tb, const char* __restrict__ wo, const char* __restrict__ w1, const char* __restrict__ w3,
                   const char* __restrict__ w2, unsigned& tch) {
  const int tid = threadIdx.x, lane = tid & 63, wv = uni(tid >> 6);
  const int g = lane >> 4, i = lane & 15, q4 = (lane >> 2) & 3, p4 = lane & 3;
  const int tg = wv & 3, nh = wv >> 2;
  const int bi = blockIdx.x >> 3;

  const char* gx = x + (size_t)(tb * 128 + wv * 16) * 1024;
  const unsigned vt = (lane >> 3) * 1024 + (((lane & 7) ^ (lane >> 3)) << 4);
  const char* gw = w + cb * 192;
  unsigned vw0, vw1;
  {
    int qq = wv * 64 + lane;
    int k = qq / 12, c = (qq % 12) ^ (((k >> 3) & 1) << 1);
    vw0 = k * 3072 + c * 16;
    qq = 512 + wv * 32 + (lane & 31);
    k = qq / 12; c = (qq % 12) ^ (((k >> 3) & 1) << 1);
    vw1 = k * 3072 + c * 16;
  }
  const int k0 = bi & 7;
  auto dma = [&](int s, int j) {
    const int kk = (k0 + s) & 7;
    const unsigned base = lds0 + (s % K1_NS) * K1_STAGE;
    if (j == 0) dma16s(vt, gx + kk * 128, base + wv * 2048);
    else if (j == 1) dma16s(vt, gx + kk * 128 + 8192, base + wv * 2048 + 1024);
    else if (j == 2) dma16s(vw0, gw + kk * (64 * 3072), base + 16384 + wv * 1024);
    else dma16sm(vw1, gw + kk * (64 * 3072), base + 16384 + 8192 + wv * 512, 0xFFFFFFFFull);
  };
#pragma unroll
  for (int s = 0; s < UP; ++s)
#pragma unroll
    for (int j = 0; j < 4; ++j) dma(s, j);

  f32x4 acc[2][3], cs = f32x4{0, 0, 0, 0}, s1v = f32x4{0, 0, 0, 0}, gr = f32x4{0, 0, 0, 0};
#pragma unroll
  for (int a = 0; a < 3; ++a) { acc[0][a] = f32x4{0, 0, 0, 0}; acc[1][a] = f32x4{0, 0, 0, 0}; }
  const bf16x8 ones = {0x3F80, 0x3F80, 0x3F80, 0x3F80, 0x3F80, 0x3F80, 0x3F80, 0x3F80};
  int toff[2][2];
#pragma unroll
  for (int tt = 0; tt < 2; ++tt) {
    toff[tt][0] = (tg * 32 + (tt ^ nh) * 16 + i) * 128 + ((g ^ (i & 7)) << 4);
    toff[tt][1] = (tg * 32 + (tt ^ nh) * 16 + i) * 128 + (((4 + g) ^ (i & 7)) << 4);
  }
  int woff[3], un[3];
#pragma unroll
  for (int nt = 0; nt < 3; ++nt) {
    int a = nt + tg; a = a >= 3 ? a - 3 : a; a = a >= 3 ? a - 3 : a;
    un[nt] = nh * 3 + a;
    woff[nt] = 16384 + (8 * g + q4) * 192 + ((un[nt] ^ (g & 1)) << 5) + p4 * 8;
  }
  auto ldB = [&](int tt, int kt, int ks) -> bf16x8 { return lds128(smem + (kt % K1_NS) * K1_STAGE + toff[tt][ks]); };
  auto ldA = [&](int nt, int kt, int ks) -> bf16x8 {
    const char* p = smem + (kt % K1_NS) * K1_STAGE + woff[nt] + ks * 6144;
    return cat(ldstr(p), ldstr(p + 768));
  };
  bf16x8 A0[3], A1[3], B0[2], B1[2];
  auto phase = [&](bf16x8 (&Ac)[3], bf16x8 (&Bc)[2], bf16x8 (&An)[3], bf16x8 (&Bn)[2], int kn, int ksn, int dk, int dj, int nd) {
#pragma unroll
    for (int m = 0; m < 9; ++m) {
      if (kn < 8) {
        if (m == 0) { Bn[0] = ldB(0, kn, ksn); An[0] = ldA(0, kn, ksn); }
        else if (m == 1) Bn[1] = ldB(1, kn, ksn);
        else if (m < 4) An[m - 1] = ldA(m - 1, kn, ksn);
      }
      if (m < 6) acc[m & 1][m >> 1] = mfma16(Ac[m >> 1], Bc[m & 1], acc[m & 1][m >> 1]);
      else if (m == 6) cs = mfma16(Ac[0], ones, cs);
      else if (m == 7) s1v = mfma16(ones, Bc[0], s1v);
      else gr = mfma16(Bc[0], Bc[0], gr);
      if (dk < 8) {
        if (nd == 2) { if (m == 2 || m == 5) dma(dk, dj + (m == 5)); }
        else { if (m == 1 || m == 2) dma(dk, m - 1); else if (m == 4 || m == 5) dma(dk, m - 2); }
      }
      SB;
    }
  };
  wait_bar<4 * UP - 4>();
  B0[0] = ldB(0, 0, 0); A0[0] = ldA(0, 0, 0); B0[1] = ldB(1, 0, 0); A0[1] = ldA(1, 0, 0); A0[2] = ldA(2, 0, 0);
#pragma unroll
  for (int kt = 0; kt < 8; ++kt) {
    const int d1 = UP == 5 ? 8 : UP == 4 ? (kt == 0 ? 4 : 8) : (kt == 0 ? 3 : kt == 1 ? 5 : 8);
    const int d2 = UP == 3 ? (kt == 0 ? 4 : kt == 1 ? 6 : kt == 2 ? 7 : 8) : kt + 5;
    phase(A0, B0, A1, B1, kt, 1, d1, 0, 4);
    if (kt == 0) wait_bar<UP == 3 ? 8 : 12>();
    else if (kt < 4) wait_bar<12>();
    else if (kt == 4) wait_bar<8 + 1>();
    else if (kt == 5) wait_bar<4 + 1>();
    else if (kt == 6) wait_bar<0 + 1>();
    if (kt == 3) {
      const unsigned bb = blockIdx.x, xc = bb & 7, t2 = tid < 112 ? tid : tid - 112;
      const char* p = tid < 224 ? (tid < 112 ? w1 : w3) + (size_t)((((xc >> 2) ^ 1) * 256 + (bb >> 3) * 8 + t2 / 14) * 3584 + (xc & 3) * 896 + (t2 % 14) * 64)
                    : tid < 256 ? wo + (bb * 32 + tid - 224) * 64 : w2 + (bb * 112 + min(tid, 367) - 256) * 64;
      asm volatile("global_load_dword %0, %1, off" : "=v"(tch) : "v"(p) : "memory");
    }
    phase(A1, B1, A0, B0, kt + 1, 0, d2, 0, 4);
  }

  float* st = (float*)(smem + 49152);
  float* csp = (float*)(smem + 49152 + 1024);
  if (g == (i >> 2)) {
    const float s2 = (i & 2) ? ((i & 1) ? gr[3] : gr[2]) : ((i & 1) ? gr[1] : gr[0]);
    float mean = s1v[0] * (1.f / 512.f);
    float var = fmaxf(s2 * (1.f / 512.f) - mean * mean, 0.f);
    float rstd = rsqrtf(var + LN_EPS);
    *(f32x2*)(st + (tg * 32 + nh * 16 + i) * 2) = f32x2{-mean * rstd, rstd};
  }
  if (i == 0 && tg < 3) *(f32x4*)(csp + un[0] * 16 + 4 * g) = cs;
  bar();
  f32x2 ms[2];
#pragma unroll
  for (int tt = 0; tt < 2; ++tt) ms[tt] = *(const f32x2*)(st + (tg * 32 + (tt ^ nh) * 16 + i) * 2);
  const int otile = (g & 1) ^ nh, ohalf = g >> 1;
  const unsigned vo = ohalf * 512 + (((otile * 16 + i) ^ (8 * ohalf)) << 4);
#pragma unroll
  for (int nt = 0; nt < 3; ++nt) {
    f32x4 c = *(const f32x4*)(csp + un[nt] * 16 + 4 * g);
    const float qs = cb * 6 + un[nt] < 32 ? 0.25f * 1.4426950408889634f : 1.f;
    unsigned o[2][2];
#pragma unroll
    for (int tt = 0; tt < 2; ++tt) {
      f32x4 y = acc[tt][nt] * (ms[tt][1] * qs) + (ms[tt][0] * qs) * c;
      o[tt][0] = pack2(y[0], y[1]); o[tt][1] = pack2(y[2], y[3]);
    }
    auto sx = __builtin_amdgcn_permlane16_swap(o[0][0], o[1][0], false, false);
    auto sy = __builtin_amdgcn_permlane16_swap(o[0][1], o[1][1], false, false);
    char* dst = qkv + (size_t)(cb * 6 + un[nt]) * (2048 * 32) + (size_t)(tb * 4 + tg) * 1024;
    st16<true>(dst + vo, u32x4{sx[0], sy[0], sx[1], sy[1]});
  }
}

constexpr int S0 = 0, S1 = 49152, S2 = 98304, CSP = 147456, MG_LDS = 163840;
typedef unsigned long long u64;
extern "C" __device__ u64 llvm_dispatch_id() __asm("llvm.amdgcn.dispatch.id");
DEV void flag_set(u64* f, u64 tag) {
  asm volatile("global_store_dwordx2 %0, %1, off sc0 sc1" ::"v"(f), "v"(tag) : "memory");
}
DEV void flag_wait(const u64* p, u64 tag) {
  for (int it = 0; it < (1 << 20); ++it) {
    u64 c;
    asm volatile("global_load_dwordx2 %0, %1, off sc0 sc1\n\ts_waitcnt vmcnt(0)" : "=v"(c) : "v"(p) : "memory");
    if (__builtin_amdgcn_ballot_w64(c != tag) == 0) break;
  }
}
template <int V> struct IC { static constexpr int value = V; };
DEV int opaque(int v) { asm volatile("" : "+v"(v)); return v; }
DEV int opaque_s(int v) { asm volatile("" : "+s"(v)); return v; }

struct CW {
  const char *g1, *g3; unsigned vw; unsigned long long wmask;
};
DEV CW c_setup(const char* w1, const char* w3, int b, int wv, int lane) {
  const int xcd = b & 7, bi = b >> 3;
  const int cb = (xcd & 3) * 4 + (bi & 3);
  CW c;
  c.g1 = w1 + (size_t)(wv * 8) * (F_ * 2) + cb * 224;
  c.g3 = w3 + (size_t)(wv * 8) * (F_ * 2) + cb * 224;
  const int unit = ((lane >> 1) & 7) ^ ((lane >> 4) | ((wv & 1) << 2));
  c.vw = (lane >> 4) * (F_ * 2) + unit * 32 + (lane & 1) * 16;
  c.wmask = __builtin_amdgcn_ballot_w64(unit < 7);
  return c;
}
DEV void c_dmaw(const CW& c, unsigned ldsw, int kt, int j) {
  const unsigned base = ldsw + (kt % 3) * 49152;
  if (j < 2) dma16sm(c.vw, c.g1 + (size_t)(kt * 64 + j * 4) * (F_ * 2), base + 16384 + j * 1024, c.wmask);
  else dma16sm(c.vw, c.g3 + (size_t)(kt * 64 + (j - 2) * 4) * (F_ * 2), base + 32768 + (j - 2) * 1024, c.wmask);
}
DEV constexpr int d_unit(int u) {
  return u < 3 ? S2 + u * 2048 : u < 6 ? S0 + (u - 3) * 2048 : u < 9 ? S1 + (u - 6) * 2048 : u == 9 ? CSP
       : u < 13 ? S2 + (u - 10) * 2048 : S0;
}
DEV constexpr int d_ustr(int u) { return u == 9 ? 2048 : 6144; }
DEV void d_dma(const char* gw2, unsigned vw, unsigned lds0, int wv, int u, int q) {
  dma16s(vw, gw2 + (size_t)(u * 16 + q * 8) * 1024, lds0 + d_unit(u) + wv * d_ustr(u) + q * 1024);
}

template <int NT>
DEV void c_body(char* smem, unsigned lds0, const char* gt, const CW& cw, unsigned vt, int wv, int lane,
                const char* __restrict__ stats, const char* __restrict__ csb, char* __restrict__ hh, int tok0, int cb, const char* gw2, unsigned vwd) {
  const int tg = wv & 3;
  const unsigned ldsw = lds0 + wv * 2048;
  const int g = lane >> 4, i = lane & 15, q4 = (lane >> 2) & 3, p4 = lane & 3;
  f32x4 acc[2][2 * NT];
#pragma unroll
  for (int a = 0; a < 2 * NT; ++a) { acc[0][a] = f32x4{0, 0, 0, 0}; acc[1][a] = f32x4{0, 0, 0, 0}; }
  const int toff0 = (tg * 32 + i) * 128 + ((g ^ (i & 7)) << 4);
  const int toff1 = (tg * 32 + i) * 128 + (((4 + g) ^ (i & 7)) << 4);
  const int odd = cb & 1;
  const int fsw = q4 | ((g & 1) << 2);
  int T[NT], wa[NT];
#pragma unroll
  for (int a = 0; a < NT; ++a) {
    T[a] = NT == 4 ? odd * 3 + a : (a < 2 ? (odd ? 1 : 4) + a : (odd ? 0 : 6));
    wa[a] = 16384 + (8 * g + q4) * 256 + ((T[a] ^ fsw) << 5) + p4 * 8;
  }
  auto dmat = [&](int kt, int j) { dma16s(vt, gt + kt * 128 + j * 8192, ldsw + (kt % 3) * 49152 + j * 1024); };
  auto dma = [&](int kt, int j) {
    if (j < 2) dmat(kt, j); else c_dmaw(cw, ldsw, kt, j - 2);
  };
  auto ldB = [&](bf16x8* B, int kt, int ks) {
    const char* sb = smem + (kt % 3) * 49152 + (ks ? toff1 : toff0);
    B[0] = lds128(sb); B[1] = lds128(sb + 2048);
  };
  auto ldA = [&](int a, int kt, int ks) -> bf16x8 {
    const char* wp = smem + (kt % 3) * 49152 + wa[a < NT ? a : a - NT] + (a < NT ? 0 : 16384) + ks * 8192;
    return cat(ldstr(wp), ldstr(wp + 1024));
  };
  auto mma = [&](int a, bf16x8 A, const bf16x8* B) {
    acc[0][a] = mfma16(A, B[0], acc[0][a]);
    acc[1][a] = mfma16(A, B[1], acc[1][a]);
  };
  bf16x8 A0[2 * NT], A1[2 * NT], B0[2], B1[2];
  bf16x8 cp[4];
  u32x2 sa[4];
#pragma unroll
  for (int kt = 0; kt < 3; ++kt) { dmat(kt, 0); dmat(kt, 1); }
#pragma unroll
  for (int m = 0; m < 4; ++m)
    gld8(sa[m], (unsigned)((i >> 3) * 512 + g * 128 + (i & 7) * 8), stats + (size_t)(tok0 + tg * 32 + (m >> 1) * 16) * 64 + (m & 1) * 64);
  const int cq = min(tg * 16 + (lane >> 2), 55);
  if (NT == 3) {
#pragma unroll
    for (int m = 0; m < 4; ++m) gld16(cp[m], (unsigned)(((lane & 3) * 4 + m) * 896 + cq * 16), csb + (size_t)cb * 14336);
  }
  constexpr int X = NT == 3 ? 8 : 4;
  wait_bar<4 + X>();
  float rs[2], mr[2];
  float* csp = (float*)(smem + CSP);
  ldB(B0, 0, 0);
#pragma unroll
  for (int a = 0; a < 2 * NT; ++a) A0[a] = ldA(a, 0, 0);
#pragma unroll
  for (int kt = 0; kt < 8; ++kt) {
    ldB(B1, kt, 1);
#pragma unroll
    for (int a = 0; a < 2 * NT; ++a) {
      if (a < NT) { A1[2 * a] = ldA(2 * a, kt, 1); A1[2 * a + 1] = ldA(2 * a + 1, kt, 1); }
      mma(a, A0[a], B0);
      __builtin_amdgcn_sched_barrier(0);
    }
    if (kt == 0) wait_bar<2 + X>();
    else if (kt != 1) wait_bar<6>();
    else if (NT == 3) asm volatile("s_waitcnt vmcnt(6) lgkmcnt(0)\n\ts_barrier" : "+v"(sa[0]), "+v"(sa[1]), "+v"(sa[2]), "+v"(sa[3]), "+v"(cp[0]), "+v"(cp[1]), "+v"(cp[2]), "+v"(cp[3]) : : "memory");
    else asm volatile("s_waitcnt vmcnt(6) lgkmcnt(0)\n\ts_barrier" : "+v"(sa[0]), "+v"(sa[1]), "+v"(sa[2]), "+v"(sa[3]) : : "memory");
    if (kt < 7) ldB(B0, kt + 1, 0);
    if (kt == 1) {
#pragma unroll
      for (int tt = 0; tt < 2; ++tt) {
        const f32x2 sv0 = __builtin_bit_cast(f32x2, sa[2 * tt]), sv1 = __builtin_bit_cast(f32x2, sa[2 * tt + 1]);
        const float s1 = xsum2(sv0[0] + sv1[0]), s2 = xsum2(sv0[1] + sv1[1]);
        const float mean = s1 * (1.f / 512.f);
        const float var = fmaxf(s2 * (1.f / 512.f) - mean * mean, 0.f);
        rs[tt] = rsqrtf(var + LN_EPS); mr[tt] = mean * rs[tt];
      }
      if (NT == 3) {
        f32x4 c = (__builtin_bit_cast(f32x4, cp[0]) + __builtin_bit_cast(f32x4, cp[1])) +
                  (__builtin_bit_cast(f32x4, cp[2]) + __builtin_bit_cast(f32x4, cp[3]));
#pragma unroll
        for (int e = 0; e < 4; ++e) { c[e] += dpp_xor1(c[e]); c[e] += dpp_xor2(c[e]); }
        if ((lane & 3) == 0) *(f32x4*)(csp + cq * 4) = c;
      }
    }
#pragma unroll
    for (int a = 0; a < 2 * NT; ++a) {
      if (kt < 7 && a < NT) { A0[2 * a] = ldA(2 * a, kt + 1, 0); A0[2 * a + 1] = ldA(2 * a + 1, kt + 1, 0); }
      mma(a, A1[a], B1);
      if (a >= 2 * NT - 6) {
        const int j = a - (2 * NT - 6);
        if (kt + 3 < 8) dma(kt + 3, j);
        else d_dma(gw2, vwd, lds0, wv, (kt - 5) * 3 + (j >> 1), j & 1);
      }
      __builtin_amdgcn_sched_barrier(0);
    }
  }
  unsigned pk[2][NT][2];
#pragma unroll
  for (int tt = 0; tt < 2; ++tt) {
    char* dst = hh + ((size_t)((tok0 >> 4) + tg * 2 + tt) * 56) * 1024 + lane * 16;
#pragma unroll
    for (int a = 0; a < NT; ++a) {
      f32x4 cg = *(const f32x4*)(csp + T[a] * 16 + 4 * g);
      f32x4 cu = *(const f32x4*)(csp + (7 + T[a]) * 16 + 4 * g);
      float r[4];
#pragma unroll
      for (int e = 0; e < 4; ++e) {
        float ga = acc[tt][a][e] * rs[tt] - mr[tt] * cg[e];
        float up = acc[tt][NT + a][e] * rs[tt] - mr[tt] * cu[e];
        float sg = __builtin_amdgcn_rcpf(1.f + __builtin_amdgcn_exp2f(-1.4426950408889634f * ga));
        r[e] = ga * sg * up;
      }
      pk[tt][a][0] = pack2(r[0], r[1]); pk[tt][a][1] = pack2(r[2], r[3]);
      const int G = cb * 7 + T[a];
      if (a & 1) {
        st16<true>(dst + (size_t)(G >> 1) * 1024, u32x4{pk[tt][a - 1][0], pk[tt][a - 1][1], pk[tt][a][0], pk[tt][a][1]});
      } else if (a == NT - 1) {
        st8<true>(dst + (size_t)(G >> 1) * 1024 + (G & 1) * 512 - lane * 8, u32x2{pk[tt][a][0], pk[tt][a][1]});
      }
    }
  }
}

__global__ __launch_bounds__(512) void k_mega(const char* __restrict__ x, const char* __restrict__ wqkv, const char* __restrict__ wo,
                                              const char* __restrict__ w1, const char* __restrict__ w3,
                                              const char* __restrict__ w2, char* __restrict__ ws, char* __restrict__ out) {
  extern __shared__ __attribute__((aligned(16))) char smem[];
  char* qkv = ws;
  char* o = ws + (6u << 20);
  char* h = ws + (8u << 20);
  char* stats = ws + (10u << 20);
  u64* flags = (u64*)(ws + (10u << 20) + (512u << 10));
  char* csb = ws + (10u << 20) + (768u << 10);
  char* hh = ws + (11u << 20);
  const u64 tag = ((u64)(size_t)__builtin_amdgcn_dispatch_ptr() << 20) + llvm_dispatch_id();
  const int b = blockIdx.x;
  const unsigned lds0 = (unsigned)(size_t)(LAS char*)smem;
  const int cbG = (b >> 3) & 7, tbG = (b & 7) * 4 + (b >> 6);
  const int cbC = (b & 3) * 4 + ((b >> 3) & 3), tbC = ((b >> 2) & 1) * 8 + (b >> 5);

  unsigned cpf = 0;
  {
    u64 pc;
    unsigned len;
    asm volatile("s_getpc_b64 %0\n.Lmega_pc:\n\ts_mov_b32 %1, .Lmega_end-.Lmega_pc" : "=s"(pc), "=s"(len));
    const unsigned off = ((b >> 3) * 26 + threadIdx.x) * 64;
    if (threadIdx.x < 26 && off < len) asm volatile("global_load_dword %0, %1, %2" : "=v"(cpf) : "v"(off), "s"(pc) : "memory");
  }
  {
    const int cb = (b & 1) * 8 + ((b >> 3) & 7), tb = ((b & 7) >> 1) * 4 + (b >> 6);
    unsigned tch;
    qkv_phase(smem, lds0, x, wqkv, qkv, cb, tb, wo, w1, w3, w2, tch);
    asm volatile("s_waitcnt vmcnt(0) lgkmcnt(0)\n\ts_barrier" ::: "memory");
    asm volatile("" :: "v"(cpf), "v"(tch));
    const int tid = threadIdx.x;
    if (tid < 9) {
      const int P = cb * 3 + tid / 3, d = tid % 3, which = P >> 4, hp = P & 15, qb = tb + d;
      if (which ? qb < 16 : d == 0)
        flag_set(flags + 24576 + ((hp >> 1) | ((hp & 1) << 3) | (qb << 4)) * 32 + (which ? (which - 1) * 3 + d : 6), tag);
    }
  }
  {
    const int tid = opaque(threadIdx.x), lane = tid & 63, wv = uni(tid >> 6);
    const int c32 = lane & 31, hf = lane >> 5, q4 = (lane >> 2) & 3, p4 = lane & 3;
    const int hp = (b & 7) * 2 + ((b >> 3) & 1), qb = b >> 4;
    const int q0 = qb * 128;
    const size_t HS = 2048 * 32;
    bf16x8 Q, cw0, cw1;
    {
      const unsigned one = qb * 4 - 8 + wv >= 0 ? 0x3F803F80u : 0u;
      *(LAS u32x4*)(LAS char*)(smem + S1 + tid * 16) = u32x4{one, one, one, one};
      if (tid < 256) *(LAS u32x4*)(LAS char*)(smem + S1 + 8192 + tid * 16) = u32x4{0x3F803F80u, 0x3F803F80u, 0x3F803F80u, 0x3F803F80u};
    }
    const unsigned vwo = (lane >> 3) * 1024 + ((((lane & 7) >> 1) ^ ((lane >> 4) & 3)) << 5) + (lane & 1) * 16;
    const char* gwo = wo + (size_t)(wv * 64) * 1024 + cbG * 128;
    const CW cw1s = c_setup(w1, w3, b, wv, lane);
    auto pf = [&](int k) {
      if (k < 2) {
        const int ch = max(wv - 1, 0) * 4 + (lane & 3);
        const char* pc = (ch >= 14 ? w3 + (ch - 14) * 16 : w1 + ch * 16) + (size_t)(tbC * 32 + (lane >> 2)) * (F_ * 2) + cbC * 224;
        if (k == 0) asm volatile("global_load_dwordx4 %0, %1, off" : "=v"(cw0) : "v"(pc) : "memory");
        else asm volatile("global_load_dwordx4 %0, %1, off" : "=v"(cw1) : "v"(pc + 16 * (F_ * 2)) : "memory");
      } else if (k < 10) dma16s(vwo, gwo + (size_t)((k - 2) * 8) * 1024, lds0 + S2 + wv * 8192 + (k - 2) * 1024);
      else if (k < 14) c_dmaw(cw1s, lds0 + wv * 2048, 1, k - 10);
      else if (k == 14) {
        const int c0 = b * 8 + wv;
        dma16s(lane * 16, w2 + (size_t)(c0 < 1792 ? c0 : c0 - 1792) * 1024, lds0 + S1 + 12288 + (wv & 3) * 1024);
      }
    };
#pragma unroll
    for (int k = 0; k < PFE; ++k) pf(k);
    auto pfs = [&](int slot) {
#pragma unroll
      for (int k = PFE; k < NPF; ++k) if (((k - PFE) * PM) / 4 == slot) pf(k);
    };
    if (wv == 0) {
      const int d = lane % 3;
      flag_wait(flags + 24576 + b * 32 + (lane >= 6 ? 6 : qb - d < 0 ? 6 : lane), tag);
    }
    asm volatile("s_barrier" ::: "memory");
    {
      const int r = wv >> 1;
      const char* src = qkv + (size_t)((1 + (r >> 1)) * 32 + hp * 2 + (r & 1)) * HS;
#pragma unroll
      for (int j = 0; j < 6; ++j) {
        const int kb = qb * 4 - 8 + (wv & 1) * 6 + j;
        if (kb < 0 && r >= 2) *(LAS u32x4*)(LAS char*)(smem + r * 12288 + ((wv & 1) * 6 + j) * 1024 + lane * 16) = u32x4{0, 0, 0, 0};
        else dma16s(kb < 0 ? hf * 640 : lane * 16, src + (size_t)max(kb, 0) * 1024, lds0 + r * 12288 + ((wv & 1) * 6 + j) * 1024);
      }
    }
    const int u = wv >> 2, qg = wv & 3;
    const int qs = q0 + qg * 32;
    const unsigned vq = hf * 512 + ((c32 ^ (8 * hf)) << 4);
    gld16(Q, vq, qkv + (size_t)(hp * 2 + u) * HS + (size_t)qs * 32);
    const float NEG = -1e30f;
    f32x16 cin0, cin8;
    u32x4 mk[2];
#pragma unroll
    for (int r = 0; r < 16; ++r) {
      const int kl = (r & 3) + 8 * (r >> 2) + 4 * hf;
      cin0[r] = kl > c32 ? 0.f : NEG; cin8[r] = kl <= c32 ? 0.f : NEG;
      if (r & 1) mk[r >> 3][(r >> 1) & 3] = (kl > c32 ? 0xFFFF0000u : 0u) | (kl - 1 > c32 ? 0xFFFFu : 0u);
    }
    asm volatile("s_waitcnt vmcnt(0) lgkmcnt(0)\n\ts_barrier" : "+v"(Q), "+v"(cin0), "+v"(cin8), "+v"(mk[0]), "+v"(mk[1]), "+v"(cw0), "+v"(cw1) : : "memory");
    {
      const char* Kl = smem + u * 12288 + qg * 1024 + vq;
      const char* Vl[2];
#pragma unroll
      for (int e = 0; e < 2; ++e)
        Vl[e] = c32 < 16 ? smem + 24576 + u * 12288 + qg * 1024 + (p4 >> 1) * 512 + (((e * 8 + 4 * hf + q4) ^ (8 * (p4 >> 1))) << 4) + (p4 & 1) * 8
                         : smem + S1 + qg * 1024 + (hf ? 0 : 64);
      f32x16 z;
#pragma unroll
      for (int r = 0; r < 16; ++r) z[r] = 0.f;
      bf16x8 Kf[9];
#pragma unroll
      for (int t = 0; t < 9; ++t) Kf[t] = lds128(Kl + t * 1024);
      f32x16 sm = mfma32(Kf[0], Q, cin0);
      {
        const f32x16 s8 = mfma32(Kf[8], Q, cin8);
#pragma unroll
        for (int r = 0; r < 16; ++r) sm[r] = fmaxf(sm[r], s8[r]);
      }
      float m = sm[0];
#pragma unroll
      for (int r = 1; r < 16; ++r) m = fmaxf(m, sm[r]);
#pragma unroll
      for (int t = 1; t < 8; ++t) {
        const f32x16 s = mfma32(Kf[t], Q, z);
#pragma unroll
        for (int r = 0; r < 16; ++r) m = fmaxf(m, s[r]);
      }
      {
        auto sw = __builtin_amdgcn_permlane32_swap(__float_as_uint(m), __float_as_uint(m), false, false);
        m = fmaxf(__uint_as_float(sw[0]), __uint_as_float(sw[1]));
      }
      f32x16 cm, oacc;
#pragma unroll
      for (int r = 0; r < 16; ++r) { cm[r] = -m; oacc[r] = 0.f; }
      f32x16 sn = mfma32(Kf[1], Q, cm);
      {
        u32x4 pb[2];
#pragma unroll
        for (int j = 0; j < 8; ++j)
          pb[j >> 2][j & 3] = pack2(__builtin_amdgcn_exp2f(sm[2 * j] - m), __builtin_amdgcn_exp2f(sm[2 * j + 1] - m));
#pragma unroll
        for (int sh = 0; sh < 2; ++sh) {
          const u32x4 p0 = pb[sh] & mk[sh], p8 = pb[sh] ^ p0;
          oacc = mfma32(cat(ldstr(Vl[0] + sh * 256), ldstr(Vl[1] + sh * 256)), __builtin_bit_cast(bf16x8, p0), oacc);
          pfs(2 * sh);
          oacc = mfma32(cat(ldstr(Vl[0] + 8192 + sh * 256), ldstr(Vl[1] + 8192 + sh * 256)), __builtin_bit_cast(bf16x8, p8), oacc);
          pfs(2 * sh + 1);
        }
      }
      SB;
#pragma unroll
      for (int t = 1; t < 8; ++t) {
        const f32x16 s = sn;
        if (t < 7) sn = mfma32(Kf[t + 1], Q, cm);
#pragma unroll
        for (int sh = 0; sh < 2; ++sh) {
          u32x4 pb;
#pragma unroll
          for (int e = 0; e < 4; ++e)
            pb[e] = pack2(__builtin_amdgcn_exp2f(s[8 * sh + 2 * e]), __builtin_amdgcn_exp2f(s[8 * sh + 2 * e + 1]));
          const int vp = t * 1024 + sh * 256;
          oacc = mfma32(cat(ldstr(Vl[0] + vp), ldstr(Vl[1] + vp)), __builtin_bit_cast(bf16x8, pb), oacc);
          pfs(2 * t + sh + 2);
        }
        SB;
      }
      const float inv = __builtin_amdgcn_rcpf(oacc[8]);
      const u32x4 ov = {pack2(oacc[0] * inv, oacc[1] * inv), pack2(oacc[2] * inv, oacc[3] * inv),
                        pack2(oacc[4] * inv, oacc[5] * inv), pack2(oacc[6] * inv, oacc[7] * inv)};
      st16<true>(o + ((size_t)((qs >> 4) + (c32 >> 4)) * 16 + hp) * 1024 + ((2 * u + hf) * 16 + (c32 & 15)) * 16, ov);
    }
    asm volatile("s_waitcnt vmcnt(0) lgkmcnt(0)\n\ts_barrier" : "+v"(cw0), "+v"(cw1) : : "memory");
    if (tid < 16) flag_set(flags + (((qb >> 1) | (tid << 3) | ((qb & 1) << 7)) * 32 + hp), tag);
    {
      const CW cw = c_setup(w1, w3, b, wv, lane);
#pragma unroll
      for (int j = 0; j < 4; ++j) c_dmaw(cw, lds0 + wv * 2048, 0, j);
    }
    if (wv == 0) { flag_wait(flags + b * 32 + (lane & 15), tag); }
    else {
      const u32x4 ua = __builtin_bit_cast(u32x4, cw0), ub = __builtin_bit_cast(u32x4, cw1);
      f32x4 lo, hi;
#pragma unroll
      for (int e = 0; e < 2; ++e) {
        lo[2 * e] = bflo(ua[e]) + bflo(ub[e]); lo[2 * e + 1] = bfhi(ua[e]) + bfhi(ub[e]);
        hi[2 * e] = bflo(ua[2 + e]) + bflo(ub[2 + e]); hi[2 * e + 1] = bfhi(ua[2 + e]) + bfhi(ub[2 + e]);
      }
#pragma unroll
      for (int e = 0; e < 4; ++e) {
        lo[e] += dpp_ror4(lo[e]); lo[e] += dpp_ror8(lo[e]); lo[e] = xsum2(lo[e]);
        hi[e] += dpp_ror4(hi[e]); hi[e] += dpp_ror8(hi[e]); hi[e] = xsum2(hi[e]);
      }
      if (lane < 8) {
        char* d = csb + (size_t)(cbC * 16 + tbC) * 896 + ((wv - 1) * 4 + (lane & 3)) * 32 + (lane >> 2) * 16;
        st16<true>(d, __builtin_bit_cast(u32x4, lane < 4 ? lo : hi));
      }
      asm volatile("s_waitcnt vmcnt(0)" ::: "memory");
    }
    asm volatile("s_barrier" ::: "memory");
    if (tid >= 64 && tid < 80) flag_set(flags + 8192 + ((cbC >> 2) | ((tid & 1) << 2) | ((cbC & 3) << 3) | (((tid & 15) >> 1) << 5)) * 32 + 16 + tbC, tag);
  }
  {
    const int tid = opaque(threadIdx.x), lane = tid & 63, wv = uni(tid >> 6);
    const int g = lane >> 4, i = lane & 15, q4 = (lane >> 2) & 3, p4 = lane & 3;
    const int kp = wv & 3, th = wv >> 2;
    bf16x8 B[4][2], rs;
    const char* ga = o + ((size_t)(tbG * 4 + th * 2) * 16 + kp * 4) * 1024;
#pragma unroll
    for (int s = 0; s < 4; ++s) {
      gld16(B[s][0], lane * 16, ga + s * 1024);
      gld16(B[s][1], lane * 16, ga + (16 + s) * 1024);
    }
    const size_t ooff = (size_t)(tbG * 64 + wv * 8) * 1024 + cbG * 128;
    const unsigned vo = (lane >> 3) * 1024 + (lane & 7) * 16;
    gld16(rs, vo, x + ooff);
    const int sw = (2 * (g & 1) + (q4 >> 1)) & 3;
    const char* wr = smem + S2 + kp * 16384 + (16 * (g >> 1) + 4 * (g & 1) + q4) * 128 + p4 * 8;
    bf16x8 A[4][4];
#pragma unroll
    for (int s = 0; s < 4; ++s)
#pragma unroll
      for (int nt = 0; nt < 4; ++nt) {
        const char* p = wr + s * 4096 + ((nt ^ sw) << 5);
        A[s][nt] = cat(ldstr(p), ldstr(p + 1024));
      }
    f32x4 acc[2][4];
#pragma unroll
    for (int t = 0; t < 2; ++t)
#pragma unroll
      for (int n = 0; n < 4; ++n) acc[t][n] = f32x4{0, 0, 0, 0};
    asm volatile("s_waitcnt vmcnt(7)" : "+v"(B[0][0]), "+v"(B[0][1]) : : "memory");
#pragma unroll
    for (int nt = 0; nt < 4; ++nt) { acc[0][nt] = mfma16(A[0][nt], B[0][0], acc[0][nt]); acc[1][nt] = mfma16(A[0][nt], B[0][1], acc[1][nt]); }
    asm volatile("s_waitcnt vmcnt(5)" : "+v"(B[1][0]), "+v"(B[1][1]) : : "memory");
#pragma unroll
    for (int nt = 0; nt < 4; ++nt) { acc[0][nt] = mfma16(A[1][nt], B[1][0], acc[0][nt]); acc[1][nt] = mfma16(A[1][nt], B[1][1], acc[1][nt]); }
    asm volatile("s_waitcnt vmcnt(3)" : "+v"(B[2][0]), "+v"(B[2][1]) : : "memory");
#pragma unroll
    for (int nt = 0; nt < 4; ++nt) { acc[0][nt] = mfma16(A[2][nt], B[2][0], acc[0][nt]); acc[1][nt] = mfma16(A[2][nt], B[2][1], acc[1][nt]); }
    asm volatile("s_waitcnt vmcnt(1)" : "+v"(B[3][0]), "+v"(B[3][1]) : : "memory");
#pragma unroll
    for (int nt = 0; nt < 4; ++nt) { acc[0][nt] = mfma16(A[3][nt], B[3][0], acc[0][nt]); acc[1][nt] = mfma16(A[3][nt], B[3][1], acc[1][nt]); }
    bar();
    {
      const int mo = S2 + kp * 16384 + g * 256 + ((i ^ ((g >> 1) * 2)) << 4);
#pragma unroll
      for (int tt = 0; tt < 2; ++tt)
#pragma unroll
        for (int nt = 0; nt < 4; ++nt)
          *(LAS f32x4*)(LAS char*)(smem + (mo ^ (nt * 64)) + ((th * 2 + tt) * 4 + nt) * 1024) = acc[tt][nt];
    }
    asm volatile("s_waitcnt vmcnt(0) lgkmcnt(0)\n\ts_barrier" : "+v"(rs) : : "memory");
    {
      const int c = lane & 7;
      const int tok = (lane >> 3) + 8 * (wv & 1);
      const char* rp = smem + S2 + ((wv >> 1) * 4 + (c >> 1)) * 1024 + (c & 1) * 512 + ((tok ^ (2 * c)) << 4);
      f32x4 v0 = f32x4{0, 0, 0, 0}, v1 = f32x4{0, 0, 0, 0};
#pragma unroll
      for (int p = 0; p < 4; ++p) {
        v0 += *(const LAS f32x4*)(LAS const char*)(rp + p * 16384);
        v1 += *(const LAS f32x4*)(LAS const char*)(rp + p * 16384 + 256);
      }
      u32x4 r = __builtin_bit_cast(u32x4, rs);
      u32x4 ov = {pack2(v0[0] + bflo(r[0]), v0[1] + bfhi(r[0])), pack2(v0[2] + bflo(r[1]), v0[3] + bfhi(r[1])),
                  pack2(v1[0] + bflo(r[2]), v1[1] + bfhi(r[2])), pack2(v1[2] + bflo(r[3]), v1[3] + bfhi(r[3]))};
      st16<true>(h + ooff + vo, ov);
      float s1 = 0.f, s2 = 0.f;
#pragma unroll
      for (int e = 0; e < 4; ++e) { s1 = dot2(ov[e], 0x3F803F80u, s1); s2 = dot2(ov[e], ov[e], s2); }
#pragma unroll
      for (int m = 1; m < 8; m <<= 1) { s1 += __shfl_xor(s1, m); s2 += __shfl_xor(s2, m); }
      if (c == 0) st8<true>(stats + ((size_t)(tbG * 8 + wv) * 8 + cbG) * 64 + (lane >> 3) * 8,
                            u32x2{__float_as_uint(s1), __float_as_uint(s2)});
    }
    asm volatile("s_waitcnt vmcnt(0) lgkmcnt(0)\n\ts_barrier" ::: "memory");
    if (tid < 16) flag_set(flags + 8192 + ((tid & 3) | ((tbG >> 4) << 2) | ((tid >> 2) << 3) | (((tbG >> 1) & 7) << 5)) * 32 + (tbG & 1) * 8 + cbG, tag);
    {
      const CW cw = c_setup(w1, w3, b, wv, lane);
#pragma unroll
      for (int j = 0; j < 4; ++j) c_dmaw(cw, lds0 + wv * 2048, 2, j);
    }
    if (wv == 0) flag_wait(flags + 8192 + b * 32 + (lane & 31), tag);
    asm volatile("s_barrier" ::: "memory");
  }
  const unsigned vwd_ = 0;
  {
    const int tid = opaque(threadIdx.x), lane = tid & 63, wv = uni(tid >> 6);
    const char* gt = h + (size_t)(tbC * 128 + wv * 16) * 1024;
    const unsigned vt = (lane >> 3) * 1024 + (((lane & 7) ^ (lane >> 3)) << 4);
    const CW cw = c_setup(w1, w3, b, wv, lane);
    const unsigned vwd = (lane >> 3) * 1024 + ((((lane & 7) >> 1) ^ ((lane >> 4) & 3)) << 5) + (lane & 1) * 16;
    const char* gw2 = w2 + (size_t)(wv * 224) * 1024 + cbG * 128;
    if ((wv >> 2) == 0) c_body<4>(smem, lds0, gt, cw, vt, wv, lane, stats, csb, hh, tbC * 128, cbC, gw2, vwd);
    else c_body<3>(smem, lds0, gt, cw, vt, wv, lane, stats, csb, hh, tbC * 128, cbC, gw2, vwd);
  }
  {
    const int tid = opaque(threadIdx.x), lane = tid & 63, wv = uni(tid >> 6);
    const int g = lane >> 4, i = lane & 15, q4 = (lane >> 2) & 3, p4 = lane & 3;
    asm volatile("s_waitcnt vmcnt(0) lgkmcnt(0)\n\ts_barrier" ::: "memory");
    if (tid < 16) flag_set(flags + 16384 + ((tbC >> 1) | (tid << 3) | ((tbC & 1) << 7)) * 32 + cbC, tag);
    const unsigned vwd = (lane >> 3) * 1024 + ((((lane & 7) >> 1) ^ ((lane >> 4) & 3)) << 5) + (lane & 1) * 16;
    const char* gw2 = w2 + (size_t)(wv * 224) * 1024 + cbG * 128;
    d_dma(gw2, vwd, lds0, wv, 9, 0); d_dma(gw2, vwd, lds0, wv, 9, 1);
    const int sw = (2 * g + (q4 >> 1)) & 3;
    const int ro = (4 * g + q4) * 128 + p4 * 8;
    int rn[4];
#pragma unroll
    for (int nt = 0; nt < 4; ++nt) rn[nt] = ro + ((nt ^ sw) << 5);
    auto lda = [&](bf16x8* Af, int j) {
      const char* p0 = smem + d_unit(2 * j) + wv * d_ustr(2 * j);
      const char* p1 = smem + d_unit(2 * j + 1) + wv * d_ustr(2 * j + 1);
#pragma unroll
      for (int nt = 0; nt < 4; ++nt) Af[nt] = cat(ldstr(p0 + rn[nt]), ldstr(p1 + rn[nt]));
    };
    bf16x8 A[2][4], B[7][4], rs;
    lda(A[0], 0); lda(A[1], 1);
    asm volatile("s_waitcnt lgkmcnt(0)" : "+v"(A[0][0]), "+v"(A[0][1]), "+v"(A[0][2]), "+v"(A[0][3]), "+v"(A[1][0]), "+v"(A[1][1]), "+v"(A[1][2]), "+v"(A[1][3]) : : "memory");
#pragma unroll
    for (int u = 10; u < 14; ++u) { d_dma(gw2, vwd, lds0, wv, u, 0); d_dma(gw2, vwd, lds0, wv, u, 1); }
    if (wv == 0) flag_wait(flags + 16384 + b * 32 + (lane & 15), tag);
    asm volatile("s_barrier" ::: "memory");
    const char* ga = hh + ((size_t)(tbG * 4) * 56 + wv * 7) * 1024;
    u32x2 Bl[4], Bh[4];
#pragma unroll
    for (int j = 0; j < 7; ++j)
#pragma unroll
      for (int tt = 0; tt < 4; ++tt) {
        if (j == 3) {
          gld8(Bl[tt], lane * 8, ga + (size_t)(tt * 56 + j) * 1024);
          gld8(Bh[tt], lane * 8, ga + (size_t)(tt * 56 + j) * 1024 + 512);
        } else gld16(B[j][tt], lane * 16, ga + (size_t)(tt * 56 + j) * 1024);
      }
    const size_t ooff = (size_t)(tbG * 64 + wv * 8) * 1024 + cbG * 128;
    const unsigned vo = (lane >> 3) * 1024 + (lane & 7) * 16;
    gld16(rs, vo, h + ooff);
    f32x4 acc[4][4];
#pragma unroll
    for (int t = 0; t < 4; ++t)
#pragma unroll
      for (int n = 0; n < 4; ++n) acc[t][n] = f32x4{0, 0, 0, 0};
    auto step = [&](auto J) {
      constexpr int j = decltype(J)::value;
      if constexpr (j == 3) {
        asm volatile("s_waitcnt vmcnt(13)" : "+v"(Bl[0]), "+v"(Bl[1]), "+v"(Bl[2]), "+v"(Bl[3]), "+v"(Bh[0]), "+v"(Bh[1]), "+v"(Bh[2]), "+v"(Bh[3]) : : "memory");
#pragma unroll
        for (int tt = 0; tt < 4; ++tt) B[3][tt] = __builtin_bit_cast(bf16x8, u32x4{Bl[tt][0], Bl[tt][1], Bh[tt][0], Bh[tt][1]});
      } else
        asm volatile("s_waitcnt vmcnt(%4)" : "+v"(B[j][0]), "+v"(B[j][1]), "+v"(B[j][2]), "+v"(B[j][3]) : "n"((j < 3 ? 29 : 25) - 4 * j) : "memory");
#pragma unroll
      for (int nt = 0; nt < 4; ++nt)
#pragma unroll
        for (int tt = 0; tt < 4; ++tt) acc[tt][nt] = mfma16(A[j & 1][nt], B[j][tt], acc[tt][nt]);
      __builtin_amdgcn_sched_barrier(0);
      if constexpr (j + 2 < 7) lda(A[j & 1], j + 2);
    };
    step(IC<0>{}); step(IC<1>{}); step(IC<2>{}); step(IC<3>{}); step(IC<4>{}); step(IC<5>{}); step(IC<6>{});
    {
      const int mo = wv * 6144 + g * 256 + ((i ^ ((g >> 1) * 2)) << 4);
#pragma unroll
      for (int tt = 0; tt < 4; ++tt)
#pragma unroll
        for (int nt = 0; nt < 4; ++nt) {
          const int t = tt * 4 + nt;
          const int base = t < 6 ? S2 + t * 1024 : t < 12 ? S0 + (t - 6) * 1024 : S1 + (t - 12) * 1024;
          *(LAS f32x4*)(LAS char*)(smem + (mo ^ (nt * 64)) + base) = acc[tt][nt];
        }
    }
    asm volatile("s_waitcnt vmcnt(0) lgkmcnt(0)\n\ts_barrier" : "+v"(rs) : : "memory");
    {
      const int c = lane & 7;
      const int tok = (lane >> 3) + 8 * (wv & 1);
      const int t = (wv >> 1) * 4 + (c >> 1);
      const int base = t < 6 ? S2 + t * 1024 : t < 12 ? S0 + (t - 6) * 1024 : S1 + (t - 12) * 1024;
      const char* rp = smem + base + (c & 1) * 512 + ((tok ^ (2 * c)) << 4);
      f32x4 v0 = f32x4{0, 0, 0, 0}, v1 = f32x4{0, 0, 0, 0};
#pragma unroll
      for (int p = 0; p < 8; ++p) {
        v0 += *(const LAS f32x4*)(LAS const char*)(rp + p * 6144);
        v1 += *(const LAS f32x4*)(LAS const char*)(rp + p * 6144 + 256);
      }
      u32x4 r = __builtin_bit_cast(u32x4, rs);
      u32x4 ov = {pack2(v0[0] + bflo(r[0]), v0[1] + bfhi(r[0])), pack2(v0[2] + bflo(r[1]), v0[3] + bfhi(r[1])),
                  pack2(v1[0] + bflo(r[2]), v1[1] + bfhi(r[2])), pack2(v1[2] + bflo(r[3]), v1[3] + bfhi(r[3]))};
      st16<true>(out + ooff + vo, ov);
    }
  }
  asm volatile(".Lmega_end:");
}

extern "C" void kernel_launch(void* const* d_in, const int* in_sizes, int n_in, void* d_out, int out_size,
                              void* d_ws, size_t ws_size, hipStream_t stream) {
  const char* x = (const char*)d_in[0];
  const char* wqkv = (const char*)d_in[1];
  const char* wo = (const char*)d_in[2];
  const char* w1 = (const char*)d_in[4];
  const char* w3 = (const char*)d_in[5];
  const char* w2 = (const char*)d_in[6];
  char* ws = (char*)d_ws;
  (void)hipFuncSetAttribute((const void*)k_mega, hipFuncAttributeMaxDynamicSharedMemorySize, MG_LDS);
  k_mega<<<256, 512, MG_LDS, stream>>>(x, wqkv, wo, w1, w3, w2, ws, (char*)d_out);
}
```

```cpp
#include <hip/hip_runtime.h>
#include <hip/hip_bf16.h>
#include <stdint.h>

typedef short bf16x8 __attribute__((ext_vector_type(8)));
typedef short s16x4 __attribute__((ext_vector_type(4)));
typedef float f32x4 __attribute__((ext_vector_type(4)));
typedef float f32x2 __attribute__((ext_vector_type(2)));
typedef unsigned u32x4 __attribute__((ext_vector_type(4)));
typedef unsigned u32x2 __attribute__((ext_vector_type(2)));
typedef __bf16 bf16x2 __attribute__((ext_vector_type(2)));
#define LAS __attribute__((address_space(3)))
#define DEV __device__ __forceinline__

constexpr int S_ = 2048, D_ = 512, H_ = 32, F_ = 1792;
constexpr float LN_EPS = 1e-5f;

typedef float f32x16 __attribute__((ext_vector_type(16)));
DEV f32x16 mfma32(bf16x8 a, bf16x8 b, f32x16 c) { return __builtin_amdgcn_mfma_f32_32x32x16_bf16(a, b, c, 0, 0, 0); }
DEV f32x4 mfma16(bf16x8 a, bf16x8 b, f32x4 c) { return __builtin_amdgcn_mfma_f32_16x16x32_bf16(a, b, c, 0, 0, 0); }
DEV unsigned pack2(float a, float b) {
  f32x2 v = {a, b};
  return __builtin_bit_cast(unsigned, __builtin_convertvector(v, bf16x2));
}
DEV float bflo(unsigned u) { return __builtin_bit_cast(float, u << 16); }
DEV float bfhi(unsigned u) { return __builtin_bit_cast(float, u & 0xffff0000u); }
DEV float dot2(unsigned a, unsigned b, float c) {
  return __builtin_amdgcn_fdot2_f32_bf16(__builtin_bit_cast(bf16x2, a), __builtin_bit_cast(bf16x2, b), c, false);
}
DEV void dma16(const char* gptr, unsigned ldsbase) {
  unsigned sv;
  asm volatile("s_mov_b32 %0, m0\n\ts_mov_b32 m0, %2\n\ts_nop 0\n\tglobal_load_lds_dwordx4 %1, off\n\ts_mov_b32 m0, %0"
               : "=&s"(sv) : "v"(gptr), "s"(ldsbase) : "memory");
}
DEV void dma16s(unsigned voff, const char* sbase, unsigned ldsbase) {
  unsigned sv;
  asm volatile("s_mov_b32 %0, m0\n\ts_mov_b32 m0, %3\n\ts_nop 0\n\tglobal_load_lds_dwordx4 %1, %2\n\ts_mov_b32 m0, %0"
               : "=&s"(sv) : "v"(voff), "s"(sbase), "s"(ldsbase) : "memory");
}
DEV void dma16sm(unsigned voff, const char* sbase, unsigned ldsbase, unsigned long long mask) {
  unsigned sv;
  asm volatile("s_mov_b32 %0, m0\n\ts_mov_b32 m0, %3\n\ts_mov_b64 exec, %4\n\tglobal_load_lds_dwordx4 %1, %2\n\ts_mov_b64 exec, -1\n\ts_mov_b32 m0, %0"
               : "=&s"(sv) : "v"(voff), "s"(sbase), "s"(ldsbase), "s"(mask) : "memory");
}
DEV void gld16(bf16x8& d, unsigned voff, const char* sbase) {
  asm volatile("global_load_dwordx4 %0, %1, %2" : "=v"(d) : "v"(voff), "s"(sbase) : "memory");
}
DEV void gld8(u32x2& d, unsigned voff, const char* sbase) {
  asm volatile("global_load_dwordx2 %0, %1, %2" : "=v"(d) : "v"(voff), "s"(sbase) : "memory");
}
template <int N> DEV void wait_bar() { asm volatile("s_waitcnt vmcnt(%0) lgkmcnt(0)\n\ts_barrier" ::"n"(N) : "memory"); }
DEV void bar() { asm volatile("s_waitcnt lgkmcnt(0)\n\ts_barrier" ::: "memory"); }
DEV bf16x8 lds128(const char* p) { return *(const LAS bf16x8*)(LAS const char*)p; }
DEV s16x4 ldstr(const char* p) { return __builtin_amdgcn_ds_read_tr16_b64_v4i16((LAS s16x4*)p); }
DEV bf16x8 cat(s16x4 a, s16x4 b) { return __builtin_shufflevector(a, b, 0, 1, 2, 3, 4, 5, 6, 7); }
DEV float xsum(float v) { v += __shfl_xor(v, 16); v += __shfl_xor(v, 32); return v; }
DEV float xmax(float v) { v = fmaxf(v, __shfl_xor(v, 16)); v = fmaxf(v, __shfl_xor(v, 32)); return v; }
DEV float dpp_xor1(float v) { return __builtin_bit_cast(float, __builtin_amdgcn_mov_dpp(__builtin_bit_cast(int, v), 0xB1, 0xF, 0xF, true)); }
DEV float dpp_xor2(float v) { return __builtin_bit_cast(float, __builtin_amdgcn_mov_dpp(__builtin_bit_cast(int, v), 0x4E, 0xF, 0xF, true)); }
DEV float dpp_ror4(float v) { return __builtin_bit_cast(float, __builtin_amdgcn_mov_dpp(__builtin_bit_cast(int, v), 0x124, 0xF, 0xF, true)); }
DEV float dpp_ror8(float v) { return __builtin_bit_cast(float, __builtin_amdgcn_mov_dpp(__builtin_bit_cast(int, v), 0x128, 0xF, 0xF, true)); }
DEV unsigned uni(unsigned v) { return __builtin_amdgcn_readfirstlane(v); }
#define UP 3
#define NPF 14
#define PFE 2
#define PM 4
#define WT 31
template <bool W> DEV void st16(char* p, u32x4 v) {
  if constexpr (W) asm volatile("global_store_dwordx4 %0, %1, off sc0 sc1\n\ts_nop 1" ::"v"(p), "v"(v) : "memory");
  else *(u32x4*)p = v;
}
template <bool W> DEV void st8(char* p, u32x2 v) {
  if constexpr (W) asm volatile("global_store_dwordx2 %0, %1, off sc0 sc1" ::"v"(p), "v"(v) : "memory");
  else *(u32x2*)p = v;
}

template <int A, int B> struct Min { static constexpr int v = A < B ? A : B; };

template <int KT, int NK, int NS, int P, class Issue, class Compute>
DEV void pipe_steps(Issue& issue, Compute& compute) {
  if constexpr (KT < NK) {
    wait_bar<(Min<KT + NS - 1, NK>::v - KT - 1) * P>();
    if constexpr (KT + NS - 1 < NK) issue((KT + NS - 1) % NS);
    compute(KT % NS, KT);
    pipe_steps<KT + 1, NK, NS, P>(issue, compute);
  }
}

constexpr int K1_STAGE = 16384 + 12288, K1_NS = 5;
#define SB __builtin_amdgcn_sched_barrier(0)
DEV float xsum2(float v) {
  auto a = __builtin_amdgcn_permlane16_swap(__float_as_uint(v), __float_as_uint(v), false, false);
  v = __uint_as_float(a[0]) + __uint_as_float(a[1]);
  auto b = __builtin_amdgcn_permlane32_swap(__float_as_uint(v), __float_as_uint(v), false, false);
  return __uint_as_float(b[0]) + __uint_as_float(b[1]);
}

DEV void qkv_phase(char* smem, unsigned lds0, const char* __restrict__ x, const char* __restrict__ w, char* __restrict__ qkv,
                   int cb, int tb, const char* __restrict__ wo, const char* __restrict__ w1, const char* __restrict__ w3,
                   const char* __restrict__ w2, unsigned& tch) {
  const int tid = threadIdx.x, lane = tid & 63, wv = uni(tid >> 6);
  const int g = lane >> 4, i = lane & 15, q4 = (lane >> 2) & 3, p4 = lane & 3;
  const int tg = wv & 3, nh = wv >> 2;
  const int bi = blockIdx.x >> 3;

  const char* gx = x + (size_t)(tb * 128 + wv * 16) * 1024;
  const unsigned vt = (lane >> 3) * 1024 + (((lane & 7) ^ (lane >> 3)) << 4);
  const char* gw = w + cb * 192;
  unsigned vw0, vw1;
  {
    int qq = wv * 64 + lane;
    int k = qq / 12, c = (qq % 12) ^ (((k >> 3) & 1) << 1);
    vw0 = k * 3072 + c * 16;
    qq = 512 + wv * 32 + (lane & 31);
    k = qq / 12; c = (qq % 12) ^ (((k >> 3) & 1) << 1);
    vw1 = k * 3072 + c * 16;
  }
  const int k0 = bi & 7;
  auto dma = [&](int s, int j) {
    const int kk = (k0 + s) & 7;
    const unsigned base = lds0 + (s % K1_NS) * K1_STAGE;
    if (j == 0) dma16s(vt, gx + kk * 128, base + wv * 2048);
    else if (j == 1) dma16s(vt, gx + kk * 128 + 8192, base + wv * 2048 + 1024);
    else if (j == 2) dma16s(vw0, gw + kk * (64 * 3072), base + 16384 + wv * 1024);
    else dma16sm(vw1, gw + kk * (64 * 3072), base + 16384 + 8192 + wv * 512, 0xFFFFFFFFull);
  };
#pragma unroll
  for (int s = 0; s < UP; ++s)
#pragma unroll
    for (int j = 0; j < 4; ++j) dma(s, j);

  f32x4 acc[2][3], cs = f32x4{0, 0, 0, 0}, s1v = f32x4{0, 0, 0, 0}, gr = f32x4{0, 0, 0, 0};
#pragma unroll
  for (int a = 0; a < 3; ++a) { acc[0][a] = f32x4{0, 0, 0, 0}; acc[1][a] = f32x4{0, 0, 0, 0}; }
  const bf16x8 ones = {0x3F80, 0x3F80, 0x3F80, 0x3F80, 0x3F80, 0x3F80, 0x3F80, 0x3F80};
  int toff[2][2];
#pragma unroll
  for (int tt = 0; tt < 2; ++tt) {
    toff[tt][0] = (tg * 32 + (tt ^ nh) * 16 + i) * 128 + ((g ^ (i & 7)) << 4);
    toff[tt][1] = (tg * 32 + (tt ^ nh) * 16 + i) * 128 + (((4 + g) ^ (i & 7)) << 4);
  }
  int woff[3], un[3];
#pragma unroll
  for (int nt = 0; nt < 3; ++nt) {
    int a = nt + tg; a = a >= 3 ? a - 3 : a; a = a >= 3 ? a - 3 : a;
    un[nt] = nh * 3 + a;
    woff[nt] = 16384 + (8 * g + q4) * 192 + ((un[nt] ^ (g & 1)) << 5) + p4 * 8;
  }
  auto ldB = [&](int tt, int kt, int ks) -> bf16x8 { return lds128(smem + (kt % K1_NS) * K1_STAGE + toff[tt][ks]); };
  auto ldA = [&](int nt, int kt, int ks) -> bf16x8 {
    const char* p = smem + (kt % K1_NS) * K1_STAGE + woff[nt] + ks * 6144;
    return cat(ldstr(p), ldstr(p + 768));
  };
  bf16x8 A0[3], A1[3], B0[2], B1[2];
  auto phase = [&](bf16x8 (&Ac)[3], bf16x8 (&Bc)[2], bf16x8 (&An)[3], bf16x8 (&Bn)[2], int kn, int ksn, int dk, int dj, int nd) {
#pragma unroll
    for (int m = 0; m < 9; ++m) {
      if (kn < 8) {
        if (m == 0) { Bn[0] = ldB(0, kn, ksn); An[0] = ldA(0, kn, ksn); }
        else if (m == 1) Bn[1] = ldB(1, kn, ksn);
        else if (m < 4) An[m - 1] = ldA(m - 1, kn, ksn);
      }
      if (m < 6) acc[m & 1][m >> 1] = mfma16(Ac[m >> 1], Bc[m & 1], acc[m & 1][m >> 1]);
      else if (m == 6) cs = mfma16(Ac[0], ones, cs);
      else if (m == 7) s1v = mfma16(ones, Bc[0], s1v);
      else gr = mfma16(Bc[0], Bc[0], gr);
      if (dk < 8) {
        if (nd == 2) { if (m == 2 || m == 5) dma(dk, dj + (m == 5)); }
        else { if (m == 1 || m == 2) dma(dk, m - 1); else if (m == 4 || m == 5) dma(dk, m - 2); }
      }
      SB;
    }
  };
  wait_bar<4 * UP - 4>();
  B0[0] = ldB(0, 0, 0); A0[0] = ldA(0, 0, 0); B0[1] = ldB(1, 0, 0); A0[1] = ldA(1, 0, 0); A0[2] = ldA(2, 0, 0);
#pragma unroll
  for (int kt = 0; kt < 8; ++kt) {
    const int d1 = UP == 5 ? 8 : UP == 4 ? (kt == 0 ? 4 : 8) : (kt == 0 ? 3 : kt == 1 ? 5 : 8);
    const int d2 = UP == 3 ? (kt == 0 ? 4 : kt == 1 ? 6 : kt == 2 ? 7 : 8) : kt + 5;
    phase(A0, B0, A1, B1, kt, 1, d1, 0, 4);
    if (kt == 0) wait_bar<UP == 3 ? 8 : 12>();
    else if (kt < 4) wait_bar<12>();
    else if (kt == 4) wait_bar<8 + 1>();
    else if (kt == 5) wait_bar<4 + 1>();
    else if (kt == 6) wait_bar<0 + 1>();
    if (kt == 3) {
      const unsigned bb = blockIdx.x, xc = bb & 7, t2 = tid < 112 ? tid : tid - 112;
      const char* p = tid < 224 ? (tid < 112 ? w1 : w3) + (size_t)((((xc >> 2) ^ 1) * 256 + (bb >> 3) * 8 + t2 / 14) * 3584 + (xc & 3) * 896 + (t2 % 14) * 64)
                    : tid < 256 ? wo + (bb * 32 + tid - 224) * 64 : w2 + (bb * 112 + min(tid, 367) - 256) * 64;
      asm volatile("global_load_dword %0, %1, off" : "=v"(tch) : "v"(p) : "memory");
    }
    phase(A1, B1, A0, B0, kt + 1, 0, d2, 0, 4);
  }

  float* st = (float*)(smem + 49152);
  float* csp = (float*)(smem + 49152 + 1024);
  if (g == (i >> 2)) {
    const float s2 = (i & 2) ? ((i & 1) ? gr[3] : gr[2]) : ((i & 1) ? gr[1] : gr[0]);
    float mean = s1v[0] * (1.f / 512.f);
    float var = fmaxf(s2 * (1.f / 512.f) - mean * mean, 0.f);
    float rstd = rsqrtf(var + LN_EPS);
    *(f32x2*)(st + (tg * 32 + nh * 16 + i) * 2) = f32x2{-mean * rstd, rstd};
  }
  if (i == 0 && tg < 3) *(f32x4*)(csp + un[0] * 16 + 4 * g) = cs;
  bar();
  f32x2 ms[2];
#pragma unroll
  for (int tt = 0; tt < 2; ++tt) ms[tt] = *(const f32x2*)(st + (tg * 32 + (tt ^ nh) * 16 + i) * 2);
  const int otile = (g & 1) ^ nh, ohalf = g >> 1;
  const unsigned vo = ohalf * 512 + (((otile * 16 + i) ^ (8 * ohalf)) << 4);
#pragma unroll
  for (int nt = 0; nt < 3; ++nt) {
    f32x4 c = *(const f32x4*)(csp + un[nt] * 16 + 4 * g);
    const float qs = cb * 6 + un[nt] < 32 ? 0.25f * 1.4426950408889634f : 1.f;
    unsigned o[2][2];
#pragma unroll
    for (int tt = 0; tt < 2; ++tt) {
      f32x4 y = acc[tt][nt] * (ms[tt][1] * qs) + (ms[tt][0] * qs) * c;
      o[tt][0] = pack2(y[0], y[1]); o[tt][1] = pack2(y[2], y[3]);
    }
    auto sx = __builtin_amdgcn_permlane16_swap(o[0][0], o[1][0], false, false);
    auto sy = __builtin_amdgcn_permlane16_swap(o[0][1], o[1][1], false, false);
    char* dst = qkv + (size_t)(cb * 6 + un[nt]) * (2048 * 32) + (size_t)(tb * 4 + tg) * 1024;
    st16<true>(dst + vo, u32x4{sx[0], sy[0], sx[1], sy[1]});
  }
}

constexpr int S0 = 0, S1 = 49152, S2 = 98304, CSP = 147456, MG_LDS = 163840;
typedef unsigned long long u64;
extern "C" __device__ u64 llvm_dispatch_id() __asm("llvm.amdgcn.dispatch.id");
DEV void flag_set(u64* f, u64 tag) {
  asm volatile("global_store_dwordx2 %0, %1, off sc0 sc1" ::"v"(f), "v"(tag) : "memory");
}
DEV void flag_wait(const u64* p, u64 tag) {
  for (int it = 0; it < (1 << 20); ++it) {
    u64 c;
    asm volatile("global_load_dwordx2 %0, %1, off sc0 sc1\n\ts_waitcnt vmcnt(0)" : "=v"(c) : "v"(p) : "memory");
    if (__builtin_amdgcn_ballot_w64(c != tag) == 0) break;
  }
}
template <int V> struct IC { static constexpr int value = V; };
DEV int opaque(int v) { asm volatile("" : "+v"(v)); return v; }
DEV int opaque_s(int v) { asm volatile("" : "+s"(v)); return v; }

struct CW {
  const char *g1, *g3; unsigned vw; unsigned long long wmask;
};
DEV CW c_setup(const char* w1, const char* w3, int b, int wv, int lane) {
  const int xcd = b & 7, bi = b >> 3;
  const int cb = (xcd & 3) * 4 + (bi & 3);
  CW c;
  c.g1 = w1 + (size_t)(wv * 8) * (F_ * 2) + cb * 224;
  c.g3 = w3 + (size_t)(wv * 8) * (F_ * 2) + cb * 224;
  const int unit = ((lane >> 1) & 7) ^ ((lane >> 4) | ((wv & 1) << 2));
  c.vw = (lane >> 4) * (F_ * 2) + unit * 32 + (lane & 1) * 16;
  c.wmask = __builtin_amdgcn_ballot_w64(unit < 7);
  return c;
}
DEV void c_dmaw(const CW& c, unsigned ldsw, int kt, int j) {
  const unsigned base = ldsw + (kt % 3) * 49152;
  if (j < 2) dma16sm(c.vw, c.g1 + (size_t)(kt * 64 + j * 4) * (F_ * 2), base + 16384 + j * 1024, c.wmask);
  else dma16sm(c.vw, c.g3 + (size_t)(kt * 64 + (j - 2) * 4) * (F_ * 2), base + 32768 + (j - 2) * 1024, c.wmask);
}
DEV constexpr int d_unit(int u) {
  return u < 3 ? S2 + u * 2048 : u < 6 ? S0 + (u - 3) * 2048 : u < 9 ? S1 + (u - 6) * 2048 : u == 9 ? CSP
       : u < 13 ? S2 + (u - 10) * 2048 : S0;
}
DEV constexpr int d_ustr(int u) { return u == 9 ? 2048 : 6144; }
DEV void d_dma(const char* gw2, unsigned vw, unsigned lds0, int wv, int u, int q) {
  dma16s(vw, gw2 + (size_t)(u * 16 + q * 8) * 1024, lds0 + d_unit(u) + wv * d_ustr(u) + q * 1024);
}

template <int NT>
DEV void c_body(char* smem, unsigned lds0, const char* gt, const CW& cw, unsigned vt, int wv, int lane,
                const char* __restrict__ stats, const char* __restrict__ csb, char* __restrict__ hh, int tok0, int cb, const char* gw2, unsigned vwd) {
  const int tg = wv & 3;
  const unsigned ldsw = lds0 + wv * 2048;
  const int g = lane >> 4, i = lane & 15, q4 = (lane >> 2) & 3, p4 = lane & 3;
  f32x4 acc[2][2 * NT];
#pragma unroll
  for (int a = 0; a < 2 * NT; ++a) { acc[0][a] = f32x4{0, 0, 0, 0}; acc[1][a] = f32x4{0, 0, 0, 0}; }
  const int toff0 = (tg * 32 + i) * 128 + ((g ^ (i & 7)) << 4);
  const int toff1 = (tg * 32 + i) * 128 + (((4 + g) ^ (i & 7)) << 4);
  const int odd = cb & 1;
  const int fsw = q4 | ((g & 1) << 2);
  int T[NT], wa[NT];
#pragma unroll
  for (int a = 0; a < NT; ++a) {
    T[a] = NT == 4 ? odd * 3 + a : (a < 2 ? (odd ? 1 : 4) + a : (odd ? 0 : 6));
    wa[a] = 16384 + (8 * g + q4) * 256 + ((T[a] ^ fsw) << 5) + p4 * 8;
  }
  auto dmat = [&](int kt, int j) { dma16s(vt, gt + kt * 128 + j * 8192, ldsw + (kt % 3) * 49152 + j * 1024); };
  auto dma = [&](int kt, int j) {
    if (j < 2) dmat(kt, j); else c_dmaw(cw, ldsw, kt, j - 2);
  };
  auto ldB = [&](bf16x8* B, int kt, int ks) {
    const char* sb = smem + (kt % 3) * 49152 + (ks ? toff1 : toff0);
    B[0] = lds128(sb); B[1] = lds128(sb + 2048);
  };
  auto ldA = [&](int a, int kt, int ks) -> bf16x8 {
    const char* wp = smem + (kt % 3) * 49152 + wa[a < NT ? a : a - NT] + (a < NT ? 0 : 16384) + ks * 8192;
    return cat(ldstr(wp), ldstr(wp + 1024));
  };
  auto mma = [&](int a, bf16x8 A, const bf16x8* B) {
    acc[0][a] = mfma16(A, B[0], acc[0][a]);
    acc[1][a] = mfma16(A, B[1], acc[1][a]);
  };
  bf16x8 A0[2 * NT], A1[2 * NT], B0[2], B1[2];
  bf16x8 cp[4];
  u32x2 sa[4];
#pragma unroll
  for (int kt = 0; kt < 3; ++kt) { dmat(kt, 0); dmat(kt, 1); }
#pragma unroll
  for (int m = 0; m < 4; ++m)
    gld8(sa[m], (unsigned)((i >> 3) * 512 + g * 128 + (i & 7) * 8), stats + (size_t)(tok0 + tg * 32 + (m >> 1) * 16) * 64 + (m & 1) * 64);
  const int cq = min(tg * 16 + (lane >> 2), 55);
  if (NT == 3) {
#pragma unroll
    for (int m = 0; m < 4; ++m) gld16(cp[m], (unsigned)(((lane & 3) * 4 + m) * 896 + cq * 16), csb + (size_t)cb * 14336);
  }
  constexpr int X = NT == 3 ? 8 : 4;
  wait_bar<4 + X>();
  float rs[2], mr[2];
  float* csp = (float*)(smem + CSP);
  ldB(B0, 0, 0);
#pragma unroll
  for (int a = 0; a < 2 * NT; ++a) A0[a] = ldA(a, 0, 0);
#pragma unroll
  for (int kt = 0; kt < 8; ++kt) {
    ldB(B1, kt, 1);
#pragma unroll
    for (int a = 0; a < 2 * NT; ++a) {
      if (a < NT) { A1[2 * a] = ldA(2 * a, kt, 1); A1[2 * a + 1] = ldA(2 * a + 1, kt, 1); }
      mma(a, A0[a], B0);
      __builtin_amdgcn_sched_barrier(0);
    }
    if (kt == 0) wait_bar<2 + X>();
    else if (kt != 1) wait_bar<6>();
    else if (NT == 3) asm volatile("s_waitcnt vmcnt(6) lgkmcnt(0)\n\ts_barrier" : "+v"(sa[0]), "+v"(sa[1]), "+v"(sa[2]), "+v"(sa[3]), "+v"(cp[0]), "+v"(cp[1]), "+v"(cp[2]), "+v"(cp[3]) : : "memory");
    else asm volatile("s_waitcnt vmcnt(6) lgkmcnt(0)\n\ts_barrier" : "+v"(sa[0]), "+v"(sa[1]), "+v"(sa[2]), "+v"(sa[3]) : : "memory");
    if (kt < 7) ldB(B0, kt + 1, 0);
    if (kt == 1) {
#pragma unroll
      for (int tt = 0; tt < 2; ++tt) {
        const f32x2 sv0 = __builtin_bit_cast(f32x2, sa[2 * tt]), sv1 = __builtin_bit_cast(f32x2, sa[2 * tt + 1]);
        const float s1 = xsum2(sv0[0] + sv1[0]), s2 = xsum2(sv0[1] + sv1[1]);
        const float mean = s1 * (1.f / 512.f);
        const float var = fmaxf(s2 * (1.f / 512.f) - mean * mean, 0.f);
        rs[tt] = rsqrtf(var + LN_EPS); mr[tt] = mean * rs[tt];
      }
      if (NT == 3) {
        f32x4 c = (__builtin_bit_cast(f32x4, cp[0]) + __builtin_bit_cast(f32x4, cp[1])) +
                  (__builtin_bit_cast(f32x4, cp[2]) + __builtin_bit_cast(f32x4, cp[3]));
#pragma unroll
        for (int e = 0; e < 4; ++e) { c[e] += dpp_xor1(c[e]); c[e] += dpp_xor2(c[e]); }
        if ((lane & 3) == 0) *(f32x4*)(csp + cq * 4) = c;
      }
    }
#pragma unroll
    for (int a = 0; a < 2 * NT; ++a) {
      if (kt < 7 && a < NT) { A0[2 * a] = ldA(2 * a, kt + 1, 0); A0[2 * a + 1] = ldA(2 * a + 1, kt + 1, 0); }
      mma(a, A1[a], B1);
      if (a >= 2 * NT - 6) {
        const int j = a - (2 * NT - 6);
        if (kt + 3 < 8) dma(kt + 3, j);
        else d_dma(gw2, vwd, lds0, wv, (kt - 5) * 3 + (j >> 1), j & 1);
      }
      __builtin_amdgcn_sched_barrier(0);
    }
  }
  unsigned pk[2][NT][2];
#pragma unroll
  for (int tt = 0; tt < 2; ++tt) {
    char* dst = hh + ((size_t)((tok0 >> 4) + tg * 2 + tt) * 56) * 1024 + lane * 16;
#pragma unroll
    for (int a = 0; a < NT; ++a) {
      f32x4 cg = *(const f32x4*)(csp + T[a] * 16 + 4 * g);
      f32x4 cu = *(const f32x4*)(csp + (7 + T[a]) * 16 + 4 * g);
      float r[4];
#pragma unroll
      for (int e = 0; e < 4; ++e) {
        float ga = acc[tt][a][e] * rs[tt] - mr[tt] * cg[e];
        float up = acc[tt][NT + a][e] * rs[tt] - mr[tt] * cu[e];
        float sg = __builtin_amdgcn_rcpf(1.f + __builtin_amdgcn_exp2f(-1.4426950408889634f * ga));
        r[e] = ga * sg * up;
      }
      pk[tt][a][0] = pack2(r[0], r[1]); pk[tt][a][1] = pack2(r[2], r[3]);
      const int G = cb * 7 + T[a];
      if (a & 1) {
        st16<true>(dst + (size_t)(G >> 1) * 1024, u32x4{pk[tt][a - 1][0], pk[tt][a - 1][1], pk[tt][a][0], pk[tt][a][1]});
      } else if (a == NT - 1) {
        st8<true>(dst + (size_t)(G >> 1) * 1024 + (G & 1) * 512 - lane * 8, u32x2{pk[tt][a][0], pk[tt][a][1]});
      }
    }
  }
}

__global__ __launch_bounds__(512) void k_mega(const char* __restrict__ x, const char* __restrict__ wqkv, const char* __restrict__ wo,
                                              const char* __restrict__ w1, const char* __restrict__ w3,
                                              const char* __restrict__ w2, char* __restrict__ ws, char* __restrict__ out) {
  extern __shared__ __attribute__((aligned(16))) char smem[];
  char* qkv = ws;
  char* o = ws + (6u << 20);
  char* h = ws + (8u << 20);
  char* stats = ws + (10u << 20);
  u64* flags = (u64*)(ws + (10u << 20) + (512u << 10));
  char* csb = ws + (10u << 20) + (768u << 10);
  char* hh = ws + (11u << 20);
  const u64 tag = ((u64)(size_t)__builtin_amdgcn_dispatch_ptr() << 20) + llvm_dispatch_id();
  const int b = blockIdx.x;
  const unsigned lds0 = (unsigned)(size_t)(LAS char*)smem;
  const int cbG = (b >> 3) & 7, tbG = (b & 7) * 4 + (b >> 6);
  const int cbC = (b & 3) * 4 + ((b >> 3) & 3), tbC = ((b >> 2) & 1) * 8 + (b >> 5);

  unsigned cpf = 0;
  {
    u64 pc;
    unsigned len;
    asm volatile("s_getpc_b64 %0\n.Lmega_pc:\n\ts_mov_b32 %1, .Lmega_end-.Lmega_pc" : "=s"(pc), "=s"(len));
    const unsigned off = ((b >> 3) * 26 + threadIdx.x) * 64;
    if (threadIdx.x < 26 && off < len) asm volatile("global_load_dword %0, %1, %2" : "=v"(cpf) : "v"(off), "s"(pc) : "memory");
  }
  {
    const int cb = (b & 1) * 8 + ((b >> 3) & 7), tb = ((b & 7) >> 1) * 4 + (b >> 6);
    unsigned tch;
    qkv_phase(smem, lds0, x, wqkv, qkv, cb, tb, wo, w1, w3, w2, tch);
    asm volatile("s_waitcnt vmcnt(0) lgkmcnt(0)\n\ts_barrier" ::: "memory");
    asm volatile("" :: "v"(cpf), "v"(tch));
    const int tid = threadIdx.x;
    if (tid < 9) {
      const int P = cb * 3 + tid / 3, d = tid % 3, which = P >> 4, hp = P & 15, qb = tb + d;
      if (which ? qb < 16 : d == 0)
        flag_set(flags + 24576 + ((hp >> 1) | ((hp & 1) << 3) | (qb << 4)) * 32 + (which ? (which - 1) * 3 + d : 6), tag);
    }
  }
  {
    const int tid = opaque(threadIdx.x), lane = tid & 63, wv = uni(tid >> 6);
    const int c32 = lane & 31, hf = lane >> 5, q4 = (lane >> 2) & 3, p4 = lane & 3;
    const int hp = (b & 7) * 2 + ((b >> 3) & 1), qb = b >> 4;
    const int q0 = qb * 128;
    const size_t HS = 2048 * 32;
    bf16x8 Q, cw0, cw1;
    {
      const unsigned one = qb * 4 - 8 + wv >= 0 ? 0x3F803F80u : 0u;
      *(LAS u32x4*)(LAS char*)(smem + S1 + tid * 16) = u32x4{one, one, one, one};
      if (tid < 256) *(LAS u32x4*)(LAS char*)(smem + S1 + 8192 + tid * 16) = u32x4{0x3F803F80u, 0x3F803F80u, 0x3F803F80u, 0x3F803F80u};
    }
    const unsigned vwo = (lane >> 3) * 1024 + ((((lane & 7) >> 1) ^ ((lane >> 4) & 3)) << 5) + (lane & 1) * 16;
    const char* gwo = wo + (size_t)(wv * 64) * 1024 + cbG * 128;
    const CW cw1s = c_setup(w1, w3, b, wv, lane);
    auto pf = [&](int k) {
      if (k < 2) {
        const int ch = max(wv - 1, 0) * 4 + (lane & 3);
        const char* pc = (ch >= 14 ? w3 + (ch - 14) * 16 : w1 + ch * 16) + (size_t)(tbC * 32 + (lane >> 2)) * (F_ * 2) + cbC * 224;
        if (k == 0) asm volatile("global_load_dwordx4 %0, %1, off" : "=v"(cw0) : "v"(pc) : "memory");
        else asm volatile("global_load_dwordx4 %0, %1, off" : "=v"(cw1) : "v"(pc + 16 * (F_ * 2)) : "memory");
      } else if (k < 10) dma16s(vwo, gwo + (size_t)((k - 2) * 8) * 1024, lds0 + S2 + wv * 8192 + (k - 2) * 1024);
      else if (k < 14) c_dmaw(cw1s, lds0 + wv * 2048, 1, k - 10);
      else if (k == 14) {
        const int c0 = b * 8 + wv;
        dma16s(lane * 16, w2 + (size_t)(c0 < 1792 ? c0 : c0 - 1792) * 1024, lds0 + S1 + 12288 + (wv & 3) * 1024);
      }
    };
#pragma unroll
    for (int k = 0; k < PFE; ++k) pf(k);
    auto pfs = [&](int slot) {
#pragma unroll
      for (int k = PFE; k < NPF; ++k) if (((k - PFE) * PM) / 4 == slot) pf(k);
    };
    if (wv == 0) {
      const int d = lane % 3;
      flag_wait(flags + 24576 + b * 32 + (lane >= 6 ? 6 : qb - d < 0 ? 6 : lane), tag);
    }
    asm volatile("s_barrier" ::: "memory");
    {
      const int r = wv >> 1;
      const char* src = qkv + (size_t)((1 + (r >> 1)) * 32 + hp * 2 + (r & 1)) * HS;
#pragma unroll
      for (int j = 0; j < 6; ++j) {
        const int kb = qb * 4 - 8 + (wv & 1) * 6 + j;
        if (kb < 0 && r >= 2) *(LAS u32x4*)(LAS char*)(smem + r * 12288 + ((wv & 1) * 6 + j) * 1024 + lane * 16) = u32x4{0, 0, 0, 0};
        else dma16s(kb < 0 ? hf * 640 : lane * 16, src + (size_t)max(kb, 0) * 1024, lds0 + r * 12288 + ((wv & 1) * 6 + j) * 1024);
      }
    }
    const int u = wv >> 2, qg = wv & 3;
    const int qs = q0 + qg * 32;
    const unsigned vq = hf * 512 + ((c32 ^ (8 * hf)) << 4);
    gld16(Q, vq, qkv + (size_t)(hp * 2 + u) * HS + (size_t)qs * 32);
    const float NEG = -1e30f;
    f32x16 cin0, cin8;
    u32x4 mk[2];
#pragma unroll
    for (int r = 0; r < 16; ++r) {
      const int kl = (r & 3) + 8 * (r >> 2) + 4 * hf;
      cin0[r] = kl > c32 ? 0.f : NEG; cin8[r] = kl <= c32 ? 0.f : NEG;
      if (r & 1) mk[r >> 3][(r >> 1) & 3] = (kl > c32 ? 0xFFFF0000u : 0u) | (kl - 1 > c32 ? 0xFFFFu : 0u);
    }
    asm volatile("s_waitcnt vmcnt(0) lgkmcnt(0)\n\ts_barrier" : "+v"(Q), "+v"(cin0), "+v"(cin8), "+v"(mk[0]), "+v"(mk[1]), "+v"(cw0), "+v"(cw1) : : "memory");
    {
      const char* Kl = smem + u * 12288 + qg * 1024 + vq;
      const char* Vl[2];
#pragma unroll
      for (int e = 0; e < 2; ++e)
        Vl[e] = c32 < 16 ? smem + 24576 + u * 12288 + qg * 1024 + (p4 >> 1) * 512 + (((e * 8 + 4 * hf + q4) ^ (8 * (p4 >> 1))) << 4) + (p4 & 1) * 8
                         : smem + S1 + qg * 1024 + (hf ? 0 : 64);
      f32x16 z;
#pragma unroll
      for (int r = 0; r < 16; ++r) z[r] = 0.f;
      bf16x8 Kf[9];
#pragma unroll
      for (int t = 0; t < 9; ++t) Kf[t] = lds128(Kl + t * 1024);
      f32x16 sm = mfma32(Kf[0], Q, cin0);
      {
        const f32x16 s8 = mfma32(Kf[8], Q, cin8);
#pragma unroll
        for (int r = 0; r < 16; ++r) sm[r] = fmaxf(sm[r], s8[r]);
      }
      float m = sm[0];
#pragma unroll
      for (int r = 1; r < 16; ++r) m = fmaxf(m, sm[r]);
#pragma unroll
      for (int t = 1; t < 8; ++t) {
        const f32x16 s = mfma32(Kf[t], Q, z);
#pragma unroll
        for (int r = 0; r < 16; ++r) m = fmaxf(m, s[r]);
      }
      {
        auto sw = __builtin_amdgcn_permlane32_swap(__float_as_uint(m), __float_as_uint(m), false, false);
        m = fmaxf(__uint_as_float(sw[0]), __uint_as_float(sw[1]));
      }
      f32x16 cm, oacc;
#pragma unroll
      for (int r = 0; r < 16; ++r) { cm[r] = -m; oacc[r] = 0.f; }
      f32x16 sn = mfma32(Kf[1], Q, cm);
      {
        u32x4 pb[2];
#pragma unroll
        for (int j = 0; j < 8; ++j)
          pb[j >> 2][j & 3] = pack2(__builtin_amdgcn_exp2f(sm[2 * j] - m), __builtin_amdgcn_exp2f(sm[2 * j + 1] - m));
#pragma unroll
        for (int sh = 0; sh < 2; ++sh) {
          const u32x4 p0 = pb[sh] & mk[sh], p8 = pb[sh] ^ p0;
          oacc = mfma32(cat(ldstr(Vl[0] + sh * 256), ldstr(Vl[1] + sh * 256)), __builtin_bit_cast(bf16x8, p0), oacc);
          pfs(2 * sh);
          oacc = mfma32(cat(ldstr(Vl[0] + 8192 + sh * 256), ldstr(Vl[1] + 8192 + sh * 256)), __builtin_bit_cast(bf16x8, p8), oacc);
          pfs(2 * sh + 1);
        }
      }
      SB;
#pragma unroll
      for (int t = 1; t < 8; ++t) {
        const f32x16 s = sn;
        if (t < 7) sn = mfma32(Kf[t + 1], Q, cm);
#pragma unroll
        for (int sh = 0; sh < 2; ++sh) {
          u32x4 pb;
#pragma unroll
          for (int e = 0; e < 4; ++e)
            pb[e] = pack2(__builtin_amdgcn_exp2f(s[8 * sh + 2 * e]), __builtin_amdgcn_exp2f(s[8 * sh + 2 * e + 1]));
          const int vp = t * 1024 + sh * 256;
          oacc = mfma32(cat(ldstr(Vl[0] + vp), ldstr(Vl[1] + vp)), __builtin_bit_cast(bf16x8, pb), oacc);
          pfs(2 * t + sh + 2);
        }
        SB;
      }
      const float inv = __builtin_amdgcn_rcpf(oacc[8]);
      const u32x4 ov = {pack2(oacc[0] * inv, oacc[1] * inv), pack2(oacc[2] * inv, oacc[3] * inv),
                        pack2(oacc[4] * inv, oacc[5] * inv), pack2(oacc[6] * inv, oacc[7] * inv)};
      st16<true>(o + ((size_t)((qs >> 4) + (c32 >> 4)) * 16 + hp) * 1024 + ((2 * u + hf) * 16 + (c32 & 15)) * 16, ov);
    }
    asm volatile("s_waitcnt vmcnt(0) lgkmcnt(0)\n\ts_barrier" : "+v"(cw0), "+v"(cw1) : : "memory");
    if (tid < 16) flag_set(flags + (((qb >> 1) | (tid << 3) | ((qb & 1) << 7)) * 32 + hp), tag);
    {
      const CW cw = c_setup(w1, w3, b, wv, lane);
#pragma unroll
      for (int j = 0; j < 4; ++j) c_dmaw(cw, lds0 + wv * 2048, 0, j);
    }
    if (wv == 0) { flag_wait(flags + b * 32 + (lane & 15), tag); }
    else {
      const u32x4 ua = __builtin_bit_cast(u32x4, cw0), ub = __builtin_bit_cast(u32x4, cw1);
      f32x4 lo, hi;
#pragma unroll
      for (int e = 0; e < 2; ++e) {
        lo[2 * e] = bflo(ua[e]) + bflo(ub[e]); lo[2 * e + 1] = bfhi(ua[e]) + bfhi(ub[e]);
        hi[2 * e] = bflo(ua[2 + e]) + bflo(ub[2 + e]); hi[2 * e + 1] = bfhi(ua[2 + e]) + bfhi(ub[2 + e]);
      }
#pragma unroll
      for (int e = 0; e < 4; ++e) {
        lo[e] += dpp_ror4(lo[e]); lo[e] += dpp_ror8(lo[e]); lo[e] = xsum2(lo[e]);
        hi[e] += dpp_ror4(hi[e]); hi[e] += dpp_ror8(hi[e]); hi[e] = xsum2(hi[e]);
      }
      if (lane < 8) {
        char* d = csb + (size_t)(cbC * 16 + tbC) * 896 + ((wv - 1) * 4 + (lane & 3)) * 32 + (lane >> 2) * 16;
        st16<true>(d, __builtin_bit_cast(u32x4, lane < 4 ? lo : hi));
      }
      asm volatile("s_waitcnt vmcnt(0)" ::: "memory");
    }
    asm volatile("s_barrier" ::: "memory");
    if (tid >= 64 && tid < 80) flag_set(flags + 8192 + ((cbC >> 2) | ((tid & 1) << 2) | ((cbC & 3) << 3) | (((tid & 15) >> 1) << 5)) * 32 + 16 + tbC, tag);
  }
  u32x4 hres;
  {
    const int tid = opaque(threadIdx.x), lane = tid & 63, wv = uni(tid >> 6);
    const int g = lane >> 4, i = lane & 15, q4 = (lane >> 2) & 3, p4 = lane & 3;
    const int kp = wv & 3, th = wv >> 2;
    bf16x8 B[4][2], rs;
    const char* ga = o + ((size_t)(tbG * 4 + th * 2) * 16 + kp * 4) * 1024;
#pragma unroll
    for (int s = 0; s < 4; ++s) {
      gld16(B[s][0], lane * 16, ga + s * 1024);
      gld16(B[s][1], lane * 16, ga + (16 + s) * 1024);
    }
    const size_t ooff = (size_t)(tbG * 64 + wv * 8) * 1024 + cbG * 128;
    const unsigned vo = (lane >> 3) * 1024 + (lane & 7) * 16;
    gld16(rs, vo, x + ooff);
    const int sw = (2 * (g & 1) + (q4 >> 1)) & 3;
    const char* wr = smem + S2 + kp * 16384 + (16 * (g >> 1) + 4 * (g & 1) + q4) * 128 + p4 * 8;
    bf16x8 A[4][4];
#pragma unroll
    for (int s = 0; s < 4; ++s)
#pragma unroll
      for (int nt = 0; nt < 4; ++nt) {
        const char* p = wr + s * 4096 + ((nt ^ sw) << 5);
        A[s][nt] = cat(ldstr(p), ldstr(p + 1024));
      }
    f32x4 acc[2][4];
#pragma unroll
    for (int t = 0; t < 2; ++t)
#pragma unroll
      for (int n = 0; n < 4; ++n) acc[t][n] = f32x4{0, 0, 0, 0};
    asm volatile("s_waitcnt vmcnt(7)" : "+v"(B[0][0]), "+v"(B[0][1]) : : "memory");
#pragma unroll
    for (int nt = 0; nt < 4; ++nt) { acc[0][nt] = mfma16(A[0][nt], B[0][0], acc[0][nt]); acc[1][nt] = mfma16(A[0][nt], B[0][1], acc[1][nt]); }
    asm volatile("s_waitcnt vmcnt(5)" : "+v"(B[1][0]), "+v"(B[1][1]) : : "memory");
#pragma unroll
    for (int nt = 0; nt < 4; ++nt) { acc[0][nt] = mfma16(A[1][nt], B[1][0], acc[0][nt]); acc[1][nt] = mfma16(A[1][nt], B[1][1], acc[1][nt]); }
    asm volatile("s_waitcnt vmcnt(3)" : "+v"(B[2][0]), "+v"(B[2][1]) : : "memory");
#pragma unroll
    for (int nt = 0; nt < 4; ++nt) { acc[0][nt] = mfma16(A[2][nt], B[2][0], acc[0][nt]); acc[1][nt] = mfma16(A[2][nt], B[2][1], acc[1][nt]); }
    asm volatile("s_waitcnt vmcnt(1)" : "+v"(B[3][0]), "+v"(B[3][1]) : : "memory");
#pragma unroll
    for (int nt = 0; nt < 4; ++nt) { acc[0][nt] = mfma16(A[3][nt], B[3][0], acc[0][nt]); acc[1][nt] = mfma16(A[3][nt], B[3][1], acc[1][nt]); }
    bar();
    {
      const int mo = S2 + kp * 16384 + g * 256 + ((i ^ ((g >> 1) * 2)) << 4);
#pragma unroll
      for (int tt = 0; tt < 2; ++tt)
#pragma unroll
        for (int nt = 0; nt < 4; ++nt)
          *(LAS f32x4*)(LAS char*)(smem + (mo ^ (nt * 64)) + ((th * 2 + tt) * 4 + nt) * 1024) = acc[tt][nt];
    }
    asm volatile("s_waitcnt vmcnt(0) lgkmcnt(0)\n\ts_barrier" : "+v"(rs) : : "memory");
    {
      const int c = lane & 7;
      const int tok = (lane >> 3) + 8 * (wv & 1);
      const char* rp = smem + S2 + ((wv >> 1) * 4 + (c >> 1)) * 1024 + (c & 1) * 512 + ((tok ^ (2 * c)) << 4);
      f32x4 v0 = f32x4{0, 0, 0, 0}, v1 = f32x4{0, 0, 0, 0};
#pragma unroll
      for (int p = 0; p < 4; ++p) {
        v0 += *(const LAS f32x4*)(LAS const char*)(rp + p * 16384);
        v1 += *(const LAS f32x4*)(LAS const char*)(rp + p * 16384 + 256);
      }
      u32x4 r = __builtin_bit_cast(u32x4, rs);
      u32x4 ov = {pack2(v0[0] + bflo(r[0]), v0[1] + bfhi(r[0])), pack2(v0[2] + bflo(r[1]), v0[3] + bfhi(r[1])),
                  pack2(v1[0] + bflo(r[2]), v1[1] + bfhi(r[2])), pack2(v1[2] + bflo(r[3]), v1[3] + bfhi(r[3]))};
      st16<true>(h + ooff + vo, ov);
      hres = ov;
      float s1 = 0.f, s2 = 0.f;
#pragma unroll
      for (int e = 0; e < 4; ++e) { s1 = dot2(ov[e], 0x3F803F80u, s1); s2 = dot2(ov[e], ov[e], s2); }
#pragma unroll
      for (int m = 1; m < 8; m <<= 1) { s1 += __shfl_xor(s1, m); s2 += __shfl_xor(s2, m); }
      if (c == 0) st8<true>(stats + ((size_t)(tbG * 8 + wv) * 8 + cbG) * 64 + (lane >> 3) * 8,
                            u32x2{__float_as_uint(s1), __float_as_uint(s2)});
    }
    asm volatile("s_waitcnt vmcnt(0) lgkmcnt(0)\n\ts_barrier" ::: "memory");
    if (tid < 16) flag_set(flags + 8192 + ((tid & 3) | ((tbG >> 4) << 2) | ((tid >> 2) << 3) | (((tbG >> 1) & 7) << 5)) * 32 + (tbG & 1) * 8 + cbG, tag);
    {
      const CW cw = c_setup(w1, w3, b, wv, lane);
#pragma unroll
      for (int j = 0; j < 4; ++j) c_dmaw(cw, lds0 + wv * 2048, 2, j);
    }
    if (wv == 0) flag_wait(flags + 8192 + b * 32 + (lane & 31), tag);
    asm volatile("s_barrier" ::: "memory");
  }
  const unsigned vwd_ = 0;
  {
    const int tid = opaque(threadIdx.x), lane = tid & 63, wv = uni(tid >> 6);
    const char* gt = h + (size_t)(tbC * 128 + wv * 16) * 1024;
    const unsigned vt = (lane >> 3) * 1024 + (((lane & 7) ^ (lane >> 3)) << 4);
    const CW cw = c_setup(w1, w3, b, wv, lane);
    const unsigned vwd = (lane >> 3) * 1024 + ((((lane & 7) >> 1) ^ ((lane >> 4) & 3)) << 5) + (lane & 1) * 16;
    const char* gw2 = w2 + (size_t)(wv * 224) * 1024 + cbG * 128;
    if ((wv >> 2) == 0) c_body<4>(smem, lds0, gt, cw, vt, wv, lane, stats, csb, hh, tbC * 128, cbC, gw2, vwd);
    else c_body<3>(smem, lds0, gt, cw, vt, wv, lane, stats, csb, hh, tbC * 128, cbC, gw2, vwd);
  }
  {
    const int tid = opaque(threadIdx.x), lane = tid & 63, wv = uni(tid >> 6);
    const int g = lane >> 4, i = lane & 15, q4 = (lane >> 2) & 3, p4 = lane & 3;
    asm volatile("s_waitcnt vmcnt(0) lgkmcnt(0)\n\ts_barrier" ::: "memory");
    if (tid < 16) flag_set(flags + 16384 + ((tbC >> 1) | (tid << 3) | ((tbC & 1) << 7)) * 32 + cbC, tag);
    const unsigned vwd = (lane >> 3) * 1024 + ((((lane & 7) >> 1) ^ ((lane >> 4) & 3)) << 5) + (lane & 1) * 16;
    const char* gw2 = w2 + (size_t)(wv * 224) * 1024 + cbG * 128;
    d_dma(gw2, vwd, lds0, wv, 9, 0); d_dma(gw2, vwd, lds0, wv, 9, 1);
    const int sw = (2 * g + (q4 >> 1)) & 3;
    const int ro = (4 * g + q4) * 128 + p4 * 8;
    int rn[4];
#pragma unroll
    for (int nt = 0; nt < 4; ++nt) rn[nt] = ro + ((nt ^ sw) << 5);
    auto lda = [&](bf16x8* Af, int j) {
      const char* p0 = smem + d_unit(2 * j) + wv * d_ustr(2 * j);
      const char* p1 = smem + d_unit(2 * j + 1) + wv * d_ustr(2 * j + 1);
#pragma unroll
      for (int nt = 0; nt < 4; ++nt) Af[nt] = cat(ldstr(p0 + rn[nt]), ldstr(p1 + rn[nt]));
    };
    bf16x8 A[2][4], B[7][4];
    lda(A[0], 0); lda(A[1], 1);
    asm volatile("s_waitcnt lgkmcnt(0)" : "+v"(A[0][0]), "+v"(A[0][1]), "+v"(A[0][2]), "+v"(A[0][3]), "+v"(A[1][0]), "+v"(A[1][1]), "+v"(A[1][2]), "+v"(A[1][3]) : : "memory");
#pragma unroll
    for (int u = 10; u < 14; ++u) { d_dma(gw2, vwd, lds0, wv, u, 0); d_dma(gw2, vwd, lds0, wv, u, 1); }
    if (wv == 0) flag_wait(flags + 16384 + b * 32 + (lane & 15), tag);
    asm volatile("s_barrier" ::: "memory");
    const char* ga = hh + ((size_t)(tbG * 4) * 56 + wv * 7) * 1024;
    u32x2 Bl[4], Bh[4];
#pragma unroll
    for (int j = 0; j < 7; ++j)
#pragma unroll
      for (int tt = 0; tt < 4; ++tt) {
        if (j == 3) {
          gld8(Bl[tt], lane * 8, ga + (size_t)(tt * 56 + j) * 1024);
          gld8(Bh[tt], lane * 8, ga + (size_t)(tt * 56 + j) * 1024 + 512);
        } else gld16(B[j][tt], lane * 16, ga + (size_t)(tt * 56 + j) * 1024);
      }
    const size_t ooff = (size_t)(tbG * 64 + wv * 8) * 1024 + cbG * 128;
    const unsigned vo = (lane >> 3) * 1024 + (lane & 7) * 16;
    f32x4 acc[4][4];
#pragma unroll
    for (int t = 0; t < 4; ++t)
#pragma unroll
      for (int n = 0; n < 4; ++n) acc[t][n] = f32x4{0, 0, 0, 0};
    auto step = [&](auto J) {
      constexpr int j = decltype(J)::value;
      if constexpr (j == 3) {
        asm volatile("s_waitcnt vmcnt(12)" : "+v"(Bl[0]), "+v"(Bl[1]), "+v"(Bl[2]), "+v"(Bl[3]), "+v"(Bh[0]), "+v"(Bh[1]), "+v"(Bh[2]), "+v"(Bh[3]) : : "memory");
#pragma unroll
        for (int tt = 0; tt < 4; ++tt) B[3][tt] = __builtin_bit_cast(bf16x8, u32x4{Bl[tt][0], Bl[tt][1], Bh[tt][0], Bh[tt][1]});
      } else
        asm volatile("s_waitcnt vmcnt(%4)" : "+v"(B[j][0]), "+v"(B[j][1]), "+v"(B[j][2]), "+v"(B[j][3]) : "n"((j < 3 ? 28 : 24) - 4 * j) : "memory");
#pragma unroll
      for (int nt = 0; nt < 4; ++nt)
#pragma unroll
        for (int tt = 0; tt < 4; ++tt) acc[tt][nt] = mfma16(A[j & 1][nt], B[j][tt], acc[tt][nt]);
      __builtin_amdgcn_sched_barrier(0);
      if constexpr (j + 2 < 7) lda(A[j & 1], j + 2);
    };
    step(IC<0>{}); step(IC<1>{}); step(IC<2>{}); step(IC<3>{}); step(IC<4>{}); step(IC<5>{}); step(IC<6>{});
    {
      const int mo = wv * 6144 + g * 256 + ((i ^ ((g >> 1) * 2)) << 4);
#pragma unroll
      for (int tt = 0; tt < 4; ++tt)
#pragma unroll
        for (int nt = 0; nt < 4; ++nt) {
          const int t = tt * 4 + nt;
          const int base = t < 6 ? S2 + t * 1024 : t < 12 ? S0 + (t - 6) * 1024 : S1 + (t - 12) * 1024;
          *(LAS f32x4*)(LAS char*)(smem + (mo ^ (nt * 64)) + base) = acc[tt][nt];
        }
    }
    asm volatile("s_waitcnt vmcnt(0) lgkmcnt(0)\n\ts_barrier" ::: "memory");
    {
      const int c = lane & 7;
      const int tok = (lane >> 3) + 8 * (wv & 1);
      const int t = (wv >> 1) * 4 + (c >> 1);
      const int base = t < 6 ? S2 + t * 1024 : t < 12 ? S0 + (t - 6) * 1024 : S1 + (t - 12) * 1024;
      const char* rp = smem + base + (c & 1) * 512 + ((tok ^ (2 * c)) << 4);
      f32x4 v0 = f32x4{0, 0, 0, 0}, v1 = f32x4{0, 0, 0, 0};
#pragma unroll
      for (int p = 0; p < 8; ++p) {
        v0 += *(const LAS f32x4*)(LAS const char*)(rp + p * 6144);
        v1 += *(const LAS f32x4*)(LAS const char*)(rp + p * 6144 + 256);
      }
      const u32x4 r = hres;
      u32x4 ov = {pack2(v0[0] + bflo(r[0]), v0[1] + bfhi(r[0])), pack2(v0[2] + bflo(r[1]), v0[3] + bfhi(r[1])),
                  pack2(v1[0] + bflo(r[2]), v1[1] + bfhi(r[2])), pack2(v1[2] + bflo(r[3]), v1[3] + bfhi(r[3]))};
      st16<true>(out + ooff + vo, ov);
    }
  }
  asm volatile(".Lmega_end:");
}

extern "C" void kernel_launch(void* const* d_in, const int* in_sizes, int n_in, void* d_out, int out_size,
                              void* d_ws, size_t ws_size, hipStream_t stream) {
  const char* x = (const char*)d_in[0];
  const char* wqkv = (const char*)d_in[1];
  const char* wo = (const char*)d_in[2];
  const char* w1 = (const char*)d_in[4];
  const char* w3 = (const char*)d_in[5];
  const char* w2 = (const char*)d_in[6];
  char* ws = (char*)d_ws;
  (void)hipFuncSetAttribute((const void*)k_mega, hipFuncAttributeMaxDynamicSharedMemorySize, MG_LDS);
  k_mega<<<256, 512, MG_LDS, stream>>>(x, wqkv, wo, w1, w3, w2, ws, (char*)d_out);
}
```

```cpp
#include <hip/hip_runtime.h>
#include <hip/hip_bf16.h>
#include <stdint.h>

typedef short bf16x8 __attribute__((ext_vector_type(8)));
typedef short s16x4 __attribute__((ext_vector_type(4)));
typedef float f32x4 __attribute__((ext_vector_type(4)));
typedef float f32x2 __attribute__((ext_vector_type(2)));
typedef unsigned u32x4 __attribute__((ext_vector_type(4)));
typedef unsigned u32x2 __attribute__((ext_vector_type(2)));
typedef __bf16 bf16x2 __attribute__((ext_vector_type(2)));
#define LAS __attribute__((address_space(3)))
#define DEV __device__ __forceinline__

constexpr int S_ = 2048, D_ = 512, H_ = 32, F_ = 1792;
constexpr float LN_EPS = 1e-5f;

typedef float f32x16 __attribute__((ext_vector_type(16)));
DEV f32x16 mfma32(bf16x8 a, bf16x8 b, f32x16 c) { return __builtin_amdgcn_mfma_f32_32x32x16_bf16(a, b, c, 0, 0, 0); }
DEV f32x4 mfma16(bf16x8 a, bf16x8 b, f32x4 c) { return __builtin_amdgcn_mfma_f32_16x16x32_bf16(a, b, c, 0, 0, 0); }
DEV unsigned pack2(float a, float b) {
  f32x2 v = {a, b};
  return __builtin_bit_cast(unsigned, __builtin_convertvector(v, bf16x2));
}
DEV float bflo(unsigned u) { return __builtin_bit_cast(float, u << 16); }
DEV float bfhi(unsigned u) { return __builtin_bit_cast(float, u & 0xffff0000u); }
DEV float dot2(unsigned a, unsigned b, float c) {
  return __builtin_amdgcn_fdot2_f32_bf16(__builtin_bit_cast(bf16x2, a), __builtin_bit_cast(bf16x2, b), c, false);
}
DEV void dma16(const char* gptr, unsigned ldsbase) {
  unsigned sv;
  asm volatile("s_mov_b32 %0, m0\n\ts_mov_b32 m0, %2\n\ts_nop 0\n\tglobal_load_lds_dwordx4 %1, off\n\ts_mov_b32 m0, %0"
               : "=&s"(sv) : "v"(gptr), "s"(ldsbase) : "memory");
}
DEV void dma16s(unsigned voff, const char* sbase, unsigned ldsbase) {
  unsigned sv;
  asm volatile("s_mov_b32 %0, m0\n\ts_mov_b32 m0, %3\n\ts_nop 0\n\tglobal_load_lds_dwordx4 %1, %2\n\ts_mov_b32 m0, %0"
               : "=&s"(sv) : "v"(voff), "s"(sbase), "s"(ldsbase) : "memory");
}
DEV void dma16sm(unsigned voff, const char* sbase, unsigned ldsbase, unsigned long long mask) {
  unsigned sv;
  asm volatile("s_mov_b32 %0, m0\n\ts_mov_b32 m0, %3\n\ts_mov_b64 exec, %4\n\tglobal_load_lds_dwordx4 %1, %2\n\ts_mov_b64 exec, -1\n\ts_mov_b32 m0, %0"
               : "=&s"(sv) : "v"(voff), "s"(sbase), "s"(ldsbase), "s"(mask) : "memory");
}
DEV void gld16(bf16x8& d, unsigned voff, const char* sbase) {
  asm volatile("global_load_dwordx4 %0, %1, %2" : "=v"(d) : "v"(voff), "s"(sbase) : "memory");
}
DEV void gld8(u32x2& d, unsigned voff, const char* sbase) {
  asm volatile("global_load_dwordx2 %0, %1, %2" : "=v"(d) : "v"(voff), "s"(sbase) : "memory");
}
template <int N> DEV void wait_bar() { asm volatile("s_waitcnt vmcnt(%0) lgkmcnt(0)\n\ts_barrier" ::"n"(N) : "memory"); }
DEV void bar() { asm volatile("s_waitcnt lgkmcnt(0)\n\ts_barrier" ::: "memory"); }
DEV bf16x8 lds128(const char* p) { return *(const LAS bf16x8*)(LAS const char*)p; }
DEV s16x4 ldstr(const char* p) { return __builtin_amdgcn_ds_read_tr16_b64_v4i16((LAS s16x4*)p); }
DEV bf16x8 cat(s16x4 a, s16x4 b) { return __builtin_shufflevector(a, b, 0, 1, 2, 3, 4, 5, 6, 7); }
DEV float xsum(float v) { v += __shfl_xor(v, 16); v += __shfl_xor(v, 32); return v; }
DEV float xmax(float v) { v = fmaxf(v, __shfl_xor(v, 16)); v = fmaxf(v, __shfl_xor(v, 32)); return v; }
DEV float dpp_xor1(float v) { return __builtin_bit_cast(float, __builtin_amdgcn_mov_dpp(__builtin_bit_cast(int, v), 0xB1, 0xF, 0xF, true)); }
DEV float dpp_xor2(float v) { return __builtin_bit_cast(float, __builtin_amdgcn_mov_dpp(__builtin_bit_cast(int, v), 0x4E, 0xF, 0xF, true)); }
DEV float dpp_ror4(float v) { return __builtin_bit_cast(float, __builtin_amdgcn_mov_dpp(__builtin_bit_cast(int, v), 0x124, 0xF, 0xF, true)); }
DEV float dpp_ror8(float v) { return __builtin_bit_cast(float, __builtin_amdgcn_mov_dpp(__builtin_bit_cast(int, v), 0x128, 0xF, 0xF, true)); }
DEV unsigned uni(unsigned v) { return __builtin_amdgcn_readfirstlane(v); }
#define UP 3
#define NPF 14
#define PFE 2
#define PM 4
#define WT 31
template <bool W> DEV void st16(char* p, u32x4 v) {
  if constexpr (W) asm volatile("global_store_dwordx4 %0, %1, off sc0 sc1\n\ts_nop 1" ::"v"(p), "v"(v) : "memory");
  else *(u32x4*)p = v;
}
template <bool W> DEV void st8(char* p, u32x2 v) {
  if constexpr (W) asm volatile("global_store_dwordx2 %0, %1, off sc0 sc1" ::"v"(p), "v"(v) : "memory");
  else *(u32x2*)p = v;
}

template <int A, int B> struct Min { static constexpr int v = A < B ? A : B; };

template <int KT, int NK, int NS, int P, class Issue, class Compute>
DEV void pipe_steps(Issue& issue, Compute& compute) {
  if constexpr (KT < NK) {
    wait_bar<(Min<KT + NS - 1, NK>::v - KT - 1) * P>();
    if constexpr (KT + NS - 1 < NK) issue((KT + NS - 1) % NS);
    compute(KT % NS, KT);
    pipe_steps<KT + 1, NK, NS, P>(issue, compute);
  }
}

constexpr int K1_STAGE = 16384 + 12288, K1_NS = 5;
#define SB __builtin_amdgcn_sched_barrier(0)
DEV float xsum2(float v) {
  auto a = __builtin_amdgcn_permlane16_swap(__float_as_uint(v), __float_as_uint(v), false, false);
  v = __uint_as_float(a[0]) + __uint_as_float(a[1]);
  auto b = __builtin_amdgcn_permlane32_swap(__float_as_uint(v), __float_as_uint(v), false, false);
  return __uint_as_float(b[0]) + __uint_as_float(b[1]);
}

DEV void qkv_phase(char* smem, unsigned lds0, const char* __restrict__ x, const char* __restrict__ w, char* __restrict__ qkv,
                   int cb, int tb, const char* __restrict__ wo, const char* __restrict__ w1, const char* __restrict__ w3,
                   const char* __restrict__ w2, unsigned& tch) {
  const int tid = threadIdx.x, lane = tid & 63, wv = uni(tid >> 6);
  const int g = lane >> 4, i = lane & 15, q4 = (lane >> 2) & 3, p4 = lane & 3;
  const int tg = wv & 3, nh = wv >> 2;
  const int bi = blockIdx.x >> 3;

  const char* gx = x + (size_t)(tb * 128 + wv * 16) * 1024;
  const unsigned vt = (lane >> 3) * 1024 + (((lane & 7) ^ (lane >> 3)) << 4);
  const char* gw = w + cb * 192;
  unsigned vw0, vw1;
  {
    int qq = wv * 64 + lane;
    int k = qq / 12, c = (qq % 12) ^ (((k >> 3) & 1) << 1);
    vw0 = k * 3072 + c * 16;
    qq = 512 + wv * 32 + (lane & 31);
    k = qq / 12; c = (qq % 12) ^ (((k >> 3) & 1) << 1);
    vw1 = k * 3072 + c * 16;
  }
  const int k0 = bi & 7;
  auto dma = [&](int s, int j) {
    const int kk = (k0 + s) & 7;
    const unsigned base = lds0 + (s % K1_NS) * K1_STAGE;
    if (j == 0) dma16s(vt, gx + kk * 128, base + wv * 2048);
    else if (j == 1) dma16s(vt, gx + kk * 128 + 8192, base + wv * 2048 + 1024);
    else if (j == 2) dma16s(vw0, gw + kk * (64 * 3072), base + 16384 + wv * 1024);
    else dma16sm(vw1, gw + kk * (64 * 3072), base + 16384 + 8192 + wv * 512, 0xFFFFFFFFull);
  };
#pragma unroll
  for (int s = 0; s < UP; ++s)
#pragma unroll
    for (int j = 0; j < 4; ++j) dma(s, j);

  f32x4 acc[2][3], cs = f32x4{0, 0, 0, 0}, s1v = f32x4{0, 0, 0, 0}, gr = f32x4{0, 0, 0, 0};
#pragma unroll
  for (int a = 0; a < 3; ++a) { acc[0][a] = f32x4{0, 0, 0, 0}; acc[1][a] = f32x4{0, 0, 0, 0}; }
  const bf16x8 ones = {0x3F80, 0x3F80, 0x3F80, 0x3F80, 0x3F80, 0x3F80, 0x3F80, 0x3F80};
  int toff[2][2];
#pragma unroll
  for (int tt = 0; tt < 2; ++tt) {
    toff[tt][0] = (tg * 32 + (tt ^ nh) * 16 + i) * 128 + ((g ^ (i & 7)) << 4);
    toff[tt][1] = (tg * 32 + (tt ^ nh) * 16 + i) * 128 + (((4 + g) ^ (i & 7)) << 4);
  }
  int woff[3], un[3];
#pragma unroll
  for (int nt = 0; nt < 3; ++nt) {
    int a = nt + tg; a = a >= 3 ? a - 3 : a; a = a >= 3 ? a - 3 : a;
    un[nt] = nh * 3 + a;
    woff[nt] = 16384 + (8 * g + q4) * 192 + ((un[nt] ^ (g & 1)) << 5) + p4 * 8;
  }
  auto ldB = [&](int tt, int kt, int ks) -> bf16x8 { return lds128(smem + (kt % K1_NS) * K1_STAGE + toff[tt][ks]); };
  auto ldA = [&](int nt, int kt, int ks) -> bf16x8 {
    const char* p = smem + (kt % K1_NS) * K1_STAGE + woff[nt] + ks * 6144;
    return cat(ldstr(p), ldstr(p + 768));
  };
  bf16x8 A0[3], A1[3], B0[2], B1[2];
  auto phase = [&](bf16x8 (&Ac)[3], bf16x8 (&Bc)[2], bf16x8 (&An)[3], bf16x8 (&Bn)[2], int kn, int ksn, int dk, int dj, int nd) {
#pragma unroll
    for (int m = 0; m < 9; ++m) {
      if (kn < 8) {
        if (m == 0) { Bn[0] = ldB(0, kn, ksn); An[0] = ldA(0, kn, ksn); }
        else if (m == 1) Bn[1] = ldB(1, kn, ksn);
        else if (m < 4) An[m - 1] = ldA(m - 1, kn, ksn);
      }
      if (m < 6) acc[m & 1][m >> 1] = mfma16(Ac[m >> 1], Bc[m & 1], acc[m & 1][m >> 1]);
      else if (m == 6) cs = mfma16(Ac[0], ones, cs);
      else if (m == 7) s1v = mfma16(ones, Bc[0], s1v);
      else gr = mfma16(Bc[0], Bc[0], gr);
      if (dk < 8) {
        if (nd == 2) { if (m == 2 || m == 5) dma(dk, dj + (m == 5)); }
        else { if (m == 1 || m == 2) dma(dk, m - 1); else if (m == 4 || m == 5) dma(dk, m - 2); }
      }
      SB;
    }
  };
  wait_bar<4 * UP - 4>();
  B0[0] = ldB(0, 0, 0); A0[0] = ldA(0, 0, 0); B0[1] = ldB(1, 0, 0); A0[1] = ldA(1, 0, 0); A0[2] = ldA(2, 0, 0);
#pragma unroll
  for (int kt = 0; kt < 8; ++kt) {
    const int d1 = UP == 5 ? 8 : UP == 4 ? (kt == 0 ? 4 : 8) : (kt == 0 ? 3 : kt == 1 ? 5 : 8);
    const int d2 = UP == 3 ? (kt == 0 ? 4 : kt == 1 ? 6 : kt == 2 ? 7 : 8) : kt + 5;
    phase(A0, B0, A1, B1, kt, 1, d1, 0, 4);
    if (kt == 0) wait_bar<UP == 3 ? 8 : 12>();
    else if (kt < 4) wait_bar<12>();
    else if (kt == 4) wait_bar<8 + 1>();
    else if (kt == 5) wait_bar<4 + 1>();
    else if (kt == 6) wait_bar<0 + 1>();
    if (kt == 3) {
      const unsigned bb = blockIdx.x, xc = bb & 7, t2 = tid < 112 ? tid : tid - 112;
      const char* p = tid < 224 ? (tid < 112 ? w1 : w3) + (size_t)((((xc >> 2) ^ 1) * 256 + (bb >> 3) * 8 + t2 / 14) * 3584 + (xc & 3) * 896 + (t2 % 14) * 64)
                    : tid < 256 ? wo + (bb * 32 + tid - 224) * 64 : w2 + (bb * 112 + min(tid, 367) - 256) * 64;
      asm volatile("global_load_dword %0, %1, off" : "=v"(tch) : "v"(p) : "memory");
    }
    phase(A1, B1, A0, B0, kt + 1, 0, d2, 0, 4);
  }

  float* st = (float*)(smem + 49152);
  float* csp = (float*)(smem + 49152 + 1024);
  if (g == (i >> 2)) {
    const float s2 = (i & 2) ? ((i & 1) ? gr[3] : gr[2]) : ((i & 1) ? gr[1] : gr[0]);
    float mean = s1v[0] * (1.f / 512.f);
    float var = fmaxf(s2 * (1.f / 512.f) - mean * mean, 0.f);
    float rstd = rsqrtf(var + LN_EPS);
    *(f32x2*)(st + (tg * 32 + nh * 16 + i) * 2) = f32x2{-mean * rstd, rstd};
  }
  if (i == 0 && tg < 3) *(f32x4*)(csp + un[0] * 16 + 4 * g) = cs;
  bar();
  f32x2 ms[2];
#pragma unroll
  for (int tt = 0; tt < 2; ++tt) ms[tt] = *(const f32x2*)(st + (tg * 32 + (tt ^ nh) * 16 + i) * 2);
  const int otile = (g & 1) ^ nh, ohalf = g >> 1;
  const unsigned vo = ohalf * 512 + (((otile * 16 + i) ^ (8 * ohalf)) << 4);
#pragma unroll
  for (int nt = 0; nt < 3; ++nt) {
    f32x4 c = *(const f32x4*)(csp + un[nt] * 16 + 4 * g);
    const float qs = cb * 6 + un[nt] < 32 ? 0.25f * 1.4426950408889634f : 1.f;
    unsigned o[2][2];
#pragma unroll
    for (int tt = 0; tt < 2; ++tt) {
      f32x4 y = acc[tt][nt] * (ms[tt][1] * qs) + (ms[tt][0] * qs) * c;
      o[tt][0] = pack2(y[0], y[1]); o[tt][1] = pack2(y[2], y[3]);
    }
    auto sx = __builtin_amdgcn_permlane16_swap(o[0][0], o[1][0], false, false);
    auto sy = __builtin_amdgcn_permlane16_swap(o[0][1], o[1][1], false, false);
    char* dst = qkv + (size_t)(cb * 6 + un[nt]) * (2048 * 32) + (size_t)(tb * 4 + tg) * 1024;
    st16<true>(dst + vo, u32x4{sx[0], sy[0], sx[1], sy[1]});
  }
}

constexpr int S0 = 0, S1 = 49152, S2 = 98304, CSP = 147456, MG_LDS = 163840;
typedef unsigned long long u64;
extern "C" __device__ u64 llvm_dispatch_id() __asm("llvm.amdgcn.dispatch.id");
DEV void flag_set(u64* f, u64 tag) {
  asm volatile("global_store_dwordx2 %0, %1, off sc0 sc1" ::"v"(f), "v"(tag) : "memory");
}
DEV void flag_wait(const u64* p, u64 tag) {
  for (int it = 0; it < (1 << 20); ++it) {
    u64 c;
    asm volatile("global_load_dwordx2 %0, %1, off sc0 sc1\n\ts_waitcnt vmcnt(0)" : "=v"(c) : "v"(p) : "memory");
    if (__builtin_amdgcn_ballot_w64(c != tag) == 0) break;
  }
}
template <int V> struct IC { static constexpr int value = V; };
DEV int opaque(int v) { asm volatile("" : "+v"(v)); return v; }
DEV int opaque_s(int v) { asm volatile("" : "+s"(v)); return v; }

struct CW {
  const char *g1, *g3; unsigned vw; unsigned long long wmask;
};
DEV CW c_setup(const char* w1, const char* w3, int b, int wv, int lane) {
  const int xcd = b & 7, bi = b >> 3;
  const int cb = (xcd & 3) * 4 + (bi & 3);
  CW c;
  c.g1 = w1 + (size_t)(wv * 8) * (F_ * 2) + cb * 224;
  c.g3 = w3 + (size_t)(wv * 8) * (F_ * 2) + cb * 224;
  const int unit = ((lane >> 1) & 7) ^ ((lane >> 4) | ((wv & 1) << 2));
  c.vw = (lane >> 4) * (F_ * 2) + unit * 32 + (lane & 1) * 16;
  c.wmask = __builtin_amdgcn_ballot_w64(unit < 7);
  return c;
}
DEV void c_dmaw(const CW& c, unsigned ldsw, int kt, int j) {
  const unsigned base = ldsw + (kt % 3) * 49152;
  if (j < 2) dma16sm(c.vw, c.g1 + (size_t)(kt * 64 + j * 4) * (F_ * 2), base + 16384 + j * 1024, c.wmask);
  else dma16sm(c.vw, c.g3 + (size_t)(kt * 64 + (j - 2) * 4) * (F_ * 2), base + 32768 + (j - 2) * 1024, c.wmask);
}
DEV constexpr int d_unit(int u) {
  return u < 3 ? S2 + u * 2048 : u < 6 ? S0 + (u - 3) * 2048 : u < 9 ? S1 + (u - 6) * 2048 : u == 9 ? CSP
       : u < 13 ? S2 + (u - 10) * 2048 : S0;
}
DEV constexpr int d_ustr(int u) { return u == 9 ? 2048 : 6144; }
DEV void d_dma(const char* gw2, unsigned vw, unsigned lds0, int wv, int u, int q) {
  dma16s(vw, gw2 + (size_t)(u * 16 + q * 8) * 1024, lds0 + d_unit(u) + wv * d_ustr(u) + q * 1024);
}

template <int NT>
DEV void c_body(char* smem, unsigned lds0, const char* gt, const CW& cw, unsigned vt, int wv, int lane,
                const char* __restrict__ stats, const char* __restrict__ csb, char* __restrict__ hh, int tok0, int cb, const char* gw2, unsigned vwd) {
  const int tg = wv & 3;
  const unsigned ldsw = lds0 + wv * 2048;
  const int g = lane >> 4, i = lane & 15, q4 = (lane >> 2) & 3, p4 = lane & 3;
  f32x4 acc[2][2 * NT];
#pragma unroll
  for (int a = 0; a < 2 * NT; ++a) { acc[0][a] = f32x4{0, 0, 0, 0}; acc[1][a] = f32x4{0, 0, 0, 0}; }
  const int toff0 = (tg * 32 + i) * 128 + ((g ^ (i & 7)) << 4);
  const int toff1 = (tg * 32 + i) * 128 + (((4 + g) ^ (i & 7)) << 4);
  const int odd = cb & 1;
  const int fsw = q4 | ((g & 1) << 2);
  int T[NT], wa[NT];
#pragma unroll
  for (int a = 0; a < NT; ++a) {
    T[a] = NT == 4 ? odd * 3 + a : (a < 2 ? (odd ? 1 : 4) + a : (odd ? 0 : 6));
    wa[a] = 16384 + (8 * g + q4) * 256 + ((T[a] ^ fsw) << 5) + p4 * 8;
  }
  auto dmat = [&](int kt, int j) { dma16s(vt, gt + kt * 128 + j * 8192, ldsw + (kt % 3) * 49152 + j * 1024); };
  auto dma = [&](int kt, int j) {
    if (j < 2) dmat(kt, j); else c_dmaw(cw, ldsw, kt, j - 2);
  };
  auto ldB = [&](bf16x8* B, int kt, int ks) {
    const char* sb = smem + (kt % 3) * 49152 + (ks ? toff1 : toff0);
    B[0] = lds128(sb); B[1] = lds128(sb + 2048);
  };
  auto ldA = [&](int a, int kt, int ks) -> bf16x8 {
    const char* wp = smem + (kt % 3) * 49152 + wa[a < NT ? a : a - NT] + (a < NT ? 0 : 16384) + ks * 8192;
    return cat(ldstr(wp), ldstr(wp + 1024));
  };
  auto mma = [&](int a, bf16x8 A, const bf16x8* B) {
    acc[0][a] = mfma16(A, B[0], acc[0][a]);
    acc[1][a] = mfma16(A, B[1], acc[1][a]);
  };
  bf16x8 A0[2 * NT], A1[2 * NT], B0[2], B1[2];
  bf16x8 cp[4];
  u32x2 sa[4];
#pragma unroll
  for (int kt = 0; kt < 3; ++kt) { dmat(kt, 0); dmat(kt, 1); }
#pragma unroll
  for (int m = 0; m < 4; ++m)
    gld8(sa[m], (unsigned)((i >> 3) * 512 + g * 128 + (i & 7) * 8), stats + (size_t)(tok0 + tg * 32 + (m >> 1) * 16) * 64 + (m & 1) * 64);
  const int cq = min(tg * 16 + (lane >> 2), 55);
  if (NT == 3) {
#pragma unroll
    for (int m = 0; m < 4; ++m) gld16(cp[m], (unsigned)(((lane & 3) * 4 + m) * 896 + cq * 16), csb + (size_t)cb * 14336);
  }
  constexpr int X = NT == 3 ? 8 : 4;
  wait_bar<4 + X>();
  float rs[2], mr[2];
  float* csp = (float*)(smem + CSP);
  ldB(B0, 0, 0);
#pragma unroll
  for (int a = 0; a < 2 * NT; ++a) A0[a] = ldA(a, 0, 0);
#pragma unroll
  for (int kt = 0; kt < 8; ++kt) {
    ldB(B1, kt, 1);
#pragma unroll
    for (int a = 0; a < 2 * NT; ++a) {
      if (a < NT) { A1[2 * a] = ldA(2 * a, kt, 1); A1[2 * a + 1] = ldA(2 * a + 1, kt, 1); }
      mma(a, A0[a], B0);
      __builtin_amdgcn_sched_barrier(0);
    }
    if (kt == 0) wait_bar<2 + X>();
    else if (kt != 1) wait_bar<6>();
    else if (NT == 3) asm volatile("s_waitcnt vmcnt(6) lgkmcnt(0)\n\ts_barrier" : "+v"(sa[0]), "+v"(sa[1]), "+v"(sa[2]), "+v"(sa[3]), "+v"(cp[0]), "+v"(cp[1]), "+v"(cp[2]), "+v"(cp[3]) : : "memory");
    else asm volatile("s_waitcnt vmcnt(6) lgkmcnt(0)\n\ts_barrier" : "+v"(sa[0]), "+v"(sa[1]), "+v"(sa[2]), "+v"(sa[3]) : : "memory");
    if (kt < 7) ldB(B0, kt + 1, 0);
    if (kt == 1) {
#pragma unroll
      for (int tt = 0; tt < 2; ++tt) {
        const f32x2 sv0 = __builtin_bit_cast(f32x2, sa[2 * tt]), sv1 = __builtin_bit_cast(f32x2, sa[2 * tt + 1]);
        const float s1 = xsum2(sv0[0] + sv1[0]), s2 = xsum2(sv0[1] + sv1[1]);
        const float mean = s1 * (1.f / 512.f);
        const float var = fmaxf(s2 * (1.f / 512.f) - mean * mean, 0.f);
        rs[tt] = rsqrtf(var + LN_EPS); mr[tt] = mean * rs[tt];
      }
      if (NT == 3) {
        f32x4 c = (__builtin_bit_cast(f32x4, cp[0]) + __builtin_bit_cast(f32x4, cp[1])) +
                  (__builtin_bit_cast(f32x4, cp[2]) + __builtin_bit_cast(f32x4, cp[3]));
#pragma unroll
        for (int e = 0; e < 4; ++e) { c[e] += dpp_xor1(c[e]); c[e] += dpp_xor2(c[e]); }
        if ((lane & 3) == 0) *(f32x4*)(csp + cq * 4) = c;
      }
    }
#pragma unroll
    for (int a = 0; a < 2 * NT; ++a) {
      if (kt < 7 && a < NT) { A0[2 * a] = ldA(2 * a, kt + 1, 0); A0[2 * a + 1] = ldA(2 * a + 1, kt + 1, 0); }
      mma(a, A1[a], B1);
      if (a >= 2 * NT - 6) {
        const int j = a - (2 * NT - 6);
        if (kt + 3 < 8) dma(kt + 3, j);
        else d_dma(gw2, vwd, lds0, wv, (kt - 5) * 3 + (j >> 1), j & 1);
      }
      __builtin_amdgcn_sched_barrier(0);
    }
  }
  unsigned pk[2][NT][2];
#pragma unroll
  for (int tt = 0; tt < 2; ++tt) {
    char* dst = hh + ((size_t)((tok0 >> 4) + tg * 2 + tt) * 56) * 1024 + lane * 16;
#pragma unroll
    for (int a = 0; a < NT; ++a) {
      f32x4 cg = *(const f32x4*)(csp + T[a] * 16 + 4 * g);
      f32x4 cu = *(const f32x4*)(csp + (7 + T[a]) * 16 + 4 * g);
      float r[4];
#pragma unroll
      for (int e = 0; e < 4; ++e) {
        float ga = acc[tt][a][e] * rs[tt] - mr[tt] * cg[e];
        float up = acc[tt][NT + a][e] * rs[tt] - mr[tt] * cu[e];
        float sg = __builtin_amdgcn_rcpf(1.f + __builtin_amdgcn_exp2f(-1.4426950408889634f * ga));
        r[e] = ga * sg * up;
      }
      pk[tt][a][0] = pack2(r[0], r[1]); pk[tt][a][1] = pack2(r[2], r[3]);
      const int G = cb * 7 + T[a];
      if (a & 1) {
        st16<true>(dst + (size_t)(G >> 1) * 1024, u32x4{pk[tt][a - 1][0], pk[tt][a - 1][1], pk[tt][a][0], pk[tt][a][1]});
      } else if (a == NT - 1) {
        st8<true>(dst + (size_t)(G >> 1) * 1024 + (G & 1) * 512 - lane * 8, u32x2{pk[tt][a][0], pk[tt][a][1]});
      }
    }
  }
}

__global__ __launch_bounds__(512) void k_mega(const char* __restrict__ x, const char* __restrict__ wqkv, const char* __restrict__ wo,
                                              const char* __restrict__ w1, const char* __restrict__ w3,
                                              const char* __restrict__ w2, char* __restrict__ ws, char* __restrict__ out) {
  extern __shared__ __attribute__((aligned(16))) char smem[];
  char* qkv = ws;
  char* o = ws + (6u << 20);
  char* h = ws + (8u << 20);
  char* stats = ws + (10u << 20);
  u64* flags = (u64*)(ws + (10u << 20) + (512u << 10));
  char* csb = ws + (10u << 20) + (768u << 10);
  char* hh = ws + (11u << 20);
  const u64 tag = ((u64)(size_t)__builtin_amdgcn_dispatch_ptr() << 20) + llvm_dispatch_id();
  const int b = blockIdx.x;
  const unsigned lds0 = (unsigned)(size_t)(LAS char*)smem;
  const int cbG = (b >> 3) & 7, tbG = (b & 7) * 4 + (b >> 6);
  const int cbC = (b & 3) * 4 + ((b >> 3) & 3), tbC = ((b >> 2) & 1) * 8 + (b >> 5);

  unsigned cpf = 0;
  {
    u64 pc;
    unsigned len;
    asm volatile("s_getpc_b64 %0\n.Lmega_pc:\n\ts_mov_b32 %1, .Lmega_end-.Lmega_pc" : "=s"(pc), "=s"(len));
    const unsigned off = ((b >> 3) * 26 + threadIdx.x) * 64;
    if (threadIdx.x < 26 && off < len) asm volatile("global_load_dword %0, %1, %2" : "=v"(cpf) : "v"(off), "s"(pc) : "memory");
  }
  {
    const int cb = (b & 1) * 8 + ((b >> 3) & 7), tb = ((b & 7) >> 1) * 4 + (b >> 6);
    unsigned tch;
    qkv_phase(smem, lds0, x, wqkv, qkv, cb, tb, wo, w1, w3, w2, tch);
    asm volatile("s_waitcnt vmcnt(0) lgkmcnt(0)\n\ts_barrier" ::: "memory");
    asm volatile("" :: "v"(cpf), "v"(tch));
    const int tid = threadIdx.x;
    if (tid < 9) {
      const int P = cb * 3 + tid / 3, d = tid % 3, which = P >> 4, hp = P & 15, qb = tb + d;
      if (which ? qb < 16 : d == 0)
        flag_set(flags + 24576 + ((hp >> 1) | ((hp & 1) << 3) | (qb << 4)) * 32 + (which ? (which - 1) * 3 + d : 6), tag);
    }
  }
  {
    const int tid = opaque(threadIdx.x), lane = tid & 63, wv = uni(tid >> 6);
    const int c32 = lane & 31, hf = lane >> 5, q4 = (lane >> 2) & 3, p4 = lane & 3;
    const int hp = (b & 7) * 2 + ((b >> 3) & 1), qb = b >> 4;
    const int q0 = qb * 128;
    const size_t HS = 2048 * 32;
    bf16x8 Q, cw0, cw1;
    {
      const unsigned one = qb * 4 - 8 + wv >= 0 ? 0x3F803F80u : 0u;
      *(LAS u32x4*)(LAS char*)(smem + S1 + tid * 16) = u32x4{one, one, one, one};
      if (tid < 256) *(LAS u32x4*)(LAS char*)(smem + S1 + 8192 + tid * 16) = u32x4{0x3F803F80u, 0x3F803F80u, 0x3F803F80u, 0x3F803F80u};
    }
    const unsigned vwo = (lane >> 3) * 1024 + ((((lane & 7) >> 1) ^ ((lane >> 4) & 3)) << 5) + (lane & 1) * 16;
    const char* gwo = wo + (size_t)(wv * 64) * 1024 + cbG * 128;
    const CW cw1s = c_setup(w1, w3, b, wv, lane);
    auto pf = [&](int k) {
      if (k < 2) {
        const int ch = max(wv - 1, 0) * 4 + (lane & 3);
        const char* pc = (ch >= 14 ? w3 + (ch - 14) * 16 : w1 + ch * 16) + (size_t)(tbC * 32 + (lane >> 2)) * (F_ * 2) + cbC * 224;
        if (k == 0) asm volatile("global_load_dwordx4 %0, %1, off" : "=v"(cw0) : "v"(pc) : "memory");
        else asm volatile("global_load_dwordx4 %0, %1, off" : "=v"(cw1) : "v"(pc + 16 * (F_ * 2)) : "memory");
      } else if (k < 10) dma16s(vwo, gwo + (size_t)((k - 2) * 8) * 1024, lds0 + S2 + wv * 8192 + (k - 2) * 1024);
      else if (k < 14) c_dmaw(cw1s, lds0 + wv * 2048, 1, k - 10);
      else if (k == 14) {
        const int c0 = b * 8 + wv;
        dma16s(lane * 16, w2 + (size_t)(c0 < 1792 ? c0 : c0 - 1792) * 1024, lds0 + S1 + 12288 + (wv & 3) * 1024);
      }
    };
#pragma unroll
    for (int k = 0; k < PFE; ++k) pf(k);
    auto pfs = [&](int slot) {
#pragma unroll
      for (int k = PFE; k < NPF; ++k) if (((k - PFE) * PM) / 4 == slot) pf(k);
    };
    if (wv == 0) {
      const int d = lane % 3;
      flag_wait(flags + 24576 + b * 32 + (lane >= 6 ? 6 : qb - d < 0 ? 6 : lane), tag);
    }
    asm volatile("s_barrier" ::: "memory");
    {
      const int r = wv >> 1;
      const char* src = qkv + (size_t)((1 + (r >> 1)) * 32 + hp * 2 + (r & 1)) * HS;
#pragma unroll
      for (int j = 0; j < 6; ++j) {
        const int kb = qb * 4 - 8 + (wv & 1) * 6 + j;
        if (kb < 0 && r >= 2) *(LAS u32x4*)(LAS char*)(smem + r * 12288 + ((wv & 1) * 6 + j) * 1024 + lane * 16) = u32x4{0, 0, 0, 0};
        else dma16s(kb < 0 ? hf * 640 : lane * 16, src + (size_t)max(kb, 0) * 1024, lds0 + r * 12288 + ((wv & 1) * 6 + j) * 1024);
      }
    }
    const int u = wv >> 2, qg = wv & 3;
    const int qs = q0 + qg * 32;
    const unsigned vq = hf * 512 + ((c32 ^ (8 * hf)) << 4);
    gld16(Q, vq, qkv + (size_t)(hp * 2 + u) * HS + (size_t)qs * 32);
    const float NEG = -1e30f;
    f32x16 cin0, cin8;
    u32x4 mk[2];
#pragma unroll
    for (int r = 0; r < 16; ++r) {
      const int kl = (r & 3) + 8 * (r >> 2) + 4 * hf;
      cin0[r] = kl > c32 ? 0.f : NEG; cin8[r] = kl <= c32 ? 0.f : NEG;
      if (r & 1) mk[r >> 3][(r >> 1) & 3] = (kl > c32 ? 0xFFFF0000u : 0u) | (kl - 1 > c32 ? 0xFFFFu : 0u);
    }
    asm volatile("s_waitcnt vmcnt(0) lgkmcnt(0)\n\ts_barrier" : "+v"(Q), "+v"(cin0), "+v"(cin8), "+v"(mk[0]), "+v"(mk[1]), "+v"(cw0), "+v"(cw1) : : "memory");
    {
      const char* Kl = smem + u * 12288 + qg * 1024 + vq;
      const char* Vl[2];
#pragma unroll
      for (int e = 0; e < 2; ++e)
        Vl[e] = c32 < 16 ? smem + 24576 + u * 12288 + qg * 1024 + (p4 >> 1) * 512 + (((e * 8 + 4 * hf + q4) ^ (8 * (p4 >> 1))) << 4) + (p4 & 1) * 8
                         : smem + S1 + qg * 1024 + (hf ? 0 : 64);
      f32x16 z;
#pragma unroll
      for (int r = 0; r < 16; ++r) z[r] = 0.f;
      bf16x8 Kf[9];
#pragma unroll
      for (int t = 0; t < 9; ++t) Kf[t] = lds128(Kl + t * 1024);
      f32x16 sm = mfma32(Kf[0], Q, cin0);
      {
        const f32x16 s8 = mfma32(Kf[8], Q, cin8);
#pragma unroll
        for (int r = 0; r < 16; ++r) sm[r] = fmaxf(sm[r], s8[r]);
      }
      float m = sm[0];
#pragma unroll
      for (int r = 1; r < 16; ++r) m = fmaxf(m, sm[r]);
#pragma unroll
      for (int t = 1; t < 8; ++t) {
        const f32x16 s = mfma32(Kf[t], Q, z);
#pragma unroll
        for (int r = 0; r < 16; ++r) m = fmaxf(m, s[r]);
      }
      {
        auto sw = __builtin_amdgcn_permlane32_swap(__float_as_uint(m), __float_as_uint(m), false, false);
        m = fmaxf(__uint_as_float(sw[0]), __uint_as_float(sw[1]));
      }
      f32x16 cm, oacc;
#pragma unroll
      for (int r = 0; r < 16; ++r) { cm[r] = -m; oacc[r] = 0.f; }
      f32x16 sn = mfma32(Kf[1], Q, cm);
      {
        u32x4 pb[2];
#pragma unroll
        for (int j = 0; j < 8; ++j)
          pb[j >> 2][j & 3] = pack2(__builtin_amdgcn_exp2f(sm[2 * j] - m), __builtin_amdgcn_exp2f(sm[2 * j + 1] - m));
#pragma unroll
        for (int sh = 0; sh < 2; ++sh) {
          const u32x4 p0 = pb[sh] & mk[sh], p8 = pb[sh] ^ p0;
          oacc = mfma32(cat(ldstr(Vl[0] + sh * 256), ldstr(Vl[1] + sh * 256)), __builtin_bit_cast(bf16x8, p0), oacc);
          pfs(2 * sh);
          oacc = mfma32(cat(ldstr(Vl[0] + 8192 + sh * 256), ldstr(Vl[1] + 8192 + sh * 256)), __builtin_bit_cast(bf16x8, p8), oacc);
          pfs(2 * sh + 1);
        }
      }
      SB;
#pragma unroll
      for (int t = 1; t < 8; ++t) {
        const f32x16 s = sn;
        if (t < 7) sn = mfma32(Kf[t + 1], Q, cm);
#pragma unroll
        for (int sh = 0; sh < 2; ++sh) {
          u32x4 pb;
#pragma unroll
          for (int e = 0; e < 4; ++e)
            pb[e] = pack2(__builtin_amdgcn_exp2f(s[8 * sh + 2 * e]), __builtin_amdgcn_exp2f(s[8 * sh + 2 * e + 1]));
          const int vp = t * 1024 + sh * 256;
          oacc = mfma32(cat(ldstr(Vl[0] + vp), ldstr(Vl[1] + vp)), __builtin_bit_cast(bf16x8, pb), oacc);
          pfs(2 * t + sh + 2);
        }
        SB;
      }
      const float inv = __builtin_amdgcn_rcpf(oacc[8]);
      const u32x4 ov = {pack2(oacc[0] * inv, oacc[1] * inv), pack2(oacc[2] * inv, oacc[3] * inv),
                        pack2(oacc[4] * inv, oacc[5] * inv), pack2(oacc[6] * inv, oacc[7] * inv)};
      st16<true>(o + ((size_t)((qs >> 4) + (c32 >> 4)) * 16 + hp) * 1024 + ((2 * u + hf) * 16 + (c32 & 15)) * 16, ov);
    }
    asm volatile("s_waitcnt vmcnt(0) lgkmcnt(0)\n\ts_barrier" : "+v"(cw0), "+v"(cw1) : : "memory");
    if (tid < 16) flag_set(flags + (((qb >> 1) | (tid << 3) | ((qb & 1) << 7)) * 32 + hp), tag);
    {
      const CW cw = c_setup(w1, w3, b, wv, lane);
#pragma unroll
      for (int j = 0; j < 4; ++j) c_dmaw(cw, lds0 + wv * 2048, 0, j);
    }
    if (wv != 0) {
      const u32x4 ua = __builtin_bit_cast(u32x4, cw0), ub = __builtin_bit_cast(u32x4, cw1);
      f32x4 lo, hi;
#pragma unroll
      for (int e = 0; e < 2; ++e) {
        lo[2 * e] = bflo(ua[e]) + bflo(ub[e]); lo[2 * e + 1] = bfhi(ua[e]) + bfhi(ub[e]);
        hi[2 * e] = bflo(ua[2 + e]) + bflo(ub[2 + e]); hi[2 * e + 1] = bfhi(ua[2 + e]) + bfhi(ub[2 + e]);
      }
#pragma unroll
      for (int e = 0; e < 4; ++e) {
        lo[e] += dpp_ror4(lo[e]); lo[e] += dpp_ror8(lo[e]); lo[e] = xsum2(lo[e]);
        hi[e] += dpp_ror4(hi[e]); hi[e] += dpp_ror8(hi[e]); hi[e] = xsum2(hi[e]);
      }
      if (lane < 8) {
        char* d = csb + (size_t)(cbC * 16 + tbC) * 896 + ((wv - 1) * 4 + (lane & 3)) * 32 + (lane >> 2) * 16;
        st16<true>(d, __builtin_bit_cast(u32x4, lane < 4 ? lo : hi));
      }
    }
  }
  u32x4 hres;
  {
    const int tid = opaque(threadIdx.x), lane = tid & 63, wv = uni(tid >> 6);
    const int g = lane >> 4, i = lane & 15, q4 = (lane >> 2) & 3, p4 = lane & 3;
    const int kp = wv & 3, th = wv >> 2;
    bf16x8 B[4][2], rs;
    const int sw = (2 * (g & 1) + (q4 >> 1)) & 3;
    const char* wr = smem + S2 + kp * 16384 + (16 * (g >> 1) + 4 * (g & 1) + q4) * 128 + p4 * 8;
    bf16x8 A[4][4];
#pragma unroll
    for (int s = 0; s < 4; ++s)
#pragma unroll
      for (int nt = 0; nt < 4; ++nt) {
        const char* p = wr + s * 4096 + ((nt ^ sw) << 5);
        A[s][nt] = cat(ldstr(p), ldstr(p + 1024));
      }
    const size_t ooff = (size_t)(tbG * 64 + wv * 8) * 1024 + cbG * 128;
    const unsigned vo = (lane >> 3) * 1024 + (lane & 7) * 16;
    gld16(rs, vo, x + ooff);
    flag_wait(flags + b * 32 + kp * 4 + (lane & 3), tag);
    asm volatile("" : "+v"(rs));
    const char* ga = o + ((size_t)(tbG * 4 + th * 2) * 16 + kp * 4) * 1024;
#pragma unroll
    for (int s = 0; s < 4; ++s) {
      gld16(B[s][0], lane * 16, ga + s * 1024);
      gld16(B[s][1], lane * 16, ga + (16 + s) * 1024);
    }
    f32x4 acc[2][4];
#pragma unroll
    for (int t = 0; t < 2; ++t)
#pragma unroll
      for (int n = 0; n < 4; ++n) acc[t][n] = f32x4{0, 0, 0, 0};
    asm volatile("s_waitcnt vmcnt(6)" : "+v"(B[0][0]), "+v"(B[0][1]) : : "memory");
#pragma unroll
    for (int nt = 0; nt < 4; ++nt) { acc[0][nt] = mfma16(A[0][nt], B[0][0], acc[0][nt]); acc[1][nt] = mfma16(A[0][nt], B[0][1], acc[1][nt]); }
    asm volatile("s_waitcnt vmcnt(4)" : "+v"(B[1][0]), "+v"(B[1][1]) : : "memory");
#pragma unroll
    for (int nt = 0; nt < 4; ++nt) { acc[0][nt] = mfma16(A[1][nt], B[1][0], acc[0][nt]); acc[1][nt] = mfma16(A[1][nt], B[1][1], acc[1][nt]); }
    asm volatile("s_waitcnt vmcnt(2)" : "+v"(B[2][0]), "+v"(B[2][1]) : : "memory");
#pragma unroll
    for (int nt = 0; nt < 4; ++nt) { acc[0][nt] = mfma16(A[2][nt], B[2][0], acc[0][nt]); acc[1][nt] = mfma16(A[2][nt], B[2][1], acc[1][nt]); }
    asm volatile("s_waitcnt vmcnt(0)" : "+v"(B[3][0]), "+v"(B[3][1]) : : "memory");
#pragma unroll
    for (int nt = 0; nt < 4; ++nt) { acc[0][nt] = mfma16(A[3][nt], B[3][0], acc[0][nt]); acc[1][nt] = mfma16(A[3][nt], B[3][1], acc[1][nt]); }
    bar();
    if (tid >= 64 && tid < 80) flag_set(flags + 8192 + ((cbC >> 2) | ((tid & 1) << 2) | ((cbC & 3) << 3) | (((tid & 15) >> 1) << 5)) * 32 + 16 + tbC, tag);
    {
      const int mo = S2 + kp * 16384 + g * 256 + ((i ^ ((g >> 1) * 2)) << 4);
#pragma unroll
      for (int tt = 0; tt < 2; ++tt)
#pragma unroll
        for (int nt = 0; nt < 4; ++nt)
          *(LAS f32x4*)(LAS char*)(smem + (mo ^ (nt * 64)) + ((th * 2 + tt) * 4 + nt) * 1024) = acc[tt][nt];
    }
    asm volatile("s_waitcnt vmcnt(0) lgkmcnt(0)\n\ts_barrier" : "+v"(rs) : : "memory");
    {
      const int c = lane & 7;
      const int tok = (lane >> 3) + 8 * (wv & 1);
      const char* rp = smem + S2 + ((wv >> 1) * 4 + (c >> 1)) * 1024 + (c & 1) * 512 + ((tok ^ (2 * c)) << 4);
      f32x4 v0 = f32x4{0, 0, 0, 0}, v1 = f32x4{0, 0, 0, 0};
#pragma unroll
      for (int p = 0; p < 4; ++p) {
        v0 += *(const LAS f32x4*)(LAS const char*)(rp + p * 16384);
        v1 += *(const LAS f32x4*)(LAS const char*)(rp + p * 16384 + 256);
      }
      u32x4 r = __builtin_bit_cast(u32x4, rs);
      u32x4 ov = {pack2(v0[0] + bflo(r[0]), v0[1] + bfhi(r[0])), pack2(v0[2] + bflo(r[1]), v0[3] + bfhi(r[1])),
                  pack2(v1[0] + bflo(r[2]), v1[1] + bfhi(r[2])), pack2(v1[2] + bflo(r[3]), v1[3] + bfhi(r[3]))};
      st16<true>(h + ooff + vo, ov);
      hres = ov;
      float s1 = 0.f, s2 = 0.f;
#pragma unroll
      for (int e = 0; e < 4; ++e) { s1 = dot2(ov[e], 0x3F803F80u, s1); s2 = dot2(ov[e], ov[e], s2); }
#pragma unroll
      for (int m = 1; m < 8; m <<= 1) { s1 += __shfl_xor(s1, m); s2 += __shfl_xor(s2, m); }
      if (c == 0) st8<true>(stats + ((size_t)(tbG * 8 + wv) * 8 + cbG) * 64 + (lane >> 3) * 8,
                            u32x2{__float_as_uint(s1), __float_as_uint(s2)});
    }
    asm volatile("s_waitcnt vmcnt(0) lgkmcnt(0)\n\ts_barrier" ::: "memory");
    if (tid < 16) flag_set(flags + 8192 + ((tid & 3) | ((tbG >> 4) << 2) | ((tid >> 2) << 3) | (((tbG >> 1) & 7) << 5)) * 32 + (tbG & 1) * 8 + cbG, tag);
    {
      const CW cw = c_setup(w1, w3, b, wv, lane);
#pragma unroll
      for (int j = 0; j < 4; ++j) c_dmaw(cw, lds0 + wv * 2048, 2, j);
    }
    if (wv == 0) flag_wait(flags + 8192 + b * 32 + (lane & 31), tag);
    asm volatile("s_barrier" ::: "memory");
  }
  const unsigned vwd_ = 0;
  {
    const int tid = opaque(threadIdx.x), lane = tid & 63, wv = uni(tid >> 6);
    const char* gt = h + (size_t)(tbC * 128 + wv * 16) * 1024;
    const unsigned vt = (lane >> 3) * 1024 + (((lane & 7) ^ (lane >> 3)) << 4);
    const CW cw = c_setup(w1, w3, b, wv, lane);
    const unsigned vwd = (lane >> 3) * 1024 + ((((lane & 7) >> 1) ^ ((lane >> 4) & 3)) << 5) + (lane & 1) * 16;
    const char* gw2 = w2 + (size_t)(wv * 224) * 1024 + cbG * 128;
    if ((wv >> 2) == 0) c_body<4>(smem, lds0, gt, cw, vt, wv, lane, stats, csb, hh, tbC * 128, cbC, gw2, vwd);
    else c_body<3>(smem, lds0, gt, cw, vt, wv, lane, stats, csb, hh, tbC * 128, cbC, gw2, vwd);
  }
  {
    const int tid = opaque(threadIdx.x), lane = tid & 63, wv = uni(tid >> 6);
    const int g = lane >> 4, i = lane & 15, q4 = (lane >> 2) & 3, p4 = lane & 3;
    asm volatile("s_waitcnt vmcnt(0) lgkmcnt(0)\n\ts_barrier" ::: "memory");
    if (tid < 16) flag_set(flags + 16384 + ((tbC >> 1) | (tid << 3) | ((tbC & 1) << 7)) * 32 + cbC, tag);
    const unsigned vwd = (lane >> 3) * 1024 + ((((lane & 7) >> 1) ^ ((lane >> 4) & 3)) << 5) + (lane & 1) * 16;
    const char* gw2 = w2 + (size_t)(wv * 224) * 1024 + cbG * 128;
    d_dma(gw2, vwd, lds0, wv, 9, 0); d_dma(gw2, vwd, lds0, wv, 9, 1);
    const int sw = (2 * g + (q4 >> 1)) & 3;
    const int ro = (4 * g + q4) * 128 + p4 * 8;
    int rn[4];
#pragma unroll
    for (int nt = 0; nt < 4; ++nt) rn[nt] = ro + ((nt ^ sw) << 5);
    auto lda = [&](bf16x8* Af, int j) {
      const char* p0 = smem + d_unit(2 * j) + wv * d_ustr(2 * j);
      const char* p1 = smem + d_unit(2 * j + 1) + wv * d_ustr(2 * j + 1);
#pragma unroll
      for (int nt = 0; nt < 4; ++nt) Af[nt] = cat(ldstr(p0 + rn[nt]), ldstr(p1 + rn[nt]));
    };
    bf16x8 A[2][4], B[7][4];
    lda(A[0], 0); lda(A[1], 1);
    asm volatile("s_waitcnt lgkmcnt(0)" : "+v"(A[0][0]), "+v"(A[0][1]), "+v"(A[0][2]), "+v"(A[0][3]), "+v"(A[1][0]), "+v"(A[1][1]), "+v"(A[1][2]), "+v"(A[1][3]) : : "memory");
#pragma unroll
    for (int u = 10; u < 14; ++u) { d_dma(gw2, vwd, lds0, wv, u, 0); d_dma(gw2, vwd, lds0, wv, u, 1); }
    if (wv == 0) flag_wait(flags + 16384 + b * 32 + (lane & 15), tag);
    asm volatile("s_barrier" ::: "memory");
    const char* ga = hh + ((size_t)(tbG * 4) * 56 + wv * 7) * 1024;
    u32x2 Bl[4], Bh[4];
#pragma unroll
    for (int j = 0; j < 7; ++j)
#pragma unroll
      for (int tt = 0; tt < 4; ++tt) {
        if (j == 3) {
          gld8(Bl[tt], lane * 8, ga + (size_t)(tt * 56 + j) * 1024);
          gld8(Bh[tt], lane * 8, ga + (size_t)(tt * 56 + j) * 1024 + 512);
        } else gld16(B[j][tt], lane * 16, ga + (size_t)(tt * 56 + j) * 1024);
      }
    const size_t ooff = (size_t)(tbG * 64 + wv * 8) * 1024 + cbG * 128;
    const unsigned vo = (lane >> 3) * 1024 + (lane & 7) * 16;
    f32x4 acc[4][4];
#pragma unroll
    for (int t = 0; t < 4; ++t)
#pragma unroll
      for (int n = 0; n < 4; ++n) acc[t][n] = f32x4{0, 0, 0, 0};
    auto step = [&](auto J) {
      constexpr int j = decltype(J)::value;
      if constexpr (j == 3) {
        asm volatile("s_waitcnt vmcnt(12)" : "+v"(Bl[0]), "+v"(Bl[1]), "+v"(Bl[2]), "+v"(Bl[3]), "+v"(Bh[0]), "+v"(Bh[1]), "+v"(Bh[2]), "+v"(Bh[3]) : : "memory");
#pragma unroll
        for (int tt = 0; tt < 4; ++tt) B[3][tt] = __builtin_bit_cast(bf16x8, u32x4{Bl[tt][0], Bl[tt][1], Bh[tt][0], Bh[tt][1]});
      } else
        asm volatile("s_waitcnt vmcnt(%4)" : "+v"(B[j][0]), "+v"(B[j][1]), "+v"(B[j][2]), "+v"(B[j][3]) : "n"((j < 3 ? 28 : 24) - 4 * j) : "memory");
#pragma unroll
      for (int nt = 0; nt < 4; ++nt)
#pragma unroll
        for (int tt = 0; tt < 4; ++tt) acc[tt][nt] = mfma16(A[j & 1][nt], B[j][tt], acc[tt][nt]);
      __builtin_amdgcn_sched_barrier(0);
      if constexpr (j + 2 < 7) lda(A[j & 1], j + 2);
    };
    step(IC<0>{}); step(IC<1>{}); step(IC<2>{}); step(IC<3>{}); step(IC<4>{}); step(IC<5>{}); step(IC<6>{});
    {
      const int mo = wv * 6144 + g * 256 + ((i ^ ((g >> 1) * 2)) << 4);
#pragma unroll
      for (int tt = 0; tt < 4; ++tt)
#pragma unroll
        for (int nt = 0; nt < 4; ++nt) {
          const int t = tt * 4 + nt;
          const int base = t < 6 ? S2 + t * 1024 : t < 12 ? S0 + (t - 6) * 1024 : S1 + (t - 12) * 1024;
          *(LAS f32x4*)(LAS char*)(smem + (mo ^ (nt * 64)) + base) = acc[tt][nt];
        }
    }
    asm volatile("s_waitcnt vmcnt(0) lgkmcnt(0)\n\ts_barrier" ::: "memory");
    {
      const int c = lane & 7;
      const int tok = (lane >> 3) + 8 * (wv & 1);
      const int t = (wv >> 1) * 4 + (c >> 1);
      const int base = t < 6 ? S2 + t * 1024 : t < 12 ? S0 + (t - 6) * 1024 : S1 + (t - 12) * 1024;
      const char* rp = smem + base + (c & 1) * 512 + ((tok ^ (2 * c)) << 4);
      f32x4 v0 = f32x4{0, 0, 0, 0}, v1 = f32x4{0, 0, 0, 0};
#pragma unroll
      for (int p = 0; p < 8; ++p) {
        v0 += *(const LAS f32x4*)(LAS const char*)(rp + p * 6144);
        v1 += *(const LAS f32x4*)(LAS const char*)(rp + p * 6144 + 256);
      }
      const u32x4 r = hres;
      u32x4 ov = {pack2(v0[0] + bflo(r[0]), v0[1] + bfhi(r[0])), pack2(v0[2] + bflo(r[1]), v0[3] + bfhi(r[1])),
                  pack2(v1[0] + bflo(r[2]), v1[1] + bfhi(r[2])), pack2(v1[2] + bflo(r[3]), v1[3] + bfhi(r[3]))};
      st16<true>(out + ooff + vo, ov);
    }
  }
  asm volatile(".Lmega_end:");
}

extern "C" void kernel_launch(void* const* d_in, const int* in_sizes, int n_in, void* d_out, int out_size,
                              void* d_ws, size_t ws_size, hipStream_t stream) {
  const char* x = (const char*)d_in[0];
  const char* wqkv = (const char*)d_in[1];
  const char* wo = (const char*)d_in[2];
  const char* w1 = (const char*)d_in[4];
  const char* w3 = (const char*)d_in[5];
  const char* w2 = (const char*)d_in[6];
  char* ws = (char*)d_ws;
  (void)hipFuncSetAttribute((const void*)k_mega, hipFuncAttributeMaxDynamicSharedMemorySize, MG_LDS);
  k_mega<<<256, 512, MG_LDS, stream>>>(x, wqkv, wo, w1, w3, w2, ws, (char*)d_out);
}
```
